```python
import torch

def _softmax(x, axis=-1):
    return torch.softmax(x, dim=axis)
def _gelu(x):
    return torch.nn.functional.gelu(x, approximate="tanh")
def _silu(x):
    return torch.nn.functional.silu(x)
def _sigmoid(x):
    return torch.sigmoid(x)
def _layernorm(x, axis=-1, eps=1e-5):
    m = x.mean(dim=axis, keepdim=True)
    v = x.var(dim=axis, keepdim=True, unbiased=False)
    return (x - m) / torch.sqrt(v + eps)
def _scatter_add(tgt, idx, src, axis=0):
    return tgt.clone().index_add_(axis, idx, src)
def _segment_sum(src, seg, n):
    out = torch.zeros((n,) + src.shape[1:], dtype=src.dtype)
    return out.index_add_(0, seg, src)
def _pad(x, pw):
    flat = tuple(v for pair in reversed(pw) for v in pair)
    return torch.nn.functional.pad(x, flat)

kernel_name = 'concat_heads__mha_block_cached__gqa_block_cached__near_ridge'

def setup_inputs():

    _w = (lambda a: (lambda t: t.to(torch.bfloat16) if t.is_floating_point() else t)(torch.as_tensor(a)))
    def _A_setup():
        B, Sq, D = 16, 112, 448
        H, Sk, Dh = 16, 512, 256
        return {
            "x": _w((lambda *s: torch.randn(*s, dtype=torch.bfloat16) * 0.8)(B, Sq, D)),
            "k_cache": _w((lambda *s: torch.randn(*s, dtype=torch.bfloat16) * 0.8)(B, H, Sk, Dh)),
            "v_cache": _w((lambda *s: torch.randn(*s, dtype=torch.bfloat16) * 0.05)(B, H, Sk, Dh)),
            "wq": _w((lambda *s: torch.randn(*s, dtype=torch.bfloat16) * 0.8)(D, H * Dh) * (D ** -0.5)),
            "wo": _w((lambda *s: torch.randn(*s, dtype=torch.bfloat16) * 0.05)(H * Dh, D) * ((H * Dh) ** -0.5)),
        }
    _A_in = _A_setup()

    def _B_setup():
        B, Sq, D = 16, 112, 448
        Hq, Hkv, Sk, Dh = 16, 16, 512, 256
        assert Hq % Hkv == 0, (Hq, Hkv)
        return {
            "x": _w((lambda *s: torch.randn(*s, dtype=torch.bfloat16) * 0.8)(B, Sq, D)),
            "k_cache": _w((lambda *s: torch.randn(*s, dtype=torch.bfloat16) * 0.8)(B, Hkv, Sk, Dh)),
            "v_cache": _w((lambda *s: torch.randn(*s, dtype=torch.bfloat16) * 0.05)(B, Hkv, Sk, Dh)),
            "wq": _w((lambda *s: torch.randn(*s, dtype=torch.bfloat16) * 0.8)(D, Hq * Dh) * (D ** -0.5)),
            "wo": _w((lambda *s: torch.randn(*s, dtype=torch.bfloat16) * 0.05)(Hq * Dh, D) * ((Hq * Dh) ** -0.5)),
        }
    _B_in = _B_setup()

    return {
        "A_x": _A_in["x"],
        "A_k_cache": _A_in["k_cache"],
        "A_v_cache": _A_in["v_cache"],
        "A_wq": _A_in["wq"],
        "A_wo": _A_in["wo"],
        "B_x": _B_in["x"],
        "B_k_cache": _B_in["k_cache"],
        "B_v_cache": _B_in["v_cache"],
        "B_wq": _B_in["wq"],
        "B_wo": _B_in["wo"],
    }

def reference(A_x, A_k_cache, A_v_cache, A_wq, A_wo, B_x, B_k_cache, B_v_cache, B_wq, B_wo):
    def _A_ref(x, k_cache, v_cache, wq, wo):
        B, Sq, D = x.shape
        H, Dh = 16, 256
        q = (lambda x, *d: x.permute(*d))((x @ wq).reshape(B, Sq, H, Dh), 0, 2, 1, 3)
        scores = torch.einsum("bhqd,bhkd->bhqk", q, k_cache) * (Dh ** -0.5)
        p = _softmax(scores, axis=-1)
        o = torch.einsum("bhqk,bhkd->bhqd", p, v_cache)
        return (lambda x, *d: x.permute(*d))(o, 0, 2, 1, 3).reshape(B, Sq, H * Dh) @ wo

    def _B_ref(x, k_cache, v_cache, wq, wo):
        B, Sq, D = x.shape
        Hq, Dh = 16, 256
        Hkv = k_cache.shape[1]
        rep = Hq // Hkv
        q = (lambda x, *d: x.permute(*d))((x @ wq).reshape(B, Sq, Hq, Dh), 0, 2, 1, 3)
        k = (lambda x, n, axis: torch.repeat_interleave(x, n, dim=axis))(k_cache, rep, axis=1)
        v = (lambda x, n, axis: torch.repeat_interleave(x, n, dim=axis))(v_cache, rep, axis=1)
        scores = torch.einsum("bhqd,bhkd->bhqk", q, k) * (Dh ** -0.5)
        p = _softmax(scores, axis=-1)
        o = torch.einsum("bhqk,bhkd->bhqd", p, v)
        return (lambda x, *d: x.permute(*d))(o, 0, 2, 1, 3).reshape(B, Sq, Hq * Dh) @ wo

    a = _A_ref(A_x, A_k_cache, A_v_cache, A_wq, A_wo)
    b = _B_ref(B_x, B_k_cache, B_v_cache, B_wq, B_wo)
    return torch.concatenate([a, b], axis=-1)
```

```cpp
#include <hip/hip_runtime.h>
#include <hip/hip_bf16.h>
#include <stdint.h>

struct Args { const char* p[10]; char* part; char* out; };

__device__ __forceinline__ float lo(unsigned u) { return __uint_as_float(u << 16); }
__device__ __forceinline__ float hi(unsigned u) { return __uint_as_float(u & 0xffff0000u); }
__device__ __forceinline__ unsigned pk(float a, float b) { unsigned r; asm("v_cvt_pk_bf16_f32 %0, %1, %2" : "=v"(r) : "v"(a), "v"(b)); return r; }

typedef unsigned u4 __attribute__((ext_vector_type(4)));
typedef unsigned u2 __attribute__((ext_vector_type(2)));
__device__ __forceinline__ u4 sum8(const u4* r) {
  float s[8];
#pragma unroll
  for (int i = 0; i < 8; i++) s[i] = 0.f;
#pragma unroll
  for (int h = 0; h < 8; h++) {
    s[0] += lo(r[h].x); s[1] += hi(r[h].x); s[2] += lo(r[h].y); s[3] += hi(r[h].y);
    s[4] += lo(r[h].z); s[5] += hi(r[h].z); s[6] += lo(r[h].w); s[7] += hi(r[h].w);
  }
  const u4 o = {pk(s[0], s[1]), pk(s[2], s[3]), pk(s[4], s[5]), pk(s[6], s[7])};
  return o;
}
__device__ __forceinline__ u2 where(unsigned e) {
  const unsigned w = e / 1568, f = e % 1568;
  const unsigned g = f - 896, t = g / 96, q = g % 96, l = (q - 64) * 2;
  const unsigned a = ((f >> 7) * 16 + (f & 15)) * 1792 + (w * 64 + ((f >> 6) & 1) * 32 + ((f & 63) >> 4) * 4) * 2;
  const unsigned b = (t * 16 + (q & 15)) * 1792 + (256 + w * 48 + (q >> 4) * 4) * 2;
  const unsigned c = (t * 16 + (l & 15)) * 1792 + (288 + w * 48 + (l >> 4) * 4) * 2;
  const unsigned o = f < 896 ? a : (q < 64 ? b : c);
  const u2 r = {o, o + ((f < 896 || q < 64) ? 32u : 1792u)};
  return r;
}

__global__ __launch_bounds__(256, 1) void attn_fused(Args a) {
  __shared__ __attribute__((aligned(1024))) char lds[163840];
  unsigned bid = blockIdx.x;
  asm volatile("s_getpc_b64 s[72:73]\nv_and_b32 v101, 63, %1\nv_lshlrev_b32 v100, 7, v101\nglobal_load_dword v104, v100, s[72:73]\n"
               "s_lshr_b32 s74, %0, 3\nv_lshl_add_u32 v101, s74, 8, %1\nv_lshlrev_b32 v101, 5, v101\nv_min_u32 v101, 150*1024, v101\nglobal_load_dword v105, v101, s[72:73]\n"
               : "+s"(bid) : "v"(threadIdx.x) : "s72", "s73", "s74", "scc", "v100", "v101", "v104", "v105");
  const unsigned xcd = bid & 7, j = bid >> 3;
  const unsigned br = xcd >> 2, hp = (xcd & 3) * 2 + (j & 1), b = j >> 1;
  const unsigned w = __builtin_amdgcn_readfirstlane(threadIdx.x >> 6);
  const char* x  = (br ? a.p[5] : a.p[0]) + (size_t)b * 100352;
  const char* k  = (br ? a.p[6] : a.p[1]) + (size_t)(b * 16 + hp * 2) * 262144;
  const char* v  = (br ? a.p[7] : a.p[2]) + (size_t)(b * 16 + hp * 2) * 262144;
  const char* wq = (br ? a.p[8] : a.p[3]) + hp * 1024;
  const char* wo = (br ? a.p[9] : a.p[4]) + (size_t)hp * 458752;
  char* part = a.part + (size_t)((br * 16 + b) * 8 + hp) * 100352;
  const unsigned won = 3670016u - hp * 458752u;
  char* flags = a.part + 33554432 + (br * 16 + b) * 2048;
  char* ob = a.out + (size_t)b * 200704 + br * 896;
  asm volatile(
"s_bitcmp1_b32 %10,3\ns_cbranch_scc1 77f\n"
"s_mov_b32 s70,%6\nv_mbcnt_lo_u32_b32 v0,-1,0\nv_mbcnt_hi_u32_b32 v0,-1,v0\nv_lshlrev_b32 v239,4,v0\ns_mov_b64 s[40:41],%0\ns_mov_b64 s[44:45],%1\ns_mov_b64 s[48:49],%2\ns_mov_b64 s[52:53],%3\n"
"s_mov_b64 s[56:57],%4\ns_mov_b64 s[60:61],%5\ns_mov_b32 s42,100352\ns_mov_b32 s46,3662848\ns_mov_b32 s50,524288\ns_mov_b32 s54,524288\ns_mov_b32 s58,%7\ns_mov_b32 s62,100352\n"
"s_mov_b32 s43,0x20000\ns_mov_b32 s47,0x20000\ns_mov_b32 s51,0x20000\ns_mov_b32 s55,0x20000\ns_mov_b32 s59,0x20000\ns_mov_b32 s63,0x20000\ns_mov_b64 s[96:97],%13\ns_mov_b32 s98,2048\n"
"s_mov_b32 s99,0x20000\ns_mul_i32 s64,s70,24576\ns_add_u32 s64,s64,65536\ns_lshl_b32 s65,s70,12\ns_cmp_lt_u32 s70,3\ns_cselect_b32 s67,14336,0\ns_cselect_b32 s66,2048,0\ns_add_u32 s66,s66,s65\n"
"s_lshl_b32 s68,s70,5\ns_sub_u32 s68,512,s68\ns_mov_b32 s69,0x3db8aa3b\ns_mov_b32 s35,0x3f803f80\ns_mul_i32 s71,s70,25088\nv_lshrrev_b32 v1,3,v0\nv_and_b32 v2,7,v0\nv_lshrrev_b32 v4,1,v1\n"
"v_xor_b32 v5,v2,v4\nv_lshl_add_u32 v6,s70,5,v1\nv_mul_u32_u24 v6,896,v6\nv_lshl_add_u32 v229,v5,4,v6\nv_xor_b32 v230,64,v229\nv_and_b32 v3,4,v1\nv_add_u32 v3,v3,v1\nv_lshlrev_b32 v6,1,v4\n"
"v_xor_b32 v6,v2,v6\nv_lshlrev_b32 v6,4,v6\nv_lshl_add_u32 v6,s70,7,v6\nv_lshl_add_u32 v224,v3,13,v6\nv_lshl_add_u32 v225,v3,9,v6\nv_mul_u32_u24 v7,896,v1\nv_add_u32 v226,v7,v6\ns_lshr_b32 s74,s70,1\n"
"s_and_b32 s75,s70,1\nv_lshl_add_u32 v7,s74,5,v3\nv_lshl_add_u32 v7,s75,2,v7\nv_lshlrev_b32 v7,9,v7\nv_lshl_add_u32 v227,v5,4,v7\nv_xor_b32 v228,64,v227\nv_lshl_add_u32 v240,s74,10,v239\nv_lshl_add_u32 v240,s75,3,v240\n"
"v_and_b32 v1,15,v0\nv_lshrrev_b32 v2,4,v0\nv_lshrrev_b32 v3,1,v1\nv_xor_b32 v3,v3,v2\nv_lshlrev_b32 v3,4,v3\nv_lshl_add_u32 v237,v1,7,v3\nv_xor_b32 v238,64,v237\nv_add_u32 v235,s64,v237\n"
"v_add_u32 v236,s64,v238\nv_bfe_u32 v1,v0,2,3\nv_lshrrev_b32 v2,5,v0\nv_lshlrev_b32 v2,11,v2\nv_lshl_add_u32 v2,v1,7,v2\nv_lshrrev_b32 v1,1,v1\nv_lshl_add_u32 v2,v1,5,v2\nv_and_b32 v1,3,v0\n"
"v_lshl_add_u32 v2,v1,3,v2\nv_add_u32 v231,s64,v2\nv_xor_b32 v232,32,v231\nv_xor_b32 v233,64,v231\nv_xor_b32 v234,0x60,v231\ns_add_u32 m0,s65,0\ns_mov_b32 s73,0\nbuffer_load_dwordx4 v229,s[40:43],s73 offen offset:0 lds\n"
"s_mov_b32 s74,6144\nbuffer_load_dwordx4 v230,s[40:43],s74 offen offset:1024 lds\ns_add_u32 m0,s66,0\ns_add_u32 s75,s67,0\nbuffer_load_dwordx4 v229,s[40:43],s75 offen offset:0 lds\ns_add_u32 s72,s67,6144\nbuffer_load_dwordx4 v230,s[40:43],s72 offen offset:1024 lds\ns_add_u32 m0,s64,0\n"
"s_mov_b32 s73,0\nbuffer_load_dwordx4 v224,s[44:47],s73 offen offset:0 lds\ns_mov_b32 s74,31744\nbuffer_load_dwordx4 v224,s[44:47],s74 offen offset:1024 lds\ns_mov_b32 s75,129024\nbuffer_load_dwordx4 v224,s[44:47],s75 offen offset:2048 lds\ns_mov_b32 s72,160768\nbuffer_load_dwordx4 v224,s[44:47],s72 offen offset:3072 lds\n"
"v_min_u32 v1,55,v0\nv_lshrrev_b32 v2,3,v1\nv_and_b32 v1,7,v1\nv_lshlrev_b32 v1,7,v1\nv_lshl_add_u32 v1,v2,13,v1\ns_mul_i32 s72,%10,57344\nbuffer_load_dword v255,v1,s[44:47],s72 offen offset:0\ns_add_u32 m0,s64,4096\n"
"s_mov_b32 s73,262144\nbuffer_load_dwordx4 v224,s[44:47],s73 offen offset:0 lds\ns_mov_b32 s74,293888\nbuffer_load_dwordx4 v224,s[44:47],s74 offen offset:1024 lds\ns_mov_b32 s75,391168\nbuffer_load_dwordx4 v224,s[44:47],s75 offen offset:2048 lds\ns_mov_b32 s72,422912\nbuffer_load_dwordx4 v224,s[44:47],s72 offen offset:3072 lds\n"
"s_add_u32 m0,s65,14336\ns_mov_b32 s73,128\nbuffer_load_dwordx4 v229,s[40:43],s73 offen offset:0 lds\ns_mov_b32 s74,6272\nbuffer_load_dwordx4 v230,s[40:43],s74 offen offset:1024 lds\ns_add_u32 m0,s66,14336\ns_add_u32 s75,s67,128\nbuffer_load_dwordx4 v229,s[40:43],s75 offen offset:0 lds\n"
"s_add_u32 s72,s67,6272\nbuffer_load_dwordx4 v230,s[40:43],s72 offen offset:1024 lds\ns_add_u32 m0,s64,8192\ns_mov_b32 s73,524288\nbuffer_load_dwordx4 v224,s[44:47],s73 offen offset:0 lds\ns_mov_b32 s74,556032\nbuffer_load_dwordx4 v224,s[44:47],s74 offen offset:1024 lds\ns_mov_b32 s75,653312\n"
"buffer_load_dwordx4 v224,s[44:47],s75 offen offset:2048 lds\ns_mov_b32 s72,685056\nbuffer_load_dwordx4 v224,s[44:47],s72 offen offset:3072 lds\ns_add_u32 m0,s65,28672\ns_mov_b32 s73,256\nbuffer_load_dwordx4 v229,s[40:43],s73 offen offset:0 lds\ns_mov_b32 s74,6400\nbuffer_load_dwordx4 v230,s[40:43],s74 offen offset:1024 lds\n"
"s_add_u32 m0,s66,28672\ns_add_u32 s75,s67,256\nbuffer_load_dwordx4 v229,s[40:43],s75 offen offset:0 lds\ns_add_u32 s72,s67,6400\nbuffer_load_dwordx4 v230,s[40:43],s72 offen offset:1024 lds\ns_add_u32 m0,s64,12288\ns_mov_b32 s73,786432\nbuffer_load_dwordx4 v224,s[44:47],s73 offen offset:0 lds\n"
"s_mov_b32 s74,818176\nbuffer_load_dwordx4 v224,s[44:47],s74 offen offset:1024 lds\ns_mov_b32 s75,915456\nbuffer_load_dwordx4 v224,s[44:47],s75 offen offset:2048 lds\ns_mov_b32 s72,947200\nbuffer_load_dwordx4 v224,s[44:47],s72 offen offset:3072 lds\ns_add_u32 m0,s65,43008\ns_mov_b32 s73,384\n"
"buffer_load_dwordx4 v229,s[40:43],s73 offen offset:0 lds\ns_mov_b32 s74,6528\nbuffer_load_dwordx4 v230,s[40:43],s74 offen offset:1024 lds\ns_add_u32 m0,s66,43008\ns_add_u32 s75,s67,384\nbuffer_load_dwordx4 v229,s[40:43],s75 offen offset:0 lds\ns_add_u32 s72,s67,6528\nbuffer_load_dwordx4 v230,s[40:43],s72 offen offset:1024 lds\n"
"v_lshlrev_b32 v1,7,v0\ns_mul_i32 s72,%10,7168\nbuffer_load_dword v255,v1,s[56:59],s72 offen offset:0\ns_waitcnt vmcnt(26)\nds_read_b64_tr_b16 a[196:197],v231 offset:0\nds_read_b64_tr_b16 a[198:199],v231 offset:1024\nds_read_b64_tr_b16 a[200:201],v232 offset:0\nds_read_b64_tr_b16 a[202:203],v232 offset:1024\n"
"ds_read_b64_tr_b16 a[204:205],v233 offset:0\nds_read_b64_tr_b16 a[206:207],v233 offset:1024\nds_read_b64_tr_b16 a[208:209],v234 offset:0\nds_read_b64_tr_b16 a[210:211],v234 offset:1024\ns_barrier\nds_read_b128 a[228:231],v237 offset:0\nds_read_b128 a[232:235],v237 offset:2048\nds_read_b128 a[236:239],v237 offset:4096\n"
"ds_read_b128 a[240:243],v237 offset:6144\nds_read_b128 a[244:247],v237 offset:8192\nds_read_b128 a[248:251],v237 offset:10240\nds_read_b128 a[252:255],v237 offset:12288\ns_waitcnt lgkmcnt(6)\nv_mfma_f32_16x16x32_bf16 v[0:3],a[196:199],a[228:231],0\ns_waitcnt vmcnt(21)\nds_read_b64_tr_b16 a[212:213],v231 offset:4096\n"
"v_mfma_f32_16x16x32_bf16 v[28:31],a[200:203],a[228:231],0\ns_add_u32 m0,s64,16384\nv_lshl_add_u32 v252,s70,11,v239\nv_lshl_add_u32 v253,s70,11,v237\nv_mfma_f32_16x16x32_bf16 v[56:59],a[204:207],a[228:231],0\nds_read_b64_tr_b16 a[214:215],v231 offset:5120\nv_lshl_add_u32 v254,s70,11,v238\nv_mfma_f32_16x16x32_bf16 v[84:87],a[208:211],a[228:231],0\n"
"ds_read_b128 a[228:231],v238 offset:0\ns_waitcnt lgkmcnt(8)\nv_mfma_f32_16x16x32_bf16 v[4:7],a[196:199],a[232:235],0\ns_mov_b32 s73,1048576\nbuffer_load_dwordx4 v224,s[44:47],s73 offen offset:0 lds\nv_mfma_f32_16x16x32_bf16 v[32:35],a[200:203],a[232:235],0\nds_read_b64_tr_b16 a[216:217],v232 offset:4096\ns_mov_b32 s74,1080320\n"
"v_mfma_f32_16x16x32_bf16 v[60:63],a[204:207],a[232:235],0\nv_mfma_f32_16x16x32_bf16 v[88:91],a[208:211],a[232:235],0\nds_read_b128 a[232:235],v238 offset:2048\ns_waitcnt lgkmcnt(9)\nv_mfma_f32_16x16x32_bf16 v[8:11],a[196:199],a[236:239],0\nbuffer_load_dwordx4 v224,s[44:47],s74 offen offset:1024 lds\ns_mov_b32 s75,1177600\nv_mfma_f32_16x16x32_bf16 v[36:39],a[200:203],a[236:239],0\n"
"ds_read_b64_tr_b16 a[218:219],v232 offset:5120\nv_mfma_f32_16x16x32_bf16 v[64:67],a[204:207],a[236:239],0\nv_mfma_f32_16x16x32_bf16 v[92:95],a[208:211],a[236:239],0\nds_read_b128 a[236:239],v238 offset:4096\ns_waitcnt lgkmcnt(9)\nv_mfma_f32_16x16x32_bf16 v[12:15],a[196:199],a[240:243],0\nbuffer_load_dwordx4 v224,s[44:47],s75 offen offset:2048 lds\ns_mov_b32 s72,1209344\n"
"v_mfma_f32_16x16x32_bf16 v[40:43],a[200:203],a[240:243],0\nds_read_b64_tr_b16 a[220:221],v233 offset:4096\nv_mfma_f32_16x16x32_bf16 v[68:71],a[204:207],a[240:243],0\nds_read_b64_tr_b16 a[222:223],v233 offset:5120\nv_mfma_f32_16x16x32_bf16 v[96:99],a[208:211],a[240:243],0\nds_read_b128 a[240:243],v238 offset:6144\nv_mfma_f32_16x16x32_bf16 v[16:19],a[196:199],a[244:247],0\nbuffer_load_dwordx4 v224,s[44:47],s72 offen offset:3072 lds\n"
"s_add_u32 m0,s64,20480\nv_mfma_f32_16x16x32_bf16 v[44:47],a[200:203],a[244:247],0\nds_read_b64_tr_b16 a[224:225],v234 offset:4096\ns_mov_b32 s73,1310720\nv_mfma_f32_16x16x32_bf16 v[72:75],a[204:207],a[244:247],0\nv_mfma_f32_16x16x32_bf16 v[100:103],a[208:211],a[244:247],0\nds_read_b128 a[244:247],v238 offset:8192\ns_waitcnt lgkmcnt(12)\n"
"v_mfma_f32_16x16x32_bf16 v[20:23],a[196:199],a[248:251],0\nbuffer_load_dwordx4 v224,s[44:47],s73 offen offset:0 lds\ns_mov_b32 s74,1342464\nv_mfma_f32_16x16x32_bf16 v[48:51],a[200:203],a[248:251],0\nds_read_b64_tr_b16 a[226:227],v234 offset:5120\nv_mfma_f32_16x16x32_bf16 v[76:79],a[204:207],a[248:251],0\nv_mfma_f32_16x16x32_bf16 v[104:107],a[208:211],a[248:251],0\nds_read_b128 a[248:251],v238 offset:10240\n"
"v_mfma_f32_16x16x32_bf16 v[24:27],a[196:199],a[252:255],0\nbuffer_load_dwordx4 v224,s[44:47],s74 offen offset:1024 lds\ns_mov_b32 s75,1439744\nv_mfma_f32_16x16x32_bf16 v[52:55],a[200:203],a[252:255],0\nv_mfma_f32_16x16x32_bf16 v[80:83],a[204:207],a[252:255],0\nv_mfma_f32_16x16x32_bf16 v[108:111],a[208:211],a[252:255],0\nds_read_b128 a[252:255],v238 offset:12288\ns_waitcnt lgkmcnt(2)\n"
"v_mfma_f32_16x16x32_bf16 v[0:3],a[212:215],a[228:231],v[0:3]\nbuffer_load_dwordx4 v224,s[44:47],s75 offen offset:2048 lds\nv_mov_b32 v250,0xff800000\nv_mfma_f32_16x16x32_bf16 v[28:31],a[216:219],a[228:231],v[28:31]\ns_waitcnt vmcnt(20)\nds_read_b64_tr_b16 a[196:197],v231 offset:8192\nv_mfma_f32_16x16x32_bf16 v[56:59],a[220:223],a[228:231],v[56:59]\nds_read_b64_tr_b16 a[198:199],v231 offset:9216\n"
"v_lshrrev_b32 v248,2,v239\nv_mfma_f32_16x16x32_bf16 v[84:87],a[224:227],a[228:231],v[84:87]\ns_waitcnt lgkmcnt(2)\ns_barrier\nds_read_b128 a[228:231],v237 offset:14336\nv_mfma_f32_16x16x32_bf16 v[4:7],a[212:215],a[232:235],v[4:7]\ns_mov_b32 s72,1471488\nbuffer_load_dwordx4 v224,s[44:47],s72 offen offset:3072 lds\n"
"v_mfma_f32_16x16x32_bf16 v[32:35],a[216:219],a[232:235],v[32:35]\nds_read_b64_tr_b16 a[200:201],v232 offset:8192\nv_lshrrev_b32 v242,9,v239\nv_mfma_f32_16x16x32_bf16 v[60:63],a[220:223],a[232:235],v[60:63]\ns_add_u32 m0,s65,0\nv_and_b32 v241,0xf0,v239\nv_lshl_or_b32 v241,v242,8,v241\nv_mfma_f32_16x16x32_bf16 v[88:91],a[224:227],a[232:235],v[88:91]\n"
"ds_read_b128 a[232:235],v237 offset:16384\nv_bfe_u32 v242,v239,8,1\nv_mfma_f32_16x16x32_bf16 v[8:11],a[212:215],a[236:239],v[8:11]\ns_mov_b32 s73,512\nbuffer_load_dwordx4 v229,s[40:43],s73 offen offset:0 lds\nv_mfma_f32_16x16x32_bf16 v[36:39],a[216:219],a[236:239],v[36:39]\nds_read_b64_tr_b16 a[202:203],v232 offset:9216\nv_lshl_or_b32 v241,v242,3,v241\n"
"v_mfma_f32_16x16x32_bf16 v[64:67],a[220:223],a[236:239],v[64:67]\ns_mov_b32 s74,6656\nv_mov_b32 v242,14336\nv_mad_u32_u24 v241,v242,s70,v241\nv_mfma_f32_16x16x32_bf16 v[92:95],a[224:227],a[236:239],v[92:95]\nds_read_b128 a[236:239],v237 offset:18432\nv_mfma_f32_16x16x32_bf16 v[12:15],a[212:215],a[240:243],v[12:15]\nbuffer_load_dwordx4 v230,s[40:43],s74 offen offset:1024 lds\n"
"s_add_u32 m0,s66,0\nv_mfma_f32_16x16x32_bf16 v[40:43],a[216:219],a[240:243],v[40:43]\nds_read_b64_tr_b16 a[204:205],v233 offset:8192\ns_add_u32 s75,s67,512\nv_mfma_f32_16x16x32_bf16 v[68:71],a[220:223],a[240:243],v[68:71]\nds_read_b64_tr_b16 a[206:207],v233 offset:9216\nv_mfma_f32_16x16x32_bf16 v[96:99],a[224:227],a[240:243],v[96:99]\nds_read_b128 a[240:243],v237 offset:20480\n"
"v_mfma_f32_16x16x32_bf16 v[16:19],a[212:215],a[244:247],v[16:19]\nbuffer_load_dwordx4 v229,s[40:43],s75 offen offset:0 lds\ns_add_u32 s72,s67,6656\nv_mfma_f32_16x16x32_bf16 v[44:47],a[216:219],a[244:247],v[44:47]\nds_read_b64_tr_b16 a[208:209],v234 offset:8192\nv_mfma_f32_16x16x32_bf16 v[72:75],a[220:223],a[244:247],v[72:75]\nv_mfma_f32_16x16x32_bf16 v[100:103],a[224:227],a[244:247],v[100:103]\nds_read_b128 a[244:247],v237 offset:22528\n"
"v_mfma_f32_16x16x32_bf16 v[20:23],a[212:215],a[248:251],v[20:23]\nbuffer_load_dwordx4 v230,s[40:43],s72 offen offset:1024 lds\ns_add_u32 m0,s64,0\nv_mfma_f32_16x16x32_bf16 v[48:51],a[216:219],a[248:251],v[48:51]\nds_read_b64_tr_b16 a[210:211],v234 offset:9216\ns_mov_b32 s73,1572864\nv_mfma_f32_16x16x32_bf16 v[76:79],a[220:223],a[248:251],v[76:79]\nv_mfma_f32_16x16x32_bf16 v[104:107],a[224:227],a[248:251],v[104:107]\n"
"ds_read_b128 a[248:251],v237 offset:24576\nv_mfma_f32_16x16x32_bf16 v[24:27],a[212:215],a[252:255],v[24:27]\nbuffer_load_dwordx4 v224,s[44:47],s73 offen offset:0 lds\ns_mov_b32 s74,1604608\nv_mfma_f32_16x16x32_bf16 v[52:55],a[216:219],a[252:255],v[52:55]\nv_mfma_f32_16x16x32_bf16 v[80:83],a[220:223],a[252:255],v[80:83]\nv_mfma_f32_16x16x32_bf16 v[108:111],a[224:227],a[252:255],v[108:111]\nds_read_b128 a[252:255],v237 offset:26624\n"
"s_waitcnt lgkmcnt(2)\nv_mfma_f32_16x16x32_bf16 v[0:3],a[196:199],a[228:231],v[0:3]\nbuffer_load_dwordx4 v224,s[44:47],s74 offen offset:1024 lds\nv_lshrrev_b32 v244,2,v239\nv_mfma_f32_16x16x32_bf16 v[28:31],a[200:203],a[228:231],v[28:31]\ns_waitcnt vmcnt(19)\nds_read_b64_tr_b16 a[212:213],v231 offset:12288\nv_mfma_f32_16x16x32_bf16 v[56:59],a[204:207],a[228:231],v[56:59]\n"
"ds_read_b64_tr_b16 a[214:215],v231 offset:13312\nv_lshlrev_b32 v245,3,v239\nv_mfma_f32_16x16x32_bf16 v[84:87],a[208:211],a[228:231],v[84:87]\nds_read_b128 a[228:231],v238 offset:14336\nv_lshl_add_u32 v244,%12,7,v244\nv_mfma_f32_16x16x32_bf16 v[4:7],a[196:199],a[232:235],v[4:7]\ns_mov_b32 s75,1701888\nbuffer_load_dwordx4 v224,s[44:47],s75 offen offset:2048 lds\n"
"v_mfma_f32_16x16x32_bf16 v[32:35],a[200:203],a[232:235],v[32:35]\nds_read_b64_tr_b16 a[216:217],v232 offset:12288\nv_lshl_add_u32 v245,%12,2,v245\nv_mfma_f32_16x16x32_bf16 v[60:63],a[204:207],a[232:235],v[60:63]\ns_mov_b32 s72,1733632\nv_lshrrev_b32 v246,7,v239\nv_add_u32 v246,s70,v246\nv_mfma_f32_16x16x32_bf16 v[88:91],a[208:211],a[232:235],v[88:91]\n"
"ds_read_b128 a[232:235],v238 offset:16384\nv_lshl_add_u32 v244,v246,16,v244\nv_mfma_f32_16x16x32_bf16 v[8:11],a[196:199],a[236:239],v[8:11]\nbuffer_load_dwordx4 v224,s[44:47],s72 offen offset:3072 lds\nv_lshl_add_u32 v245,v246,16,v245\nv_mfma_f32_16x16x32_bf16 v[36:39],a[200:203],a[236:239],v[36:39]\nds_read_b64_tr_b16 a[218:219],v232 offset:13312\ns_add_u32 m0,s64,4096\n"
"v_mfma_f32_16x16x32_bf16 v[64:67],a[204:207],a[236:239],v[64:67]\ns_mov_b32 s73,1835008\nbuffer_load_dword v244,v244,s[96:99],0 offen offset:1024 sc1\nv_mfma_f32_16x16x32_bf16 v[92:95],a[208:211],a[236:239],v[92:95]\nds_read_b128 a[236:239],v238 offset:18432\nv_mfma_f32_16x16x32_bf16 v[12:15],a[196:199],a[240:243],v[12:15]\nbuffer_load_dwordx4 v224,s[44:47],s73 offen offset:0 lds\ns_mov_b32 s74,1866752\n"
"v_mfma_f32_16x16x32_bf16 v[40:43],a[200:203],a[240:243],v[40:43]\nds_read_b64_tr_b16 a[220:221],v233 offset:12288\nv_mfma_f32_16x16x32_bf16 v[68:71],a[204:207],a[240:243],v[68:71]\nds_read_b64_tr_b16 a[222:223],v233 offset:13312\nv_mfma_f32_16x16x32_bf16 v[96:99],a[208:211],a[240:243],v[96:99]\nds_read_b128 a[240:243],v238 offset:20480\nv_mfma_f32_16x16x32_bf16 v[16:19],a[196:199],a[244:247],v[16:19]\nbuffer_load_dwordx4 v224,s[44:47],s74 offen offset:1024 lds\n"
"s_mov_b32 s75,1964032\nv_mfma_f32_16x16x32_bf16 v[44:47],a[200:203],a[244:247],v[44:47]\nds_read_b64_tr_b16 a[224:225],v234 offset:12288\nv_mfma_f32_16x16x32_bf16 v[72:75],a[204:207],a[244:247],v[72:75]\nv_mfma_f32_16x16x32_bf16 v[100:103],a[208:211],a[244:247],v[100:103]\nds_read_b128 a[244:247],v238 offset:22528\ns_waitcnt lgkmcnt(12)\nv_mfma_f32_16x16x32_bf16 v[20:23],a[196:199],a[248:251],v[20:23]\n"
"buffer_load_dwordx4 v224,s[44:47],s75 offen offset:2048 lds\ns_mov_b32 s72,1995776\nv_mfma_f32_16x16x32_bf16 v[48:51],a[200:203],a[248:251],v[48:51]\nds_read_b64_tr_b16 a[226:227],v234 offset:13312\nv_mfma_f32_16x16x32_bf16 v[76:79],a[204:207],a[248:251],v[76:79]\nv_mfma_f32_16x16x32_bf16 v[104:107],a[208:211],a[248:251],v[104:107]\nds_read_b128 a[248:251],v238 offset:24576\nv_mfma_f32_16x16x32_bf16 v[24:27],a[196:199],a[252:255],v[24:27]\n"
"buffer_load_dwordx4 v224,s[44:47],s72 offen offset:3072 lds\ns_add_u32 m0,s64,8192\nv_mfma_f32_16x16x32_bf16 v[52:55],a[200:203],a[252:255],v[52:55]\ns_mov_b32 s73,2097152\nv_mfma_f32_16x16x32_bf16 v[80:83],a[204:207],a[252:255],v[80:83]\nv_mfma_f32_16x16x32_bf16 v[108:111],a[208:211],a[252:255],v[108:111]\nds_read_b128 a[252:255],v238 offset:26624\ns_waitcnt lgkmcnt(2)\n"
"v_mfma_f32_16x16x32_bf16 v[0:3],a[212:215],a[228:231],v[0:3]\nbuffer_load_dwordx4 v224,s[44:47],s73 offen offset:0 lds\ns_mov_b32 s74,2128896\nv_mfma_f32_16x16x32_bf16 v[28:31],a[216:219],a[228:231],v[28:31]\ns_waitcnt vmcnt(18)\nds_read_b64_tr_b16 a[196:197],v231 offset:16384\nv_mfma_f32_16x16x32_bf16 v[56:59],a[220:223],a[228:231],v[56:59]\nds_read_b64_tr_b16 a[198:199],v231 offset:17408\n"
"v_mfma_f32_16x16x32_bf16 v[84:87],a[224:227],a[228:231],v[84:87]\ns_waitcnt lgkmcnt(2)\ns_barrier\nds_read_b128 a[228:231],v237 offset:28672\nv_mfma_f32_16x16x32_bf16 v[4:7],a[212:215],a[232:235],v[4:7]\nbuffer_load_dwordx4 v224,s[44:47],s74 offen offset:1024 lds\ns_add_u32 m0,s65,14336\nv_mfma_f32_16x16x32_bf16 v[32:35],a[216:219],a[232:235],v[32:35]\n"
"ds_read_b64_tr_b16 a[200:201],v232 offset:16384\ns_mov_b32 s75,640\nv_mfma_f32_16x16x32_bf16 v[60:63],a[220:223],a[232:235],v[60:63]\nv_mfma_f32_16x16x32_bf16 v[88:91],a[224:227],a[232:235],v[88:91]\nds_read_b128 a[232:235],v237 offset:30720\nv_mfma_f32_16x16x32_bf16 v[8:11],a[212:215],a[236:239],v[8:11]\nbuffer_load_dwordx4 v229,s[40:43],s75 offen offset:0 lds\ns_mov_b32 s72,6784\n"
"v_mfma_f32_16x16x32_bf16 v[36:39],a[216:219],a[236:239],v[36:39]\nds_read_b64_tr_b16 a[202:203],v232 offset:17408\nv_mfma_f32_16x16x32_bf16 v[64:67],a[220:223],a[236:239],v[64:67]\nv_mfma_f32_16x16x32_bf16 v[92:95],a[224:227],a[236:239],v[92:95]\nds_read_b128 a[236:239],v237 offset:32768\nv_mfma_f32_16x16x32_bf16 v[12:15],a[212:215],a[240:243],v[12:15]\nbuffer_load_dwordx4 v230,s[40:43],s72 offen offset:1024 lds\ns_add_u32 m0,s66,14336\n"
"v_mfma_f32_16x16x32_bf16 v[40:43],a[216:219],a[240:243],v[40:43]\nds_read_b64_tr_b16 a[204:205],v233 offset:16384\ns_add_u32 s73,s67,640\nv_mfma_f32_16x16x32_bf16 v[68:71],a[220:223],a[240:243],v[68:71]\nds_read_b64_tr_b16 a[206:207],v233 offset:17408\nv_mfma_f32_16x16x32_bf16 v[96:99],a[224:227],a[240:243],v[96:99]\nds_read_b128 a[240:243],v237 offset:34816\nv_mfma_f32_16x16x32_bf16 v[16:19],a[212:215],a[244:247],v[16:19]\n"
"buffer_load_dwordx4 v229,s[40:43],s73 offen offset:0 lds\ns_add_u32 s74,s67,6784\nv_mfma_f32_16x16x32_bf16 v[44:47],a[216:219],a[244:247],v[44:47]\nds_read_b64_tr_b16 a[208:209],v234 offset:16384\nv_mfma_f32_16x16x32_bf16 v[72:75],a[220:223],a[244:247],v[72:75]\nv_mfma_f32_16x16x32_bf16 v[100:103],a[224:227],a[244:247],v[100:103]\nds_read_b128 a[244:247],v237 offset:36864\nv_mfma_f32_16x16x32_bf16 v[20:23],a[212:215],a[248:251],v[20:23]\n"
"buffer_load_dwordx4 v230,s[40:43],s74 offen offset:1024 lds\ns_add_u32 m0,s64,8192\nv_mfma_f32_16x16x32_bf16 v[48:51],a[216:219],a[248:251],v[48:51]\nds_read_b64_tr_b16 a[210:211],v234 offset:17408\ns_mov_b32 s75,2226176\nv_mfma_f32_16x16x32_bf16 v[76:79],a[220:223],a[248:251],v[76:79]\nv_mfma_f32_16x16x32_bf16 v[104:107],a[224:227],a[248:251],v[104:107]\nds_read_b128 a[248:251],v237 offset:38912\n"
"v_mfma_f32_16x16x32_bf16 v[24:27],a[212:215],a[252:255],v[24:27]\nbuffer_load_dwordx4 v224,s[44:47],s75 offen offset:2048 lds\ns_mov_b32 s72,2257920\nv_mfma_f32_16x16x32_bf16 v[52:55],a[216:219],a[252:255],v[52:55]\nv_mfma_f32_16x16x32_bf16 v[80:83],a[220:223],a[252:255],v[80:83]\nv_mfma_f32_16x16x32_bf16 v[108:111],a[224:227],a[252:255],v[108:111]\nds_read_b128 a[252:255],v237 offset:40960\ns_waitcnt lgkmcnt(2)\n"
"v_mfma_f32_16x16x32_bf16 v[0:3],a[196:199],a[228:231],v[0:3]\nbuffer_load_dwordx4 v224,s[44:47],s72 offen offset:3072 lds\ns_add_u32 m0,s64,12288\nv_mfma_f32_16x16x32_bf16 v[28:31],a[200:203],a[228:231],v[28:31]\ns_waitcnt vmcnt(21)\nds_read_b64_tr_b16 a[212:213],v231 offset:20480\nv_mfma_f32_16x16x32_bf16 v[56:59],a[204:207],a[228:231],v[56:59]\nds_read_b64_tr_b16 a[214:215],v231 offset:21504\n"
"s_mov_b32 s73,2359296\nv_mfma_f32_16x16x32_bf16 v[84:87],a[208:211],a[228:231],v[84:87]\nds_read_b128 a[228:231],v238 offset:28672\nv_mfma_f32_16x16x32_bf16 v[4:7],a[196:199],a[232:235],v[4:7]\nbuffer_load_dwordx4 v224,s[44:47],s73 offen offset:0 lds\ns_mov_b32 s74,2391040\nv_mfma_f32_16x16x32_bf16 v[32:35],a[200:203],a[232:235],v[32:35]\nds_read_b64_tr_b16 a[216:217],v232 offset:20480\n"
"v_mfma_f32_16x16x32_bf16 v[60:63],a[204:207],a[232:235],v[60:63]\nv_mfma_f32_16x16x32_bf16 v[88:91],a[208:211],a[232:235],v[88:91]\nds_read_b128 a[232:235],v238 offset:30720\nv_mfma_f32_16x16x32_bf16 v[8:11],a[196:199],a[236:239],v[8:11]\nbuffer_load_dwordx4 v224,s[44:47],s74 offen offset:1024 lds\ns_mov_b32 s75,2488320\nv_mfma_f32_16x16x32_bf16 v[36:39],a[200:203],a[236:239],v[36:39]\nds_read_b64_tr_b16 a[218:219],v232 offset:21504\n"
"v_mfma_f32_16x16x32_bf16 v[64:67],a[204:207],a[236:239],v[64:67]\nv_mfma_f32_16x16x32_bf16 v[92:95],a[208:211],a[236:239],v[92:95]\nds_read_b128 a[236:239],v238 offset:32768\nv_mfma_f32_16x16x32_bf16 v[12:15],a[196:199],a[240:243],v[12:15]\nbuffer_load_dwordx4 v224,s[44:47],s75 offen offset:2048 lds\ns_mov_b32 s72,2520064\nv_mfma_f32_16x16x32_bf16 v[40:43],a[200:203],a[240:243],v[40:43]\nds_read_b64_tr_b16 a[220:221],v233 offset:20480\n"
"v_mfma_f32_16x16x32_bf16 v[68:71],a[204:207],a[240:243],v[68:71]\nds_read_b64_tr_b16 a[222:223],v233 offset:21504\nv_mfma_f32_16x16x32_bf16 v[96:99],a[208:211],a[240:243],v[96:99]\nds_read_b128 a[240:243],v238 offset:34816\nv_mfma_f32_16x16x32_bf16 v[16:19],a[196:199],a[244:247],v[16:19]\nbuffer_load_dwordx4 v224,s[44:47],s72 offen offset:3072 lds\ns_add_u32 m0,s64,16384\nv_mfma_f32_16x16x32_bf16 v[44:47],a[200:203],a[244:247],v[44:47]\n"
"ds_read_b64_tr_b16 a[224:225],v234 offset:20480\ns_mov_b32 s73,2621440\nv_mfma_f32_16x16x32_bf16 v[72:75],a[204:207],a[244:247],v[72:75]\nv_mfma_f32_16x16x32_bf16 v[100:103],a[208:211],a[244:247],v[100:103]\nds_read_b128 a[244:247],v238 offset:36864\ns_waitcnt lgkmcnt(12)\nv_mfma_f32_16x16x32_bf16 v[20:23],a[196:199],a[248:251],v[20:23]\nbuffer_load_dwordx4 v224,s[44:47],s73 offen offset:0 lds\n"
"s_mov_b32 s74,2653184\nv_mfma_f32_16x16x32_bf16 v[48:51],a[200:203],a[248:251],v[48:51]\nds_read_b64_tr_b16 a[226:227],v234 offset:21504\nv_mfma_f32_16x16x32_bf16 v[76:79],a[204:207],a[248:251],v[76:79]\nv_mfma_f32_16x16x32_bf16 v[104:107],a[208:211],a[248:251],v[104:107]\nds_read_b128 a[248:251],v238 offset:38912\nv_mfma_f32_16x16x32_bf16 v[24:27],a[196:199],a[252:255],v[24:27]\nbuffer_load_dwordx4 v224,s[44:47],s74 offen offset:1024 lds\n"
"s_mov_b32 s75,2750464\nv_mfma_f32_16x16x32_bf16 v[52:55],a[200:203],a[252:255],v[52:55]\nv_mfma_f32_16x16x32_bf16 v[80:83],a[204:207],a[252:255],v[80:83]\nv_mfma_f32_16x16x32_bf16 v[108:111],a[208:211],a[252:255],v[108:111]\nds_read_b128 a[252:255],v238 offset:40960\ns_waitcnt lgkmcnt(2)\nv_mfma_f32_16x16x32_bf16 v[0:3],a[212:215],a[228:231],v[0:3]\nbuffer_load_dwordx4 v224,s[44:47],s75 offen offset:2048 lds\n"
"s_mov_b32 s72,2782208\nv_mfma_f32_16x16x32_bf16 v[28:31],a[216:219],a[228:231],v[28:31]\ns_waitcnt vmcnt(20)\nds_read_b64_tr_b16 a[196:197],v231 offset:0\nv_mfma_f32_16x16x32_bf16 v[56:59],a[220:223],a[228:231],v[56:59]\nds_read_b64_tr_b16 a[198:199],v231 offset:1024\nv_mfma_f32_16x16x32_bf16 v[84:87],a[224:227],a[228:231],v[84:87]\ns_waitcnt lgkmcnt(2)\n"
"s_barrier\nds_read_b128 a[228:231],v237 offset:43008\nv_mfma_f32_16x16x32_bf16 v[4:7],a[212:215],a[232:235],v[4:7]\nbuffer_load_dwordx4 v224,s[44:47],s72 offen offset:3072 lds\ns_add_u32 m0,s65,28672\nv_mfma_f32_16x16x32_bf16 v[32:35],a[216:219],a[232:235],v[32:35]\nds_read_b64_tr_b16 a[200:201],v232 offset:0\ns_mov_b32 s73,768\n"
"v_mfma_f32_16x16x32_bf16 v[60:63],a[220:223],a[232:235],v[60:63]\nv_mfma_f32_16x16x32_bf16 v[88:91],a[224:227],a[232:235],v[88:91]\nds_read_b128 a[232:235],v237 offset:45056\nv_mfma_f32_16x16x32_bf16 v[8:11],a[212:215],a[236:239],v[8:11]\nbuffer_load_dwordx4 v229,s[40:43],s73 offen offset:0 lds\ns_mov_b32 s74,6912\nv_mfma_f32_16x16x32_bf16 v[36:39],a[216:219],a[236:239],v[36:39]\nds_read_b64_tr_b16 a[202:203],v232 offset:1024\n"
"v_mfma_f32_16x16x32_bf16 v[64:67],a[220:223],a[236:239],v[64:67]\nv_mfma_f32_16x16x32_bf16 v[92:95],a[224:227],a[236:239],v[92:95]\nds_read_b128 a[236:239],v237 offset:47104\nv_mfma_f32_16x16x32_bf16 v[12:15],a[212:215],a[240:243],v[12:15]\nbuffer_load_dwordx4 v230,s[40:43],s74 offen offset:1024 lds\ns_add_u32 m0,s66,28672\nv_mfma_f32_16x16x32_bf16 v[40:43],a[216:219],a[240:243],v[40:43]\nds_read_b64_tr_b16 a[204:205],v233 offset:0\n"
"s_add_u32 s75,s67,768\nv_mfma_f32_16x16x32_bf16 v[68:71],a[220:223],a[240:243],v[68:71]\nds_read_b64_tr_b16 a[206:207],v233 offset:1024\nv_mfma_f32_16x16x32_bf16 v[96:99],a[224:227],a[240:243],v[96:99]\nds_read_b128 a[240:243],v237 offset:49152\nv_mfma_f32_16x16x32_bf16 v[16:19],a[212:215],a[244:247],v[16:19]\nbuffer_load_dwordx4 v229,s[40:43],s75 offen offset:0 lds\ns_add_u32 s72,s67,6912\n"
"v_mfma_f32_16x16x32_bf16 v[44:47],a[216:219],a[244:247],v[44:47]\nds_read_b64_tr_b16 a[208:209],v234 offset:0\nv_mfma_f32_16x16x32_bf16 v[72:75],a[220:223],a[244:247],v[72:75]\nv_mfma_f32_16x16x32_bf16 v[100:103],a[224:227],a[244:247],v[100:103]\nds_read_b128 a[244:247],v237 offset:51200\nv_mfma_f32_16x16x32_bf16 v[20:23],a[212:215],a[248:251],v[20:23]\nbuffer_load_dwordx4 v230,s[40:43],s72 offen offset:1024 lds\ns_add_u32 m0,s64,20480\n"
"v_mfma_f32_16x16x32_bf16 v[48:51],a[216:219],a[248:251],v[48:51]\nds_read_b64_tr_b16 a[210:211],v234 offset:1024\ns_mov_b32 s73,2883584\nv_mfma_f32_16x16x32_bf16 v[76:79],a[220:223],a[248:251],v[76:79]\nv_mfma_f32_16x16x32_bf16 v[104:107],a[224:227],a[248:251],v[104:107]\nds_read_b128 a[248:251],v237 offset:53248\nv_mfma_f32_16x16x32_bf16 v[24:27],a[212:215],a[252:255],v[24:27]\nbuffer_load_dwordx4 v224,s[44:47],s73 offen offset:0 lds\n"
"s_mov_b32 s74,2915328\nv_mfma_f32_16x16x32_bf16 v[52:55],a[216:219],a[252:255],v[52:55]\nv_mfma_f32_16x16x32_bf16 v[80:83],a[220:223],a[252:255],v[80:83]\nv_mfma_f32_16x16x32_bf16 v[108:111],a[224:227],a[252:255],v[108:111]\nds_read_b128 a[252:255],v237 offset:55296\ns_waitcnt lgkmcnt(2)\nv_mfma_f32_16x16x32_bf16 v[0:3],a[196:199],a[228:231],v[0:3]\nbuffer_load_dwordx4 v224,s[44:47],s74 offen offset:1024 lds\n"
"s_mov_b32 s75,3012608\nv_mfma_f32_16x16x32_bf16 v[28:31],a[200:203],a[228:231],v[28:31]\ns_waitcnt vmcnt(22)\nds_read_b64_tr_b16 a[212:213],v231 offset:4096\nv_mfma_f32_16x16x32_bf16 v[56:59],a[204:207],a[228:231],v[56:59]\nds_read_b64_tr_b16 a[214:215],v231 offset:5120\nv_mfma_f32_16x16x32_bf16 v[84:87],a[208:211],a[228:231],v[84:87]\nds_read_b128 a[228:231],v238 offset:43008\n"
"v_mfma_f32_16x16x32_bf16 v[4:7],a[196:199],a[232:235],v[4:7]\nbuffer_load_dwordx4 v224,s[44:47],s75 offen offset:2048 lds\ns_mov_b32 s72,3044352\nv_mfma_f32_16x16x32_bf16 v[32:35],a[200:203],a[232:235],v[32:35]\nds_read_b64_tr_b16 a[216:217],v232 offset:4096\nv_mfma_f32_16x16x32_bf16 v[60:63],a[204:207],a[232:235],v[60:63]\nv_mfma_f32_16x16x32_bf16 v[88:91],a[208:211],a[232:235],v[88:91]\nds_read_b128 a[232:235],v238 offset:45056\n"
"v_mfma_f32_16x16x32_bf16 v[8:11],a[196:199],a[236:239],v[8:11]\nbuffer_load_dwordx4 v224,s[44:47],s72 offen offset:3072 lds\ns_add_u32 m0,s64,0\nv_mfma_f32_16x16x32_bf16 v[36:39],a[200:203],a[236:239],v[36:39]\nds_read_b64_tr_b16 a[218:219],v232 offset:5120\ns_mov_b32 s73,3145728\nv_mfma_f32_16x16x32_bf16 v[64:67],a[204:207],a[236:239],v[64:67]\nv_mfma_f32_16x16x32_bf16 v[92:95],a[208:211],a[236:239],v[92:95]\n"
"ds_read_b128 a[236:239],v238 offset:47104\nv_mfma_f32_16x16x32_bf16 v[12:15],a[196:199],a[240:243],v[12:15]\nbuffer_load_dwordx4 v224,s[44:47],s73 offen offset:0 lds\ns_mov_b32 s74,3177472\nv_mfma_f32_16x16x32_bf16 v[40:43],a[200:203],a[240:243],v[40:43]\nds_read_b64_tr_b16 a[220:221],v233 offset:4096\nv_mfma_f32_16x16x32_bf16 v[68:71],a[204:207],a[240:243],v[68:71]\nds_read_b64_tr_b16 a[222:223],v233 offset:5120\n"
"v_mfma_f32_16x16x32_bf16 v[96:99],a[208:211],a[240:243],v[96:99]\nds_read_b128 a[240:243],v238 offset:49152\nv_mfma_f32_16x16x32_bf16 v[16:19],a[196:199],a[244:247],v[16:19]\nbuffer_load_dwordx4 v224,s[44:47],s74 offen offset:1024 lds\ns_mov_b32 s75,3274752\nv_mfma_f32_16x16x32_bf16 v[44:47],a[200:203],a[244:247],v[44:47]\nds_read_b64_tr_b16 a[224:225],v234 offset:4096\nv_mfma_f32_16x16x32_bf16 v[72:75],a[204:207],a[244:247],v[72:75]\n"
"v_mfma_f32_16x16x32_bf16 v[100:103],a[208:211],a[244:247],v[100:103]\nds_read_b128 a[244:247],v238 offset:51200\ns_waitcnt lgkmcnt(12)\nv_mfma_f32_16x16x32_bf16 v[20:23],a[196:199],a[248:251],v[20:23]\nbuffer_load_dwordx4 v224,s[44:47],s75 offen offset:2048 lds\ns_mov_b32 s72,3306496\nv_mfma_f32_16x16x32_bf16 v[48:51],a[200:203],a[248:251],v[48:51]\nds_read_b64_tr_b16 a[226:227],v234 offset:5120\n"
"v_mfma_f32_16x16x32_bf16 v[76:79],a[204:207],a[248:251],v[76:79]\nv_mfma_f32_16x16x32_bf16 v[104:107],a[208:211],a[248:251],v[104:107]\nds_read_b128 a[248:251],v238 offset:53248\nv_mfma_f32_16x16x32_bf16 v[24:27],a[196:199],a[252:255],v[24:27]\nbuffer_load_dwordx4 v224,s[44:47],s72 offen offset:3072 lds\ns_add_u32 m0,s64,4096\nv_mfma_f32_16x16x32_bf16 v[52:55],a[200:203],a[252:255],v[52:55]\ns_mov_b32 s73,3407872\n"
"v_mfma_f32_16x16x32_bf16 v[80:83],a[204:207],a[252:255],v[80:83]\nv_mfma_f32_16x16x32_bf16 v[108:111],a[208:211],a[252:255],v[108:111]\nds_read_b128 a[252:255],v238 offset:55296\ns_waitcnt lgkmcnt(2)\nv_mfma_f32_16x16x32_bf16 v[0:3],a[212:215],a[228:231],v[0:3]\nbuffer_load_dwordx4 v224,s[44:47],s73 offen offset:0 lds\ns_mov_b32 s74,3439616\nv_mfma_f32_16x16x32_bf16 v[28:31],a[216:219],a[228:231],v[28:31]\n"
"s_waitcnt vmcnt(21)\nds_read_b64_tr_b16 a[196:197],v231 offset:8192\nv_mfma_f32_16x16x32_bf16 v[56:59],a[220:223],a[228:231],v[56:59]\nds_read_b64_tr_b16 a[198:199],v231 offset:9216\nv_mfma_f32_16x16x32_bf16 v[84:87],a[224:227],a[228:231],v[84:87]\ns_waitcnt lgkmcnt(2)\ns_barrier\nds_read_b128 a[228:231],v237 offset:0\n"
"v_mfma_f32_16x16x32_bf16 v[4:7],a[212:215],a[232:235],v[4:7]\nbuffer_load_dwordx4 v224,s[44:47],s74 offen offset:1024 lds\ns_mov_b32 s75,3536896\nv_mfma_f32_16x16x32_bf16 v[32:35],a[216:219],a[232:235],v[32:35]\nds_read_b64_tr_b16 a[200:201],v232 offset:8192\nv_mfma_f32_16x16x32_bf16 v[60:63],a[220:223],a[232:235],v[60:63]\nv_mfma_f32_16x16x32_bf16 v[88:91],a[224:227],a[232:235],v[88:91]\nds_read_b128 a[232:235],v237 offset:2048\n"
"v_mfma_f32_16x16x32_bf16 v[8:11],a[212:215],a[236:239],v[8:11]\nbuffer_load_dwordx4 v224,s[44:47],s75 offen offset:2048 lds\ns_mov_b32 s72,3568640\nv_mfma_f32_16x16x32_bf16 v[36:39],a[216:219],a[236:239],v[36:39]\nds_read_b64_tr_b16 a[202:203],v232 offset:9216\nv_mfma_f32_16x16x32_bf16 v[64:67],a[220:223],a[236:239],v[64:67]\nv_mfma_f32_16x16x32_bf16 v[92:95],a[224:227],a[236:239],v[92:95]\nds_read_b128 a[236:239],v237 offset:4096\n"
"v_mfma_f32_16x16x32_bf16 v[12:15],a[212:215],a[240:243],v[12:15]\nbuffer_load_dwordx4 v224,s[44:47],s72 offen offset:3072 lds\nv_mfma_f32_16x16x32_bf16 v[40:43],a[216:219],a[240:243],v[40:43]\nds_read_b64_tr_b16 a[204:205],v233 offset:8192\nv_mfma_f32_16x16x32_bf16 v[68:71],a[220:223],a[240:243],v[68:71]\nds_read_b64_tr_b16 a[206:207],v233 offset:9216\nv_mfma_f32_16x16x32_bf16 v[96:99],a[224:227],a[240:243],v[96:99]\nds_read_b128 a[240:243],v237 offset:6144\n"
"v_mfma_f32_16x16x32_bf16 v[16:19],a[212:215],a[244:247],v[16:19]\nds_read_b64_tr_b16 a[208:209],v234 offset:8192\nv_mfma_f32_16x16x32_bf16 v[44:47],a[216:219],a[244:247],v[44:47]\nv_mfma_f32_16x16x32_bf16 v[72:75],a[220:223],a[244:247],v[72:75]\nv_mfma_f32_16x16x32_bf16 v[100:103],a[224:227],a[244:247],v[100:103]\nds_read_b128 a[244:247],v237 offset:8192\nv_mfma_f32_16x16x32_bf16 v[20:23],a[212:215],a[248:251],v[20:23]\nds_read_b64_tr_b16 a[210:211],v234 offset:9216\n"
"s_add_u32 m0,s64,8192\nv_mfma_f32_16x16x32_bf16 v[48:51],a[216:219],a[248:251],v[48:51]\ns_mov_b32 s73,0\nv_mfma_f32_16x16x32_bf16 v[76:79],a[220:223],a[248:251],v[76:79]\nv_mfma_f32_16x16x32_bf16 v[104:107],a[224:227],a[248:251],v[104:107]\nds_read_b128 a[248:251],v237 offset:10240\nv_mfma_f32_16x16x32_bf16 v[24:27],a[212:215],a[252:255],v[24:27]\nv_mfma_f32_16x16x32_bf16 v[52:55],a[216:219],a[252:255],v[52:55]\n"
"v_mfma_f32_16x16x32_bf16 v[80:83],a[220:223],a[252:255],v[80:83]\nv_mfma_f32_16x16x32_bf16 v[108:111],a[224:227],a[252:255],v[108:111]\nds_read_b128 a[252:255],v237 offset:12288\ns_waitcnt lgkmcnt(2)\nv_mfma_f32_16x16x32_bf16 v[0:3],a[196:199],a[228:231],v[0:3]\nbuffer_load_dwordx4 v227,s[48:51],s73 offen offset:0 nt lds\ns_mov_b32 s74,7168\nv_mfma_f32_16x16x32_bf16 v[28:31],a[200:203],a[228:231],v[28:31]\n"
"s_waitcnt vmcnt(21)\nds_read_b64_tr_b16 a[212:213],v231 offset:12288\nv_mfma_f32_16x16x32_bf16 v[56:59],a[204:207],a[228:231],v[56:59]\nds_read_b64_tr_b16 a[214:215],v231 offset:13312\nv_mfma_f32_16x16x32_bf16 v[84:87],a[208:211],a[228:231],v[84:87]\nds_read_b128 a[228:231],v238 offset:0\nv_mfma_f32_16x16x32_bf16 v[4:7],a[196:199],a[232:235],v[4:7]\nbuffer_load_dwordx4 v228,s[48:51],s74 offen offset:1024 nt lds\n"
"s_mov_b32 s75,30720\nv_mfma_f32_16x16x32_bf16 v[32:35],a[200:203],a[232:235],v[32:35]\nds_read_b64_tr_b16 a[216:217],v232 offset:12288\nv_mfma_f32_16x16x32_bf16 v[60:63],a[204:207],a[232:235],v[60:63]\nv_mfma_f32_16x16x32_bf16 v[88:91],a[208:211],a[232:235],v[88:91]\nds_read_b128 a[232:235],v238 offset:2048\nv_mfma_f32_16x16x32_bf16 v[8:11],a[196:199],a[236:239],v[8:11]\nbuffer_load_dwordx4 v227,s[48:51],s75 offen offset:2048 nt lds\n"
"s_mov_b32 s72,37888\nv_mfma_f32_16x16x32_bf16 v[36:39],a[200:203],a[236:239],v[36:39]\nds_read_b64_tr_b16 a[218:219],v232 offset:13312\nv_mfma_f32_16x16x32_bf16 v[64:67],a[204:207],a[236:239],v[64:67]\nv_mfma_f32_16x16x32_bf16 v[92:95],a[208:211],a[236:239],v[92:95]\nds_read_b128 a[236:239],v238 offset:4096\nv_mfma_f32_16x16x32_bf16 v[12:15],a[196:199],a[240:243],v[12:15]\nbuffer_load_dwordx4 v228,s[48:51],s72 offen offset:3072 nt lds\n"
"v_mfma_f32_16x16x32_bf16 v[40:43],a[200:203],a[240:243],v[40:43]\nds_read_b64_tr_b16 a[220:221],v233 offset:12288\nv_mfma_f32_16x16x32_bf16 v[68:71],a[204:207],a[240:243],v[68:71]\nds_read_b64_tr_b16 a[222:223],v233 offset:13312\nv_mfma_f32_16x16x32_bf16 v[96:99],a[208:211],a[240:243],v[96:99]\nds_read_b128 a[240:243],v238 offset:6144\nv_mfma_f32_16x16x32_bf16 v[16:19],a[196:199],a[244:247],v[16:19]\nds_read_b64_tr_b16 a[224:225],v234 offset:12288\n"
"v_mfma_f32_16x16x32_bf16 v[44:47],a[200:203],a[244:247],v[44:47]\nv_mfma_f32_16x16x32_bf16 v[72:75],a[204:207],a[244:247],v[72:75]\nv_mfma_f32_16x16x32_bf16 v[100:103],a[208:211],a[244:247],v[100:103]\nds_read_b128 a[244:247],v238 offset:8192\ns_waitcnt lgkmcnt(12)\nv_mfma_f32_16x16x32_bf16 v[20:23],a[196:199],a[248:251],v[20:23]\nds_read_b64_tr_b16 a[226:227],v234 offset:13312\ns_add_u32 m0,s64,12288\n"
"v_mfma_f32_16x16x32_bf16 v[48:51],a[200:203],a[248:251],v[48:51]\ns_mov_b32 s73,65536\nv_mfma_f32_16x16x32_bf16 v[76:79],a[204:207],a[248:251],v[76:79]\nv_mfma_f32_16x16x32_bf16 v[104:107],a[208:211],a[248:251],v[104:107]\nds_read_b128 a[248:251],v238 offset:10240\nv_mfma_f32_16x16x32_bf16 v[24:27],a[196:199],a[252:255],v[24:27]\nv_mfma_f32_16x16x32_bf16 v[52:55],a[200:203],a[252:255],v[52:55]\nv_mfma_f32_16x16x32_bf16 v[80:83],a[204:207],a[252:255],v[80:83]\n"
"v_mfma_f32_16x16x32_bf16 v[108:111],a[208:211],a[252:255],v[108:111]\nds_read_b128 a[252:255],v238 offset:12288\ns_waitcnt lgkmcnt(2)\nv_mfma_f32_16x16x32_bf16 v[0:3],a[212:215],a[228:231],v[0:3]\nbuffer_load_dwordx4 v227,s[48:51],s73 offen offset:0 nt lds\nv_add_u32 v244,1,v244\nv_mfma_f32_16x16x32_bf16 v[28:31],a[216:219],a[228:231],v[28:31]\ns_waitcnt vmcnt(21)\n"
"ds_read_b64_tr_b16 a[196:197],v231 offset:16384\nv_mfma_f32_16x16x32_bf16 v[56:59],a[220:223],a[228:231],v[56:59]\nds_read_b64_tr_b16 a[198:199],v231 offset:17408\ns_mov_b32 s74,72704\nv_mfma_f32_16x16x32_bf16 v[84:87],a[224:227],a[228:231],v[84:87]\ns_waitcnt lgkmcnt(2)\ns_barrier\nds_read_b128 a[228:231],v237 offset:14336\n"
"v_mfma_f32_16x16x32_bf16 v[4:7],a[212:215],a[232:235],v[4:7]\nbuffer_load_dwordx4 v228,s[48:51],s74 offen offset:1024 nt lds\ns_mov_b32 s75,96256\nv_mfma_f32_16x16x32_bf16 v[32:35],a[216:219],a[232:235],v[32:35]\nds_read_b64_tr_b16 a[200:201],v232 offset:16384\nv_mfma_f32_16x16x32_bf16 v[60:63],a[220:223],a[232:235],v[60:63]\nbuffer_store_dword v244,v245,s[96:99],0 offen sc1\nv_mfma_f32_16x16x32_bf16 v[88:91],a[224:227],a[232:235],v[88:91]\n"
"ds_read_b128 a[232:235],v237 offset:16384\nv_mfma_f32_16x16x32_bf16 v[8:11],a[212:215],a[236:239],v[8:11]\nbuffer_load_dwordx4 v227,s[48:51],s75 offen offset:2048 nt lds\ns_mov_b32 s72,103424\nv_mfma_f32_16x16x32_bf16 v[36:39],a[216:219],a[236:239],v[36:39]\nds_read_b64_tr_b16 a[202:203],v232 offset:17408\nv_mfma_f32_16x16x32_bf16 v[64:67],a[220:223],a[236:239],v[64:67]\nv_mfma_f32_16x16x32_bf16 v[92:95],a[224:227],a[236:239],v[92:95]\n"
"ds_read_b128 a[236:239],v237 offset:18432\nv_mfma_f32_16x16x32_bf16 v[12:15],a[212:215],a[240:243],v[12:15]\nbuffer_load_dwordx4 v228,s[48:51],s72 offen offset:3072 nt lds\nv_mfma_f32_16x16x32_bf16 v[40:43],a[216:219],a[240:243],v[40:43]\nds_read_b64_tr_b16 a[204:205],v233 offset:16384\nv_mfma_f32_16x16x32_bf16 v[68:71],a[220:223],a[240:243],v[68:71]\nds_read_b64_tr_b16 a[206:207],v233 offset:17408\nv_mfma_f32_16x16x32_bf16 v[96:99],a[224:227],a[240:243],v[96:99]\n"
"ds_read_b128 a[240:243],v237 offset:20480\nv_mfma_f32_16x16x32_bf16 v[16:19],a[212:215],a[244:247],v[16:19]\nds_read_b64_tr_b16 a[208:209],v234 offset:16384\nv_mfma_f32_16x16x32_bf16 v[44:47],a[216:219],a[244:247],v[44:47]\nv_mfma_f32_16x16x32_bf16 v[72:75],a[220:223],a[244:247],v[72:75]\nv_mfma_f32_16x16x32_bf16 v[100:103],a[224:227],a[244:247],v[100:103]\nds_read_b128 a[244:247],v237 offset:22528\nv_mfma_f32_16x16x32_bf16 v[20:23],a[212:215],a[248:251],v[20:23]\n"
"ds_read_b64_tr_b16 a[210:211],v234 offset:17408\ns_add_u32 m0,s64,16384\nv_mfma_f32_16x16x32_bf16 v[48:51],a[216:219],a[248:251],v[48:51]\ns_mov_b32 s73,128\nv_mfma_f32_16x16x32_bf16 v[76:79],a[220:223],a[248:251],v[76:79]\nv_mfma_f32_16x16x32_bf16 v[104:107],a[224:227],a[248:251],v[104:107]\nds_read_b128 a[248:251],v237 offset:24576\nv_mfma_f32_16x16x32_bf16 v[24:27],a[212:215],a[252:255],v[24:27]\n"
"v_mfma_f32_16x16x32_bf16 v[52:55],a[216:219],a[252:255],v[52:55]\nv_mfma_f32_16x16x32_bf16 v[80:83],a[220:223],a[252:255],v[80:83]\nv_mfma_f32_16x16x32_bf16 v[108:111],a[224:227],a[252:255],v[108:111]\nds_read_b128 a[252:255],v237 offset:26624\ns_waitcnt lgkmcnt(2)\nv_mfma_f32_16x16x32_bf16 v[0:3],a[196:199],a[228:231],v[0:3]\nbuffer_load_dwordx4 v227,s[48:51],s73 offen offset:0 nt lds\ns_mov_b32 s74,7296\n"
"v_mfma_f32_16x16x32_bf16 v[28:31],a[200:203],a[228:231],v[28:31]\ns_waitcnt vmcnt(18)\nds_read_b64_tr_b16 a[212:213],v231 offset:20480\nv_mfma_f32_16x16x32_bf16 v[56:59],a[204:207],a[228:231],v[56:59]\nds_read_b64_tr_b16 a[214:215],v231 offset:21504\nv_mfma_f32_16x16x32_bf16 v[84:87],a[208:211],a[228:231],v[84:87]\nds_read_b128 a[228:231],v238 offset:14336\nv_mfma_f32_16x16x32_bf16 v[4:7],a[196:199],a[232:235],v[4:7]\n"
"buffer_load_dwordx4 v228,s[48:51],s74 offen offset:1024 nt lds\ns_mov_b32 s75,30848\nv_mfma_f32_16x16x32_bf16 v[32:35],a[200:203],a[232:235],v[32:35]\nds_read_b64_tr_b16 a[216:217],v232 offset:20480\nv_mfma_f32_16x16x32_bf16 v[60:63],a[204:207],a[232:235],v[60:63]\nv_mfma_f32_16x16x32_bf16 v[88:91],a[208:211],a[232:235],v[88:91]\nds_read_b128 a[232:235],v238 offset:16384\nv_mfma_f32_16x16x32_bf16 v[8:11],a[196:199],a[236:239],v[8:11]\n"
"buffer_load_dwordx4 v227,s[48:51],s75 offen offset:2048 nt lds\ns_mov_b32 s72,38016\nv_mfma_f32_16x16x32_bf16 v[36:39],a[200:203],a[236:239],v[36:39]\nds_read_b64_tr_b16 a[218:219],v232 offset:21504\nv_mfma_f32_16x16x32_bf16 v[64:67],a[204:207],a[236:239],v[64:67]\nv_mfma_f32_16x16x32_bf16 v[92:95],a[208:211],a[236:239],v[92:95]\nds_read_b128 a[236:239],v238 offset:18432\nv_mfma_f32_16x16x32_bf16 v[12:15],a[196:199],a[240:243],v[12:15]\n"
"buffer_load_dwordx4 v228,s[48:51],s72 offen offset:3072 nt lds\nv_mfma_f32_16x16x32_bf16 v[40:43],a[200:203],a[240:243],v[40:43]\nds_read_b64_tr_b16 a[220:221],v233 offset:20480\nv_mfma_f32_16x16x32_bf16 v[68:71],a[204:207],a[240:243],v[68:71]\nds_read_b64_tr_b16 a[222:223],v233 offset:21504\nv_mfma_f32_16x16x32_bf16 v[96:99],a[208:211],a[240:243],v[96:99]\nds_read_b128 a[240:243],v238 offset:20480\nv_mfma_f32_16x16x32_bf16 v[16:19],a[196:199],a[244:247],v[16:19]\n"
"ds_read_b64_tr_b16 a[224:225],v234 offset:20480\nv_mfma_f32_16x16x32_bf16 v[44:47],a[200:203],a[244:247],v[44:47]\nv_mfma_f32_16x16x32_bf16 v[72:75],a[204:207],a[244:247],v[72:75]\nv_mfma_f32_16x16x32_bf16 v[100:103],a[208:211],a[244:247],v[100:103]\nds_read_b128 a[244:247],v238 offset:22528\ns_waitcnt lgkmcnt(12)\nv_mfma_f32_16x16x32_bf16 v[20:23],a[196:199],a[248:251],v[20:23]\nds_read_b64_tr_b16 a[226:227],v234 offset:21504\n"
"s_add_u32 m0,s64,20480\nv_mfma_f32_16x16x32_bf16 v[48:51],a[200:203],a[248:251],v[48:51]\ns_mov_b32 s73,65664\nv_mfma_f32_16x16x32_bf16 v[76:79],a[204:207],a[248:251],v[76:79]\nv_mfma_f32_16x16x32_bf16 v[104:107],a[208:211],a[248:251],v[104:107]\nds_read_b128 a[248:251],v238 offset:24576\nv_mfma_f32_16x16x32_bf16 v[24:27],a[196:199],a[252:255],v[24:27]\nv_mfma_f32_16x16x32_bf16 v[52:55],a[200:203],a[252:255],v[52:55]\n"
"v_mfma_f32_16x16x32_bf16 v[80:83],a[204:207],a[252:255],v[80:83]\nv_mfma_f32_16x16x32_bf16 v[108:111],a[208:211],a[252:255],v[108:111]\nds_read_b128 a[252:255],v238 offset:26624\ns_waitcnt lgkmcnt(2)\nv_mfma_f32_16x16x32_bf16 v[0:3],a[212:215],a[228:231],v[0:3]\nbuffer_load_dwordx4 v227,s[48:51],s73 offen offset:0 nt lds\ns_mov_b32 s74,72832\nv_mfma_f32_16x16x32_bf16 v[28:31],a[216:219],a[228:231],v[28:31]\n"
"s_waitcnt vmcnt(18)\nds_read_b64_tr_b16 a[196:197],v231 offset:0\nv_mfma_f32_16x16x32_bf16 v[56:59],a[220:223],a[228:231],v[56:59]\nds_read_b64_tr_b16 a[198:199],v231 offset:1024\nv_mfma_f32_16x16x32_bf16 v[84:87],a[224:227],a[228:231],v[84:87]\ns_waitcnt lgkmcnt(2)\ns_barrier\nds_read_b128 a[228:231],v237 offset:28672\n"
"v_mfma_f32_16x16x32_bf16 v[4:7],a[212:215],a[232:235],v[4:7]\nbuffer_load_dwordx4 v228,s[48:51],s74 offen offset:1024 nt lds\ns_mov_b32 s75,96384\nv_mfma_f32_16x16x32_bf16 v[32:35],a[216:219],a[232:235],v[32:35]\nds_read_b64_tr_b16 a[200:201],v232 offset:0\nv_mfma_f32_16x16x32_bf16 v[60:63],a[220:223],a[232:235],v[60:63]\nv_mfma_f32_16x16x32_bf16 v[88:91],a[224:227],a[232:235],v[88:91]\nds_read_b128 a[232:235],v237 offset:30720\n"
"v_mfma_f32_16x16x32_bf16 v[8:11],a[212:215],a[236:239],v[8:11]\nbuffer_load_dwordx4 v227,s[48:51],s75 offen offset:2048 nt lds\ns_mov_b32 s72,103552\nv_mfma_f32_16x16x32_bf16 v[36:39],a[216:219],a[236:239],v[36:39]\nds_read_b64_tr_b16 a[202:203],v232 offset:1024\nv_mfma_f32_16x16x32_bf16 v[64:67],a[220:223],a[236:239],v[64:67]\nv_mfma_f32_16x16x32_bf16 v[92:95],a[224:227],a[236:239],v[92:95]\nds_read_b128 a[236:239],v237 offset:32768\n"
"v_mfma_f32_16x16x32_bf16 v[12:15],a[212:215],a[240:243],v[12:15]\nbuffer_load_dwordx4 v228,s[48:51],s72 offen offset:3072 nt lds\nv_mfma_f32_16x16x32_bf16 v[40:43],a[216:219],a[240:243],v[40:43]\nds_read_b64_tr_b16 a[204:205],v233 offset:0\nv_mfma_f32_16x16x32_bf16 v[68:71],a[220:223],a[240:243],v[68:71]\nds_read_b64_tr_b16 a[206:207],v233 offset:1024\nv_mfma_f32_16x16x32_bf16 v[96:99],a[224:227],a[240:243],v[96:99]\nds_read_b128 a[240:243],v237 offset:34816\n"
"v_mfma_f32_16x16x32_bf16 v[16:19],a[212:215],a[244:247],v[16:19]\nds_read_b64_tr_b16 a[208:209],v234 offset:0\nv_mfma_f32_16x16x32_bf16 v[44:47],a[216:219],a[244:247],v[44:47]\nv_mfma_f32_16x16x32_bf16 v[72:75],a[220:223],a[244:247],v[72:75]\nv_mfma_f32_16x16x32_bf16 v[100:103],a[224:227],a[244:247],v[100:103]\nds_read_b128 a[244:247],v237 offset:36864\nv_mfma_f32_16x16x32_bf16 v[20:23],a[212:215],a[248:251],v[20:23]\nds_read_b64_tr_b16 a[210:211],v234 offset:1024\n"
"s_add_u32 m0,s64,0\nv_mfma_f32_16x16x32_bf16 v[48:51],a[216:219],a[248:251],v[48:51]\ns_mov_b32 s73,256\nv_mfma_f32_16x16x32_bf16 v[76:79],a[220:223],a[248:251],v[76:79]\nv_mfma_f32_16x16x32_bf16 v[104:107],a[224:227],a[248:251],v[104:107]\nds_read_b128 a[248:251],v237 offset:38912\nv_mfma_f32_16x16x32_bf16 v[24:27],a[212:215],a[252:255],v[24:27]\nv_mfma_f32_16x16x32_bf16 v[52:55],a[216:219],a[252:255],v[52:55]\n"
"v_mfma_f32_16x16x32_bf16 v[80:83],a[220:223],a[252:255],v[80:83]\nv_mfma_f32_16x16x32_bf16 v[108:111],a[224:227],a[252:255],v[108:111]\nds_read_b128 a[252:255],v237 offset:40960\ns_waitcnt lgkmcnt(2)\nv_mfma_f32_16x16x32_bf16 v[0:3],a[196:199],a[228:231],v[0:3]\nbuffer_load_dwordx4 v227,s[48:51],s73 offen offset:0 nt lds\ns_mov_b32 s74,7424\nv_mfma_f32_16x16x32_bf16 v[28:31],a[200:203],a[228:231],v[28:31]\n"
"s_waitcnt vmcnt(18)\nds_read_b64_tr_b16 a[212:213],v231 offset:4096\nv_mfma_f32_16x16x32_bf16 v[56:59],a[204:207],a[228:231],v[56:59]\nds_read_b64_tr_b16 a[214:215],v231 offset:5120\nv_mfma_f32_16x16x32_bf16 v[84:87],a[208:211],a[228:231],v[84:87]\nds_read_b128 a[228:231],v238 offset:28672\nv_mfma_f32_16x16x32_bf16 v[4:7],a[196:199],a[232:235],v[4:7]\nbuffer_load_dwordx4 v228,s[48:51],s74 offen offset:1024 nt lds\n"
"s_mov_b32 s75,30976\nv_mfma_f32_16x16x32_bf16 v[32:35],a[200:203],a[232:235],v[32:35]\nds_read_b64_tr_b16 a[216:217],v232 offset:4096\nv_mfma_f32_16x16x32_bf16 v[60:63],a[204:207],a[232:235],v[60:63]\nv_mfma_f32_16x16x32_bf16 v[88:91],a[208:211],a[232:235],v[88:91]\nds_read_b128 a[232:235],v238 offset:30720\nv_mfma_f32_16x16x32_bf16 v[8:11],a[196:199],a[236:239],v[8:11]\nbuffer_load_dwordx4 v227,s[48:51],s75 offen offset:2048 nt lds\n"
"s_mov_b32 s72,38144\nv_mfma_f32_16x16x32_bf16 v[36:39],a[200:203],a[236:239],v[36:39]\nds_read_b64_tr_b16 a[218:219],v232 offset:5120\nv_mfma_f32_16x16x32_bf16 v[64:67],a[204:207],a[236:239],v[64:67]\nv_mfma_f32_16x16x32_bf16 v[92:95],a[208:211],a[236:239],v[92:95]\nds_read_b128 a[236:239],v238 offset:32768\nv_mfma_f32_16x16x32_bf16 v[12:15],a[196:199],a[240:243],v[12:15]\nbuffer_load_dwordx4 v228,s[48:51],s72 offen offset:3072 nt lds\n"
"v_mfma_f32_16x16x32_bf16 v[40:43],a[200:203],a[240:243],v[40:43]\nds_read_b64_tr_b16 a[220:221],v233 offset:4096\nv_mfma_f32_16x16x32_bf16 v[68:71],a[204:207],a[240:243],v[68:71]\nds_read_b64_tr_b16 a[222:223],v233 offset:5120\nv_mfma_f32_16x16x32_bf16 v[96:99],a[208:211],a[240:243],v[96:99]\nds_read_b128 a[240:243],v238 offset:34816\nv_mfma_f32_16x16x32_bf16 v[16:19],a[196:199],a[244:247],v[16:19]\nds_read_b64_tr_b16 a[224:225],v234 offset:4096\n"
"v_mfma_f32_16x16x32_bf16 v[44:47],a[200:203],a[244:247],v[44:47]\nv_mfma_f32_16x16x32_bf16 v[72:75],a[204:207],a[244:247],v[72:75]\nv_mfma_f32_16x16x32_bf16 v[100:103],a[208:211],a[244:247],v[100:103]\nds_read_b128 a[244:247],v238 offset:36864\ns_waitcnt lgkmcnt(12)\nv_mfma_f32_16x16x32_bf16 v[20:23],a[196:199],a[248:251],v[20:23]\nds_read_b64_tr_b16 a[226:227],v234 offset:5120\ns_add_u32 m0,s64,4096\n"
"v_mfma_f32_16x16x32_bf16 v[48:51],a[200:203],a[248:251],v[48:51]\ns_mov_b32 s73,65792\nv_mfma_f32_16x16x32_bf16 v[76:79],a[204:207],a[248:251],v[76:79]\nv_mfma_f32_16x16x32_bf16 v[104:107],a[208:211],a[248:251],v[104:107]\nds_read_b128 a[248:251],v238 offset:38912\nv_mfma_f32_16x16x32_bf16 v[24:27],a[196:199],a[252:255],v[24:27]\nv_mfma_f32_16x16x32_bf16 v[52:55],a[200:203],a[252:255],v[52:55]\nv_mfma_f32_16x16x32_bf16 v[80:83],a[204:207],a[252:255],v[80:83]\n"
"v_mfma_f32_16x16x32_bf16 v[108:111],a[208:211],a[252:255],v[108:111]\nds_read_b128 a[252:255],v238 offset:40960\ns_waitcnt lgkmcnt(2)\nv_mfma_f32_16x16x32_bf16 v[0:3],a[212:215],a[228:231],v[0:3]\nbuffer_load_dwordx4 v227,s[48:51],s73 offen offset:0 nt lds\ns_mov_b32 s74,72960\nv_mfma_f32_16x16x32_bf16 v[28:31],a[216:219],a[228:231],v[28:31]\ns_waitcnt vmcnt(18)\n"
"ds_read_b128 a[196:199],v235 offset:8192\nv_mfma_f32_16x16x32_bf16 v[56:59],a[220:223],a[228:231],v[56:59]\nv_mfma_f32_16x16x32_bf16 v[84:87],a[224:227],a[228:231],v[84:87]\ns_waitcnt lgkmcnt(0)\ns_barrier\nv_mfma_f32_16x16x32_bf16 v[4:7],a[212:215],a[232:235],v[4:7]\nbuffer_load_dwordx4 v228,s[48:51],s74 offen offset:1024 nt lds\ns_mov_b32 s75,96512\n"
"v_mfma_f32_16x16x32_bf16 v[32:35],a[216:219],a[232:235],v[32:35]\nds_read_b128 a[200:203],v235 offset:10240\nv_mfma_f32_16x16x32_bf16 v[60:63],a[220:223],a[232:235],v[60:63]\nds_write_b32 v248,v250 offset:57344\nv_mfma_f32_16x16x32_bf16 v[88:91],a[224:227],a[232:235],v[88:91]\nv_mfma_f32_16x16x32_bf16 v[8:11],a[212:215],a[236:239],v[8:11]\nbuffer_load_dwordx4 v227,s[48:51],s75 offen offset:2048 nt lds\nv_cvt_pk_bf16_f32 v0,v0,v1\n"
"v_mfma_f32_16x16x32_bf16 v[36:39],a[216:219],a[236:239],v[36:39]\ns_mov_b32 s72,103680\nds_write_b32 v248,v250 offset:57600\nv_mfma_f32_16x16x32_bf16 v[64:67],a[220:223],a[236:239],v[64:67]\nv_cvt_pk_bf16_f32 v1,v2,v3\nv_cvt_pk_bf16_f32 v28,v28,v29\nv_mfma_f32_16x16x32_bf16 v[92:95],a[224:227],a[236:239],v[92:95]\nds_write_b64 v241,v[0:1] offset:0\n"
"v_mfma_f32_16x16x32_bf16 v[12:15],a[212:215],a[240:243],v[12:15]\nbuffer_load_dwordx4 v228,s[48:51],s72 offen offset:3072 nt lds\nv_cvt_pk_bf16_f32 v29,v30,v31\nv_mfma_f32_16x16x32_bf16 v[40:43],a[216:219],a[240:243],v[40:43]\ns_waitcnt vmcnt(16)\nds_read_b128 a[204:207],v235 offset:12288\nv_mfma_f32_16x16x32_bf16 v[68:71],a[220:223],a[240:243],v[68:71]\nds_write_b64 v241,v[28:29] offset:512\n"
"v_mfma_f32_16x16x32_bf16 v[96:99],a[224:227],a[240:243],v[96:99]\nv_cvt_pk_bf16_f32 v56,v56,v57\nv_cvt_pk_bf16_f32 v57,v58,v59\nv_mfma_f32_16x16x32_bf16 v[16:19],a[212:215],a[244:247],v[16:19]\nds_read_b128 a[208:211],v235 offset:14336\nv_cvt_pk_bf16_f32 v84,v84,v85\nv_mfma_f32_16x16x32_bf16 v[44:47],a[216:219],a[244:247],v[44:47]\nds_write_b64 v241,v[56:57] offset:1024\n"
"v_mfma_f32_16x16x32_bf16 v[72:75],a[220:223],a[244:247],v[72:75]\nv_cvt_pk_bf16_f32 v85,v86,v87\nv_cvt_pk_bf16_f32 v4,v4,v5\nv_mfma_f32_16x16x32_bf16 v[100:103],a[224:227],a[244:247],v[100:103]\nds_write_b64 v241,v[84:85] offset:1536\nv_mfma_f32_16x16x32_bf16 v[20:23],a[212:215],a[248:251],v[20:23]\nv_cvt_pk_bf16_f32 v5,v6,v7\nv_cvt_pk_bf16_f32 v32,v32,v33\n"
"v_mfma_f32_16x16x32_bf16 v[48:51],a[216:219],a[248:251],v[48:51]\nds_write_b64 v241,v[4:5] offset:2048\nv_mfma_f32_16x16x32_bf16 v[76:79],a[220:223],a[248:251],v[76:79]\nv_cvt_pk_bf16_f32 v33,v34,v35\nv_cvt_pk_bf16_f32 v60,v60,v61\nv_mfma_f32_16x16x32_bf16 v[104:107],a[224:227],a[248:251],v[104:107]\nds_write_b64 v241,v[32:33] offset:2560\nv_mfma_f32_16x16x32_bf16 v[24:27],a[212:215],a[252:255],v[24:27]\n"
"v_cvt_pk_bf16_f32 v61,v62,v63\nv_cvt_pk_bf16_f32 v88,v88,v89\nv_mfma_f32_16x16x32_bf16 v[52:55],a[216:219],a[252:255],v[52:55]\nds_write_b64 v241,v[60:61] offset:3072\nv_mfma_f32_16x16x32_bf16 v[80:83],a[220:223],a[252:255],v[80:83]\nv_cvt_pk_bf16_f32 v89,v90,v91\nv_cvt_pk_bf16_f32 v8,v8,v9\nv_mfma_f32_16x16x32_bf16 v[108:111],a[224:227],a[252:255],v[108:111]\n"
"ds_write_b64 v241,v[88:89] offset:3584\nv_cvt_pk_bf16_f32 v9,v10,v11\nds_write_b64 v241,v[8:9] offset:4096\nv_cvt_pk_bf16_f32 v36,v36,v37\nv_cvt_pk_bf16_f32 v37,v38,v39\nds_write_b64 v241,v[36:37] offset:4608\nv_cvt_pk_bf16_f32 v64,v64,v65\nv_cvt_pk_bf16_f32 v65,v66,v67\n"
"ds_write_b64 v241,v[64:65] offset:5120\nv_cvt_pk_bf16_f32 v92,v92,v93\nv_cvt_pk_bf16_f32 v93,v94,v95\nds_write_b64 v241,v[92:93] offset:5632\nv_cvt_pk_bf16_f32 v12,v12,v13\nv_cvt_pk_bf16_f32 v13,v14,v15\nds_write_b64 v241,v[12:13] offset:6144\nv_cvt_pk_bf16_f32 v40,v40,v41\n"
"v_cvt_pk_bf16_f32 v41,v42,v43\nds_write_b64 v241,v[40:41] offset:6656\nv_cvt_pk_bf16_f32 v68,v68,v69\nv_cvt_pk_bf16_f32 v69,v70,v71\nds_write_b64 v241,v[68:69] offset:7168\nv_cvt_pk_bf16_f32 v96,v96,v97\nv_cvt_pk_bf16_f32 v97,v98,v99\nds_write_b64 v241,v[96:97] offset:7680\n"
"v_cvt_pk_bf16_f32 v16,v16,v17\nv_cvt_pk_bf16_f32 v17,v18,v19\nds_write_b64 v241,v[16:17] offset:8192\nv_cvt_pk_bf16_f32 v44,v44,v45\nv_cvt_pk_bf16_f32 v45,v46,v47\nds_write_b64 v241,v[44:45] offset:8704\nv_cvt_pk_bf16_f32 v72,v72,v73\nv_cvt_pk_bf16_f32 v73,v74,v75\n"
"ds_write_b64 v241,v[72:73] offset:9216\nv_cvt_pk_bf16_f32 v100,v100,v101\nv_cvt_pk_bf16_f32 v101,v102,v103\nds_write_b64 v241,v[100:101] offset:9728\nv_cvt_pk_bf16_f32 v20,v20,v21\nv_cvt_pk_bf16_f32 v21,v22,v23\nds_write_b64 v241,v[20:21] offset:10240\nv_cvt_pk_bf16_f32 v48,v48,v49\n"
"v_cvt_pk_bf16_f32 v49,v50,v51\nds_write_b64 v241,v[48:49] offset:10752\nv_cvt_pk_bf16_f32 v76,v76,v77\nv_cvt_pk_bf16_f32 v77,v78,v79\nds_write_b64 v241,v[76:77] offset:11264\nv_cvt_pk_bf16_f32 v104,v104,v105\nv_cvt_pk_bf16_f32 v105,v106,v107\nds_write_b64 v241,v[104:105] offset:11776\n"
"v_cvt_pk_bf16_f32 v24,v24,v25\nv_cvt_pk_bf16_f32 v25,v26,v27\nds_write_b64 v241,v[24:25] offset:12288\nv_cvt_pk_bf16_f32 v52,v52,v53\nv_cvt_pk_bf16_f32 v53,v54,v55\nds_write_b64 v241,v[52:53] offset:12800\nv_cvt_pk_bf16_f32 v80,v80,v81\nv_cvt_pk_bf16_f32 v81,v82,v83\n"
"ds_write_b64 v241,v[80:81] offset:13312\nv_cvt_pk_bf16_f32 v108,v108,v109\nv_cvt_pk_bf16_f32 v109,v110,v111\nds_write_b64 v241,v[108:109] offset:13824\ns_waitcnt lgkmcnt(0)\ns_barrier\nds_read_b128 a[228:231],v239 offset:0\nds_read_b128 a[232:235],v239 offset:2048\n"
"ds_read_b128 a[236:239],v239 offset:4096\nds_read_b128 a[240:243],v239 offset:6144\nds_read_b128 a[244:247],v239 offset:8192\nds_read_b128 a[248:251],v239 offset:10240\nds_read_b128 a[252:255],v239 offset:12288\ns_waitcnt lgkmcnt(6)\nv_mfma_f32_16x16x32_bf16 v[0:3],a[196:199],a[228:231],0\nds_read_b128 a[212:215],v236 offset:8192\n"
"v_mfma_f32_16x16x32_bf16 v[28:31],a[200:203],a[228:231],0\nv_mfma_f32_16x16x32_bf16 v[56:59],a[204:207],a[228:231],0\nv_mfma_f32_16x16x32_bf16 v[84:87],a[208:211],a[228:231],0\nds_read_b128 a[228:231],v239 offset:1024\ns_waitcnt lgkmcnt(7)\nv_mfma_f32_16x16x32_bf16 v[4:7],a[196:199],a[232:235],0\nds_read_b128 a[216:219],v236 offset:10240\ns_add_u32 m0,s64,8192\n"
"v_mfma_f32_16x16x32_bf16 v[32:35],a[200:203],a[232:235],0\ns_mov_b32 s73,384\nv_mfma_f32_16x16x32_bf16 v[60:63],a[204:207],a[232:235],0\nv_mfma_f32_16x16x32_bf16 v[88:91],a[208:211],a[232:235],0\nds_read_b128 a[232:235],v239 offset:3072\ns_waitcnt lgkmcnt(8)\nv_mfma_f32_16x16x32_bf16 v[8:11],a[196:199],a[236:239],0\nv_mfma_f32_16x16x32_bf16 v[36:39],a[200:203],a[236:239],0\n"
"v_mfma_f32_16x16x32_bf16 v[64:67],a[204:207],a[236:239],0\nv_mfma_f32_16x16x32_bf16 v[92:95],a[208:211],a[236:239],0\nds_read_b128 a[236:239],v239 offset:5120\ns_waitcnt lgkmcnt(7)\nv_mfma_f32_16x16x32_bf16 v[12:15],a[196:199],a[240:243],0\nds_read_b128 a[220:223],v236 offset:12288\nv_mfma_f32_16x16x32_bf16 v[40:43],a[200:203],a[240:243],0\nv_mfma_f32_16x16x32_bf16 v[68:71],a[204:207],a[240:243],0\n"
"v_mfma_f32_16x16x32_bf16 v[96:99],a[208:211],a[240:243],0\nds_read_b128 a[240:243],v239 offset:7168\nv_mfma_f32_16x16x32_bf16 v[16:19],a[196:199],a[244:247],0\nds_read_b128 a[224:227],v236 offset:14336\nv_mfma_f32_16x16x32_bf16 v[44:47],a[200:203],a[244:247],0\nv_mfma_f32_16x16x32_bf16 v[72:75],a[204:207],a[244:247],0\nv_mfma_f32_16x16x32_bf16 v[100:103],a[208:211],a[244:247],0\nds_read_b128 a[244:247],v239 offset:9216\n"
"s_waitcnt lgkmcnt(9)\nv_mfma_f32_16x16x32_bf16 v[20:23],a[196:199],a[248:251],0\nv_mfma_f32_16x16x32_bf16 v[48:51],a[200:203],a[248:251],0\nv_mfma_f32_16x16x32_bf16 v[76:79],a[204:207],a[248:251],0\nv_mfma_f32_16x16x32_bf16 v[104:107],a[208:211],a[248:251],0\nds_read_b128 a[248:251],v239 offset:11264\nv_mfma_f32_16x16x32_bf16 v[24:27],a[196:199],a[252:255],0\nv_mfma_f32_16x16x32_bf16 v[52:55],a[200:203],a[252:255],0\n"
"v_mfma_f32_16x16x32_bf16 v[80:83],a[204:207],a[252:255],0\nv_mfma_f32_16x16x32_bf16 v[108:111],a[208:211],a[252:255],0\nds_read_b128 a[252:255],v239 offset:13312\ns_waitcnt lgkmcnt(3)\nv_mfma_f32_16x16x32_bf16 v[0:3],a[212:215],a[228:231],v[0:3]\nbuffer_load_dwordx4 v227,s[48:51],s73 offen offset:0 nt lds\ns_mov_b32 s74,7552\nv_mfma_f32_16x16x32_bf16 v[28:31],a[216:219],a[228:231],v[28:31]\n"
"s_waitcnt vmcnt(13)\nds_read_b128 a[196:199],v235 offset:16384\nv_mfma_f32_16x16x32_bf16 v[56:59],a[220:223],a[228:231],v[56:59]\nv_mfma_f32_16x16x32_bf16 v[84:87],a[224:227],a[228:231],v[84:87]\nds_read_b128 a[228:231],v239 offset:14336\nv_mfma_f32_16x16x32_bf16 v[4:7],a[212:215],a[232:235],v[4:7]\nbuffer_load_dwordx4 v228,s[48:51],s74 offen offset:1024 nt lds\ns_mov_b32 s75,31104\n"
"v_mfma_f32_16x16x32_bf16 v[32:35],a[216:219],a[232:235],v[32:35]\nds_read_b128 a[200:203],v235 offset:18432\nv_mfma_f32_16x16x32_bf16 v[60:63],a[220:223],a[232:235],v[60:63]\nv_mfma_f32_16x16x32_bf16 v[88:91],a[224:227],a[232:235],v[88:91]\nds_read_b128 a[232:235],v239 offset:16384\nv_mfma_f32_16x16x32_bf16 v[8:11],a[212:215],a[236:239],v[8:11]\nbuffer_load_dwordx4 v227,s[48:51],s75 offen offset:2048 nt lds\ns_mov_b32 s72,38272\n"
"v_mfma_f32_16x16x32_bf16 v[36:39],a[216:219],a[236:239],v[36:39]\nv_mfma_f32_16x16x32_bf16 v[64:67],a[220:223],a[236:239],v[64:67]\nv_mfma_f32_16x16x32_bf16 v[92:95],a[224:227],a[236:239],v[92:95]\nds_read_b128 a[236:239],v239 offset:18432\ns_waitcnt lgkmcnt(7)\nv_mfma_f32_16x16x32_bf16 v[12:15],a[212:215],a[240:243],v[12:15]\nbuffer_load_dwordx4 v228,s[48:51],s72 offen offset:3072 nt lds\ns_add_u32 m0,s64,12288\n"
"v_mfma_f32_16x16x32_bf16 v[40:43],a[216:219],a[240:243],v[40:43]\ns_waitcnt vmcnt(12)\nds_read_b128 a[204:207],v235 offset:20480\nv_mfma_f32_16x16x32_bf16 v[68:71],a[220:223],a[240:243],v[68:71]\ns_mov_b32 s73,65920\nv_mfma_f32_16x16x32_bf16 v[96:99],a[224:227],a[240:243],v[96:99]\nds_read_b128 a[240:243],v239 offset:20480\nv_mfma_f32_16x16x32_bf16 v[16:19],a[212:215],a[244:247],v[16:19]\n"
"buffer_load_dwordx4 v227,s[48:51],s73 offen offset:0 nt lds\ns_mov_b32 s74,73088\nv_mfma_f32_16x16x32_bf16 v[44:47],a[216:219],a[244:247],v[44:47]\nds_read_b128 a[208:211],v235 offset:22528\nv_mfma_f32_16x16x32_bf16 v[72:75],a[220:223],a[244:247],v[72:75]\nv_mfma_f32_16x16x32_bf16 v[100:103],a[224:227],a[244:247],v[100:103]\nds_read_b128 a[244:247],v239 offset:22528\ns_waitcnt lgkmcnt(9)\n"
"v_mfma_f32_16x16x32_bf16 v[20:23],a[212:215],a[248:251],v[20:23]\nbuffer_load_dwordx4 v228,s[48:51],s74 offen offset:1024 nt lds\ns_mov_b32 s75,96640\nv_mfma_f32_16x16x32_bf16 v[48:51],a[216:219],a[248:251],v[48:51]\nv_mfma_f32_16x16x32_bf16 v[76:79],a[220:223],a[248:251],v[76:79]\nv_mfma_f32_16x16x32_bf16 v[104:107],a[224:227],a[248:251],v[104:107]\nds_read_b128 a[248:251],v239 offset:24576\nv_mfma_f32_16x16x32_bf16 v[24:27],a[212:215],a[252:255],v[24:27]\n"
"buffer_load_dwordx4 v227,s[48:51],s75 offen offset:2048 nt lds\ns_mov_b32 s72,103808\nv_mfma_f32_16x16x32_bf16 v[52:55],a[216:219],a[252:255],v[52:55]\nv_mfma_f32_16x16x32_bf16 v[80:83],a[220:223],a[252:255],v[80:83]\nv_mfma_f32_16x16x32_bf16 v[108:111],a[224:227],a[252:255],v[108:111]\nds_read_b128 a[252:255],v239 offset:26624\ns_waitcnt lgkmcnt(3)\nv_mfma_f32_16x16x32_bf16 v[0:3],a[196:199],a[228:231],v[0:3]\n"
"buffer_load_dwordx4 v228,s[48:51],s72 offen offset:3072 nt lds\nv_mfma_f32_16x16x32_bf16 v[28:31],a[200:203],a[228:231],v[28:31]\nds_read_b128 a[212:215],v236 offset:16384\nv_mfma_f32_16x16x32_bf16 v[56:59],a[204:207],a[228:231],v[56:59]\nv_mfma_f32_16x16x32_bf16 v[84:87],a[208:211],a[228:231],v[84:87]\nds_read_b128 a[228:231],v239 offset:15360\nv_mfma_f32_16x16x32_bf16 v[4:7],a[196:199],a[232:235],v[4:7]\nds_read_b128 a[216:219],v236 offset:18432\n"
"s_add_u32 m0,s64,16384\nv_mfma_f32_16x16x32_bf16 v[32:35],a[200:203],a[232:235],v[32:35]\ns_mov_b32 s73,131072\nv_mfma_f32_16x16x32_bf16 v[60:63],a[204:207],a[232:235],v[60:63]\nv_mfma_f32_16x16x32_bf16 v[88:91],a[208:211],a[232:235],v[88:91]\nds_read_b128 a[232:235],v239 offset:17408\nv_mfma_f32_16x16x32_bf16 v[8:11],a[196:199],a[236:239],v[8:11]\nv_mfma_f32_16x16x32_bf16 v[36:39],a[200:203],a[236:239],v[36:39]\n"
"v_mfma_f32_16x16x32_bf16 v[64:67],a[204:207],a[236:239],v[64:67]\nv_mfma_f32_16x16x32_bf16 v[92:95],a[208:211],a[236:239],v[92:95]\nds_read_b128 a[236:239],v239 offset:19456\ns_waitcnt lgkmcnt(7)\nv_mfma_f32_16x16x32_bf16 v[12:15],a[196:199],a[240:243],v[12:15]\nds_read_b128 a[220:223],v236 offset:20480\nv_mfma_f32_16x16x32_bf16 v[40:43],a[200:203],a[240:243],v[40:43]\nv_mfma_f32_16x16x32_bf16 v[68:71],a[204:207],a[240:243],v[68:71]\n"
"v_mfma_f32_16x16x32_bf16 v[96:99],a[208:211],a[240:243],v[96:99]\nds_read_b128 a[240:243],v239 offset:21504\nv_mfma_f32_16x16x32_bf16 v[16:19],a[196:199],a[244:247],v[16:19]\nds_read_b128 a[224:227],v236 offset:22528\nv_mfma_f32_16x16x32_bf16 v[44:47],a[200:203],a[244:247],v[44:47]\nv_mfma_f32_16x16x32_bf16 v[72:75],a[204:207],a[244:247],v[72:75]\nv_mfma_f32_16x16x32_bf16 v[100:103],a[208:211],a[244:247],v[100:103]\nds_read_b128 a[244:247],v239 offset:23552\n"
"s_waitcnt lgkmcnt(9)\nv_mfma_f32_16x16x32_bf16 v[20:23],a[196:199],a[248:251],v[20:23]\nv_mfma_f32_16x16x32_bf16 v[48:51],a[200:203],a[248:251],v[48:51]\nv_mfma_f32_16x16x32_bf16 v[76:79],a[204:207],a[248:251],v[76:79]\nv_mfma_f32_16x16x32_bf16 v[104:107],a[208:211],a[248:251],v[104:107]\nds_read_b128 a[248:251],v239 offset:25600\nv_mfma_f32_16x16x32_bf16 v[24:27],a[196:199],a[252:255],v[24:27]\nv_mfma_f32_16x16x32_bf16 v[52:55],a[200:203],a[252:255],v[52:55]\n"
"v_mfma_f32_16x16x32_bf16 v[80:83],a[204:207],a[252:255],v[80:83]\nv_mfma_f32_16x16x32_bf16 v[108:111],a[208:211],a[252:255],v[108:111]\nds_read_b128 a[252:255],v239 offset:27648\ns_waitcnt lgkmcnt(3)\nv_mfma_f32_16x16x32_bf16 v[0:3],a[212:215],a[228:231],v[0:3]\nbuffer_load_dwordx4 v227,s[48:51],s73 offen offset:0 nt lds\ns_mov_b32 s74,138240\nv_mfma_f32_16x16x32_bf16 v[28:31],a[216:219],a[228:231],v[28:31]\n"
"s_waitcnt vmcnt(13)\nds_read_b128 a[196:199],v235 offset:0\nv_mfma_f32_16x16x32_bf16 v[56:59],a[220:223],a[228:231],v[56:59]\nv_mfma_f32_16x16x32_bf16 v[84:87],a[224:227],a[228:231],v[84:87]\nds_read_b128 a[228:231],v239 offset:28672\nv_mfma_f32_16x16x32_bf16 v[4:7],a[212:215],a[232:235],v[4:7]\nbuffer_load_dwordx4 v228,s[48:51],s74 offen offset:1024 nt lds\ns_mov_b32 s75,161792\n"
"v_mfma_f32_16x16x32_bf16 v[32:35],a[216:219],a[232:235],v[32:35]\nds_read_b128 a[200:203],v235 offset:2048\nv_mfma_f32_16x16x32_bf16 v[60:63],a[220:223],a[232:235],v[60:63]\nv_mfma_f32_16x16x32_bf16 v[88:91],a[224:227],a[232:235],v[88:91]\nds_read_b128 a[232:235],v239 offset:30720\nv_mfma_f32_16x16x32_bf16 v[8:11],a[212:215],a[236:239],v[8:11]\nbuffer_load_dwordx4 v227,s[48:51],s75 offen offset:2048 nt lds\ns_mov_b32 s72,168960\n"
"v_mfma_f32_16x16x32_bf16 v[36:39],a[216:219],a[236:239],v[36:39]\nv_mfma_f32_16x16x32_bf16 v[64:67],a[220:223],a[236:239],v[64:67]\nv_mfma_f32_16x16x32_bf16 v[92:95],a[224:227],a[236:239],v[92:95]\nds_read_b128 a[236:239],v239 offset:32768\ns_waitcnt lgkmcnt(7)\nv_mfma_f32_16x16x32_bf16 v[12:15],a[212:215],a[240:243],v[12:15]\nbuffer_load_dwordx4 v228,s[48:51],s72 offen offset:3072 nt lds\ns_add_u32 m0,s64,20480\n"
"v_mfma_f32_16x16x32_bf16 v[40:43],a[216:219],a[240:243],v[40:43]\ns_waitcnt vmcnt(12)\nds_read_b128 a[204:207],v235 offset:4096\nv_mfma_f32_16x16x32_bf16 v[68:71],a[220:223],a[240:243],v[68:71]\ns_mov_b32 s73,196608\nv_mfma_f32_16x16x32_bf16 v[96:99],a[224:227],a[240:243],v[96:99]\nds_read_b128 a[240:243],v239 offset:34816\nv_mfma_f32_16x16x32_bf16 v[16:19],a[212:215],a[244:247],v[16:19]\n"
"buffer_load_dwordx4 v227,s[48:51],s73 offen offset:0 nt lds\ns_mov_b32 s74,203776\nv_mfma_f32_16x16x32_bf16 v[44:47],a[216:219],a[244:247],v[44:47]\nds_read_b128 a[208:211],v235 offset:6144\nv_mfma_f32_16x16x32_bf16 v[72:75],a[220:223],a[244:247],v[72:75]\nv_mfma_f32_16x16x32_bf16 v[100:103],a[224:227],a[244:247],v[100:103]\nds_read_b128 a[244:247],v239 offset:36864\ns_waitcnt lgkmcnt(9)\n"
"v_mfma_f32_16x16x32_bf16 v[20:23],a[212:215],a[248:251],v[20:23]\nbuffer_load_dwordx4 v228,s[48:51],s74 offen offset:1024 nt lds\ns_mov_b32 s75,227328\nv_mfma_f32_16x16x32_bf16 v[48:51],a[216:219],a[248:251],v[48:51]\nv_mfma_f32_16x16x32_bf16 v[76:79],a[220:223],a[248:251],v[76:79]\nv_mfma_f32_16x16x32_bf16 v[104:107],a[224:227],a[248:251],v[104:107]\nds_read_b128 a[248:251],v239 offset:38912\nv_mfma_f32_16x16x32_bf16 v[24:27],a[212:215],a[252:255],v[24:27]\n"
"buffer_load_dwordx4 v227,s[48:51],s75 offen offset:2048 nt lds\ns_mov_b32 s72,234496\nv_mfma_f32_16x16x32_bf16 v[52:55],a[216:219],a[252:255],v[52:55]\nv_mfma_f32_16x16x32_bf16 v[80:83],a[220:223],a[252:255],v[80:83]\nv_mfma_f32_16x16x32_bf16 v[108:111],a[224:227],a[252:255],v[108:111]\nds_read_b128 a[252:255],v239 offset:40960\ns_waitcnt lgkmcnt(3)\nv_mfma_f32_16x16x32_bf16 v[0:3],a[196:199],a[228:231],v[0:3]\n"
"buffer_load_dwordx4 v228,s[48:51],s72 offen offset:3072 nt lds\nv_mfma_f32_16x16x32_bf16 v[28:31],a[200:203],a[228:231],v[28:31]\nds_read_b128 a[212:215],v236 offset:0\nv_mfma_f32_16x16x32_bf16 v[56:59],a[204:207],a[228:231],v[56:59]\nv_mfma_f32_16x16x32_bf16 v[84:87],a[208:211],a[228:231],v[84:87]\nds_read_b128 a[228:231],v239 offset:29696\nv_mfma_f32_16x16x32_bf16 v[4:7],a[196:199],a[232:235],v[4:7]\nds_read_b128 a[216:219],v236 offset:2048\n"
"s_add_u32 m0,s64,0\nv_mfma_f32_16x16x32_bf16 v[32:35],a[200:203],a[232:235],v[32:35]\ns_mov_b32 s73,131200\nv_mfma_f32_16x16x32_bf16 v[60:63],a[204:207],a[232:235],v[60:63]\nv_mfma_f32_16x16x32_bf16 v[88:91],a[208:211],a[232:235],v[88:91]\nds_read_b128 a[232:235],v239 offset:31744\nv_mfma_f32_16x16x32_bf16 v[8:11],a[196:199],a[236:239],v[8:11]\nv_mfma_f32_16x16x32_bf16 v[36:39],a[200:203],a[236:239],v[36:39]\n"
"v_mfma_f32_16x16x32_bf16 v[64:67],a[204:207],a[236:239],v[64:67]\nv_mfma_f32_16x16x32_bf16 v[92:95],a[208:211],a[236:239],v[92:95]\nds_read_b128 a[236:239],v239 offset:33792\ns_waitcnt lgkmcnt(7)\nv_mfma_f32_16x16x32_bf16 v[12:15],a[196:199],a[240:243],v[12:15]\nds_read_b128 a[220:223],v236 offset:4096\nv_mfma_f32_16x16x32_bf16 v[40:43],a[200:203],a[240:243],v[40:43]\nv_mfma_f32_16x16x32_bf16 v[68:71],a[204:207],a[240:243],v[68:71]\n"
"v_mfma_f32_16x16x32_bf16 v[96:99],a[208:211],a[240:243],v[96:99]\nds_read_b128 a[240:243],v239 offset:35840\nv_mfma_f32_16x16x32_bf16 v[16:19],a[196:199],a[244:247],v[16:19]\nds_read_b128 a[224:227],v236 offset:6144\nv_mfma_f32_16x16x32_bf16 v[44:47],a[200:203],a[244:247],v[44:47]\nv_mfma_f32_16x16x32_bf16 v[72:75],a[204:207],a[244:247],v[72:75]\nv_mfma_f32_16x16x32_bf16 v[100:103],a[208:211],a[244:247],v[100:103]\nds_read_b128 a[244:247],v239 offset:37888\n"
"s_waitcnt lgkmcnt(9)\nv_mfma_f32_16x16x32_bf16 v[20:23],a[196:199],a[248:251],v[20:23]\nv_mfma_f32_16x16x32_bf16 v[48:51],a[200:203],a[248:251],v[48:51]\nv_mfma_f32_16x16x32_bf16 v[76:79],a[204:207],a[248:251],v[76:79]\nv_mfma_f32_16x16x32_bf16 v[104:107],a[208:211],a[248:251],v[104:107]\nds_read_b128 a[248:251],v239 offset:39936\nv_mfma_f32_16x16x32_bf16 v[24:27],a[196:199],a[252:255],v[24:27]\nv_mfma_f32_16x16x32_bf16 v[52:55],a[200:203],a[252:255],v[52:55]\n"
"v_mfma_f32_16x16x32_bf16 v[80:83],a[204:207],a[252:255],v[80:83]\nv_mfma_f32_16x16x32_bf16 v[108:111],a[208:211],a[252:255],v[108:111]\nds_read_b128 a[252:255],v239 offset:41984\ns_waitcnt lgkmcnt(3)\nv_mfma_f32_16x16x32_bf16 v[0:3],a[212:215],a[228:231],v[0:3]\nbuffer_load_dwordx4 v227,s[48:51],s73 offen offset:0 nt lds\ns_mov_b32 s74,138368\nv_mfma_f32_16x16x32_bf16 v[28:31],a[216:219],a[228:231],v[28:31]\n"
"s_waitcnt vmcnt(13)\nds_read_b128 a[196:199],v235 offset:8192\nv_mfma_f32_16x16x32_bf16 v[56:59],a[220:223],a[228:231],v[56:59]\nv_mfma_f32_16x16x32_bf16 v[84:87],a[224:227],a[228:231],v[84:87]\nds_read_b128 a[228:231],v239 offset:43008\nv_mfma_f32_16x16x32_bf16 v[4:7],a[212:215],a[232:235],v[4:7]\nbuffer_load_dwordx4 v228,s[48:51],s74 offen offset:1024 nt lds\ns_mov_b32 s75,161920\n"
"v_mfma_f32_16x16x32_bf16 v[32:35],a[216:219],a[232:235],v[32:35]\nds_read_b128 a[200:203],v235 offset:10240\nv_mfma_f32_16x16x32_bf16 v[60:63],a[220:223],a[232:235],v[60:63]\nv_mfma_f32_16x16x32_bf16 v[88:91],a[224:227],a[232:235],v[88:91]\nds_read_b128 a[232:235],v239 offset:45056\nv_mfma_f32_16x16x32_bf16 v[8:11],a[212:215],a[236:239],v[8:11]\nbuffer_load_dwordx4 v227,s[48:51],s75 offen offset:2048 nt lds\ns_mov_b32 s72,169088\n"
"v_mfma_f32_16x16x32_bf16 v[36:39],a[216:219],a[236:239],v[36:39]\nv_mfma_f32_16x16x32_bf16 v[64:67],a[220:223],a[236:239],v[64:67]\nv_mfma_f32_16x16x32_bf16 v[92:95],a[224:227],a[236:239],v[92:95]\nds_read_b128 a[236:239],v239 offset:47104\ns_waitcnt lgkmcnt(7)\nv_mfma_f32_16x16x32_bf16 v[12:15],a[212:215],a[240:243],v[12:15]\nbuffer_load_dwordx4 v228,s[48:51],s72 offen offset:3072 nt lds\ns_add_u32 m0,s64,4096\n"
"v_mfma_f32_16x16x32_bf16 v[40:43],a[216:219],a[240:243],v[40:43]\ns_waitcnt vmcnt(12)\nds_read_b128 a[204:207],v235 offset:12288\nv_mfma_f32_16x16x32_bf16 v[68:71],a[220:223],a[240:243],v[68:71]\ns_mov_b32 s73,196736\nv_mfma_f32_16x16x32_bf16 v[96:99],a[224:227],a[240:243],v[96:99]\nds_read_b128 a[240:243],v239 offset:49152\nv_mfma_f32_16x16x32_bf16 v[16:19],a[212:215],a[244:247],v[16:19]\n"
"buffer_load_dwordx4 v227,s[48:51],s73 offen offset:0 nt lds\ns_mov_b32 s74,203904\nv_mfma_f32_16x16x32_bf16 v[44:47],a[216:219],a[244:247],v[44:47]\nds_read_b128 a[208:211],v235 offset:14336\nv_mfma_f32_16x16x32_bf16 v[72:75],a[220:223],a[244:247],v[72:75]\nv_mfma_f32_16x16x32_bf16 v[100:103],a[224:227],a[244:247],v[100:103]\nds_read_b128 a[244:247],v239 offset:51200\ns_waitcnt lgkmcnt(9)\n"
"v_mfma_f32_16x16x32_bf16 v[20:23],a[212:215],a[248:251],v[20:23]\nbuffer_load_dwordx4 v228,s[48:51],s74 offen offset:1024 nt lds\ns_mov_b32 s75,227456\nv_mfma_f32_16x16x32_bf16 v[48:51],a[216:219],a[248:251],v[48:51]\nv_mfma_f32_16x16x32_bf16 v[76:79],a[220:223],a[248:251],v[76:79]\nv_mfma_f32_16x16x32_bf16 v[104:107],a[224:227],a[248:251],v[104:107]\nds_read_b128 a[248:251],v239 offset:53248\nv_mfma_f32_16x16x32_bf16 v[24:27],a[212:215],a[252:255],v[24:27]\n"
"buffer_load_dwordx4 v227,s[48:51],s75 offen offset:2048 nt lds\ns_mov_b32 s72,234624\nv_mfma_f32_16x16x32_bf16 v[52:55],a[216:219],a[252:255],v[52:55]\nv_mfma_f32_16x16x32_bf16 v[80:83],a[220:223],a[252:255],v[80:83]\nv_mfma_f32_16x16x32_bf16 v[108:111],a[224:227],a[252:255],v[108:111]\nds_read_b128 a[252:255],v239 offset:55296\ns_waitcnt lgkmcnt(3)\nv_mfma_f32_16x16x32_bf16 v[0:3],a[196:199],a[228:231],v[0:3]\n"
"buffer_load_dwordx4 v228,s[48:51],s72 offen offset:3072 nt lds\nv_mfma_f32_16x16x32_bf16 v[28:31],a[200:203],a[228:231],v[28:31]\nds_read_b128 a[212:215],v236 offset:8192\nv_mfma_f32_16x16x32_bf16 v[56:59],a[204:207],a[228:231],v[56:59]\nv_mfma_f32_16x16x32_bf16 v[84:87],a[208:211],a[228:231],v[84:87]\nds_read_b128 a[228:231],v239 offset:44032\nv_mfma_f32_16x16x32_bf16 v[4:7],a[196:199],a[232:235],v[4:7]\nds_read_b128 a[216:219],v236 offset:10240\n"
"s_add_u32 m0,s64,8192\nv_mfma_f32_16x16x32_bf16 v[32:35],a[200:203],a[232:235],v[32:35]\ns_mov_b32 s73,131328\nv_mfma_f32_16x16x32_bf16 v[60:63],a[204:207],a[232:235],v[60:63]\nv_mfma_f32_16x16x32_bf16 v[88:91],a[208:211],a[232:235],v[88:91]\nds_read_b128 a[232:235],v239 offset:46080\nv_mfma_f32_16x16x32_bf16 v[8:11],a[196:199],a[236:239],v[8:11]\nv_mfma_f32_16x16x32_bf16 v[36:39],a[200:203],a[236:239],v[36:39]\n"
"v_mfma_f32_16x16x32_bf16 v[64:67],a[204:207],a[236:239],v[64:67]\nv_mfma_f32_16x16x32_bf16 v[92:95],a[208:211],a[236:239],v[92:95]\nds_read_b128 a[236:239],v239 offset:48128\ns_waitcnt lgkmcnt(7)\nv_mfma_f32_16x16x32_bf16 v[12:15],a[196:199],a[240:243],v[12:15]\nds_read_b128 a[220:223],v236 offset:12288\nv_mfma_f32_16x16x32_bf16 v[40:43],a[200:203],a[240:243],v[40:43]\nv_mfma_f32_16x16x32_bf16 v[68:71],a[204:207],a[240:243],v[68:71]\n"
"v_mfma_f32_16x16x32_bf16 v[96:99],a[208:211],a[240:243],v[96:99]\nds_read_b128 a[240:243],v239 offset:50176\nv_mfma_f32_16x16x32_bf16 v[16:19],a[196:199],a[244:247],v[16:19]\nds_read_b128 a[224:227],v236 offset:14336\nv_mfma_f32_16x16x32_bf16 v[44:47],a[200:203],a[244:247],v[44:47]\nv_mfma_f32_16x16x32_bf16 v[72:75],a[204:207],a[244:247],v[72:75]\nv_mfma_f32_16x16x32_bf16 v[100:103],a[208:211],a[244:247],v[100:103]\nds_read_b128 a[244:247],v239 offset:52224\n"
"s_waitcnt lgkmcnt(9)\nv_mfma_f32_16x16x32_bf16 v[20:23],a[196:199],a[248:251],v[20:23]\nv_mfma_f32_16x16x32_bf16 v[48:51],a[200:203],a[248:251],v[48:51]\nv_mfma_f32_16x16x32_bf16 v[76:79],a[204:207],a[248:251],v[76:79]\nv_mfma_f32_16x16x32_bf16 v[104:107],a[208:211],a[248:251],v[104:107]\nds_read_b128 a[248:251],v239 offset:54272\nv_mfma_f32_16x16x32_bf16 v[24:27],a[196:199],a[252:255],v[24:27]\nv_mfma_f32_16x16x32_bf16 v[52:55],a[200:203],a[252:255],v[52:55]\n"
"v_mfma_f32_16x16x32_bf16 v[80:83],a[204:207],a[252:255],v[80:83]\nv_mfma_f32_16x16x32_bf16 v[108:111],a[208:211],a[252:255],v[108:111]\nds_read_b128 a[252:255],v239 offset:56320\ns_waitcnt lgkmcnt(3)\nv_mfma_f32_16x16x32_bf16 v[0:3],a[212:215],a[228:231],v[0:3]\nbuffer_load_dwordx4 v227,s[48:51],s73 offen offset:0 nt lds\nv_bfe_u32 v255,v239,2,6\nv_mfma_f32_16x16x32_bf16 v[28:31],a[216:219],a[228:231],v[28:31]\n"
"s_waitcnt vmcnt(13)\nds_read_b128 a[196:199],v235 offset:16384\nv_mfma_f32_16x16x32_bf16 v[56:59],a[220:223],a[228:231],v[56:59]\ns_mov_b32 s74,138496\nv_mfma_f32_16x16x32_bf16 v[84:87],a[224:227],a[228:231],v[84:87]\nds_read_b128 a[228:231],v239 offset:0\nv_mfma_f32_16x16x32_bf16 v[4:7],a[212:215],a[232:235],v[4:7]\nbuffer_load_dwordx4 v228,s[48:51],s74 offen offset:1024 nt lds\n"
"s_mov_b32 s75,162048\nv_mfma_f32_16x16x32_bf16 v[32:35],a[216:219],a[232:235],v[32:35]\nds_read_b128 a[200:203],v235 offset:18432\nv_mfma_f32_16x16x32_bf16 v[60:63],a[220:223],a[232:235],v[60:63]\nv_mfma_f32_16x16x32_bf16 v[88:91],a[224:227],a[232:235],v[88:91]\nds_read_b128 a[232:235],v239 offset:2048\nv_mfma_f32_16x16x32_bf16 v[8:11],a[212:215],a[236:239],v[8:11]\nbuffer_load_dwordx4 v227,s[48:51],s75 offen offset:2048 nt lds\n"
"v_max3_f32 v241,v0,v1,v0\nv_mfma_f32_16x16x32_bf16 v[36:39],a[216:219],a[236:239],v[36:39]\ns_mov_b32 s72,169216\nv_max3_f32 v241,v2,v3,v241\nv_max3_f32 v241,v28,v29,v241\nv_max3_f32 v241,v30,v31,v241\nv_mfma_f32_16x16x32_bf16 v[64:67],a[220:223],a[236:239],v[64:67]\nv_max3_f32 v241,v56,v57,v241\n"
"v_max3_f32 v241,v58,v59,v241\nv_mfma_f32_16x16x32_bf16 v[92:95],a[224:227],a[236:239],v[92:95]\nds_read_b128 a[236:239],v239 offset:4096\ns_waitcnt lgkmcnt(7)\nv_max3_f32 v241,v84,v85,v241\nv_mfma_f32_16x16x32_bf16 v[12:15],a[212:215],a[240:243],v[12:15]\nbuffer_load_dwordx4 v228,s[48:51],s72 offen offset:3072 nt lds\nv_max3_f32 v241,v86,v87,v241\n"
"v_mfma_f32_16x16x32_bf16 v[40:43],a[216:219],a[240:243],v[40:43]\ns_waitcnt vmcnt(12)\nds_read_b128 a[204:207],v235 offset:20480\nv_mul_f32 v241,s69,v241\nv_mfma_f32_16x16x32_bf16 v[68:71],a[220:223],a[240:243],v[68:71]\ns_add_u32 m0,s64,12288\nds_max_f32 v255,v241 offset:57344\nv_mfma_f32_16x16x32_bf16 v[96:99],a[224:227],a[240:243],v[96:99]\n"
"ds_read_b128 a[240:243],v239 offset:6144\nv_max3_f32 v242,v4,v5,v4\nv_max3_f32 v242,v6,v7,v242\nv_mfma_f32_16x16x32_bf16 v[16:19],a[212:215],a[244:247],v[16:19]\ns_mov_b32 s73,196864\nbuffer_load_dwordx4 v227,s[48:51],s73 offen offset:0 nt lds\nv_mfma_f32_16x16x32_bf16 v[44:47],a[216:219],a[244:247],v[44:47]\nds_read_b128 a[208:211],v235 offset:22528\n"
"v_max3_f32 v242,v32,v33,v242\nv_max3_f32 v242,v34,v35,v242\nv_mfma_f32_16x16x32_bf16 v[72:75],a[220:223],a[244:247],v[72:75]\ns_mov_b32 s74,204032\nv_max3_f32 v242,v60,v61,v242\nv_max3_f32 v242,v62,v63,v242\nv_mfma_f32_16x16x32_bf16 v[100:103],a[224:227],a[244:247],v[100:103]\nds_read_b128 a[244:247],v239 offset:8192\n"
"s_waitcnt lgkmcnt(10)\nv_max3_f32 v242,v88,v89,v242\nv_mfma_f32_16x16x32_bf16 v[20:23],a[212:215],a[248:251],v[20:23]\nbuffer_load_dwordx4 v228,s[48:51],s74 offen offset:1024 nt lds\nv_max3_f32 v242,v90,v91,v242\nv_mfma_f32_16x16x32_bf16 v[48:51],a[216:219],a[248:251],v[48:51]\ns_mov_b32 s75,227584\nv_mul_f32 v242,s69,v242\n"
"v_max3_f32 v243,v8,v9,v8\nv_max3_f32 v243,v10,v11,v243\nv_mfma_f32_16x16x32_bf16 v[76:79],a[220:223],a[248:251],v[76:79]\nds_max_f32 v255,v242 offset:57408\nv_mfma_f32_16x16x32_bf16 v[104:107],a[224:227],a[248:251],v[104:107]\nds_read_b128 a[248:251],v239 offset:10240\nv_max3_f32 v243,v36,v37,v243\nv_max3_f32 v243,v38,v39,v243\n"
"v_mfma_f32_16x16x32_bf16 v[24:27],a[212:215],a[252:255],v[24:27]\nbuffer_load_dwordx4 v227,s[48:51],s75 offen offset:2048 nt lds\nv_max3_f32 v243,v64,v65,v243\nv_mfma_f32_16x16x32_bf16 v[52:55],a[216:219],a[252:255],v[52:55]\ns_mov_b32 s72,234752\nv_max3_f32 v243,v66,v67,v243\nv_max3_f32 v243,v92,v93,v243\nv_max3_f32 v243,v94,v95,v243\n"
"v_mfma_f32_16x16x32_bf16 v[80:83],a[220:223],a[252:255],v[80:83]\nv_mul_f32 v243,s69,v243\nv_max3_f32 v244,v12,v13,v12\nv_mfma_f32_16x16x32_bf16 v[108:111],a[224:227],a[252:255],v[108:111]\nds_read_b128 a[252:255],v239 offset:12288\ns_waitcnt lgkmcnt(4)\nv_max3_f32 v244,v14,v15,v244\nv_mfma_f32_16x16x32_bf16 v[112:115],a[196:199],a[228:231],0\n"
"buffer_load_dwordx4 v228,s[48:51],s72 offen offset:3072 nt lds\nv_max3_f32 v244,v40,v41,v244\nv_mfma_f32_16x16x32_bf16 v[140:143],a[200:203],a[228:231],0\nds_read_b128 a[212:215],v236 offset:16384\nv_max3_f32 v244,v42,v43,v244\nv_max3_f32 v244,v68,v69,v244\nv_mfma_f32_16x16x32_bf16 v[168:171],a[204:207],a[228:231],0\nds_max_f32 v255,v243 offset:57472\n"
"v_mfma_f32_16x16x32_bf16 v[196:199],a[208:211],a[228:231],0\nds_read_b128 a[228:231],v239 offset:1024\nv_max3_f32 v244,v70,v71,v244\nv_max3_f32 v244,v96,v97,v244\nv_mfma_f32_16x16x32_bf16 v[116:119],a[196:199],a[232:235],0\nds_read_b128 a[216:219],v236 offset:18432\nv_max3_f32 v244,v98,v99,v244\nv_mfma_f32_16x16x32_bf16 v[144:147],a[200:203],a[232:235],0\n"
"s_add_u32 m0,s64,16384\nv_mul_f32 v244,s69,v244\nv_max3_f32 v245,v16,v17,v16\nv_max3_f32 v245,v18,v19,v245\nv_mfma_f32_16x16x32_bf16 v[172:175],a[204:207],a[232:235],0\ns_mov_b32 s73,131456\nds_max_f32 v255,v244 offset:57536\nv_mfma_f32_16x16x32_bf16 v[200:203],a[208:211],a[232:235],0\n"
"ds_read_b128 a[232:235],v239 offset:3072\nv_max3_f32 v245,v44,v45,v245\nv_max3_f32 v245,v46,v47,v245\nv_mfma_f32_16x16x32_bf16 v[120:123],a[196:199],a[236:239],0\nv_max3_f32 v245,v72,v73,v245\nv_max3_f32 v245,v74,v75,v245\nv_mfma_f32_16x16x32_bf16 v[148:151],a[200:203],a[236:239],0\nv_max3_f32 v245,v100,v101,v245\n"
"v_max3_f32 v245,v102,v103,v245\nv_mul_f32 v245,s69,v245\nv_mfma_f32_16x16x32_bf16 v[176:179],a[204:207],a[236:239],0\nds_max_f32 v255,v245 offset:57600\nv_mfma_f32_16x16x32_bf16 v[204:207],a[208:211],a[236:239],0\nds_read_b128 a[236:239],v239 offset:5120\ns_waitcnt lgkmcnt(11)\nv_max3_f32 v246,v20,v21,v20\n"
"v_mfma_f32_16x16x32_bf16 v[124:127],a[196:199],a[240:243],0\nds_read_b128 a[220:223],v236 offset:20480\nv_max3_f32 v246,v22,v23,v246\nv_mfma_f32_16x16x32_bf16 v[152:155],a[200:203],a[240:243],0\nv_max3_f32 v246,v48,v49,v246\nv_max3_f32 v246,v50,v51,v246\nv_max3_f32 v246,v76,v77,v246\nv_mfma_f32_16x16x32_bf16 v[180:183],a[204:207],a[240:243],0\n"
"v_max3_f32 v246,v78,v79,v246\nv_max3_f32 v246,v104,v105,v246\nv_mfma_f32_16x16x32_bf16 v[208:211],a[208:211],a[240:243],0\nds_read_b128 a[240:243],v239 offset:7168\nv_max3_f32 v246,v106,v107,v246\nv_mul_f32 v246,s69,v246\nv_mfma_f32_16x16x32_bf16 v[128:131],a[196:199],a[244:247],0\nds_read_b128 a[224:227],v236 offset:22528\n"
"v_max3_f32 v247,v24,v25,v24\nv_mfma_f32_16x16x32_bf16 v[156:159],a[200:203],a[244:247],0\nds_max_f32 v255,v246 offset:57664\nv_mfma_f32_16x16x32_bf16 v[184:187],a[204:207],a[244:247],0\nv_max3_f32 v247,v26,v27,v247\nv_max3_f32 v247,v52,v53,v247\nv_mfma_f32_16x16x32_bf16 v[212:215],a[208:211],a[244:247],0\nds_read_b128 a[244:247],v239 offset:9216\n"
"s_waitcnt lgkmcnt(13)\nv_max3_f32 v247,v54,v55,v247\nv_mfma_f32_16x16x32_bf16 v[132:135],a[196:199],a[248:251],0\nv_max3_f32 v247,v80,v81,v247\nv_max3_f32 v247,v82,v83,v247\nv_mfma_f32_16x16x32_bf16 v[160:163],a[200:203],a[248:251],0\nv_max3_f32 v247,v108,v109,v247\nv_max3_f32 v247,v110,v111,v247\n"
"v_mul_f32 v247,s69,v247\nv_mfma_f32_16x16x32_bf16 v[188:191],a[204:207],a[248:251],0\nds_max_f32 v255,v247 offset:57728\nv_mfma_f32_16x16x32_bf16 v[216:219],a[208:211],a[248:251],0\nds_read_b128 a[248:251],v239 offset:11264\nv_mfma_f32_16x16x32_bf16 v[136:139],a[196:199],a[252:255],0\nv_mfma_f32_16x16x32_bf16 v[164:167],a[200:203],a[252:255],0\nv_mfma_f32_16x16x32_bf16 v[192:195],a[204:207],a[252:255],0\n"
"v_mfma_f32_16x16x32_bf16 v[220:223],a[208:211],a[252:255],0\nds_read_b128 a[252:255],v239 offset:13312\ns_waitcnt lgkmcnt(5)\ns_waitcnt lgkmcnt(1)\nv_mfma_f32_16x16x32_bf16 v[112:115],a[212:215],a[228:231],v[112:115]\nbuffer_load_dwordx4 v227,s[48:51],s73 offen offset:0 nt lds\ns_barrier\nv_mfma_f32_16x16x32_bf16 v[140:143],a[216:219],a[228:231],v[140:143]\n"
"s_waitcnt vmcnt(13)\nds_read_b128 a[196:199],v235 offset:0\nv_mfma_f32_16x16x32_bf16 v[168:171],a[220:223],a[228:231],v[168:171]\ns_mov_b32 s74,138624\nds_read_b32 v241,v255 offset:57344\nv_mfma_f32_16x16x32_bf16 v[196:199],a[224:227],a[228:231],v[196:199]\nds_read_b128 a[228:231],v239 offset:14336\nds_read_b32 v242,v255 offset:57408\n"
"v_mfma_f32_16x16x32_bf16 v[116:119],a[212:215],a[232:235],v[116:119]\nbuffer_load_dwordx4 v228,s[48:51],s74 offen offset:1024 nt lds\ns_mov_b32 s75,162176\nv_mfma_f32_16x16x32_bf16 v[144:147],a[216:219],a[232:235],v[144:147]\nds_read_b128 a[200:203],v235 offset:2048\nds_read_b32 v243,v255 offset:57472\nv_mfma_f32_16x16x32_bf16 v[172:175],a[220:223],a[232:235],v[172:175]\nds_read_b32 v244,v255 offset:57536\n"
"v_mfma_f32_16x16x32_bf16 v[200:203],a[224:227],a[232:235],v[200:203]\nds_read_b128 a[232:235],v239 offset:16384\nds_read_b32 v245,v255 offset:57600\nv_mfma_f32_16x16x32_bf16 v[120:123],a[212:215],a[236:239],v[120:123]\nbuffer_load_dwordx4 v227,s[48:51],s75 offen offset:2048 nt lds\ns_mov_b32 s72,169344\nv_mfma_f32_16x16x32_bf16 v[148:151],a[216:219],a[236:239],v[148:151]\nds_read_b32 v246,v255 offset:57664\n"
"ds_read_b32 v247,v255 offset:57728\nv_mfma_f32_16x16x32_bf16 v[176:179],a[220:223],a[236:239],v[176:179]\nv_mfma_f32_16x16x32_bf16 v[204:207],a[224:227],a[236:239],v[204:207]\nds_read_b128 a[236:239],v239 offset:18432\nv_mfma_f32_16x16x32_bf16 v[124:127],a[212:215],a[240:243],v[124:127]\nbuffer_load_dwordx4 v228,s[48:51],s72 offen offset:3072 nt lds\ns_add_u32 m0,s64,20480\nv_mfma_f32_16x16x32_bf16 v[152:155],a[216:219],a[240:243],v[152:155]\n"
"s_waitcnt vmcnt(12)\nds_read_b128 a[204:207],v235 offset:4096\ns_waitcnt lgkmcnt(2)\nv_mfma_f32_16x16x32_bf16 v[180:183],a[220:223],a[240:243],v[180:183]\ns_mov_b32 s73,196992\nv_fma_f32 v0,v0,s69,-v241\nv_fma_f32 v1,v1,s69,-v241\nv_mfma_f32_16x16x32_bf16 v[208:211],a[224:227],a[240:243],v[208:211]\n"
"ds_read_b128 a[240:243],v239 offset:20480\nv_fma_f32 v2,v2,s69,-v241\nv_fma_f32 v3,v3,s69,-v241\nv_mfma_f32_16x16x32_bf16 v[128:131],a[212:215],a[244:247],v[128:131]\nbuffer_load_dwordx4 v227,s[48:51],s73 offen offset:0 nt lds\nv_fma_f32 v4,v4,s69,-v242\nv_mfma_f32_16x16x32_bf16 v[156:159],a[216:219],a[244:247],v[156:159]\nds_read_b128 a[208:211],v235 offset:6144\n"
"v_exp_f32 v0,v0\nv_mfma_f32_16x16x32_bf16 v[184:187],a[220:223],a[244:247],v[184:187]\ns_mov_b32 s74,204160\nv_exp_f32 v1,v1\nv_mfma_f32_16x16x32_bf16 v[212:215],a[224:227],a[244:247],v[212:215]\nds_read_b128 a[244:247],v239 offset:22528\nv_exp_f32 v2,v2\nv_mfma_f32_16x16x32_bf16 v[132:135],a[212:215],a[248:251],v[132:135]\n"
"buffer_load_dwordx4 v228,s[48:51],s74 offen offset:1024 nt lds\nv_cvt_pk_bf16_f32 v0,v0,v1\nv_mfma_f32_16x16x32_bf16 v[160:163],a[216:219],a[248:251],v[160:163]\ns_mov_b32 s75,227712\nv_exp_f32 v3,v3\nv_fma_f32 v5,v5,s69,-v242\nv_mfma_f32_16x16x32_bf16 v[188:191],a[220:223],a[248:251],v[188:191]\nv_cvt_pk_bf16_f32 v1,v2,v3\n"
"v_fma_f32 v6,v6,s69,-v242\nv_mfma_f32_16x16x32_bf16 v[216:219],a[224:227],a[248:251],v[216:219]\nds_read_b128 a[248:251],v239 offset:24576\nv_fma_f32 v7,v7,s69,-v242\nv_fma_f32 v8,v8,s69,-v243\nv_mfma_f32_16x16x32_bf16 v[136:139],a[212:215],a[252:255],v[136:139]\nbuffer_load_dwordx4 v227,s[48:51],s75 offen offset:2048 nt lds\nv_fma_f32 v9,v9,s69,-v243\n"
"v_mfma_f32_16x16x32_bf16 v[164:167],a[216:219],a[252:255],v[164:167]\ns_mov_b32 s72,234880\nv_exp_f32 v4,v4\nv_fma_f32 v10,v10,s69,-v243\nv_mfma_f32_16x16x32_bf16 v[192:195],a[220:223],a[252:255],v[192:195]\nv_exp_f32 v5,v5\nv_mfma_f32_16x16x32_bf16 v[220:223],a[224:227],a[252:255],v[220:223]\nds_read_b128 a[252:255],v239 offset:26624\n"
"s_waitcnt lgkmcnt(3)\nv_cvt_pk_bf16_f32 v4,v4,v5\nv_mfma_f32_16x16x32_bf16 v[112:115],a[196:199],a[228:231],v[112:115]\nbuffer_load_dwordx4 v228,s[48:51],s72 offen offset:3072 nt lds\nv_fma_f32 v11,v11,s69,-v243\nv_mfma_f32_16x16x32_bf16 v[140:143],a[200:203],a[228:231],v[140:143]\nds_read_b128 a[212:215],v236 offset:0\nv_exp_f32 v6,v6\n"
"v_mfma_f32_16x16x32_bf16 v[168:171],a[204:207],a[228:231],v[168:171]\nv_exp_f32 v7,v7\nv_mfma_f32_16x16x32_bf16 v[196:199],a[208:211],a[228:231],v[196:199]\nds_read_b128 a[228:231],v239 offset:15360\nv_cvt_pk_bf16_f32 v5,v6,v7\nv_fma_f32 v12,v12,s69,-v244\nv_mfma_f32_16x16x32_bf16 v[116:119],a[196:199],a[232:235],v[116:119]\nds_read_b128 a[216:219],v236 offset:2048\n"
"v_fma_f32 v13,v13,s69,-v244\nv_mfma_f32_16x16x32_bf16 v[144:147],a[200:203],a[232:235],v[144:147]\ns_add_u32 m0,s64,0\nv_exp_f32 v8,v8\nv_fma_f32 v14,v14,s69,-v244\nv_mfma_f32_16x16x32_bf16 v[172:175],a[204:207],a[232:235],v[172:175]\ns_mov_b32 s73,0\nv_exp_f32 v9,v9\n"
"v_mfma_f32_16x16x32_bf16 v[200:203],a[208:211],a[232:235],v[200:203]\nds_read_b128 a[232:235],v239 offset:17408\nv_exp_f32 v10,v10\nv_mfma_f32_16x16x32_bf16 v[120:123],a[196:199],a[236:239],v[120:123]\nv_exp_f32 v11,v11\nv_mfma_f32_16x16x32_bf16 v[148:151],a[200:203],a[236:239],v[148:151]\nv_cvt_pk_bf16_f32 v8,v8,v9\nv_cvt_pk_bf16_f32 v9,v10,v11\n"
"v_fma_f32 v15,v15,s69,-v244\nv_mfma_f32_16x16x32_bf16 v[176:179],a[204:207],a[236:239],v[176:179]\nv_exp_f32 v12,v12\nv_mfma_f32_16x16x32_bf16 v[204:207],a[208:211],a[236:239],v[204:207]\nds_read_b128 a[236:239],v239 offset:19456\ns_waitcnt lgkmcnt(7)\nv_fma_f32 v16,v16,s69,-v245\nv_mfma_f32_16x16x32_bf16 v[124:127],a[196:199],a[240:243],v[124:127]\n"
"ds_read_b128 a[220:223],v236 offset:4096\nv_fma_f32 v17,v17,s69,-v245\nv_mfma_f32_16x16x32_bf16 v[152:155],a[200:203],a[240:243],v[152:155]\nv_exp_f32 v13,v13\nv_fma_f32 v18,v18,s69,-v245\nv_mfma_f32_16x16x32_bf16 v[180:183],a[204:207],a[240:243],v[180:183]\nv_exp_f32 v14,v14\nv_mfma_f32_16x16x32_bf16 v[208:211],a[208:211],a[240:243],v[208:211]\n"
"ds_read_b128 a[240:243],v239 offset:21504\nv_exp_f32 v15,v15\nv_mfma_f32_16x16x32_bf16 v[128:131],a[196:199],a[244:247],v[128:131]\nds_read_b128 a[224:227],v236 offset:6144\nv_cvt_pk_bf16_f32 v12,v12,v13\nv_mfma_f32_16x16x32_bf16 v[156:159],a[200:203],a[244:247],v[156:159]\nv_cvt_pk_bf16_f32 v13,v14,v15\nv_fma_f32 v19,v19,s69,-v245\n"
"v_fma_f32 v20,v20,s69,-v246\nv_mfma_f32_16x16x32_bf16 v[184:187],a[204:207],a[244:247],v[184:187]\nv_exp_f32 v16,v16\nv_mfma_f32_16x16x32_bf16 v[212:215],a[208:211],a[244:247],v[212:215]\nds_read_b128 a[244:247],v239 offset:23552\ns_waitcnt lgkmcnt(9)\nv_fma_f32 v21,v21,s69,-v246\nv_mfma_f32_16x16x32_bf16 v[132:135],a[196:199],a[248:251],v[132:135]\n"
"v_exp_f32 v17,v17\nv_mfma_f32_16x16x32_bf16 v[160:163],a[200:203],a[248:251],v[160:163]\nv_exp_f32 v18,v18\nv_cvt_pk_bf16_f32 v16,v16,v17\nv_mfma_f32_16x16x32_bf16 v[188:191],a[204:207],a[248:251],v[188:191]\nv_exp_f32 v19,v19\nv_mfma_f32_16x16x32_bf16 v[216:219],a[208:211],a[248:251],v[216:219]\nds_read_b128 a[248:251],v239 offset:25600\n"
"v_cvt_pk_bf16_f32 v17,v18,v19\nv_fma_f32 v22,v22,s69,-v246\nv_mfma_f32_16x16x32_bf16 v[136:139],a[196:199],a[252:255],v[136:139]\nv_fma_f32 v23,v23,s69,-v246\nv_fma_f32 v24,v24,s69,-v247\nv_mfma_f32_16x16x32_bf16 v[164:167],a[200:203],a[252:255],v[164:167]\nv_exp_f32 v20,v20\nv_fma_f32 v25,v25,s69,-v247\n"
"v_mfma_f32_16x16x32_bf16 v[192:195],a[204:207],a[252:255],v[192:195]\nv_exp_f32 v21,v21\nv_mfma_f32_16x16x32_bf16 v[220:223],a[208:211],a[252:255],v[220:223]\nds_read_b128 a[252:255],v239 offset:27648\ns_waitcnt lgkmcnt(3)\nv_cvt_pk_bf16_f32 v20,v20,v21\nv_mfma_f32_16x16x32_bf16 v[112:115],a[212:215],a[228:231],v[112:115]\nbuffer_load_dwordx4 v225,s[52:55],s73 offen offset:0 nt lds\n"
"v_fma_f32 v26,v26,s69,-v247\nv_mfma_f32_16x16x32_bf16 v[140:143],a[216:219],a[228:231],v[140:143]\ns_waitcnt vmcnt(13)\nds_read_b128 a[196:199],v235 offset:8192\nv_fma_f32 v27,v27,s69,-v247\nv_mfma_f32_16x16x32_bf16 v[168:171],a[220:223],a[228:231],v[168:171]\ns_mov_b32 s74,1024\nv_exp_f32 v22,v22\n"
"v_mfma_f32_16x16x32_bf16 v[196:199],a[224:227],a[228:231],v[196:199]\nds_read_b128 a[228:231],v239 offset:28672\nv_exp_f32 v23,v23\nv_mfma_f32_16x16x32_bf16 v[116:119],a[212:215],a[232:235],v[116:119]\nbuffer_load_dwordx4 v225,s[52:55],s74 offen offset:1024 nt lds\nv_cvt_pk_bf16_f32 v21,v22,v23\nv_mfma_f32_16x16x32_bf16 v[144:147],a[216:219],a[232:235],v[144:147]\nds_read_b128 a[200:203],v235 offset:10240\n"
"v_exp_f32 v24,v24\nv_mfma_f32_16x16x32_bf16 v[172:175],a[220:223],a[232:235],v[172:175]\ns_mov_b32 s75,6144\nv_exp_f32 v25,v25\nv_mfma_f32_16x16x32_bf16 v[200:203],a[224:227],a[232:235],v[200:203]\nds_read_b128 a[232:235],v239 offset:30720\nv_exp_f32 v26,v26\nv_mfma_f32_16x16x32_bf16 v[120:123],a[212:215],a[236:239],v[120:123]\n"
"buffer_load_dwordx4 v225,s[52:55],s75 offen offset:2048 nt lds\nv_cvt_pk_bf16_f32 v24,v24,v25\nv_mfma_f32_16x16x32_bf16 v[148:151],a[216:219],a[236:239],v[148:151]\ns_mov_b32 s72,7168\nv_exp_f32 v27,v27\nv_fma_f32 v28,v28,s69,-v241\nv_mfma_f32_16x16x32_bf16 v[176:179],a[220:223],a[236:239],v[176:179]\nv_cvt_pk_bf16_f32 v25,v26,v27\n"
"v_fma_f32 v29,v29,s69,-v241\nv_mfma_f32_16x16x32_bf16 v[204:207],a[224:227],a[236:239],v[204:207]\nds_read_b128 a[236:239],v239 offset:32768\ns_waitcnt lgkmcnt(7)\nv_fma_f32 v30,v30,s69,-v241\nv_mfma_f32_16x16x32_bf16 v[124:127],a[212:215],a[240:243],v[124:127]\nbuffer_load_dwordx4 v225,s[52:55],s72 offen offset:3072 nt lds\nv_fma_f32 v31,v31,s69,-v241\n"
"v_mfma_f32_16x16x32_bf16 v[152:155],a[216:219],a[240:243],v[152:155]\ns_waitcnt vmcnt(12)\nds_read_b128 a[204:207],v235 offset:12288\nv_fma_f32 v32,v32,s69,-v242\nv_mfma_f32_16x16x32_bf16 v[180:183],a[220:223],a[240:243],v[180:183]\ns_add_u32 m0,s64,4096\nv_exp_f32 v28,v28\nv_mfma_f32_16x16x32_bf16 v[208:211],a[224:227],a[240:243],v[208:211]\n"
"ds_read_b128 a[240:243],v239 offset:34816\nv_exp_f32 v29,v29\nv_mfma_f32_16x16x32_bf16 v[128:131],a[212:215],a[244:247],v[128:131]\ns_mov_b32 s73,16384\nbuffer_load_dwordx4 v225,s[52:55],s73 offen offset:0 nt lds\nv_mfma_f32_16x16x32_bf16 v[156:159],a[216:219],a[244:247],v[156:159]\nds_read_b128 a[208:211],v235 offset:14336\nv_exp_f32 v30,v30\n"
"v_mfma_f32_16x16x32_bf16 v[184:187],a[220:223],a[244:247],v[184:187]\ns_mov_b32 s74,17408\nv_exp_f32 v31,v31\nv_mfma_f32_16x16x32_bf16 v[212:215],a[224:227],a[244:247],v[212:215]\nds_read_b128 a[244:247],v239 offset:36864\ns_waitcnt lgkmcnt(9)\nv_cvt_pk_bf16_f32 v28,v28,v29\nv_mfma_f32_16x16x32_bf16 v[132:135],a[212:215],a[248:251],v[132:135]\n"
"buffer_load_dwordx4 v225,s[52:55],s74 offen offset:1024 nt lds\nv_cvt_pk_bf16_f32 v29,v30,v31\nv_mfma_f32_16x16x32_bf16 v[160:163],a[216:219],a[248:251],v[160:163]\ns_mov_b32 s75,22528\nv_fma_f32 v33,v33,s69,-v242\nv_fma_f32 v34,v34,s69,-v242\nv_fma_f32 v35,v35,s69,-v242\nv_mfma_f32_16x16x32_bf16 v[188:191],a[220:223],a[248:251],v[188:191]\n"
"v_exp_f32 v32,v32\nv_mfma_f32_16x16x32_bf16 v[216:219],a[224:227],a[248:251],v[216:219]\nds_read_b128 a[248:251],v239 offset:38912\nv_exp_f32 v33,v33\nv_mfma_f32_16x16x32_bf16 v[136:139],a[212:215],a[252:255],v[136:139]\nbuffer_load_dwordx4 v225,s[52:55],s75 offen offset:2048 nt lds\nv_cvt_pk_bf16_f32 v32,v32,v33\nv_mfma_f32_16x16x32_bf16 v[164:167],a[216:219],a[252:255],v[164:167]\n"
"s_mov_b32 s72,23552\nv_exp_f32 v34,v34\nv_fma_f32 v36,v36,s69,-v243\nv_mfma_f32_16x16x32_bf16 v[192:195],a[220:223],a[252:255],v[192:195]\nv_exp_f32 v35,v35\nv_mfma_f32_16x16x32_bf16 v[220:223],a[224:227],a[252:255],v[220:223]\nds_read_b128 a[252:255],v239 offset:40960\ns_waitcnt lgkmcnt(3)\n"
"v_cvt_pk_bf16_f32 v33,v34,v35\nv_mfma_f32_16x16x32_bf16 v[112:115],a[196:199],a[228:231],v[112:115]\nbuffer_load_dwordx4 v225,s[52:55],s72 offen offset:3072 nt lds\nv_fma_f32 v37,v37,s69,-v243\nv_mfma_f32_16x16x32_bf16 v[140:143],a[200:203],a[228:231],v[140:143]\nds_read_b128 a[212:215],v236 offset:8192\nv_fma_f32 v38,v38,s69,-v243\nv_fma_f32 v39,v39,s69,-v243\n"
"v_mfma_f32_16x16x32_bf16 v[168:171],a[204:207],a[228:231],v[168:171]\nv_exp_f32 v36,v36\nv_mfma_f32_16x16x32_bf16 v[196:199],a[208:211],a[228:231],v[196:199]\nds_read_b128 a[228:231],v239 offset:29696\nv_exp_f32 v37,v37\nv_mfma_f32_16x16x32_bf16 v[116:119],a[196:199],a[232:235],v[116:119]\nds_read_b128 a[216:219],v236 offset:10240\nv_cvt_pk_bf16_f32 v36,v36,v37\n"
"v_mfma_f32_16x16x32_bf16 v[144:147],a[200:203],a[232:235],v[144:147]\ns_add_u32 m0,s64,8192\nv_exp_f32 v38,v38\nv_fma_f32 v40,v40,s69,-v244\nv_mfma_f32_16x16x32_bf16 v[172:175],a[204:207],a[232:235],v[172:175]\ns_mov_b32 s73,32768\nv_exp_f32 v39,v39\nv_mfma_f32_16x16x32_bf16 v[200:203],a[208:211],a[232:235],v[200:203]\n"
"ds_read_b128 a[232:235],v239 offset:31744\nv_cvt_pk_bf16_f32 v37,v38,v39\nv_fma_f32 v41,v41,s69,-v244\nv_mfma_f32_16x16x32_bf16 v[120:123],a[196:199],a[236:239],v[120:123]\nv_fma_f32 v42,v42,s69,-v244\nv_fma_f32 v43,v43,s69,-v244\nv_mfma_f32_16x16x32_bf16 v[148:151],a[200:203],a[236:239],v[148:151]\nv_exp_f32 v40,v40\n"
"v_fma_f32 v44,v44,s69,-v245\nv_mfma_f32_16x16x32_bf16 v[176:179],a[204:207],a[236:239],v[176:179]\nv_exp_f32 v41,v41\nv_mfma_f32_16x16x32_bf16 v[204:207],a[208:211],a[236:239],v[204:207]\nds_read_b128 a[236:239],v239 offset:33792\ns_waitcnt lgkmcnt(7)\nv_cvt_pk_bf16_f32 v40,v40,v41\nv_mfma_f32_16x16x32_bf16 v[124:127],a[196:199],a[240:243],v[124:127]\n"
"ds_read_b128 a[220:223],v236 offset:12288\nv_fma_f32 v45,v45,s69,-v245\nv_mfma_f32_16x16x32_bf16 v[152:155],a[200:203],a[240:243],v[152:155]\nv_exp_f32 v42,v42\nv_fma_f32 v46,v46,s69,-v245\nv_mfma_f32_16x16x32_bf16 v[180:183],a[204:207],a[240:243],v[180:183]\nv_exp_f32 v43,v43\nv_mfma_f32_16x16x32_bf16 v[208:211],a[208:211],a[240:243],v[208:211]\n"
"ds_read_b128 a[240:243],v239 offset:35840\nv_cvt_pk_bf16_f32 v41,v42,v43\nv_fma_f32 v47,v47,s69,-v245\nv_mfma_f32_16x16x32_bf16 v[128:131],a[196:199],a[244:247],v[128:131]\nds_read_b128 a[224:227],v236 offset:14336\nv_fma_f32 v48,v48,s69,-v246\nv_mfma_f32_16x16x32_bf16 v[156:159],a[200:203],a[244:247],v[156:159]\nv_exp_f32 v44,v44\n"
"v_fma_f32 v49,v49,s69,-v246\nv_mfma_f32_16x16x32_bf16 v[184:187],a[204:207],a[244:247],v[184:187]\nv_exp_f32 v45,v45\nv_mfma_f32_16x16x32_bf16 v[212:215],a[208:211],a[244:247],v[212:215]\nds_read_b128 a[244:247],v239 offset:37888\ns_waitcnt lgkmcnt(9)\nv_cvt_pk_bf16_f32 v44,v44,v45\nv_mfma_f32_16x16x32_bf16 v[132:135],a[196:199],a[248:251],v[132:135]\n"
"v_exp_f32 v46,v46\nv_mfma_f32_16x16x32_bf16 v[160:163],a[200:203],a[248:251],v[160:163]\nv_exp_f32 v47,v47\nv_fma_f32 v50,v50,s69,-v246\nv_mfma_f32_16x16x32_bf16 v[188:191],a[204:207],a[248:251],v[188:191]\nv_cvt_pk_bf16_f32 v45,v46,v47\nv_fma_f32 v51,v51,s69,-v246\nv_mfma_f32_16x16x32_bf16 v[216:219],a[208:211],a[248:251],v[216:219]\n"
"ds_read_b128 a[248:251],v239 offset:39936\nv_exp_f32 v48,v48\nv_mfma_f32_16x16x32_bf16 v[136:139],a[196:199],a[252:255],v[136:139]\nv_exp_f32 v49,v49\nv_mfma_f32_16x16x32_bf16 v[164:167],a[200:203],a[252:255],v[164:167]\nv_exp_f32 v50,v50\nv_cvt_pk_bf16_f32 v48,v48,v49\nv_mfma_f32_16x16x32_bf16 v[192:195],a[204:207],a[252:255],v[192:195]\n"
"v_exp_f32 v51,v51\nv_mfma_f32_16x16x32_bf16 v[220:223],a[208:211],a[252:255],v[220:223]\nds_read_b128 a[252:255],v239 offset:41984\ns_waitcnt lgkmcnt(3)\nv_cvt_pk_bf16_f32 v49,v50,v51\nv_mfma_f32_16x16x32_bf16 v[112:115],a[212:215],a[228:231],v[112:115]\nbuffer_load_dwordx4 v225,s[52:55],s73 offen offset:0 nt lds\nv_fma_f32 v52,v52,s69,-v247\n"
"v_mfma_f32_16x16x32_bf16 v[140:143],a[216:219],a[228:231],v[140:143]\ns_waitcnt vmcnt(13)\nds_read_b128 a[196:199],v235 offset:16384\nv_fma_f32 v53,v53,s69,-v247\nv_mfma_f32_16x16x32_bf16 v[168:171],a[220:223],a[228:231],v[168:171]\ns_mov_b32 s74,33792\nv_fma_f32 v54,v54,s69,-v247\nv_fma_f32 v55,v55,s69,-v247\n"
"v_mfma_f32_16x16x32_bf16 v[196:199],a[224:227],a[228:231],v[196:199]\nds_read_b128 a[228:231],v239 offset:43008\nv_exp_f32 v52,v52\nv_mfma_f32_16x16x32_bf16 v[116:119],a[212:215],a[232:235],v[116:119]\nbuffer_load_dwordx4 v225,s[52:55],s74 offen offset:1024 nt lds\nv_fma_f32 v56,v56,s69,-v241\nv_mfma_f32_16x16x32_bf16 v[144:147],a[216:219],a[232:235],v[144:147]\nds_read_b128 a[200:203],v235 offset:18432\n"
"v_exp_f32 v53,v53\nv_mfma_f32_16x16x32_bf16 v[172:175],a[220:223],a[232:235],v[172:175]\ns_mov_b32 s75,38912\nv_exp_f32 v54,v54\nv_mfma_f32_16x16x32_bf16 v[200:203],a[224:227],a[232:235],v[200:203]\nds_read_b128 a[232:235],v239 offset:45056\nv_exp_f32 v55,v55\nv_mfma_f32_16x16x32_bf16 v[120:123],a[212:215],a[236:239],v[120:123]\n"
"buffer_load_dwordx4 v225,s[52:55],s75 offen offset:2048 nt lds\nv_cvt_pk_bf16_f32 v52,v52,v53\nv_mfma_f32_16x16x32_bf16 v[148:151],a[216:219],a[236:239],v[148:151]\ns_mov_b32 s72,39936\nv_cvt_pk_bf16_f32 v53,v54,v55\nv_fma_f32 v57,v57,s69,-v241\nv_fma_f32 v58,v58,s69,-v241\nv_mfma_f32_16x16x32_bf16 v[176:179],a[220:223],a[236:239],v[176:179]\n"
"v_fma_f32 v59,v59,s69,-v241\nv_fma_f32 v60,v60,s69,-v242\nv_mfma_f32_16x16x32_bf16 v[204:207],a[224:227],a[236:239],v[204:207]\nds_read_b128 a[236:239],v239 offset:47104\ns_waitcnt lgkmcnt(7)\nv_fma_f32 v61,v61,s69,-v242\nv_mfma_f32_16x16x32_bf16 v[124:127],a[212:215],a[240:243],v[124:127]\nbuffer_load_dwordx4 v225,s[52:55],s72 offen offset:3072 nt lds\n"
"v_fma_f32 v62,v62,s69,-v242\nv_mfma_f32_16x16x32_bf16 v[152:155],a[216:219],a[240:243],v[152:155]\ns_waitcnt vmcnt(12)\nds_read_b128 a[204:207],v235 offset:20480\nv_fma_f32 v63,v63,s69,-v242\nv_mfma_f32_16x16x32_bf16 v[180:183],a[220:223],a[240:243],v[180:183]\ns_add_u32 m0,s64,12288\nv_exp_f32 v56,v56\n"
"v_mfma_f32_16x16x32_bf16 v[208:211],a[224:227],a[240:243],v[208:211]\nds_read_b128 a[240:243],v239 offset:49152\nv_exp_f32 v57,v57\nv_mfma_f32_16x16x32_bf16 v[128:131],a[212:215],a[244:247],v[128:131]\ns_mov_b32 s73,49152\nbuffer_load_dwordx4 v225,s[52:55],s73 offen offset:0 nt lds\nv_mfma_f32_16x16x32_bf16 v[156:159],a[216:219],a[244:247],v[156:159]\nds_read_b128 a[208:211],v235 offset:22528\n"
"v_exp_f32 v58,v58\nv_mfma_f32_16x16x32_bf16 v[184:187],a[220:223],a[244:247],v[184:187]\ns_mov_b32 s74,50176\nv_exp_f32 v59,v59\nv_mfma_f32_16x16x32_bf16 v[212:215],a[224:227],a[244:247],v[212:215]\nds_read_b128 a[244:247],v239 offset:51200\ns_waitcnt lgkmcnt(9)\nv_cvt_pk_bf16_f32 v56,v56,v57\n"
"v_mfma_f32_16x16x32_bf16 v[132:135],a[212:215],a[248:251],v[132:135]\nbuffer_load_dwordx4 v225,s[52:55],s74 offen offset:1024 nt lds\nv_cvt_pk_bf16_f32 v57,v58,v59\nv_mfma_f32_16x16x32_bf16 v[160:163],a[216:219],a[248:251],v[160:163]\ns_mov_b32 s75,55296\nv_exp_f32 v60,v60\nv_fma_f32 v64,v64,s69,-v243\nv_mfma_f32_16x16x32_bf16 v[188:191],a[220:223],a[248:251],v[188:191]\n"
"v_exp_f32 v61,v61\nv_mfma_f32_16x16x32_bf16 v[216:219],a[224:227],a[248:251],v[216:219]\nds_read_b128 a[248:251],v239 offset:53248\nv_exp_f32 v62,v62\nv_mfma_f32_16x16x32_bf16 v[136:139],a[212:215],a[252:255],v[136:139]\nbuffer_load_dwordx4 v225,s[52:55],s75 offen offset:2048 nt lds\nv_cvt_pk_bf16_f32 v60,v60,v61\nv_mfma_f32_16x16x32_bf16 v[164:167],a[216:219],a[252:255],v[164:167]\n"
"s_mov_b32 s72,56320\nv_exp_f32 v63,v63\nv_fma_f32 v65,v65,s69,-v243\nv_mfma_f32_16x16x32_bf16 v[192:195],a[220:223],a[252:255],v[192:195]\nv_cvt_pk_bf16_f32 v61,v62,v63\nv_fma_f32 v66,v66,s69,-v243\nv_mfma_f32_16x16x32_bf16 v[220:223],a[224:227],a[252:255],v[220:223]\nds_read_b128 a[252:255],v239 offset:55296\n"
"s_waitcnt lgkmcnt(3)\nv_fma_f32 v67,v67,s69,-v243\nv_mfma_f32_16x16x32_bf16 v[112:115],a[196:199],a[228:231],v[112:115]\nbuffer_load_dwordx4 v225,s[52:55],s72 offen offset:3072 nt lds\nv_fma_f32 v68,v68,s69,-v244\nv_mfma_f32_16x16x32_bf16 v[140:143],a[200:203],a[228:231],v[140:143]\nds_read_b128 a[212:215],v236 offset:16384\nv_exp_f32 v64,v64\n"
"v_mfma_f32_16x16x32_bf16 v[168:171],a[204:207],a[228:231],v[168:171]\nv_exp_f32 v65,v65\nv_mfma_f32_16x16x32_bf16 v[196:199],a[208:211],a[228:231],v[196:199]\nds_read_b128 a[228:231],v239 offset:44032\nv_exp_f32 v66,v66\nv_mfma_f32_16x16x32_bf16 v[116:119],a[196:199],a[232:235],v[116:119]\nds_read_b128 a[216:219],v236 offset:18432\nv_cvt_pk_bf16_f32 v64,v64,v65\n"
"v_mfma_f32_16x16x32_bf16 v[144:147],a[200:203],a[232:235],v[144:147]\ns_add_u32 m0,s64,16384\nv_exp_f32 v67,v67\nv_fma_f32 v69,v69,s69,-v244\nv_mfma_f32_16x16x32_bf16 v[172:175],a[204:207],a[232:235],v[172:175]\ns_mov_b32 s73,65536\nv_cvt_pk_bf16_f32 v65,v66,v67\nv_fma_f32 v70,v70,s69,-v244\n"
"v_mfma_f32_16x16x32_bf16 v[200:203],a[208:211],a[232:235],v[200:203]\nds_read_b128 a[232:235],v239 offset:46080\nv_fma_f32 v71,v71,s69,-v244\nv_fma_f32 v72,v72,s69,-v245\nv_mfma_f32_16x16x32_bf16 v[120:123],a[196:199],a[236:239],v[120:123]\nv_exp_f32 v68,v68\nv_mfma_f32_16x16x32_bf16 v[148:151],a[200:203],a[236:239],v[148:151]\nv_exp_f32 v69,v69\n"
"v_fma_f32 v73,v73,s69,-v245\nv_mfma_f32_16x16x32_bf16 v[176:179],a[204:207],a[236:239],v[176:179]\nv_exp_f32 v70,v70\nv_mfma_f32_16x16x32_bf16 v[204:207],a[208:211],a[236:239],v[204:207]\nds_read_b128 a[236:239],v239 offset:48128\ns_waitcnt lgkmcnt(7)\nv_cvt_pk_bf16_f32 v68,v68,v69\nv_mfma_f32_16x16x32_bf16 v[124:127],a[196:199],a[240:243],v[124:127]\n"
"ds_read_b128 a[220:223],v236 offset:20480\nv_fma_f32 v74,v74,s69,-v245\nv_mfma_f32_16x16x32_bf16 v[152:155],a[200:203],a[240:243],v[152:155]\nv_exp_f32 v71,v71\nv_fma_f32 v75,v75,s69,-v245\nv_mfma_f32_16x16x32_bf16 v[180:183],a[204:207],a[240:243],v[180:183]\nv_cvt_pk_bf16_f32 v69,v70,v71\nv_fma_f32 v76,v76,s69,-v246\n"
"v_mfma_f32_16x16x32_bf16 v[208:211],a[208:211],a[240:243],v[208:211]\nds_read_b128 a[240:243],v239 offset:50176\nv_exp_f32 v72,v72\nv_mfma_f32_16x16x32_bf16 v[128:131],a[196:199],a[244:247],v[128:131]\nds_read_b128 a[224:227],v236 offset:22528\nv_fma_f32 v77,v77,s69,-v246\nv_mfma_f32_16x16x32_bf16 v[156:159],a[200:203],a[244:247],v[156:159]\nv_exp_f32 v73,v73\n"
"v_fma_f32 v78,v78,s69,-v246\nv_mfma_f32_16x16x32_bf16 v[184:187],a[204:207],a[244:247],v[184:187]\nv_exp_f32 v74,v74\nv_mfma_f32_16x16x32_bf16 v[212:215],a[208:211],a[244:247],v[212:215]\nds_read_b128 a[244:247],v239 offset:52224\ns_waitcnt lgkmcnt(9)\nv_cvt_pk_bf16_f32 v72,v72,v73\nv_mfma_f32_16x16x32_bf16 v[132:135],a[196:199],a[248:251],v[132:135]\n"
"v_exp_f32 v75,v75\nv_mfma_f32_16x16x32_bf16 v[160:163],a[200:203],a[248:251],v[160:163]\nv_cvt_pk_bf16_f32 v73,v74,v75\nv_fma_f32 v79,v79,s69,-v246\nv_fma_f32 v80,v80,s69,-v247\nv_mfma_f32_16x16x32_bf16 v[188:191],a[204:207],a[248:251],v[188:191]\nv_exp_f32 v76,v76\nv_mfma_f32_16x16x32_bf16 v[216:219],a[208:211],a[248:251],v[216:219]\n"
"ds_read_b128 a[248:251],v239 offset:54272\nv_exp_f32 v77,v77\nv_mfma_f32_16x16x32_bf16 v[136:139],a[196:199],a[252:255],v[136:139]\nv_exp_f32 v78,v78\nv_mfma_f32_16x16x32_bf16 v[164:167],a[200:203],a[252:255],v[164:167]\nv_exp_f32 v79,v79\nv_cvt_pk_bf16_f32 v76,v76,v77\nv_mfma_f32_16x16x32_bf16 v[192:195],a[204:207],a[252:255],v[192:195]\n"
"v_cvt_pk_bf16_f32 v77,v78,v79\nv_fma_f32 v81,v81,s69,-v247\nv_mfma_f32_16x16x32_bf16 v[220:223],a[208:211],a[252:255],v[220:223]\nds_read_b128 a[252:255],v239 offset:56320\ns_waitcnt lgkmcnt(3)\nv_fma_f32 v82,v82,s69,-v247\nv_mfma_f32_16x16x32_bf16 v[112:115],a[212:215],a[228:231],v[112:115]\nbuffer_load_dwordx4 v225,s[52:55],s73 offen offset:0 nt lds\n"
"v_fma_f32 v83,v83,s69,-v247\nv_mfma_f32_16x16x32_bf16 v[140:143],a[216:219],a[228:231],v[140:143]\ns_waitcnt vmcnt(13)\nds_read_b64_tr_b16 a[196:197],v231 offset:0\nv_fma_f32 v84,v84,s69,-v241\nv_mfma_f32_16x16x32_bf16 v[168:171],a[220:223],a[228:231],v[168:171]\nds_read_b64_tr_b16 a[198:199],v231 offset:1024\nv_fma_f32 v85,v85,s69,-v241\n"
"v_mfma_f32_16x16x32_bf16 v[196:199],a[224:227],a[228:231],v[196:199]\ns_waitcnt lgkmcnt(2)\ns_barrier\nv_exp_f32 v80,v80\nv_mfma_f32_16x16x32_bf16 v[116:119],a[212:215],a[232:235],v[116:119]\ns_mov_b32 s74,66560\nbuffer_load_dwordx4 v225,s[52:55],s74 offen offset:1024 nt lds\nds_write_b64 v240,v[0:1] offset:0\n"
"v_mfma_f32_16x16x32_bf16 v[144:147],a[216:219],a[232:235],v[144:147]\nds_read_b64_tr_b16 a[200:201],v232 offset:0\nv_exp_f32 v81,v81\nv_mfma_f32_16x16x32_bf16 v[172:175],a[220:223],a[232:235],v[172:175]\ns_mov_b32 s75,71680\nv_exp_f32 v82,v82\nv_mfma_f32_16x16x32_bf16 v[200:203],a[224:227],a[232:235],v[200:203]\nv_exp_f32 v83,v83\n"
"v_cvt_pk_bf16_f32 v80,v80,v81\nv_mfma_f32_16x16x32_bf16 v[120:123],a[212:215],a[236:239],v[120:123]\nbuffer_load_dwordx4 v225,s[52:55],s75 offen offset:2048 nt lds\nds_write_b64 v240,v[4:5] offset:2048\nv_mfma_f32_16x16x32_bf16 v[148:151],a[216:219],a[236:239],v[148:151]\nds_read_b64_tr_b16 a[202:203],v232 offset:1024\nv_cvt_pk_bf16_f32 v81,v82,v83\nv_fma_f32 v86,v86,s69,-v241\n"
"v_mfma_f32_16x16x32_bf16 v[176:179],a[220:223],a[236:239],v[176:179]\ns_mov_b32 s72,72704\nv_fma_f32 v87,v87,s69,-v241\nv_fma_f32 v88,v88,s69,-v242\nv_mfma_f32_16x16x32_bf16 v[204:207],a[224:227],a[236:239],v[204:207]\nv_exp_f32 v84,v84\nv_fma_f32 v89,v89,s69,-v242\nv_mfma_f32_16x16x32_bf16 v[124:127],a[212:215],a[240:243],v[124:127]\n"
"buffer_load_dwordx4 v225,s[52:55],s72 offen offset:3072 nt lds\nds_write_b64 v240,v[8:9] offset:4096\nv_mfma_f32_16x16x32_bf16 v[152:155],a[216:219],a[240:243],v[152:155]\nds_read_b64_tr_b16 a[204:205],v233 offset:0\nv_exp_f32 v85,v85\nv_mfma_f32_16x16x32_bf16 v[180:183],a[220:223],a[240:243],v[180:183]\nds_read_b64_tr_b16 a[206:207],v233 offset:1024\nv_cvt_pk_bf16_f32 v84,v84,v85\n"
"v_mfma_f32_16x16x32_bf16 v[208:211],a[224:227],a[240:243],v[208:211]\ns_add_u32 m0,s64,20480\nv_exp_f32 v86,v86\nv_fma_f32 v90,v90,s69,-v242\nv_mfma_f32_16x16x32_bf16 v[128:131],a[212:215],a[244:247],v[128:131]\ns_mov_b32 s73,81920\nbuffer_load_dwordx4 v225,s[52:55],s73 offen offset:0 nt lds\nds_write_b64 v240,v[12:13] offset:6144\n"
"v_mfma_f32_16x16x32_bf16 v[156:159],a[216:219],a[244:247],v[156:159]\nds_read_b64_tr_b16 a[208:209],v234 offset:0\nv_exp_f32 v87,v87\nv_mfma_f32_16x16x32_bf16 v[184:187],a[220:223],a[244:247],v[184:187]\ns_mov_b32 s74,82944\nv_cvt_pk_bf16_f32 v85,v86,v87\nv_fma_f32 v91,v91,s69,-v242\nv_mfma_f32_16x16x32_bf16 v[212:215],a[224:227],a[244:247],v[212:215]\n"
"v_exp_f32 v88,v88\nv_fma_f32 v92,v92,s69,-v243\nv_mfma_f32_16x16x32_bf16 v[132:135],a[212:215],a[248:251],v[132:135]\nbuffer_load_dwordx4 v225,s[52:55],s74 offen offset:1024 nt lds\nds_write_b64 v240,v[16:17] offset:8192\nv_mfma_f32_16x16x32_bf16 v[160:163],a[216:219],a[248:251],v[160:163]\nds_read_b64_tr_b16 a[210:211],v234 offset:1024\nv_exp_f32 v89,v89\n"
"v_mfma_f32_16x16x32_bf16 v[188:191],a[220:223],a[248:251],v[188:191]\ns_mov_b32 s75,88064\nv_exp_f32 v90,v90\nv_mfma_f32_16x16x32_bf16 v[216:219],a[224:227],a[248:251],v[216:219]\nv_exp_f32 v91,v91\nv_cvt_pk_bf16_f32 v88,v88,v89\nv_mfma_f32_16x16x32_bf16 v[136:139],a[212:215],a[252:255],v[136:139]\nbuffer_load_dwordx4 v225,s[52:55],s75 offen offset:2048 nt lds\n"
"ds_write_b64 v240,v[20:21] offset:10240\nv_mfma_f32_16x16x32_bf16 v[164:167],a[216:219],a[252:255],v[164:167]\ns_mov_b32 s72,89088\nv_cvt_pk_bf16_f32 v89,v90,v91\nv_fma_f32 v93,v93,s69,-v243\nv_fma_f32 v94,v94,s69,-v243\nv_mfma_f32_16x16x32_bf16 v[192:195],a[220:223],a[252:255],v[192:195]\nv_fma_f32 v95,v95,s69,-v243\n"
"v_fma_f32 v96,v96,s69,-v244\nv_mfma_f32_16x16x32_bf16 v[220:223],a[224:227],a[252:255],v[220:223]\nv_exp_f32 v92,v92\nv_fma_f32 v97,v97,s69,-v244\nv_exp_f32 v93,v93\nv_exp_f32 v94,v94\nv_exp_f32 v95,v95\nv_cvt_pk_bf16_f32 v92,v92,v93\n"
"v_cvt_pk_bf16_f32 v93,v94,v95\nv_fma_f32 v98,v98,s69,-v244\nv_fma_f32 v99,v99,s69,-v244\nv_exp_f32 v96,v96\nv_exp_f32 v97,v97\nv_exp_f32 v98,v98\nv_exp_f32 v99,v99\nv_cvt_pk_bf16_f32 v96,v96,v97\n"
"v_cvt_pk_bf16_f32 v97,v98,v99\nv_fma_f32 v100,v100,s69,-v245\nv_fma_f32 v101,v101,s69,-v245\nv_fma_f32 v102,v102,s69,-v245\nv_fma_f32 v103,v103,s69,-v245\nv_exp_f32 v100,v100\nv_exp_f32 v101,v101\nv_exp_f32 v102,v102\n"
"v_exp_f32 v103,v103\nv_cvt_pk_bf16_f32 v100,v100,v101\nv_cvt_pk_bf16_f32 v101,v102,v103\nv_fma_f32 v104,v104,s69,-v246\nv_fma_f32 v105,v105,s69,-v246\nv_fma_f32 v106,v106,s69,-v246\nv_fma_f32 v107,v107,s69,-v246\nv_exp_f32 v104,v104\n"
"v_exp_f32 v105,v105\nv_exp_f32 v106,v106\nv_exp_f32 v107,v107\nv_cvt_pk_bf16_f32 v104,v104,v105\nv_cvt_pk_bf16_f32 v105,v106,v107\nv_fma_f32 v108,v108,s69,-v247\nv_fma_f32 v109,v109,s69,-v247\nv_fma_f32 v110,v110,s69,-v247\n"
"v_fma_f32 v111,v111,s69,-v247\nv_exp_f32 v108,v108\nv_exp_f32 v109,v109\nv_exp_f32 v110,v110\nv_exp_f32 v111,v111\nv_cvt_pk_bf16_f32 v108,v108,v109\nv_cvt_pk_bf16_f32 v109,v110,v111\nds_write_b64 v240,v[24:25] offset:12288\n"
"v_max3_f32 v248,v112,v113,v112\nv_max3_f32 v248,v114,v115,v248\nv_max3_f32 v248,v140,v141,v248\nv_max3_f32 v248,v142,v143,v248\nv_max3_f32 v248,v168,v169,v248\nv_max3_f32 v248,v170,v171,v248\nds_write_b64 v240,v[28:29] offset:14336\nv_max3_f32 v248,v196,v197,v248\n"
"v_max3_f32 v248,v198,v199,v248\nv_mul_f32 v248,s69,v248\nds_max_f32 v255,v248 offset:57344\nv_max3_f32 v249,v116,v117,v116\nv_max3_f32 v249,v118,v119,v249\nbuffer_load_dwordx4 v225,s[52:55],s72 offen offset:3072 nt lds\nds_write_b64 v240,v[32:33] offset:16384\nv_max3_f32 v249,v144,v145,v249\n"
"v_max3_f32 v249,v146,v147,v249\nv_max3_f32 v249,v172,v173,v249\nv_max3_f32 v249,v174,v175,v249\nv_max3_f32 v249,v200,v201,v249\nv_max3_f32 v249,v202,v203,v249\nds_write_b64 v240,v[36:37] offset:18432\nv_mul_f32 v249,s69,v249\nds_max_f32 v255,v249 offset:57408\n"
"v_max3_f32 v250,v120,v121,v120\nv_max3_f32 v250,v122,v123,v250\nv_max3_f32 v250,v148,v149,v250\nv_max3_f32 v250,v150,v151,v250\nds_write_b64 v240,v[40:41] offset:20480\nv_max3_f32 v250,v176,v177,v250\nv_max3_f32 v250,v178,v179,v250\nv_max3_f32 v250,v204,v205,v250\n"
"v_max3_f32 v250,v206,v207,v250\nv_mul_f32 v250,s69,v250\nds_max_f32 v255,v250 offset:57472\nds_write_b64 v240,v[44:45] offset:22528\nv_max3_f32 v251,v124,v125,v124\nv_max3_f32 v251,v126,v127,v251\nv_max3_f32 v251,v152,v153,v251\nv_max3_f32 v251,v154,v155,v251\n"
"v_max3_f32 v251,v180,v181,v251\nv_max3_f32 v251,v182,v183,v251\nds_write_b64 v240,v[48:49] offset:24576\nv_max3_f32 v251,v208,v209,v251\nv_max3_f32 v251,v210,v211,v251\nv_mul_f32 v251,s69,v251\nds_max_f32 v255,v251 offset:57536\nv_max3_f32 v252,v128,v129,v128\n"
"v_max3_f32 v252,v130,v131,v252\nds_write_b64 v240,v[52:53] offset:26624\nv_max3_f32 v252,v156,v157,v252\nv_max3_f32 v252,v158,v159,v252\nv_max3_f32 v252,v184,v185,v252\nv_max3_f32 v252,v186,v187,v252\nv_max3_f32 v252,v212,v213,v252\nv_max3_f32 v252,v214,v215,v252\n"
"ds_write_b64 v240,v[56:57] offset:28672\nv_mul_f32 v252,s69,v252\nds_max_f32 v255,v252 offset:57600\nv_max3_f32 v253,v132,v133,v132\nv_max3_f32 v253,v134,v135,v253\nv_max3_f32 v253,v160,v161,v253\nv_max3_f32 v253,v162,v163,v253\nds_write_b64 v240,v[60:61] offset:30720\n"
"v_max3_f32 v253,v188,v189,v253\nv_max3_f32 v253,v190,v191,v253\nv_max3_f32 v253,v216,v217,v253\nv_max3_f32 v253,v218,v219,v253\nv_mul_f32 v253,s69,v253\nds_max_f32 v255,v253 offset:57664\nds_write_b64 v240,v[64:65] offset:32768\nv_max3_f32 v254,v136,v137,v136\n"
"v_max3_f32 v254,v138,v139,v254\nv_max3_f32 v254,v164,v165,v254\nv_max3_f32 v254,v166,v167,v254\nv_max3_f32 v254,v192,v193,v254\nv_max3_f32 v254,v194,v195,v254\nds_write_b64 v240,v[68:69] offset:34816\nv_max3_f32 v254,v220,v221,v254\nv_max3_f32 v254,v222,v223,v254\n"
"v_mul_f32 v254,s69,v254\nds_max_f32 v255,v254 offset:57728\nv_dot2_f32_bf16 v2,v0,s35,0\nv_dot2_f32_bf16 v2,v1,s35,v2\nds_write_b64 v240,v[72:73] offset:36864\nv_dot2_f32_bf16 v6,v4,s35,0\nv_dot2_f32_bf16 v6,v5,s35,v6\nv_dot2_f32_bf16 v10,v8,s35,0\n"
"v_dot2_f32_bf16 v10,v9,s35,v10\nv_dot2_f32_bf16 v14,v12,s35,0\nv_dot2_f32_bf16 v14,v13,s35,v14\nds_write_b64 v240,v[76:77] offset:38912\nv_dot2_f32_bf16 v18,v16,s35,0\nv_dot2_f32_bf16 v18,v17,s35,v18\nv_dot2_f32_bf16 v22,v20,s35,0\nv_dot2_f32_bf16 v22,v21,s35,v22\n"
"v_dot2_f32_bf16 v26,v24,s35,0\nv_dot2_f32_bf16 v26,v25,s35,v26\nds_write_b64 v240,v[80:81] offset:40960\nv_dot2_f32_bf16 v2,v28,s35,v2\nv_dot2_f32_bf16 v2,v29,s35,v2\nv_dot2_f32_bf16 v6,v32,s35,v6\nv_dot2_f32_bf16 v6,v33,s35,v6\nv_dot2_f32_bf16 v10,v36,s35,v10\n"
"v_dot2_f32_bf16 v10,v37,s35,v10\nds_write_b64 v240,v[84:85] offset:43008\nv_dot2_f32_bf16 v14,v40,s35,v14\nv_dot2_f32_bf16 v14,v41,s35,v14\nv_dot2_f32_bf16 v18,v44,s35,v18\nv_dot2_f32_bf16 v18,v45,s35,v18\nv_dot2_f32_bf16 v22,v48,s35,v22\nv_dot2_f32_bf16 v22,v49,s35,v22\n"
"ds_write_b64 v240,v[88:89] offset:45056\nv_dot2_f32_bf16 v26,v52,s35,v26\nv_dot2_f32_bf16 v26,v53,s35,v26\nv_dot2_f32_bf16 v2,v56,s35,v2\nv_dot2_f32_bf16 v2,v57,s35,v2\nv_dot2_f32_bf16 v6,v60,s35,v6\nv_dot2_f32_bf16 v6,v61,s35,v6\nds_write_b64 v240,v[92:93] offset:47104\n"
"v_dot2_f32_bf16 v10,v64,s35,v10\nv_dot2_f32_bf16 v10,v65,s35,v10\nv_dot2_f32_bf16 v14,v68,s35,v14\nv_dot2_f32_bf16 v14,v69,s35,v14\nv_dot2_f32_bf16 v18,v72,s35,v18\nv_dot2_f32_bf16 v18,v73,s35,v18\nds_write_b64 v240,v[96:97] offset:49152\nv_dot2_f32_bf16 v22,v76,s35,v22\n"
"v_dot2_f32_bf16 v22,v77,s35,v22\nv_dot2_f32_bf16 v26,v80,s35,v26\nv_dot2_f32_bf16 v26,v81,s35,v26\nv_dot2_f32_bf16 v2,v84,s35,v2\nv_dot2_f32_bf16 v2,v85,s35,v2\nds_write_b64 v240,v[100:101] offset:51200\nv_dot2_f32_bf16 v6,v88,s35,v6\nv_dot2_f32_bf16 v6,v89,s35,v6\n"
"v_dot2_f32_bf16 v10,v92,s35,v10\nv_dot2_f32_bf16 v10,v93,s35,v10\nv_dot2_f32_bf16 v14,v96,s35,v14\nv_dot2_f32_bf16 v14,v97,s35,v14\nds_write_b64 v240,v[104:105] offset:53248\nv_dot2_f32_bf16 v18,v100,s35,v18\nv_dot2_f32_bf16 v18,v101,s35,v18\nv_dot2_f32_bf16 v22,v104,s35,v22\n"
"v_dot2_f32_bf16 v22,v105,s35,v22\nv_dot2_f32_bf16 v26,v108,s35,v26\nv_dot2_f32_bf16 v26,v109,s35,v26\nds_write_b64 v240,v[108:109] offset:55296\nv_mov_b32 v3,0\nv_permlane32_swap_b32 v2,v6\nv_add_f32 v2,v2,v6\nv_permlane32_swap_b32 v10,v14\n"
"v_add_f32 v10,v10,v14\nv_permlane32_swap_b32 v18,v22\nv_add_f32 v18,v18,v22\nv_permlane32_swap_b32 v26,v3\nv_add_f32 v26,v26,v3\nv_bfe_u32 v6,v239,8,1\nv_lshrrev_b32 v14,9,v239\nv_permlane16_swap_b32 v2,v10\n"
"v_add_f32 v2,v2,v10\nv_permlane16_swap_b32 v18,v26\nv_add_f32 v18,v18,v26\nv_lshl_or_b32 v6,v6,1,v14\nv_and_b32 v14,0xf0,v239\nv_lshl_or_b32 v6,v6,8,v14\nv_lshl_add_u32 v6,s70,2,v6\nds_write_b32 v6,v2 offset:59392\n"
"ds_write_b32 v6,v18 offset:60416\ns_waitcnt lgkmcnt(0)\ns_barrier\nds_read_b128 a[228:231],v239 offset:0\nds_read_b128 a[232:235],v239 offset:2048\nds_read_b128 a[236:239],v239 offset:4096\nds_read_b128 a[240:243],v239 offset:6144\nds_read_b128 a[244:247],v239 offset:8192\n"
"ds_read_b128 a[248:251],v239 offset:10240\nds_read_b128 a[252:255],v239 offset:12288\ns_waitcnt lgkmcnt(6)\nv_mfma_f32_16x16x32_bf16 v[0:3],a[196:199],a[228:231],0\ns_waitcnt vmcnt(16)\nds_read_b64_tr_b16 a[212:213],v231 offset:4096\nv_mfma_f32_16x16x32_bf16 v[28:31],a[200:203],a[228:231],0\ns_add_u32 m0,s64,0\n"
"ds_read_b32 v248,v255 offset:57344\nv_mfma_f32_16x16x32_bf16 v[56:59],a[204:207],a[228:231],0\nds_read_b64_tr_b16 a[214:215],v231 offset:5120\ns_mov_b32 s73,98304\nv_mfma_f32_16x16x32_bf16 v[84:87],a[208:211],a[228:231],0\nds_read_b128 a[228:231],v239 offset:1024\ns_waitcnt lgkmcnt(9)\nv_mfma_f32_16x16x32_bf16 v[4:7],a[196:199],a[232:235],0\n"
"buffer_load_dwordx4 v225,s[52:55],s73 offen offset:0 nt lds\ns_mov_b32 s74,99328\nv_mfma_f32_16x16x32_bf16 v[32:35],a[200:203],a[232:235],0\nds_read_b64_tr_b16 a[216:217],v232 offset:4096\nds_read_b32 v249,v255 offset:57408\nv_mfma_f32_16x16x32_bf16 v[60:63],a[204:207],a[232:235],0\nds_read_b32 v250,v255 offset:57472\nv_mfma_f32_16x16x32_bf16 v[88:91],a[208:211],a[232:235],0\n"
"ds_read_b128 a[232:235],v239 offset:3072\ns_waitcnt lgkmcnt(12)\nv_mfma_f32_16x16x32_bf16 v[8:11],a[196:199],a[236:239],0\nbuffer_load_dwordx4 v225,s[52:55],s74 offen offset:1024 nt lds\ns_mov_b32 s75,104448\nv_mfma_f32_16x16x32_bf16 v[36:39],a[200:203],a[236:239],0\nds_read_b64_tr_b16 a[218:219],v232 offset:5120\nds_read_b32 v251,v255 offset:57536\n"
"v_mfma_f32_16x16x32_bf16 v[64:67],a[204:207],a[236:239],0\nds_read_b32 v252,v255 offset:57600\nv_mfma_f32_16x16x32_bf16 v[92:95],a[208:211],a[236:239],0\nds_read_b128 a[236:239],v239 offset:5120\ns_waitcnt lgkmcnt(14)\nv_mfma_f32_16x16x32_bf16 v[12:15],a[196:199],a[240:243],0\nbuffer_load_dwordx4 v225,s[52:55],s75 offen offset:2048 nt lds\ns_mov_b32 s72,105472\n"
"v_mfma_f32_16x16x32_bf16 v[40:43],a[200:203],a[240:243],0\nds_read_b64_tr_b16 a[220:221],v233 offset:4096\nds_read_b32 v253,v255 offset:57664\nv_mfma_f32_16x16x32_bf16 v[68:71],a[204:207],a[240:243],0\nds_read_b64_tr_b16 a[222:223],v233 offset:5120\nv_mfma_f32_16x16x32_bf16 v[96:99],a[208:211],a[240:243],0\nds_read_b128 a[240:243],v239 offset:7168\nds_read_b32 v254,v255 offset:57728\n"
"v_mfma_f32_16x16x32_bf16 v[16:19],a[196:199],a[244:247],0\nbuffer_load_dwordx4 v225,s[52:55],s72 offen offset:3072 nt lds\nv_mfma_f32_16x16x32_bf16 v[44:47],a[200:203],a[244:247],0\nds_read_b64_tr_b16 a[224:225],v234 offset:4096\nv_mfma_f32_16x16x32_bf16 v[72:75],a[204:207],a[244:247],0\nv_mfma_f32_16x16x32_bf16 v[100:103],a[208:211],a[244:247],0\nds_read_b128 a[244:247],v239 offset:9216\ns_waitcnt lgkmcnt(15)\n"
"s_waitcnt lgkmcnt(2)\nv_mfma_f32_16x16x32_bf16 v[20:23],a[196:199],a[248:251],0\nds_read_b64_tr_b16 a[226:227],v234 offset:5120\nv_sub_f32 v241,v241,v248\nv_mfma_f32_16x16x32_bf16 v[48:51],a[200:203],a[248:251],0\ns_add_u32 m0,s64,4096\nv_sub_f32 v242,v242,v249\nv_sub_f32 v243,v243,v250\n"
"v_sub_f32 v244,v244,v251\nv_mfma_f32_16x16x32_bf16 v[76:79],a[204:207],a[248:251],0\ns_mov_b32 s73,114688\nv_sub_f32 v245,v245,v252\nv_sub_f32 v246,v246,v253\nv_mfma_f32_16x16x32_bf16 v[104:107],a[208:211],a[248:251],0\nds_read_b128 a[248:251],v239 offset:11264\nv_sub_f32 v247,v247,v254\n"
"v_fma_f32 v112,v112,s69,-v248\nv_mfma_f32_16x16x32_bf16 v[24:27],a[196:199],a[252:255],0\nv_exp_f32 v241,v241\nv_mfma_f32_16x16x32_bf16 v[52:55],a[200:203],a[252:255],0\nv_exp_f32 v242,v242\nv_fma_f32 v113,v113,s69,-v248\nv_mfma_f32_16x16x32_bf16 v[80:83],a[204:207],a[252:255],0\nv_exp_f32 v243,v243\n"
"v_mfma_f32_16x16x32_bf16 v[108:111],a[208:211],a[252:255],0\nds_read_b128 a[252:255],v239 offset:13312\ns_waitcnt lgkmcnt(2)\nv_fma_f32 v114,v114,s69,-v248\nv_mfma_f32_16x16x32_bf16 v[0:3],a[212:215],a[228:231],v[0:3]\nbuffer_load_dwordx4 v225,s[52:55],s73 offen offset:0 nt lds\nv_fma_f32 v115,v115,s69,-v248\nv_mfma_f32_16x16x32_bf16 v[28:31],a[216:219],a[228:231],v[28:31]\n"
"s_waitcnt vmcnt(17)\nds_read_b64_tr_b16 a[196:197],v231 offset:8192\nv_fma_f32 v116,v116,s69,-v249\nv_mfma_f32_16x16x32_bf16 v[56:59],a[220:223],a[228:231],v[56:59]\nds_read_b64_tr_b16 a[198:199],v231 offset:9216\nv_fma_f32 v117,v117,s69,-v249\nv_mfma_f32_16x16x32_bf16 v[84:87],a[224:227],a[228:231],v[84:87]\ns_waitcnt lgkmcnt(2)\n"
"s_barrier\nds_read_b128 a[228:231],v239 offset:14336\nv_mfma_f32_16x16x32_bf16 v[4:7],a[212:215],a[232:235],v[4:7]\ns_mov_b32 s74,115712\nbuffer_load_dwordx4 v225,s[52:55],s74 offen offset:1024 nt lds\nv_mfma_f32_16x16x32_bf16 v[32:35],a[216:219],a[232:235],v[32:35]\nds_read_b64_tr_b16 a[200:201],v232 offset:8192\nv_exp_f32 v244,v244\n"
"v_mfma_f32_16x16x32_bf16 v[60:63],a[220:223],a[232:235],v[60:63]\ns_mov_b32 s75,120832\nv_exp_f32 v245,v245\nv_mfma_f32_16x16x32_bf16 v[88:91],a[224:227],a[232:235],v[88:91]\nds_read_b128 a[232:235],v239 offset:16384\nv_exp_f32 v246,v246\nv_mfma_f32_16x16x32_bf16 v[8:11],a[212:215],a[236:239],v[8:11]\nbuffer_load_dwordx4 v225,s[52:55],s75 offen offset:2048 nt lds\n"
"v_fma_f32 v118,v118,s69,-v249\nv_mfma_f32_16x16x32_bf16 v[36:39],a[216:219],a[236:239],v[36:39]\nds_read_b64_tr_b16 a[202:203],v232 offset:9216\nv_exp_f32 v247,v247\nv_mfma_f32_16x16x32_bf16 v[64:67],a[220:223],a[236:239],v[64:67]\ns_mov_b32 s72,121856\nv_exp_f32 v112,v112\nv_mfma_f32_16x16x32_bf16 v[92:95],a[224:227],a[236:239],v[92:95]\n"
"ds_read_b128 a[236:239],v239 offset:18432\nv_exp_f32 v113,v113\nv_mfma_f32_16x16x32_bf16 v[12:15],a[212:215],a[240:243],v[12:15]\nbuffer_load_dwordx4 v225,s[52:55],s72 offen offset:3072 nt lds\nv_add_f32 v255,v112,v113\nv_mfma_f32_16x16x32_bf16 v[40:43],a[216:219],a[240:243],v[40:43]\nds_read_b64_tr_b16 a[204:205],v233 offset:8192\nv_exp_f32 v114,v114\n"
"v_mfma_f32_16x16x32_bf16 v[68:71],a[220:223],a[240:243],v[68:71]\nds_read_b64_tr_b16 a[206:207],v233 offset:9216\nv_cvt_pk_bf16_f32 v112,v112,v113\nv_mfma_f32_16x16x32_bf16 v[96:99],a[224:227],a[240:243],v[96:99]\nds_read_b128 a[240:243],v239 offset:20480\nv_exp_f32 v115,v115\nv_mfma_f32_16x16x32_bf16 v[16:19],a[212:215],a[244:247],v[16:19]\nds_read_b64_tr_b16 a[208:209],v234 offset:8192\n"
"v_cvt_pk_bf16_f32 v113,v114,v115\nv_mfma_f32_16x16x32_bf16 v[44:47],a[216:219],a[244:247],v[44:47]\nv_add_f32 v114,v114,v115\nv_add_f32 v114,v114,v255\nv_fma_f32 v119,v119,s69,-v249\nv_mfma_f32_16x16x32_bf16 v[72:75],a[220:223],a[244:247],v[72:75]\nv_exp_f32 v116,v116\nv_mfma_f32_16x16x32_bf16 v[100:103],a[224:227],a[244:247],v[100:103]\n"
"ds_read_b128 a[244:247],v239 offset:22528\nv_exp_f32 v117,v117\nv_mfma_f32_16x16x32_bf16 v[20:23],a[212:215],a[248:251],v[20:23]\nds_read_b64_tr_b16 a[210:211],v234 offset:9216\nv_add_f32 v255,v116,v117\nv_mfma_f32_16x16x32_bf16 v[48:51],a[216:219],a[248:251],v[48:51]\ns_add_u32 m0,s64,8192\nv_exp_f32 v118,v118\n"
"v_cvt_pk_bf16_f32 v116,v116,v117\nv_mfma_f32_16x16x32_bf16 v[76:79],a[220:223],a[248:251],v[76:79]\ns_mov_b32 s73,131072\nv_exp_f32 v119,v119\nv_mfma_f32_16x16x32_bf16 v[104:107],a[224:227],a[248:251],v[104:107]\nds_read_b128 a[248:251],v239 offset:24576\nv_cvt_pk_bf16_f32 v117,v118,v119\nv_add_f32 v118,v118,v119\n"
"v_mfma_f32_16x16x32_bf16 v[24:27],a[212:215],a[252:255],v[24:27]\nv_add_f32 v118,v118,v255\nv_fma_f32 v120,v120,s69,-v250\nv_mfma_f32_16x16x32_bf16 v[52:55],a[216:219],a[252:255],v[52:55]\nv_fma_f32 v121,v121,s69,-v250\nv_fma_f32 v122,v122,s69,-v250\nv_fma_f32 v123,v123,s69,-v250\nv_mfma_f32_16x16x32_bf16 v[80:83],a[220:223],a[252:255],v[80:83]\n"
"v_exp_f32 v120,v120\nv_mfma_f32_16x16x32_bf16 v[108:111],a[224:227],a[252:255],v[108:111]\nds_read_b128 a[252:255],v239 offset:26624\ns_waitcnt lgkmcnt(2)\nv_fma_f32 v124,v124,s69,-v251\nv_mfma_f32_16x16x32_bf16 v[0:3],a[196:199],a[228:231],v[0:3]\nbuffer_load_dwordx4 v225,s[52:55],s73 offen offset:0 nt lds\nv_fma_f32 v125,v125,s69,-v251\n"
"v_mfma_f32_16x16x32_bf16 v[28:31],a[200:203],a[228:231],v[28:31]\ns_waitcnt vmcnt(17)\nds_read_b64_tr_b16 a[212:213],v231 offset:12288\nv_fma_f32 v126,v126,s69,-v251\nv_mfma_f32_16x16x32_bf16 v[56:59],a[204:207],a[228:231],v[56:59]\nds_read_b64_tr_b16 a[214:215],v231 offset:13312\nv_fma_f32 v127,v127,s69,-v251\nv_mfma_f32_16x16x32_bf16 v[84:87],a[208:211],a[228:231],v[84:87]\n"
"ds_read_b128 a[228:231],v239 offset:15360\nv_exp_f32 v121,v121\nv_mfma_f32_16x16x32_bf16 v[4:7],a[196:199],a[232:235],v[4:7]\ns_mov_b32 s74,132096\nbuffer_load_dwordx4 v225,s[52:55],s74 offen offset:1024 nt lds\nv_mfma_f32_16x16x32_bf16 v[32:35],a[200:203],a[232:235],v[32:35]\nds_read_b64_tr_b16 a[216:217],v232 offset:12288\nv_exp_f32 v122,v122\n"
"v_mfma_f32_16x16x32_bf16 v[60:63],a[204:207],a[232:235],v[60:63]\ns_mov_b32 s75,137216\nv_exp_f32 v123,v123\nv_mfma_f32_16x16x32_bf16 v[88:91],a[208:211],a[232:235],v[88:91]\nds_read_b128 a[232:235],v239 offset:17408\nv_add_f32 v255,v120,v121\nv_cvt_pk_bf16_f32 v120,v120,v121\nv_mfma_f32_16x16x32_bf16 v[8:11],a[196:199],a[236:239],v[8:11]\n"
"buffer_load_dwordx4 v225,s[52:55],s75 offen offset:2048 nt lds\nv_cvt_pk_bf16_f32 v121,v122,v123\nv_mfma_f32_16x16x32_bf16 v[36:39],a[200:203],a[236:239],v[36:39]\nds_read_b64_tr_b16 a[218:219],v232 offset:13312\nv_add_f32 v122,v122,v123\nv_add_f32 v122,v122,v255\nv_mfma_f32_16x16x32_bf16 v[64:67],a[204:207],a[236:239],v[64:67]\ns_mov_b32 s72,138240\n"
"v_exp_f32 v124,v124\nv_mfma_f32_16x16x32_bf16 v[92:95],a[208:211],a[236:239],v[92:95]\nds_read_b128 a[236:239],v239 offset:19456\nv_exp_f32 v125,v125\nv_mfma_f32_16x16x32_bf16 v[12:15],a[196:199],a[240:243],v[12:15]\nbuffer_load_dwordx4 v225,s[52:55],s72 offen offset:3072 nt lds\nv_add_f32 v255,v124,v125\nv_mfma_f32_16x16x32_bf16 v[40:43],a[200:203],a[240:243],v[40:43]\n"
"ds_read_b64_tr_b16 a[220:221],v233 offset:12288\nv_exp_f32 v126,v126\nv_mfma_f32_16x16x32_bf16 v[68:71],a[204:207],a[240:243],v[68:71]\nds_read_b64_tr_b16 a[222:223],v233 offset:13312\nv_cvt_pk_bf16_f32 v124,v124,v125\nv_mfma_f32_16x16x32_bf16 v[96:99],a[208:211],a[240:243],v[96:99]\nds_read_b128 a[240:243],v239 offset:21504\nv_exp_f32 v127,v127\n"
"v_mfma_f32_16x16x32_bf16 v[16:19],a[196:199],a[244:247],v[16:19]\nds_read_b64_tr_b16 a[224:225],v234 offset:12288\nv_cvt_pk_bf16_f32 v125,v126,v127\nv_mfma_f32_16x16x32_bf16 v[44:47],a[200:203],a[244:247],v[44:47]\nv_add_f32 v126,v126,v127\nv_add_f32 v126,v126,v255\nv_fma_f32 v128,v128,s69,-v252\nv_mfma_f32_16x16x32_bf16 v[72:75],a[204:207],a[244:247],v[72:75]\n"
"v_fma_f32 v129,v129,s69,-v252\nv_fma_f32 v130,v130,s69,-v252\nv_mfma_f32_16x16x32_bf16 v[100:103],a[208:211],a[244:247],v[100:103]\nds_read_b128 a[244:247],v239 offset:23552\ns_waitcnt lgkmcnt(12)\nv_fma_f32 v131,v131,s69,-v252\nv_mfma_f32_16x16x32_bf16 v[20:23],a[196:199],a[248:251],v[20:23]\nds_read_b64_tr_b16 a[226:227],v234 offset:13312\n"
"v_fma_f32 v132,v132,s69,-v253\nv_mfma_f32_16x16x32_bf16 v[48:51],a[200:203],a[248:251],v[48:51]\ns_add_u32 m0,s64,12288\nv_exp_f32 v128,v128\nv_fma_f32 v133,v133,s69,-v253\nv_mfma_f32_16x16x32_bf16 v[76:79],a[204:207],a[248:251],v[76:79]\ns_mov_b32 s73,147456\nv_exp_f32 v129,v129\n"
"v_mfma_f32_16x16x32_bf16 v[104:107],a[208:211],a[248:251],v[104:107]\nds_read_b128 a[248:251],v239 offset:25600\nv_exp_f32 v130,v130\nv_mfma_f32_16x16x32_bf16 v[24:27],a[196:199],a[252:255],v[24:27]\nv_exp_f32 v131,v131\nv_mfma_f32_16x16x32_bf16 v[52:55],a[200:203],a[252:255],v[52:55]\nv_add_f32 v255,v128,v129\nv_cvt_pk_bf16_f32 v128,v128,v129\n"
"v_cvt_pk_bf16_f32 v129,v130,v131\nv_mfma_f32_16x16x32_bf16 v[80:83],a[204:207],a[252:255],v[80:83]\nv_add_f32 v130,v130,v131\nv_add_f32 v130,v130,v255\nv_mfma_f32_16x16x32_bf16 v[108:111],a[208:211],a[252:255],v[108:111]\nds_read_b128 a[252:255],v239 offset:27648\ns_waitcnt lgkmcnt(2)\nv_fma_f32 v134,v134,s69,-v253\n"
"v_mfma_f32_16x16x32_bf16 v[0:3],a[212:215],a[228:231],v[0:3]\nbuffer_load_dwordx4 v225,s[52:55],s73 offen offset:0 nt lds\nv_fma_f32 v135,v135,s69,-v253\nv_mfma_f32_16x16x32_bf16 v[28:31],a[216:219],a[228:231],v[28:31]\ns_waitcnt vmcnt(17)\nds_read_b64_tr_b16 a[196:197],v231 offset:16384\nv_fma_f32 v136,v136,s69,-v254\nv_mfma_f32_16x16x32_bf16 v[56:59],a[220:223],a[228:231],v[56:59]\n"
"ds_read_b64_tr_b16 a[198:199],v231 offset:17408\nv_fma_f32 v137,v137,s69,-v254\nv_mfma_f32_16x16x32_bf16 v[84:87],a[224:227],a[228:231],v[84:87]\ns_waitcnt lgkmcnt(2)\ns_barrier\nds_read_b128 a[228:231],v239 offset:28672\nv_mfma_f32_16x16x32_bf16 v[4:7],a[212:215],a[232:235],v[4:7]\ns_mov_b32 s74,148480\n"
"buffer_load_dwordx4 v225,s[52:55],s74 offen offset:1024 nt lds\nv_mfma_f32_16x16x32_bf16 v[32:35],a[216:219],a[232:235],v[32:35]\nds_read_b64_tr_b16 a[200:201],v232 offset:16384\nv_exp_f32 v132,v132\nv_mfma_f32_16x16x32_bf16 v[60:63],a[220:223],a[232:235],v[60:63]\ns_mov_b32 s75,153600\nv_exp_f32 v133,v133\nv_mfma_f32_16x16x32_bf16 v[88:91],a[224:227],a[232:235],v[88:91]\n"
"ds_read_b128 a[232:235],v239 offset:30720\nv_exp_f32 v134,v134\nv_mfma_f32_16x16x32_bf16 v[8:11],a[212:215],a[236:239],v[8:11]\nbuffer_load_dwordx4 v225,s[52:55],s75 offen offset:2048 nt lds\nv_add_f32 v255,v132,v133\nv_mfma_f32_16x16x32_bf16 v[36:39],a[216:219],a[236:239],v[36:39]\nds_read_b64_tr_b16 a[202:203],v232 offset:17408\nv_exp_f32 v135,v135\n"
"v_mfma_f32_16x16x32_bf16 v[64:67],a[220:223],a[236:239],v[64:67]\ns_mov_b32 s72,154624\nv_cvt_pk_bf16_f32 v132,v132,v133\nv_cvt_pk_bf16_f32 v133,v134,v135\nv_mfma_f32_16x16x32_bf16 v[92:95],a[224:227],a[236:239],v[92:95]\nds_read_b128 a[236:239],v239 offset:32768\nv_add_f32 v134,v134,v135\nv_add_f32 v134,v134,v255\n"
"v_mfma_f32_16x16x32_bf16 v[12:15],a[212:215],a[240:243],v[12:15]\nbuffer_load_dwordx4 v225,s[52:55],s72 offen offset:3072 nt lds\nv_fma_f32 v138,v138,s69,-v254\nv_mfma_f32_16x16x32_bf16 v[40:43],a[216:219],a[240:243],v[40:43]\nds_read_b64_tr_b16 a[204:205],v233 offset:16384\nv_fma_f32 v139,v139,s69,-v254\nv_fma_f32 v140,v140,s69,-v248\nv_mfma_f32_16x16x32_bf16 v[68:71],a[220:223],a[240:243],v[68:71]\n"
"ds_read_b64_tr_b16 a[206:207],v233 offset:17408\nv_fma_f32 v141,v141,s69,-v248\nv_mfma_f32_16x16x32_bf16 v[96:99],a[224:227],a[240:243],v[96:99]\nds_read_b128 a[240:243],v239 offset:34816\nv_exp_f32 v136,v136\nv_mfma_f32_16x16x32_bf16 v[16:19],a[212:215],a[244:247],v[16:19]\nds_read_b64_tr_b16 a[208:209],v234 offset:16384\nv_fma_f32 v142,v142,s69,-v248\n"
"v_mfma_f32_16x16x32_bf16 v[44:47],a[216:219],a[244:247],v[44:47]\nv_exp_f32 v137,v137\nv_fma_f32 v143,v143,s69,-v248\nv_mfma_f32_16x16x32_bf16 v[72:75],a[220:223],a[244:247],v[72:75]\nv_exp_f32 v138,v138\nv_mfma_f32_16x16x32_bf16 v[100:103],a[224:227],a[244:247],v[100:103]\nds_read_b128 a[244:247],v239 offset:36864\nv_exp_f32 v139,v139\n"
"v_mfma_f32_16x16x32_bf16 v[20:23],a[212:215],a[248:251],v[20:23]\nds_read_b64_tr_b16 a[210:211],v234 offset:17408\nv_add_f32 v255,v136,v137\nv_mfma_f32_16x16x32_bf16 v[48:51],a[216:219],a[248:251],v[48:51]\ns_add_u32 m0,s64,16384\nv_cvt_pk_bf16_f32 v136,v136,v137\nv_cvt_pk_bf16_f32 v137,v138,v139\nv_add_f32 v138,v138,v139\n"
"v_mfma_f32_16x16x32_bf16 v[76:79],a[220:223],a[248:251],v[76:79]\ns_mov_b32 s73,163840\nv_add_f32 v138,v138,v255\nv_fma_f32 v144,v144,s69,-v249\nv_mfma_f32_16x16x32_bf16 v[104:107],a[224:227],a[248:251],v[104:107]\nds_read_b128 a[248:251],v239 offset:38912\nv_exp_f32 v140,v140\nv_mfma_f32_16x16x32_bf16 v[24:27],a[212:215],a[252:255],v[24:27]\n"
"ds_write_b64 v240,v[112:113] offset:0\nv_mfma_f32_16x16x32_bf16 v[52:55],a[216:219],a[252:255],v[52:55]\nv_fma_f32 v145,v145,s69,-v249\nv_fma_f32 v146,v146,s69,-v249\nv_fma_f32 v147,v147,s69,-v249\nv_mfma_f32_16x16x32_bf16 v[80:83],a[220:223],a[252:255],v[80:83]\nds_write_b64 v240,v[116:117] offset:2048\nv_mfma_f32_16x16x32_bf16 v[108:111],a[224:227],a[252:255],v[108:111]\n"
"ds_read_b128 a[252:255],v239 offset:40960\ns_waitcnt lgkmcnt(4)\nv_fma_f32 v148,v148,s69,-v250\nv_mfma_f32_16x16x32_bf16 v[0:3],a[196:199],a[228:231],v[0:3]\nbuffer_load_dwordx4 v225,s[52:55],s73 offen offset:0 nt lds\nv_fma_f32 v149,v149,s69,-v250\nv_mfma_f32_16x16x32_bf16 v[28:31],a[200:203],a[228:231],v[28:31]\ns_waitcnt vmcnt(17)\n"
"ds_read_b64_tr_b16 a[212:213],v231 offset:20480\nv_fma_f32 v150,v150,s69,-v250\nv_mfma_f32_16x16x32_bf16 v[56:59],a[204:207],a[228:231],v[56:59]\nds_read_b64_tr_b16 a[214:215],v231 offset:21504\nv_fma_f32 v151,v151,s69,-v250\nv_mfma_f32_16x16x32_bf16 v[84:87],a[208:211],a[228:231],v[84:87]\nds_read_b128 a[228:231],v239 offset:29696\nv_exp_f32 v144,v144\n"
"v_mfma_f32_16x16x32_bf16 v[4:7],a[196:199],a[232:235],v[4:7]\ns_mov_b32 s74,164864\nbuffer_load_dwordx4 v225,s[52:55],s74 offen offset:1024 nt lds\nv_mfma_f32_16x16x32_bf16 v[32:35],a[200:203],a[232:235],v[32:35]\nds_read_b64_tr_b16 a[216:217],v232 offset:20480\nv_exp_f32 v148,v148\nv_mfma_f32_16x16x32_bf16 v[60:63],a[204:207],a[232:235],v[60:63]\ns_mov_b32 s75,169984\n"
"ds_write_b64 v240,v[120:121] offset:4096\nv_mfma_f32_16x16x32_bf16 v[88:91],a[208:211],a[232:235],v[88:91]\nds_read_b128 a[232:235],v239 offset:31744\nv_fma_f32 v152,v152,s69,-v251\nv_fma_f32 v153,v153,s69,-v251\nv_mfma_f32_16x16x32_bf16 v[8:11],a[196:199],a[236:239],v[8:11]\nbuffer_load_dwordx4 v225,s[52:55],s75 offen offset:2048 nt lds\nv_fma_f32 v154,v154,s69,-v251\n"
"v_mfma_f32_16x16x32_bf16 v[36:39],a[200:203],a[236:239],v[36:39]\nds_read_b64_tr_b16 a[218:219],v232 offset:21504\nv_fma_f32 v155,v155,s69,-v251\nv_fma_f32 v156,v156,s69,-v252\nv_mfma_f32_16x16x32_bf16 v[64:67],a[204:207],a[236:239],v[64:67]\ns_mov_b32 s72,171008\nds_write_b64 v240,v[124:125] offset:6144\nv_mfma_f32_16x16x32_bf16 v[92:95],a[208:211],a[236:239],v[92:95]\n"
"ds_read_b128 a[236:239],v239 offset:33792\nv_exp_f32 v152,v152\nv_mfma_f32_16x16x32_bf16 v[12:15],a[196:199],a[240:243],v[12:15]\nbuffer_load_dwordx4 v225,s[52:55],s72 offen offset:3072 nt lds\nv_fma_f32 v157,v157,s69,-v252\nv_mfma_f32_16x16x32_bf16 v[40:43],a[200:203],a[240:243],v[40:43]\nds_read_b64_tr_b16 a[220:221],v233 offset:20480\nv_fma_f32 v158,v158,s69,-v252\n"
"v_fma_f32 v159,v159,s69,-v252\nv_mfma_f32_16x16x32_bf16 v[68:71],a[204:207],a[240:243],v[68:71]\nds_read_b64_tr_b16 a[222:223],v233 offset:21504\nv_fma_f32 v160,v160,s69,-v253\nv_mfma_f32_16x16x32_bf16 v[96:99],a[208:211],a[240:243],v[96:99]\nds_read_b128 a[240:243],v239 offset:35840\nv_exp_f32 v156,v156\nv_mfma_f32_16x16x32_bf16 v[16:19],a[196:199],a[244:247],v[16:19]\n"
"ds_read_b64_tr_b16 a[224:225],v234 offset:20480\nv_fma_f32 v161,v161,s69,-v253\nv_mfma_f32_16x16x32_bf16 v[44:47],a[200:203],a[244:247],v[44:47]\nds_write_b64 v240,v[128:129] offset:8192\nv_mfma_f32_16x16x32_bf16 v[72:75],a[204:207],a[244:247],v[72:75]\nv_fma_f32 v162,v162,s69,-v253\nv_fma_f32 v163,v163,s69,-v253\nv_mfma_f32_16x16x32_bf16 v[100:103],a[208:211],a[244:247],v[100:103]\n"
"ds_read_b128 a[244:247],v239 offset:37888\ns_waitcnt lgkmcnt(15)\nv_fma_f32 v164,v164,s69,-v254\nv_mfma_f32_16x16x32_bf16 v[20:23],a[196:199],a[248:251],v[20:23]\nds_read_b64_tr_b16 a[226:227],v234 offset:21504\nv_fma_f32 v165,v165,s69,-v254\nv_mfma_f32_16x16x32_bf16 v[48:51],a[200:203],a[248:251],v[48:51]\ns_add_u32 m0,s64,20480\n"
"ds_write_b64 v240,v[132:133] offset:10240\nv_mfma_f32_16x16x32_bf16 v[76:79],a[204:207],a[248:251],v[76:79]\ns_mov_b32 s73,180224\nv_exp_f32 v160,v160\nv_mfma_f32_16x16x32_bf16 v[104:107],a[208:211],a[248:251],v[104:107]\nds_read_b128 a[248:251],v239 offset:39936\nv_fma_f32 v166,v166,s69,-v254\nv_fma_f32 v167,v167,s69,-v254\n"
"v_mfma_f32_16x16x32_bf16 v[24:27],a[196:199],a[252:255],v[24:27]\nds_write_b64 v240,v[136:137] offset:12288\nv_mfma_f32_16x16x32_bf16 v[52:55],a[200:203],a[252:255],v[52:55]\nv_exp_f32 v164,v164\nv_add_f32 v114,v140,v114\nv_mfma_f32_16x16x32_bf16 v[80:83],a[204:207],a[252:255],v[80:83]\nv_exp_f32 v141,v141\nv_mfma_f32_16x16x32_bf16 v[108:111],a[208:211],a[252:255],v[108:111]\n"
"ds_read_b128 a[252:255],v239 offset:41984\ns_waitcnt lgkmcnt(4)\nv_add_f32 v114,v141,v114\nv_mfma_f32_16x16x32_bf16 v[0:3],a[212:215],a[228:231],v[0:3]\nbuffer_load_dwordx4 v225,s[52:55],s73 offen offset:0 nt lds\nv_cvt_pk_bf16_f32 v140,v140,v141\nv_mfma_f32_16x16x32_bf16 v[28:31],a[216:219],a[228:231],v[28:31]\ns_waitcnt vmcnt(17)\n"
"ds_read_b64_tr_b16 a[196:197],v231 offset:0\nv_add_f32 v118,v144,v118\nv_mfma_f32_16x16x32_bf16 v[56:59],a[220:223],a[228:231],v[56:59]\nds_read_b64_tr_b16 a[198:199],v231 offset:1024\nv_add_f32 v122,v148,v122\nv_mfma_f32_16x16x32_bf16 v[84:87],a[224:227],a[228:231],v[84:87]\ns_waitcnt lgkmcnt(2)\ns_barrier\n"
"ds_read_b128 a[228:231],v239 offset:43008\nv_mfma_f32_16x16x32_bf16 v[4:7],a[212:215],a[232:235],v[4:7]\ns_mov_b32 s74,181248\nbuffer_load_dwordx4 v225,s[52:55],s74 offen offset:1024 nt lds\nv_mfma_f32_16x16x32_bf16 v[32:35],a[216:219],a[232:235],v[32:35]\nds_read_b64_tr_b16 a[200:201],v232 offset:0\nv_exp_f32 v142,v142\nv_mfma_f32_16x16x32_bf16 v[60:63],a[220:223],a[232:235],v[60:63]\n"
"s_mov_b32 s75,186368\nv_exp_f32 v143,v143\nv_mfma_f32_16x16x32_bf16 v[88:91],a[224:227],a[232:235],v[88:91]\nds_read_b128 a[232:235],v239 offset:45056\nv_add_f32 v114,v142,v114\nv_cvt_pk_bf16_f32 v141,v142,v143\nv_mfma_f32_16x16x32_bf16 v[8:11],a[212:215],a[236:239],v[8:11]\nbuffer_load_dwordx4 v225,s[52:55],s75 offen offset:2048 nt lds\n"
"v_add_f32 v114,v143,v114\nv_mfma_f32_16x16x32_bf16 v[36:39],a[216:219],a[236:239],v[36:39]\nds_read_b64_tr_b16 a[202:203],v232 offset:1024\nv_exp_f32 v145,v145\nv_mfma_f32_16x16x32_bf16 v[64:67],a[220:223],a[236:239],v[64:67]\ns_mov_b32 s72,187392\nv_exp_f32 v146,v146\nv_mfma_f32_16x16x32_bf16 v[92:95],a[224:227],a[236:239],v[92:95]\n"
"ds_read_b128 a[236:239],v239 offset:47104\nv_exp_f32 v147,v147\nv_mfma_f32_16x16x32_bf16 v[12:15],a[212:215],a[240:243],v[12:15]\nbuffer_load_dwordx4 v225,s[52:55],s72 offen offset:3072 nt lds\nv_add_f32 v118,v145,v118\nv_mfma_f32_16x16x32_bf16 v[40:43],a[216:219],a[240:243],v[40:43]\nds_read_b64_tr_b16 a[204:205],v233 offset:0\nv_cvt_pk_bf16_f32 v144,v144,v145\n"
"v_add_f32 v118,v146,v118\nv_mfma_f32_16x16x32_bf16 v[68:71],a[220:223],a[240:243],v[68:71]\nds_read_b64_tr_b16 a[206:207],v233 offset:1024\nv_cvt_pk_bf16_f32 v145,v146,v147\nv_mfma_f32_16x16x32_bf16 v[96:99],a[224:227],a[240:243],v[96:99]\nds_read_b128 a[240:243],v239 offset:49152\nv_add_f32 v118,v147,v118\nv_add_f32 v126,v152,v126\n"
"v_mfma_f32_16x16x32_bf16 v[16:19],a[212:215],a[244:247],v[16:19]\nds_read_b64_tr_b16 a[208:209],v234 offset:0\nv_add_f32 v130,v156,v130\nv_mfma_f32_16x16x32_bf16 v[44:47],a[216:219],a[244:247],v[44:47]\nv_exp_f32 v149,v149\nv_mfma_f32_16x16x32_bf16 v[72:75],a[220:223],a[244:247],v[72:75]\nv_exp_f32 v150,v150\nv_mfma_f32_16x16x32_bf16 v[100:103],a[224:227],a[244:247],v[100:103]\n"
"ds_read_b128 a[244:247],v239 offset:51200\nv_exp_f32 v151,v151\nv_mfma_f32_16x16x32_bf16 v[20:23],a[212:215],a[248:251],v[20:23]\nds_read_b64_tr_b16 a[210:211],v234 offset:1024\nv_add_f32 v122,v149,v122\nv_mfma_f32_16x16x32_bf16 v[48:51],a[216:219],a[248:251],v[48:51]\ns_add_u32 m0,s64,0\nv_cvt_pk_bf16_f32 v148,v148,v149\n"
"v_add_f32 v122,v150,v122\nv_cvt_pk_bf16_f32 v149,v150,v151\nv_mfma_f32_16x16x32_bf16 v[76:79],a[220:223],a[248:251],v[76:79]\ns_mov_b32 s73,196608\nv_add_f32 v122,v151,v122\nv_add_f32 v134,v160,v134\nv_mfma_f32_16x16x32_bf16 v[104:107],a[224:227],a[248:251],v[104:107]\nds_read_b128 a[248:251],v239 offset:53248\n"
"v_exp_f32 v153,v153\nv_mfma_f32_16x16x32_bf16 v[24:27],a[212:215],a[252:255],v[24:27]\nv_exp_f32 v154,v154\nv_mfma_f32_16x16x32_bf16 v[52:55],a[216:219],a[252:255],v[52:55]\nv_exp_f32 v155,v155\nv_add_f32 v126,v153,v126\nv_mfma_f32_16x16x32_bf16 v[80:83],a[220:223],a[252:255],v[80:83]\nv_cvt_pk_bf16_f32 v152,v152,v153\n"
"v_add_f32 v126,v154,v126\nv_mfma_f32_16x16x32_bf16 v[108:111],a[224:227],a[252:255],v[108:111]\nds_read_b128 a[252:255],v239 offset:55296\ns_waitcnt lgkmcnt(2)\nv_cvt_pk_bf16_f32 v153,v154,v155\nv_mfma_f32_16x16x32_bf16 v[0:3],a[196:199],a[228:231],v[0:3]\nbuffer_load_dwordx4 v225,s[52:55],s73 offen offset:0 nt lds\nv_add_f32 v126,v155,v126\n"
"v_mfma_f32_16x16x32_bf16 v[28:31],a[200:203],a[228:231],v[28:31]\ns_waitcnt vmcnt(17)\nds_read_b64_tr_b16 a[212:213],v231 offset:4096\nv_add_f32 v138,v164,v138\nv_mfma_f32_16x16x32_bf16 v[56:59],a[204:207],a[228:231],v[56:59]\nds_read_b64_tr_b16 a[214:215],v231 offset:5120\ns_mov_b32 s74,197632\nv_mfma_f32_16x16x32_bf16 v[84:87],a[208:211],a[228:231],v[84:87]\n"
"ds_read_b128 a[228:231],v239 offset:44032\nv_exp_f32 v157,v157\nv_mfma_f32_16x16x32_bf16 v[4:7],a[196:199],a[232:235],v[4:7]\nbuffer_load_dwordx4 v225,s[52:55],s74 offen offset:1024 nt lds\nv_add_f32 v130,v157,v130\nv_mfma_f32_16x16x32_bf16 v[32:35],a[200:203],a[232:235],v[32:35]\nds_read_b64_tr_b16 a[216:217],v232 offset:4096\nv_exp_f32 v158,v158\n"
"v_mfma_f32_16x16x32_bf16 v[60:63],a[204:207],a[232:235],v[60:63]\ns_mov_b32 s75,202752\nv_exp_f32 v159,v159\nv_mfma_f32_16x16x32_bf16 v[88:91],a[208:211],a[232:235],v[88:91]\nds_read_b128 a[232:235],v239 offset:46080\nv_cvt_pk_bf16_f32 v156,v156,v157\nv_add_f32 v130,v158,v130\nv_mfma_f32_16x16x32_bf16 v[8:11],a[196:199],a[236:239],v[8:11]\n"
"buffer_load_dwordx4 v225,s[52:55],s75 offen offset:2048 nt lds\nv_cvt_pk_bf16_f32 v157,v158,v159\nv_mfma_f32_16x16x32_bf16 v[36:39],a[200:203],a[236:239],v[36:39]\nds_read_b64_tr_b16 a[218:219],v232 offset:5120\nv_add_f32 v130,v159,v130\nv_fma_f32 v168,v168,s69,-v248\nv_mfma_f32_16x16x32_bf16 v[64:67],a[204:207],a[236:239],v[64:67]\ns_mov_b32 s72,203776\n"
"v_exp_f32 v161,v161\nv_mfma_f32_16x16x32_bf16 v[92:95],a[208:211],a[236:239],v[92:95]\nds_read_b128 a[236:239],v239 offset:48128\nv_exp_f32 v162,v162\nv_mfma_f32_16x16x32_bf16 v[12:15],a[196:199],a[240:243],v[12:15]\nbuffer_load_dwordx4 v225,s[52:55],s72 offen offset:3072 nt lds\nv_add_f32 v134,v161,v134\nv_mfma_f32_16x16x32_bf16 v[40:43],a[200:203],a[240:243],v[40:43]\n"
"ds_read_b64_tr_b16 a[220:221],v233 offset:4096\nv_exp_f32 v163,v163\nv_mfma_f32_16x16x32_bf16 v[68:71],a[204:207],a[240:243],v[68:71]\nds_read_b64_tr_b16 a[222:223],v233 offset:5120\nv_cvt_pk_bf16_f32 v160,v160,v161\nv_mfma_f32_16x16x32_bf16 v[96:99],a[208:211],a[240:243],v[96:99]\nds_read_b128 a[240:243],v239 offset:50176\nv_add_f32 v134,v162,v134\n"
"v_cvt_pk_bf16_f32 v161,v162,v163\nv_mfma_f32_16x16x32_bf16 v[16:19],a[196:199],a[244:247],v[16:19]\nds_read_b64_tr_b16 a[224:225],v234 offset:4096\nv_add_f32 v134,v163,v134\nv_mfma_f32_16x16x32_bf16 v[44:47],a[200:203],a[244:247],v[44:47]\nv_exp_f32 v165,v165\nv_fma_f32 v169,v169,s69,-v248\nv_mfma_f32_16x16x32_bf16 v[72:75],a[204:207],a[244:247],v[72:75]\n"
"v_exp_f32 v166,v166\nv_mfma_f32_16x16x32_bf16 v[100:103],a[208:211],a[244:247],v[100:103]\nds_read_b128 a[244:247],v239 offset:52224\ns_waitcnt lgkmcnt(12)\nv_add_f32 v138,v165,v138\nv_mfma_f32_16x16x32_bf16 v[20:23],a[196:199],a[248:251],v[20:23]\nds_read_b64_tr_b16 a[226:227],v234 offset:5120\nv_cvt_pk_bf16_f32 v164,v164,v165\n"
"v_mfma_f32_16x16x32_bf16 v[48:51],a[200:203],a[248:251],v[48:51]\ns_add_u32 m0,s64,4096\nv_exp_f32 v167,v167\nv_add_f32 v138,v166,v138\nv_mfma_f32_16x16x32_bf16 v[76:79],a[204:207],a[248:251],v[76:79]\ns_mov_b32 s73,212992\nv_cvt_pk_bf16_f32 v165,v166,v167\nv_add_f32 v138,v167,v138\n"
"v_mfma_f32_16x16x32_bf16 v[104:107],a[208:211],a[248:251],v[104:107]\nds_read_b128 a[248:251],v239 offset:54272\nv_fma_f32 v170,v170,s69,-v248\nv_fma_f32 v171,v171,s69,-v248\nv_mfma_f32_16x16x32_bf16 v[24:27],a[196:199],a[252:255],v[24:27]\nds_write_b64 v240,v[140:141] offset:14336\nv_mfma_f32_16x16x32_bf16 v[52:55],a[200:203],a[252:255],v[52:55]\nv_exp_f32 v168,v168\n"
"v_fma_f32 v172,v172,s69,-v249\nv_mfma_f32_16x16x32_bf16 v[80:83],a[204:207],a[252:255],v[80:83]\nds_write_b64 v240,v[144:145] offset:16384\nv_mfma_f32_16x16x32_bf16 v[108:111],a[208:211],a[252:255],v[108:111]\nds_read_b128 a[252:255],v239 offset:56320\ns_waitcnt lgkmcnt(4)\nv_fma_f32 v173,v173,s69,-v249\nv_mfma_f32_16x16x32_bf16 v[0:3],a[212:215],a[228:231],v[0:3]\n"
"buffer_load_dwordx4 v225,s[52:55],s73 offen offset:0 nt lds\nv_fma_f32 v174,v174,s69,-v249\nv_mfma_f32_16x16x32_bf16 v[28:31],a[216:219],a[228:231],v[28:31]\ns_waitcnt vmcnt(17)\nds_read_b64_tr_b16 a[196:197],v231 offset:8192\nv_fma_f32 v175,v175,s69,-v249\nv_mfma_f32_16x16x32_bf16 v[56:59],a[220:223],a[228:231],v[56:59]\nds_read_b64_tr_b16 a[198:199],v231 offset:9216\n"
"v_fma_f32 v176,v176,s69,-v250\nv_mfma_f32_16x16x32_bf16 v[84:87],a[224:227],a[228:231],v[84:87]\ns_waitcnt lgkmcnt(2)\ns_barrier\nds_read_b128 a[228:231],v239 offset:0\nv_mfma_f32_16x16x32_bf16 v[4:7],a[212:215],a[232:235],v[4:7]\ns_mov_b32 s74,214016\nbuffer_load_dwordx4 v225,s[52:55],s74 offen offset:1024 nt lds\n"
"v_mfma_f32_16x16x32_bf16 v[32:35],a[216:219],a[232:235],v[32:35]\nds_read_b64_tr_b16 a[200:201],v232 offset:8192\nv_exp_f32 v172,v172\nv_mfma_f32_16x16x32_bf16 v[60:63],a[220:223],a[232:235],v[60:63]\ns_mov_b32 s75,219136\nds_write_b64 v240,v[148:149] offset:18432\nv_mfma_f32_16x16x32_bf16 v[88:91],a[224:227],a[232:235],v[88:91]\nds_read_b128 a[232:235],v239 offset:2048\n"
"v_fma_f32 v177,v177,s69,-v250\nv_fma_f32 v178,v178,s69,-v250\nv_mfma_f32_16x16x32_bf16 v[8:11],a[212:215],a[236:239],v[8:11]\nbuffer_load_dwordx4 v225,s[52:55],s75 offen offset:2048 nt lds\nv_fma_f32 v179,v179,s69,-v250\nv_mfma_f32_16x16x32_bf16 v[36:39],a[216:219],a[236:239],v[36:39]\nds_read_b64_tr_b16 a[202:203],v232 offset:9216\nv_exp_f32 v176,v176\n"
"v_mfma_f32_16x16x32_bf16 v[64:67],a[220:223],a[236:239],v[64:67]\ns_mov_b32 s72,220160\nds_write_b64 v240,v[152:153] offset:20480\nv_mfma_f32_16x16x32_bf16 v[92:95],a[224:227],a[236:239],v[92:95]\nds_read_b128 a[236:239],v239 offset:4096\nv_fma_f32 v180,v180,s69,-v251\nv_fma_f32 v181,v181,s69,-v251\nv_mfma_f32_16x16x32_bf16 v[12:15],a[212:215],a[240:243],v[12:15]\n"
"buffer_load_dwordx4 v225,s[52:55],s72 offen offset:3072 nt lds\nv_mul_f32 v0,v241,v0\nv_mul_f32 v1,v241,v1\nv_mul_f32 v2,v241,v2\nv_mul_f32 v3,v241,v3\nv_mfma_f32_16x16x32_bf16 v[40:43],a[216:219],a[240:243],v[40:43]\nds_read_b64_tr_b16 a[204:205],v233 offset:8192\nv_mul_f32 v28,v241,v28\n"
"v_mul_f32 v29,v241,v29\nv_mul_f32 v30,v241,v30\nv_mul_f32 v31,v241,v31\nv_mfma_f32_16x16x32_bf16 v[68:71],a[220:223],a[240:243],v[68:71]\nds_read_b64_tr_b16 a[206:207],v233 offset:9216\nv_mul_f32 v56,v241,v56\nv_mul_f32 v57,v241,v57\nv_mul_f32 v58,v241,v58\n"
"v_mul_f32 v59,v241,v59\nv_mfma_f32_16x16x32_bf16 v[96:99],a[224:227],a[240:243],v[96:99]\nds_read_b128 a[240:243],v239 offset:6144\nv_mul_f32 v84,v241,v84\nv_mul_f32 v85,v241,v85\nv_mul_f32 v86,v241,v86\nv_mul_f32 v87,v241,v87\nv_mfma_f32_16x16x32_bf16 v[16:19],a[212:215],a[244:247],v[16:19]\n"
"ds_read_b64_tr_b16 a[208:209],v234 offset:8192\nv_mul_f32 v4,v242,v4\nv_mul_f32 v5,v242,v5\nv_mul_f32 v6,v242,v6\nv_mul_f32 v7,v242,v7\nv_mfma_f32_16x16x32_bf16 v[44:47],a[216:219],a[244:247],v[44:47]\nv_mul_f32 v32,v242,v32\nv_mul_f32 v33,v242,v33\n"
"v_mul_f32 v34,v242,v34\nv_mul_f32 v35,v242,v35\nv_mfma_f32_16x16x32_bf16 v[72:75],a[220:223],a[244:247],v[72:75]\nv_mul_f32 v60,v242,v60\nv_mul_f32 v61,v242,v61\nv_mul_f32 v62,v242,v62\nv_mul_f32 v63,v242,v63\nv_mfma_f32_16x16x32_bf16 v[100:103],a[224:227],a[244:247],v[100:103]\n"
"ds_read_b128 a[244:247],v239 offset:8192\nv_mul_f32 v88,v242,v88\nv_mul_f32 v89,v242,v89\nv_mul_f32 v90,v242,v90\nv_mul_f32 v91,v242,v91\nv_mfma_f32_16x16x32_bf16 v[20:23],a[212:215],a[248:251],v[20:23]\nds_read_b64_tr_b16 a[210:211],v234 offset:9216\nv_mul_f32 v8,v243,v8\n"
"v_mul_f32 v9,v243,v9\nv_mul_f32 v10,v243,v10\nv_mul_f32 v11,v243,v11\nv_mfma_f32_16x16x32_bf16 v[48:51],a[216:219],a[248:251],v[48:51]\ns_add_u32 m0,s64,8192\nv_mul_f32 v36,v243,v36\nv_mul_f32 v37,v243,v37\nv_mul_f32 v38,v243,v38\n"
"v_mul_f32 v39,v243,v39\nv_mfma_f32_16x16x32_bf16 v[76:79],a[220:223],a[248:251],v[76:79]\ns_mov_b32 s73,229376\nv_mul_f32 v64,v243,v64\nv_mul_f32 v65,v243,v65\nv_mul_f32 v66,v243,v66\nv_mul_f32 v67,v243,v67\nv_mfma_f32_16x16x32_bf16 v[104:107],a[224:227],a[248:251],v[104:107]\n"
"ds_read_b128 a[248:251],v239 offset:10240\nv_mul_f32 v92,v243,v92\nv_mul_f32 v93,v243,v93\nv_mul_f32 v94,v243,v94\nv_mul_f32 v95,v243,v95\nv_mfma_f32_16x16x32_bf16 v[24:27],a[212:215],a[252:255],v[24:27]\nv_mul_f32 v12,v244,v12\nv_mul_f32 v13,v244,v13\n"
"v_mul_f32 v14,v244,v14\nv_mul_f32 v15,v244,v15\nv_mfma_f32_16x16x32_bf16 v[52:55],a[216:219],a[252:255],v[52:55]\nv_mul_f32 v40,v244,v40\nv_mul_f32 v41,v244,v41\nv_mul_f32 v42,v244,v42\nv_mul_f32 v43,v244,v43\nv_mfma_f32_16x16x32_bf16 v[80:83],a[220:223],a[252:255],v[80:83]\n"
"v_mul_f32 v68,v244,v68\nv_mul_f32 v69,v244,v69\nv_mul_f32 v70,v244,v70\nv_mul_f32 v71,v244,v71\nv_mfma_f32_16x16x32_bf16 v[108:111],a[224:227],a[252:255],v[108:111]\nds_read_b128 a[252:255],v239 offset:12288\ns_waitcnt lgkmcnt(2)\nv_mul_f32 v96,v244,v96\n"
"v_mul_f32 v97,v244,v97\nv_mul_f32 v98,v244,v98\nv_mul_f32 v99,v244,v99\nv_mfma_f32_16x16x32_bf16 v[0:3],a[196:199],a[228:231],v[0:3]\nbuffer_load_dwordx4 v225,s[52:55],s73 offen offset:0 nt lds\nv_mul_f32 v16,v245,v16\nv_mul_f32 v17,v245,v17\nv_mul_f32 v18,v245,v18\n"
"v_mul_f32 v19,v245,v19\nv_mfma_f32_16x16x32_bf16 v[28:31],a[200:203],a[228:231],v[28:31]\ns_waitcnt vmcnt(17)\nds_read_b64_tr_b16 a[212:213],v231 offset:12288\nv_mul_f32 v44,v245,v44\nv_mul_f32 v45,v245,v45\nv_mul_f32 v46,v245,v46\nv_mul_f32 v47,v245,v47\n"
"v_mfma_f32_16x16x32_bf16 v[56:59],a[204:207],a[228:231],v[56:59]\nds_read_b64_tr_b16 a[214:215],v231 offset:13312\nv_mul_f32 v72,v245,v72\nv_mul_f32 v73,v245,v73\nv_mul_f32 v74,v245,v74\nv_mul_f32 v75,v245,v75\nv_mfma_f32_16x16x32_bf16 v[84:87],a[208:211],a[228:231],v[84:87]\nds_read_b128 a[228:231],v239 offset:1024\n"
"v_mul_f32 v100,v245,v100\nv_mul_f32 v101,v245,v101\nv_mul_f32 v102,v245,v102\nv_mul_f32 v103,v245,v103\nv_mfma_f32_16x16x32_bf16 v[4:7],a[196:199],a[232:235],v[4:7]\ns_mov_b32 s74,230400\nbuffer_load_dwordx4 v225,s[52:55],s74 offen offset:1024 nt lds\nv_mul_f32 v20,v246,v20\n"
"v_mul_f32 v21,v246,v21\nv_mul_f32 v22,v246,v22\nv_mul_f32 v23,v246,v23\nv_mfma_f32_16x16x32_bf16 v[32:35],a[200:203],a[232:235],v[32:35]\nds_read_b64_tr_b16 a[216:217],v232 offset:12288\nv_mul_f32 v48,v246,v48\nv_mul_f32 v49,v246,v49\nv_mul_f32 v50,v246,v50\n"
"v_mul_f32 v51,v246,v51\nv_mfma_f32_16x16x32_bf16 v[60:63],a[204:207],a[232:235],v[60:63]\ns_mov_b32 s75,235520\nv_mul_f32 v76,v246,v76\nv_mul_f32 v77,v246,v77\nv_mul_f32 v78,v246,v78\nv_mul_f32 v79,v246,v79\nv_mfma_f32_16x16x32_bf16 v[88:91],a[208:211],a[232:235],v[88:91]\n"
"ds_read_b128 a[232:235],v239 offset:3072\nv_mul_f32 v104,v246,v104\nv_mul_f32 v105,v246,v105\nv_mul_f32 v106,v246,v106\nv_mul_f32 v107,v246,v107\nv_mfma_f32_16x16x32_bf16 v[8:11],a[196:199],a[236:239],v[8:11]\nbuffer_load_dwordx4 v225,s[52:55],s75 offen offset:2048 nt lds\nv_mul_f32 v24,v247,v24\n"
"v_mul_f32 v25,v247,v25\nv_mul_f32 v26,v247,v26\nv_mul_f32 v27,v247,v27\nv_mfma_f32_16x16x32_bf16 v[36:39],a[200:203],a[236:239],v[36:39]\nds_read_b64_tr_b16 a[218:219],v232 offset:13312\nv_mul_f32 v52,v247,v52\nv_mul_f32 v53,v247,v53\nv_mul_f32 v54,v247,v54\n"
"v_mul_f32 v55,v247,v55\nv_mfma_f32_16x16x32_bf16 v[64:67],a[204:207],a[236:239],v[64:67]\ns_mov_b32 s72,236544\nv_mul_f32 v80,v247,v80\nv_mul_f32 v81,v247,v81\nv_mul_f32 v82,v247,v82\nv_mul_f32 v83,v247,v83\nv_mfma_f32_16x16x32_bf16 v[92:95],a[208:211],a[236:239],v[92:95]\n"
"ds_read_b128 a[236:239],v239 offset:5120\nv_mul_f32 v108,v247,v108\nv_mul_f32 v109,v247,v109\nv_mul_f32 v110,v247,v110\nv_mul_f32 v111,v247,v111\nv_mfma_f32_16x16x32_bf16 v[12:15],a[196:199],a[240:243],v[12:15]\nbuffer_load_dwordx4 v225,s[52:55],s72 offen offset:3072 nt lds\nv_fma_f32 v182,v182,s69,-v251\n"
"v_mfma_f32_16x16x32_bf16 v[40:43],a[200:203],a[240:243],v[40:43]\nds_read_b64_tr_b16 a[220:221],v233 offset:12288\nv_fma_f32 v183,v183,s69,-v251\nv_fma_f32 v184,v184,s69,-v252\nv_mfma_f32_16x16x32_bf16 v[68:71],a[204:207],a[240:243],v[68:71]\nds_read_b64_tr_b16 a[222:223],v233 offset:13312\nv_fma_f32 v185,v185,s69,-v252\nv_mfma_f32_16x16x32_bf16 v[96:99],a[208:211],a[240:243],v[96:99]\n"
"ds_read_b128 a[240:243],v239 offset:7168\nv_exp_f32 v180,v180\nv_mfma_f32_16x16x32_bf16 v[16:19],a[196:199],a[244:247],v[16:19]\nds_read_b64_tr_b16 a[224:225],v234 offset:12288\nv_fma_f32 v186,v186,s69,-v252\nv_mfma_f32_16x16x32_bf16 v[44:47],a[200:203],a[244:247],v[44:47]\nds_write_b64 v240,v[156:157] offset:22528\nv_mfma_f32_16x16x32_bf16 v[72:75],a[204:207],a[244:247],v[72:75]\n"
"v_fma_f32 v187,v187,s69,-v252\nv_fma_f32 v188,v188,s69,-v253\nv_mfma_f32_16x16x32_bf16 v[100:103],a[208:211],a[244:247],v[100:103]\nds_read_b128 a[244:247],v239 offset:9216\ns_waitcnt lgkmcnt(13)\nv_fma_f32 v189,v189,s69,-v253\nv_mfma_f32_16x16x32_bf16 v[20:23],a[196:199],a[248:251],v[20:23]\nds_read_b64_tr_b16 a[226:227],v234 offset:13312\n"
"v_fma_f32 v190,v190,s69,-v253\nv_mfma_f32_16x16x32_bf16 v[48:51],a[200:203],a[248:251],v[48:51]\ns_add_u32 m0,s64,12288\nv_exp_f32 v184,v184\nv_fma_f32 v191,v191,s69,-v253\nv_mfma_f32_16x16x32_bf16 v[76:79],a[204:207],a[248:251],v[76:79]\ns_mov_b32 s73,245760\nds_write_b64 v240,v[160:161] offset:24576\n"
"v_mfma_f32_16x16x32_bf16 v[104:107],a[208:211],a[248:251],v[104:107]\nds_read_b128 a[248:251],v239 offset:11264\nv_exp_f32 v188,v188\nv_mfma_f32_16x16x32_bf16 v[24:27],a[196:199],a[252:255],v[24:27]\nds_write_b64 v240,v[164:165] offset:26624\nv_mfma_f32_16x16x32_bf16 v[52:55],a[200:203],a[252:255],v[52:55]\nv_fma_f32 v192,v192,s69,-v254\nv_fma_f32 v193,v193,s69,-v254\n"
"v_fma_f32 v194,v194,s69,-v254\nv_mfma_f32_16x16x32_bf16 v[80:83],a[204:207],a[252:255],v[80:83]\nv_fma_f32 v195,v195,s69,-v254\nv_mfma_f32_16x16x32_bf16 v[108:111],a[208:211],a[252:255],v[108:111]\nds_read_b128 a[252:255],v239 offset:13312\ns_waitcnt lgkmcnt(4)\nv_mfma_f32_16x16x32_bf16 v[0:3],a[212:215],a[228:231],v[0:3]\nbuffer_load_dwordx4 v225,s[52:55],s73 offen offset:0 nt lds\n"
"s_mov_b32 s74,246784\nv_mfma_f32_16x16x32_bf16 v[28:31],a[216:219],a[228:231],v[28:31]\ns_waitcnt vmcnt(17)\nds_read_b64_tr_b16 a[196:197],v231 offset:16384\nv_mfma_f32_16x16x32_bf16 v[56:59],a[220:223],a[228:231],v[56:59]\nds_read_b64_tr_b16 a[198:199],v231 offset:17408\nv_mfma_f32_16x16x32_bf16 v[84:87],a[224:227],a[228:231],v[84:87]\nv_exp_f32 v192,v192\n"
"s_waitcnt lgkmcnt(0)\ns_barrier\nds_read_b128 a[228:231],v239 offset:14336\nv_mfma_f32_16x16x32_bf16 v[4:7],a[212:215],a[232:235],v[4:7]\nbuffer_load_dwordx4 v225,s[52:55],s74 offen offset:1024 nt lds\nv_add_f32 v114,v168,v114\nv_mfma_f32_16x16x32_bf16 v[32:35],a[216:219],a[232:235],v[32:35]\nds_read_b64_tr_b16 a[200:201],v232 offset:16384\n"
"v_exp_f32 v169,v169\nv_mfma_f32_16x16x32_bf16 v[60:63],a[220:223],a[232:235],v[60:63]\ns_mov_b32 s75,251904\nv_exp_f32 v170,v170\nv_mfma_f32_16x16x32_bf16 v[88:91],a[224:227],a[232:235],v[88:91]\nds_read_b128 a[232:235],v239 offset:16384\nv_exp_f32 v171,v171\nv_mfma_f32_16x16x32_bf16 v[8:11],a[212:215],a[236:239],v[8:11]\n"
"buffer_load_dwordx4 v225,s[52:55],s75 offen offset:2048 nt lds\nv_add_f32 v114,v169,v114\nv_mfma_f32_16x16x32_bf16 v[36:39],a[216:219],a[236:239],v[36:39]\nds_read_b64_tr_b16 a[202:203],v232 offset:17408\nv_cvt_pk_bf16_f32 v168,v168,v169\nv_add_f32 v114,v170,v114\nv_mfma_f32_16x16x32_bf16 v[64:67],a[220:223],a[236:239],v[64:67]\ns_mov_b32 s72,252928\n"
"v_cvt_pk_bf16_f32 v169,v170,v171\nv_add_f32 v114,v171,v114\nv_mfma_f32_16x16x32_bf16 v[92:95],a[224:227],a[236:239],v[92:95]\nds_read_b128 a[236:239],v239 offset:18432\nv_exp_f32 v173,v173\nv_mfma_f32_16x16x32_bf16 v[12:15],a[212:215],a[240:243],v[12:15]\nbuffer_load_dwordx4 v225,s[52:55],s72 offen offset:3072 nt lds\nv_add_f32 v118,v172,v118\n"
"v_mfma_f32_16x16x32_bf16 v[40:43],a[216:219],a[240:243],v[40:43]\nds_read_b64_tr_b16 a[204:205],v233 offset:16384\nv_exp_f32 v174,v174\nv_mfma_f32_16x16x32_bf16 v[68:71],a[220:223],a[240:243],v[68:71]\nds_read_b64_tr_b16 a[206:207],v233 offset:17408\nv_add_f32 v118,v173,v118\nv_mfma_f32_16x16x32_bf16 v[96:99],a[224:227],a[240:243],v[96:99]\nds_read_b128 a[240:243],v239 offset:20480\n"
"v_exp_f32 v175,v175\nv_mfma_f32_16x16x32_bf16 v[16:19],a[212:215],a[244:247],v[16:19]\nds_read_b64_tr_b16 a[208:209],v234 offset:16384\nv_cvt_pk_bf16_f32 v172,v172,v173\nv_mfma_f32_16x16x32_bf16 v[44:47],a[216:219],a[244:247],v[44:47]\nv_add_f32 v118,v174,v118\nv_cvt_pk_bf16_f32 v173,v174,v175\nv_add_f32 v118,v175,v118\n"
"v_mfma_f32_16x16x32_bf16 v[72:75],a[220:223],a[244:247],v[72:75]\nv_exp_f32 v177,v177\nv_mfma_f32_16x16x32_bf16 v[100:103],a[224:227],a[244:247],v[100:103]\nds_read_b128 a[244:247],v239 offset:22528\nv_exp_f32 v178,v178\nv_mfma_f32_16x16x32_bf16 v[20:23],a[212:215],a[248:251],v[20:23]\nds_read_b64_tr_b16 a[210:211],v234 offset:17408\nv_add_f32 v122,v176,v122\n"
"v_mfma_f32_16x16x32_bf16 v[48:51],a[216:219],a[248:251],v[48:51]\ns_add_u32 m0,s64,16384\nv_exp_f32 v179,v179\nv_add_f32 v122,v177,v122\nv_mfma_f32_16x16x32_bf16 v[76:79],a[220:223],a[248:251],v[76:79]\ns_mov_b32 s73,0\nv_cvt_pk_bf16_f32 v176,v176,v177\nv_add_f32 v122,v178,v122\n"
"v_mfma_f32_16x16x32_bf16 v[104:107],a[224:227],a[248:251],v[104:107]\nds_read_b128 a[248:251],v239 offset:24576\nv_cvt_pk_bf16_f32 v177,v178,v179\nv_add_f32 v122,v179,v122\nv_mfma_f32_16x16x32_bf16 v[24:27],a[212:215],a[252:255],v[24:27]\nv_exp_f32 v181,v181\nv_mfma_f32_16x16x32_bf16 v[52:55],a[216:219],a[252:255],v[52:55]\nv_exp_f32 v182,v182\n"
"v_add_f32 v126,v180,v126\nv_mfma_f32_16x16x32_bf16 v[80:83],a[220:223],a[252:255],v[80:83]\nv_exp_f32 v183,v183\nv_mfma_f32_16x16x32_bf16 v[108:111],a[224:227],a[252:255],v[108:111]\nds_read_b128 a[252:255],v239 offset:26624\ns_waitcnt lgkmcnt(2)\nv_add_f32 v126,v181,v126\nv_mfma_f32_16x16x32_bf16 v[0:3],a[196:199],a[228:231],v[0:3]\n"
"buffer_load_dwordx4 v226,s[56:59],s73 offen offset:0 lds\nv_cvt_pk_bf16_f32 v180,v180,v181\nv_mfma_f32_16x16x32_bf16 v[28:31],a[200:203],a[228:231],v[28:31]\ns_waitcnt vmcnt(17)\nds_read_b64_tr_b16 a[212:213],v231 offset:20480\nv_add_f32 v126,v182,v126\nv_mfma_f32_16x16x32_bf16 v[56:59],a[204:207],a[228:231],v[56:59]\nds_read_b64_tr_b16 a[214:215],v231 offset:21504\n"
"v_cvt_pk_bf16_f32 v181,v182,v183\nv_mfma_f32_16x16x32_bf16 v[84:87],a[208:211],a[228:231],v[84:87]\nds_read_b128 a[228:231],v239 offset:15360\nv_add_f32 v126,v183,v126\nv_add_f32 v130,v184,v130\nv_mfma_f32_16x16x32_bf16 v[4:7],a[196:199],a[232:235],v[4:7]\ns_mov_b32 s74,13312\nbuffer_load_dwordx4 v226,s[56:59],s74 offen offset:1024 lds\n"
"v_mfma_f32_16x16x32_bf16 v[32:35],a[200:203],a[232:235],v[32:35]\nds_read_b64_tr_b16 a[216:217],v232 offset:20480\nv_exp_f32 v185,v185\nv_mfma_f32_16x16x32_bf16 v[60:63],a[204:207],a[232:235],v[60:63]\ns_mov_b32 s75,5120\nv_exp_f32 v186,v186\nv_mfma_f32_16x16x32_bf16 v[88:91],a[208:211],a[232:235],v[88:91]\nds_read_b128 a[232:235],v239 offset:17408\n"
"v_exp_f32 v187,v187\nv_mfma_f32_16x16x32_bf16 v[8:11],a[196:199],a[236:239],v[8:11]\nbuffer_load_dwordx4 v226,s[56:59],s75 offen offset:2048 lds\nv_add_f32 v130,v185,v130\nv_mfma_f32_16x16x32_bf16 v[36:39],a[200:203],a[236:239],v[36:39]\nds_read_b64_tr_b16 a[218:219],v232 offset:21504\nv_cvt_pk_bf16_f32 v184,v184,v185\nv_add_f32 v130,v186,v130\n"
"v_mfma_f32_16x16x32_bf16 v[64:67],a[204:207],a[236:239],v[64:67]\ns_mov_b32 s72,18432\nv_cvt_pk_bf16_f32 v185,v186,v187\nv_add_f32 v130,v187,v130\nv_mfma_f32_16x16x32_bf16 v[92:95],a[208:211],a[236:239],v[92:95]\nds_read_b128 a[236:239],v239 offset:19456\nv_exp_f32 v189,v189\nv_mfma_f32_16x16x32_bf16 v[12:15],a[196:199],a[240:243],v[12:15]\n"
"buffer_load_dwordx4 v226,s[56:59],s72 offen offset:3072 lds\nv_add_f32 v134,v188,v134\nv_mfma_f32_16x16x32_bf16 v[40:43],a[200:203],a[240:243],v[40:43]\nds_read_b64_tr_b16 a[220:221],v233 offset:20480\nv_exp_f32 v190,v190\nv_mfma_f32_16x16x32_bf16 v[68:71],a[204:207],a[240:243],v[68:71]\nds_read_b64_tr_b16 a[222:223],v233 offset:21504\nv_add_f32 v134,v189,v134\n"
"v_mfma_f32_16x16x32_bf16 v[96:99],a[208:211],a[240:243],v[96:99]\nds_read_b128 a[240:243],v239 offset:21504\nv_exp_f32 v191,v191\nv_mfma_f32_16x16x32_bf16 v[16:19],a[196:199],a[244:247],v[16:19]\nds_read_b64_tr_b16 a[224:225],v234 offset:20480\nv_cvt_pk_bf16_f32 v188,v188,v189\nv_mfma_f32_16x16x32_bf16 v[44:47],a[200:203],a[244:247],v[44:47]\nv_add_f32 v134,v190,v134\n"
"v_cvt_pk_bf16_f32 v189,v190,v191\nv_add_f32 v134,v191,v134\nv_mfma_f32_16x16x32_bf16 v[72:75],a[204:207],a[244:247],v[72:75]\nv_exp_f32 v193,v193\nv_mfma_f32_16x16x32_bf16 v[100:103],a[208:211],a[244:247],v[100:103]\nds_read_b128 a[244:247],v239 offset:23552\ns_waitcnt lgkmcnt(12)\nv_add_f32 v138,v192,v138\n"
"v_mfma_f32_16x16x32_bf16 v[20:23],a[196:199],a[248:251],v[20:23]\nds_read_b64_tr_b16 a[226:227],v234 offset:21504\nv_add_f32 v138,v193,v138\nv_mfma_f32_16x16x32_bf16 v[48:51],a[200:203],a[248:251],v[48:51]\ns_add_u32 m0,s64,20480\nv_exp_f32 v194,v194\nv_cvt_pk_bf16_f32 v192,v192,v193\nv_mfma_f32_16x16x32_bf16 v[76:79],a[204:207],a[248:251],v[76:79]\n"
"s_mov_b32 s73,28672\nv_exp_f32 v195,v195\nv_mfma_f32_16x16x32_bf16 v[104:107],a[208:211],a[248:251],v[104:107]\nds_read_b128 a[248:251],v239 offset:25600\nv_add_f32 v138,v194,v138\nv_cvt_pk_bf16_f32 v193,v194,v195\nv_mfma_f32_16x16x32_bf16 v[24:27],a[196:199],a[252:255],v[24:27]\nv_add_f32 v138,v195,v138\n"
"v_fma_f32 v196,v196,s69,-v248\nv_mfma_f32_16x16x32_bf16 v[52:55],a[200:203],a[252:255],v[52:55]\nds_write_b64 v240,v[168:169] offset:28672\nv_mfma_f32_16x16x32_bf16 v[80:83],a[204:207],a[252:255],v[80:83]\nv_fma_f32 v197,v197,s69,-v248\nv_fma_f32 v198,v198,s69,-v248\nv_mfma_f32_16x16x32_bf16 v[108:111],a[208:211],a[252:255],v[108:111]\nds_read_b128 a[252:255],v239 offset:27648\n"
"s_waitcnt lgkmcnt(3)\nv_fma_f32 v199,v199,s69,-v248\nv_mfma_f32_16x16x32_bf16 v[0:3],a[212:215],a[228:231],v[0:3]\nbuffer_load_dwordx4 v226,s[56:59],s73 offen offset:0 lds\nv_fma_f32 v200,v200,s69,-v249\nv_mfma_f32_16x16x32_bf16 v[28:31],a[216:219],a[228:231],v[28:31]\ns_waitcnt vmcnt(17)\nds_read_b64_tr_b16 a[196:197],v231 offset:0\n"
"v_fma_f32 v201,v201,s69,-v249\nv_mfma_f32_16x16x32_bf16 v[56:59],a[220:223],a[228:231],v[56:59]\nds_read_b64_tr_b16 a[198:199],v231 offset:1024\nv_fma_f32 v202,v202,s69,-v249\nv_mfma_f32_16x16x32_bf16 v[84:87],a[224:227],a[228:231],v[84:87]\nv_exp_f32 v196,v196\nds_write_b64 v240,v[172:173] offset:30720\nv_fma_f32 v203,v203,s69,-v249\n"
"v_exp_f32 v200,v200\nds_write_b64 v240,v[176:177] offset:32768\nv_fma_f32 v204,v204,s69,-v250\nv_fma_f32 v205,v205,s69,-v250\nv_fma_f32 v206,v206,s69,-v250\nv_fma_f32 v207,v207,s69,-v250\nv_exp_f32 v204,v204\nds_write_b64 v240,v[180:181] offset:34816\n"
"v_fma_f32 v208,v208,s69,-v251\nv_fma_f32 v209,v209,s69,-v251\nv_fma_f32 v210,v210,s69,-v251\nv_fma_f32 v211,v211,s69,-v251\nv_exp_f32 v208,v208\nds_write_b64 v240,v[184:185] offset:36864\nv_fma_f32 v212,v212,s69,-v252\nv_fma_f32 v213,v213,s69,-v252\n"
"v_fma_f32 v214,v214,s69,-v252\nv_fma_f32 v215,v215,s69,-v252\nv_exp_f32 v212,v212\nds_write_b64 v240,v[188:189] offset:38912\nv_fma_f32 v216,v216,s69,-v253\nv_fma_f32 v217,v217,s69,-v253\nv_fma_f32 v218,v218,s69,-v253\nv_fma_f32 v219,v219,s69,-v253\n"
"v_exp_f32 v216,v216\nds_write_b64 v240,v[192:193] offset:40960\nv_fma_f32 v220,v220,s69,-v254\nv_fma_f32 v221,v221,s69,-v254\nv_fma_f32 v222,v222,s69,-v254\nv_fma_f32 v223,v223,s69,-v254\nv_exp_f32 v220,v220\ns_waitcnt lgkmcnt(0)\n"
"s_barrier\nds_read_b128 a[228:231],v239 offset:28672\nv_mfma_f32_16x16x32_bf16 v[4:7],a[212:215],a[232:235],v[4:7]\ns_mov_b32 s74,41984\nbuffer_load_dwordx4 v226,s[56:59],s74 offen offset:1024 lds\nv_mfma_f32_16x16x32_bf16 v[32:35],a[216:219],a[232:235],v[32:35]\nds_read_b64_tr_b16 a[200:201],v232 offset:0\nv_exp_f32 v197,v197\n"
"v_mfma_f32_16x16x32_bf16 v[60:63],a[220:223],a[232:235],v[60:63]\ns_mov_b32 s75,33792\nv_exp_f32 v198,v198\nv_mfma_f32_16x16x32_bf16 v[88:91],a[224:227],a[232:235],v[88:91]\nds_read_b128 a[232:235],v239 offset:30720\nv_exp_f32 v199,v199\nv_mfma_f32_16x16x32_bf16 v[8:11],a[212:215],a[236:239],v[8:11]\nbuffer_load_dwordx4 v226,s[56:59],s75 offen offset:2048 lds\n"
"v_add_f32 v114,v196,v114\nv_mfma_f32_16x16x32_bf16 v[36:39],a[216:219],a[236:239],v[36:39]\nds_read_b64_tr_b16 a[202:203],v232 offset:1024\nv_add_f32 v114,v197,v114\nv_cvt_pk_bf16_f32 v196,v196,v197\nv_mfma_f32_16x16x32_bf16 v[64:67],a[220:223],a[236:239],v[64:67]\ns_mov_b32 s72,47104\nv_add_f32 v114,v198,v114\n"
"v_cvt_pk_bf16_f32 v197,v198,v199\nv_mfma_f32_16x16x32_bf16 v[92:95],a[224:227],a[236:239],v[92:95]\nds_read_b128 a[236:239],v239 offset:32768\nv_add_f32 v114,v199,v114\nv_add_f32 v118,v200,v118\nv_mfma_f32_16x16x32_bf16 v[12:15],a[212:215],a[240:243],v[12:15]\nbuffer_load_dwordx4 v226,s[56:59],s72 offen offset:3072 lds\nv_add_f32 v122,v204,v122\n"
"v_mfma_f32_16x16x32_bf16 v[40:43],a[216:219],a[240:243],v[40:43]\nds_read_b64_tr_b16 a[204:205],v233 offset:0\nv_exp_f32 v201,v201\nv_mfma_f32_16x16x32_bf16 v[68:71],a[220:223],a[240:243],v[68:71]\nds_read_b64_tr_b16 a[206:207],v233 offset:1024\nv_add_f32 v118,v201,v118\nv_mfma_f32_16x16x32_bf16 v[96:99],a[224:227],a[240:243],v[96:99]\nds_read_b128 a[240:243],v239 offset:34816\n"
"v_exp_f32 v202,v202\nv_mfma_f32_16x16x32_bf16 v[16:19],a[212:215],a[244:247],v[16:19]\nds_read_b64_tr_b16 a[208:209],v234 offset:0\nv_cvt_pk_bf16_f32 v200,v200,v201\nv_mfma_f32_16x16x32_bf16 v[44:47],a[216:219],a[244:247],v[44:47]\nv_exp_f32 v203,v203\nv_add_f32 v118,v202,v118\nv_mfma_f32_16x16x32_bf16 v[72:75],a[220:223],a[244:247],v[72:75]\n"
"v_cvt_pk_bf16_f32 v201,v202,v203\nv_add_f32 v118,v203,v118\nv_mfma_f32_16x16x32_bf16 v[100:103],a[224:227],a[244:247],v[100:103]\nds_read_b128 a[244:247],v239 offset:36864\nv_exp_f32 v205,v205\nv_mfma_f32_16x16x32_bf16 v[20:23],a[212:215],a[248:251],v[20:23]\nds_read_b64_tr_b16 a[210:211],v234 offset:1024\nv_add_f32 v122,v205,v122\n"
"v_mfma_f32_16x16x32_bf16 v[48:51],a[216:219],a[248:251],v[48:51]\ns_add_u32 m0,s64,0\nv_exp_f32 v206,v206\nv_cvt_pk_bf16_f32 v204,v204,v205\nv_mfma_f32_16x16x32_bf16 v[76:79],a[220:223],a[248:251],v[76:79]\ns_mov_b32 s73,57344\nv_exp_f32 v207,v207\nv_mfma_f32_16x16x32_bf16 v[104:107],a[224:227],a[248:251],v[104:107]\n"
"ds_read_b128 a[248:251],v239 offset:38912\nv_add_f32 v122,v206,v122\nv_cvt_pk_bf16_f32 v205,v206,v207\nv_mfma_f32_16x16x32_bf16 v[24:27],a[212:215],a[252:255],v[24:27]\nv_add_f32 v122,v207,v122\nv_add_f32 v126,v208,v126\nv_mfma_f32_16x16x32_bf16 v[52:55],a[216:219],a[252:255],v[52:55]\nv_exp_f32 v209,v209\n"
"v_add_f32 v130,v212,v130\nv_mfma_f32_16x16x32_bf16 v[80:83],a[220:223],a[252:255],v[80:83]\nv_exp_f32 v210,v210\nv_mfma_f32_16x16x32_bf16 v[108:111],a[224:227],a[252:255],v[108:111]\nds_read_b128 a[252:255],v239 offset:40960\ns_waitcnt lgkmcnt(2)\nv_add_f32 v126,v209,v126\nv_mfma_f32_16x16x32_bf16 v[0:3],a[196:199],a[228:231],v[0:3]\n"
"buffer_load_dwordx4 v226,s[56:59],s73 offen offset:0 lds\nv_cvt_pk_bf16_f32 v208,v208,v209\nv_mfma_f32_16x16x32_bf16 v[28:31],a[200:203],a[228:231],v[28:31]\ns_waitcnt vmcnt(17)\nds_read_b64_tr_b16 a[212:213],v231 offset:4096\nv_add_f32 v126,v210,v126\nv_mfma_f32_16x16x32_bf16 v[56:59],a[204:207],a[228:231],v[56:59]\nds_read_b64_tr_b16 a[214:215],v231 offset:5120\n"
"v_add_f32 v134,v216,v134\nv_mfma_f32_16x16x32_bf16 v[84:87],a[208:211],a[228:231],v[84:87]\nds_read_b128 a[228:231],v239 offset:29696\nv_exp_f32 v211,v211\nv_mfma_f32_16x16x32_bf16 v[4:7],a[196:199],a[232:235],v[4:7]\ns_mov_b32 s74,70656\nbuffer_load_dwordx4 v226,s[56:59],s74 offen offset:1024 lds\nv_mfma_f32_16x16x32_bf16 v[32:35],a[200:203],a[232:235],v[32:35]\n"
"ds_read_b64_tr_b16 a[216:217],v232 offset:4096\nv_cvt_pk_bf16_f32 v209,v210,v211\nv_add_f32 v126,v211,v126\nv_mfma_f32_16x16x32_bf16 v[60:63],a[204:207],a[232:235],v[60:63]\ns_mov_b32 s75,62464\nv_exp_f32 v213,v213\nv_mfma_f32_16x16x32_bf16 v[88:91],a[208:211],a[232:235],v[88:91]\nds_read_b128 a[232:235],v239 offset:31744\n"
"v_exp_f32 v214,v214\nv_mfma_f32_16x16x32_bf16 v[8:11],a[196:199],a[236:239],v[8:11]\nbuffer_load_dwordx4 v226,s[56:59],s75 offen offset:2048 lds\nv_add_f32 v130,v213,v130\nv_mfma_f32_16x16x32_bf16 v[36:39],a[200:203],a[236:239],v[36:39]\nds_read_b64_tr_b16 a[218:219],v232 offset:5120\nv_exp_f32 v215,v215\nv_mfma_f32_16x16x32_bf16 v[64:67],a[204:207],a[236:239],v[64:67]\n"
"s_mov_b32 s72,75776\nv_cvt_pk_bf16_f32 v212,v212,v213\nv_add_f32 v130,v214,v130\nv_mfma_f32_16x16x32_bf16 v[92:95],a[208:211],a[236:239],v[92:95]\nds_read_b128 a[236:239],v239 offset:33792\nv_cvt_pk_bf16_f32 v213,v214,v215\nv_add_f32 v130,v215,v130\nv_mfma_f32_16x16x32_bf16 v[12:15],a[196:199],a[240:243],v[12:15]\n"
"buffer_load_dwordx4 v226,s[56:59],s72 offen offset:3072 lds\nv_add_f32 v138,v220,v138\nv_mfma_f32_16x16x32_bf16 v[40:43],a[200:203],a[240:243],v[40:43]\nds_read_b64_tr_b16 a[220:221],v233 offset:4096\nv_exp_f32 v217,v217\nv_mfma_f32_16x16x32_bf16 v[68:71],a[204:207],a[240:243],v[68:71]\nds_read_b64_tr_b16 a[222:223],v233 offset:5120\nv_add_f32 v134,v217,v134\n"
"v_mfma_f32_16x16x32_bf16 v[96:99],a[208:211],a[240:243],v[96:99]\nds_read_b128 a[240:243],v239 offset:35840\nv_exp_f32 v218,v218\nv_mfma_f32_16x16x32_bf16 v[16:19],a[196:199],a[244:247],v[16:19]\nds_read_b64_tr_b16 a[224:225],v234 offset:4096\nv_cvt_pk_bf16_f32 v216,v216,v217\nv_mfma_f32_16x16x32_bf16 v[44:47],a[200:203],a[244:247],v[44:47]\nv_exp_f32 v219,v219\n"
"v_add_f32 v134,v218,v134\nv_mfma_f32_16x16x32_bf16 v[72:75],a[204:207],a[244:247],v[72:75]\nv_cvt_pk_bf16_f32 v217,v218,v219\nv_add_f32 v134,v219,v134\nv_mfma_f32_16x16x32_bf16 v[100:103],a[208:211],a[244:247],v[100:103]\nds_read_b128 a[244:247],v239 offset:37888\ns_waitcnt lgkmcnt(12)\nv_mov_b32 v115,0\n"
"v_mfma_f32_16x16x32_bf16 v[20:23],a[196:199],a[248:251],v[20:23]\nds_read_b64_tr_b16 a[226:227],v234 offset:5120\nv_permlane32_swap_b32 v114,v118\nv_mfma_f32_16x16x32_bf16 v[48:51],a[200:203],a[248:251],v[48:51]\ns_add_u32 m0,s64,4096\nv_exp_f32 v221,v221\nv_add_f32 v114,v114,v118\nv_mfma_f32_16x16x32_bf16 v[76:79],a[204:207],a[248:251],v[76:79]\n"
"s_mov_b32 s73,86016\nv_exp_f32 v222,v222\nv_mfma_f32_16x16x32_bf16 v[104:107],a[208:211],a[248:251],v[104:107]\nds_read_b128 a[248:251],v239 offset:39936\nv_exp_f32 v223,v223\nv_mfma_f32_16x16x32_bf16 v[24:27],a[196:199],a[252:255],v[24:27]\nv_add_f32 v138,v221,v138\nv_cvt_pk_bf16_f32 v220,v220,v221\n"
"v_mfma_f32_16x16x32_bf16 v[52:55],a[200:203],a[252:255],v[52:55]\nv_add_f32 v138,v222,v138\nv_cvt_pk_bf16_f32 v221,v222,v223\nv_add_f32 v138,v223,v138\nv_mfma_f32_16x16x32_bf16 v[80:83],a[204:207],a[252:255],v[80:83]\nds_write_b64 v240,v[196:197] offset:43008\nv_mfma_f32_16x16x32_bf16 v[108:111],a[208:211],a[252:255],v[108:111]\nds_read_b128 a[252:255],v239 offset:41984\n"
"s_waitcnt lgkmcnt(3)\nv_permlane32_swap_b32 v122,v126\nv_mfma_f32_16x16x32_bf16 v[0:3],a[212:215],a[228:231],v[0:3]\nbuffer_load_dwordx4 v226,s[56:59],s73 offen offset:0 lds\nv_add_f32 v122,v122,v126\nv_mfma_f32_16x16x32_bf16 v[28:31],a[216:219],a[228:231],v[28:31]\ns_waitcnt vmcnt(17)\nds_read_b64_tr_b16 a[196:197],v231 offset:8192\n"
"v_permlane32_swap_b32 v130,v134\nv_mfma_f32_16x16x32_bf16 v[56:59],a[220:223],a[228:231],v[56:59]\nds_read_b64_tr_b16 a[198:199],v231 offset:9216\nv_add_f32 v130,v130,v134\nv_mfma_f32_16x16x32_bf16 v[84:87],a[224:227],a[228:231],v[84:87]\nds_write_b64 v240,v[200:201] offset:45056\nds_write_b64 v240,v[204:205] offset:47104\nds_write_b64 v240,v[208:209] offset:49152\n"
"ds_write_b64 v240,v[212:213] offset:51200\nds_write_b64 v240,v[216:217] offset:53248\nds_write_b64 v240,v[220:221] offset:55296\nv_permlane32_swap_b32 v138,v115\nv_add_f32 v138,v138,v115\nv_bfe_u32 v118,v239,8,1\nv_lshrrev_b32 v126,9,v239\nv_permlane16_swap_b32 v114,v122\n"
"v_add_f32 v114,v114,v122\nv_permlane16_swap_b32 v130,v138\nv_add_f32 v130,v130,v138\nv_lshl_or_b32 v118,v118,1,v126\nv_and_b32 v126,0xf0,v239\nv_lshl_or_b32 v118,v118,8,v126\nv_lshl_add_u32 v118,s70,2,v118\nds_write_b32 v118,v114 offset:61440\n"
"ds_write_b32 v118,v130 offset:62464\ns_waitcnt lgkmcnt(0)\ns_barrier\nds_read_b128 a[228:231],v239 offset:43008\nv_mfma_f32_16x16x32_bf16 v[4:7],a[212:215],a[232:235],v[4:7]\ns_mov_b32 s74,99328\nbuffer_load_dwordx4 v226,s[56:59],s74 offen offset:1024 lds\nv_mfma_f32_16x16x32_bf16 v[32:35],a[216:219],a[232:235],v[32:35]\n"
"ds_read_b64_tr_b16 a[200:201],v232 offset:8192\nv_and_b32 v255,0xf0,v239\nv_mfma_f32_16x16x32_bf16 v[60:63],a[220:223],a[232:235],v[60:63]\ns_mov_b32 s75,91136\nds_read_b128 v[140:143],v255 offset:59392\nv_mfma_f32_16x16x32_bf16 v[88:91],a[224:227],a[232:235],v[88:91]\nds_read_b128 a[232:235],v239 offset:45056\nds_read_b128 v[144:147],v255 offset:59648\n"
"v_mfma_f32_16x16x32_bf16 v[8:11],a[212:215],a[236:239],v[8:11]\nbuffer_load_dwordx4 v226,s[56:59],s75 offen offset:2048 lds\ns_mov_b32 s72,104448\nv_mfma_f32_16x16x32_bf16 v[36:39],a[216:219],a[236:239],v[36:39]\nds_read_b64_tr_b16 a[202:203],v232 offset:9216\nds_read_b128 v[148:151],v255 offset:59904\nv_mfma_f32_16x16x32_bf16 v[64:67],a[220:223],a[236:239],v[64:67]\nds_read_b128 v[152:155],v255 offset:60160\n"
"v_mfma_f32_16x16x32_bf16 v[92:95],a[224:227],a[236:239],v[92:95]\nds_read_b128 a[236:239],v239 offset:47104\nds_read_b128 v[156:159],v255 offset:60416\nv_mfma_f32_16x16x32_bf16 v[12:15],a[212:215],a[240:243],v[12:15]\nbuffer_load_dwordx4 v226,s[56:59],s72 offen offset:3072 lds\nv_mfma_f32_16x16x32_bf16 v[40:43],a[216:219],a[240:243],v[40:43]\nds_read_b64_tr_b16 a[204:205],v233 offset:8192\nds_read_b128 v[160:163],v255 offset:60672\n"
"v_mfma_f32_16x16x32_bf16 v[68:71],a[220:223],a[240:243],v[68:71]\nds_read_b64_tr_b16 a[206:207],v233 offset:9216\nv_mfma_f32_16x16x32_bf16 v[96:99],a[224:227],a[240:243],v[96:99]\nds_read_b128 a[240:243],v239 offset:49152\nds_read_b128 v[164:167],v255 offset:60928\nv_mfma_f32_16x16x32_bf16 v[16:19],a[212:215],a[244:247],v[16:19]\nds_read_b64_tr_b16 a[208:209],v234 offset:8192\nv_mfma_f32_16x16x32_bf16 v[44:47],a[216:219],a[244:247],v[44:47]\n"
"ds_read_b128 v[168:171],v255 offset:61440\nds_read_b128 v[172:175],v255 offset:61696\nv_mfma_f32_16x16x32_bf16 v[72:75],a[220:223],a[244:247],v[72:75]\nds_read_b128 v[176:179],v255 offset:61952\nv_mfma_f32_16x16x32_bf16 v[100:103],a[224:227],a[244:247],v[100:103]\nds_read_b128 a[244:247],v239 offset:51200\nds_read_b128 v[180:183],v255 offset:62208\nv_mfma_f32_16x16x32_bf16 v[20:23],a[212:215],a[248:251],v[20:23]\n"
"ds_read_b64_tr_b16 a[210:211],v234 offset:9216\ns_add_u32 m0,s64,8192\nv_mfma_f32_16x16x32_bf16 v[48:51],a[216:219],a[248:251],v[48:51]\ns_mov_b32 s73,114688\nds_read_b128 v[184:187],v255 offset:62464\nv_mfma_f32_16x16x32_bf16 v[76:79],a[220:223],a[248:251],v[76:79]\nds_read_b128 v[188:191],v255 offset:62720\nv_mfma_f32_16x16x32_bf16 v[104:107],a[224:227],a[248:251],v[104:107]\n"
"ds_read_b128 a[248:251],v239 offset:53248\nds_read_b128 v[192:195],v255 offset:62976\nv_mfma_f32_16x16x32_bf16 v[24:27],a[212:215],a[252:255],v[24:27]\nv_mfma_f32_16x16x32_bf16 v[52:55],a[216:219],a[252:255],v[52:55]\nv_mfma_f32_16x16x32_bf16 v[80:83],a[220:223],a[252:255],v[80:83]\nv_mfma_f32_16x16x32_bf16 v[108:111],a[224:227],a[252:255],v[108:111]\nds_read_b128 a[252:255],v239 offset:55296\ns_waitcnt lgkmcnt(5)\n"
"v_mfma_f32_16x16x32_bf16 v[0:3],a[196:199],a[228:231],v[0:3]\nbuffer_load_dwordx4 v226,s[56:59],s73 offen offset:0 lds\ns_waitcnt lgkmcnt(1)\nv_mfma_f32_16x16x32_bf16 v[28:31],a[200:203],a[228:231],v[28:31]\ns_waitcnt vmcnt(17)\nds_read_b64_tr_b16 a[212:213],v231 offset:12288\nv_add_f32 v140,v140,v141\nv_mfma_f32_16x16x32_bf16 v[56:59],a[204:207],a[228:231],v[56:59]\n"
"ds_read_b64_tr_b16 a[214:215],v231 offset:13312\nv_add_f32 v142,v142,v143\nv_mfma_f32_16x16x32_bf16 v[84:87],a[208:211],a[228:231],v[84:87]\nds_read_b128 a[228:231],v239 offset:44032\nv_add_f32 v140,v140,v142\nv_add_f32 v168,v168,v169\nv_mfma_f32_16x16x32_bf16 v[4:7],a[196:199],a[232:235],v[4:7]\ns_mov_b32 s74,128000\n"
"buffer_load_dwordx4 v226,s[56:59],s74 offen offset:1024 lds\nv_mfma_f32_16x16x32_bf16 v[32:35],a[200:203],a[232:235],v[32:35]\nds_read_b64_tr_b16 a[216:217],v232 offset:12288\nv_add_f32 v170,v170,v171\nv_add_f32 v168,v168,v170\nv_mfma_f32_16x16x32_bf16 v[60:63],a[204:207],a[232:235],v[60:63]\ns_mov_b32 s75,119808\nv_add_f32 v144,v144,v145\n"
"v_add_f32 v146,v146,v147\nv_mfma_f32_16x16x32_bf16 v[88:91],a[208:211],a[232:235],v[88:91]\nds_read_b128 a[232:235],v239 offset:46080\nv_add_f32 v144,v144,v146\nv_add_f32 v172,v172,v173\nv_mfma_f32_16x16x32_bf16 v[8:11],a[196:199],a[236:239],v[8:11]\nbuffer_load_dwordx4 v226,s[56:59],s75 offen offset:2048 lds\nv_add_f32 v174,v174,v175\n"
"v_mfma_f32_16x16x32_bf16 v[36:39],a[200:203],a[236:239],v[36:39]\nds_read_b64_tr_b16 a[218:219],v232 offset:13312\nv_add_f32 v172,v172,v174\nv_add_f32 v148,v148,v149\nv_mfma_f32_16x16x32_bf16 v[64:67],a[204:207],a[236:239],v[64:67]\ns_mov_b32 s72,133120\nv_add_f32 v150,v150,v151\nv_add_f32 v148,v148,v150\n"
"v_mfma_f32_16x16x32_bf16 v[92:95],a[208:211],a[236:239],v[92:95]\nds_read_b128 a[236:239],v239 offset:48128\nv_add_f32 v176,v176,v177\nv_add_f32 v178,v178,v179\nv_mfma_f32_16x16x32_bf16 v[12:15],a[196:199],a[240:243],v[12:15]\nbuffer_load_dwordx4 v226,s[56:59],s72 offen offset:3072 lds\nv_add_f32 v176,v176,v178\nv_mfma_f32_16x16x32_bf16 v[40:43],a[200:203],a[240:243],v[40:43]\n"
"ds_read_b64_tr_b16 a[220:221],v233 offset:12288\nv_add_f32 v152,v152,v153\nv_add_f32 v154,v154,v155\nv_mfma_f32_16x16x32_bf16 v[68:71],a[204:207],a[240:243],v[68:71]\nds_read_b64_tr_b16 a[222:223],v233 offset:13312\nv_add_f32 v152,v152,v154\nv_mfma_f32_16x16x32_bf16 v[96:99],a[208:211],a[240:243],v[96:99]\nds_read_b128 a[240:243],v239 offset:50176\n"
"v_add_f32 v180,v180,v181\nv_add_f32 v182,v182,v183\nv_mfma_f32_16x16x32_bf16 v[16:19],a[196:199],a[244:247],v[16:19]\nds_read_b64_tr_b16 a[224:225],v234 offset:12288\nv_add_f32 v180,v180,v182\nv_mfma_f32_16x16x32_bf16 v[44:47],a[200:203],a[244:247],v[44:47]\nv_add_f32 v156,v156,v157\nv_add_f32 v158,v158,v159\n"
"v_add_f32 v156,v156,v158\nv_mfma_f32_16x16x32_bf16 v[72:75],a[204:207],a[244:247],v[72:75]\nv_add_f32 v184,v184,v185\nv_add_f32 v186,v186,v187\nv_mfma_f32_16x16x32_bf16 v[100:103],a[208:211],a[244:247],v[100:103]\nds_read_b128 a[244:247],v239 offset:52224\ns_waitcnt lgkmcnt(12)\nv_add_f32 v184,v184,v186\n"
"v_mfma_f32_16x16x32_bf16 v[20:23],a[196:199],a[248:251],v[20:23]\nds_read_b64_tr_b16 a[226:227],v234 offset:13312\nv_add_f32 v160,v160,v161\nv_mfma_f32_16x16x32_bf16 v[48:51],a[200:203],a[248:251],v[48:51]\ns_add_u32 m0,s64,12288\nv_add_f32 v162,v162,v163\nv_add_f32 v160,v160,v162\nv_add_f32 v188,v188,v189\n"
"v_mfma_f32_16x16x32_bf16 v[76:79],a[204:207],a[248:251],v[76:79]\ns_mov_b32 s73,143360\nv_add_f32 v190,v190,v191\nv_add_f32 v188,v188,v190\nv_mfma_f32_16x16x32_bf16 v[104:107],a[208:211],a[248:251],v[104:107]\nds_read_b128 a[248:251],v239 offset:54272\nv_add_f32 v164,v164,v165\nv_add_f32 v166,v166,v167\n"
"v_mfma_f32_16x16x32_bf16 v[24:27],a[196:199],a[252:255],v[24:27]\nv_add_f32 v164,v164,v166\nv_add_f32 v192,v192,v193\nv_mfma_f32_16x16x32_bf16 v[52:55],a[200:203],a[252:255],v[52:55]\nv_add_f32 v194,v194,v195\nv_add_f32 v192,v192,v194\nv_fma_f32 v140,v140,v241,v168\nv_mfma_f32_16x16x32_bf16 v[80:83],a[204:207],a[252:255],v[80:83]\n"
"v_fma_f32 v144,v144,v242,v172\nv_fma_f32 v148,v148,v243,v176\nv_mfma_f32_16x16x32_bf16 v[108:111],a[208:211],a[252:255],v[108:111]\nds_read_b128 a[252:255],v239 offset:56320\ns_waitcnt lgkmcnt(2)\nv_fma_f32 v152,v152,v244,v180\nv_mfma_f32_16x16x32_bf16 v[0:3],a[212:215],a[228:231],v[0:3]\nbuffer_load_dwordx4 v226,s[56:59],s73 offen offset:0 lds\n"
"v_fma_f32 v156,v156,v245,v184\nv_mfma_f32_16x16x32_bf16 v[28:31],a[216:219],a[228:231],v[28:31]\ns_waitcnt vmcnt(17)\nds_read_b64_tr_b16 a[196:197],v231 offset:16384\nv_fma_f32 v160,v160,v246,v188\nv_mfma_f32_16x16x32_bf16 v[56:59],a[220:223],a[228:231],v[56:59]\nds_read_b64_tr_b16 a[198:199],v231 offset:17408\nv_fma_f32 v164,v164,v247,v192\n"
"v_mfma_f32_16x16x32_bf16 v[84:87],a[224:227],a[228:231],v[84:87]\ns_waitcnt lgkmcnt(2)\ns_barrier\nv_rcp_f32 v248,v140\nv_mfma_f32_16x16x32_bf16 v[4:7],a[212:215],a[232:235],v[4:7]\ns_mov_b32 s74,156672\nbuffer_load_dwordx4 v226,s[56:59],s74 offen offset:1024 lds\nv_mfma_f32_16x16x32_bf16 v[32:35],a[216:219],a[232:235],v[32:35]\n"
"ds_read_b64_tr_b16 a[200:201],v232 offset:16384\nv_rcp_f32 v249,v144\nv_mfma_f32_16x16x32_bf16 v[60:63],a[220:223],a[232:235],v[60:63]\ns_mov_b32 s75,148480\nv_rcp_f32 v250,v148\nv_mfma_f32_16x16x32_bf16 v[88:91],a[224:227],a[232:235],v[88:91]\nv_rcp_f32 v251,v152\nv_mov_b32 v242,14336\n"
"v_mfma_f32_16x16x32_bf16 v[8:11],a[212:215],a[236:239],v[8:11]\nbuffer_load_dwordx4 v226,s[56:59],s75 offen offset:2048 lds\nv_mad_u32_u24 v241,v242,s70,v239\nv_mfma_f32_16x16x32_bf16 v[36:39],a[216:219],a[236:239],v[36:39]\nds_read_b64_tr_b16 a[202:203],v232 offset:17408\nv_rcp_f32 v252,v156\nv_mfma_f32_16x16x32_bf16 v[64:67],a[220:223],a[236:239],v[64:67]\ns_mov_b32 s72,161792\n"
"v_rcp_f32 v253,v160\nv_mfma_f32_16x16x32_bf16 v[92:95],a[224:227],a[236:239],v[92:95]\nv_rcp_f32 v254,v164\nv_mul_f32 v0,v248,v0\nv_mfma_f32_16x16x32_bf16 v[12:15],a[212:215],a[240:243],v[12:15]\nbuffer_load_dwordx4 v226,s[56:59],s72 offen offset:3072 lds\nv_mul_f32 v1,v248,v1\nv_mfma_f32_16x16x32_bf16 v[40:43],a[216:219],a[240:243],v[40:43]\n"
"ds_read_b64_tr_b16 a[204:205],v233 offset:16384\nv_mul_f32 v2,v248,v2\nv_mul_f32 v3,v248,v3\nv_mfma_f32_16x16x32_bf16 v[68:71],a[220:223],a[240:243],v[68:71]\nds_read_b64_tr_b16 a[206:207],v233 offset:17408\nv_cvt_pk_bf16_f32 v0,v0,v1\nv_mfma_f32_16x16x32_bf16 v[96:99],a[224:227],a[240:243],v[96:99]\nv_cvt_pk_bf16_f32 v1,v2,v3\n"
"v_mul_f32 v28,v248,v28\nv_mul_f32 v29,v248,v29\nv_mfma_f32_16x16x32_bf16 v[16:19],a[212:215],a[244:247],v[16:19]\nds_read_b64_tr_b16 a[208:209],v234 offset:16384\nv_mul_f32 v30,v248,v30\nv_mfma_f32_16x16x32_bf16 v[44:47],a[216:219],a[244:247],v[44:47]\nv_mul_f32 v31,v248,v31\nv_cvt_pk_bf16_f32 v2,v28,v29\n"
"v_cvt_pk_bf16_f32 v3,v30,v31\nv_mfma_f32_16x16x32_bf16 v[72:75],a[220:223],a[244:247],v[72:75]\nds_write_b128 v241,v[0:3] offset:0\nv_mfma_f32_16x16x32_bf16 v[100:103],a[224:227],a[244:247],v[100:103]\nv_mul_f32 v56,v248,v56\nv_mul_f32 v57,v248,v57\nv_mul_f32 v58,v248,v58\nv_mfma_f32_16x16x32_bf16 v[20:23],a[212:215],a[248:251],v[20:23]\n"
"ds_read_b64_tr_b16 a[210:211],v234 offset:17408\nv_mul_f32 v59,v248,v59\nv_mfma_f32_16x16x32_bf16 v[48:51],a[216:219],a[248:251],v[48:51]\ns_add_u32 m0,s64,16384\nv_cvt_pk_bf16_f32 v56,v56,v57\nv_cvt_pk_bf16_f32 v57,v58,v59\nv_mul_f32 v84,v248,v84\nv_mfma_f32_16x16x32_bf16 v[76:79],a[220:223],a[248:251],v[76:79]\n"
"s_mov_b32 s73,172032\nv_mul_f32 v85,v248,v85\nv_mul_f32 v86,v248,v86\nv_mfma_f32_16x16x32_bf16 v[104:107],a[224:227],a[248:251],v[104:107]\nv_mul_f32 v87,v248,v87\nv_cvt_pk_bf16_f32 v58,v84,v85\nv_cvt_pk_bf16_f32 v59,v86,v87\nv_mfma_f32_16x16x32_bf16 v[24:27],a[212:215],a[252:255],v[24:27]\n"
"ds_write_b128 v241,v[56:59] offset:1024\nv_mfma_f32_16x16x32_bf16 v[52:55],a[216:219],a[252:255],v[52:55]\nv_mul_f32 v4,v249,v4\nv_mul_f32 v5,v249,v5\nv_mul_f32 v6,v249,v6\nv_mfma_f32_16x16x32_bf16 v[80:83],a[220:223],a[252:255],v[80:83]\nv_mul_f32 v7,v249,v7\nv_cvt_pk_bf16_f32 v4,v4,v5\n"
"v_mfma_f32_16x16x32_bf16 v[108:111],a[224:227],a[252:255],v[108:111]\nv_cvt_pk_bf16_f32 v5,v6,v7\nv_mul_f32 v32,v249,v32\nv_mul_f32 v33,v249,v33\nv_mul_f32 v34,v249,v34\nv_mul_f32 v35,v249,v35\nv_cvt_pk_bf16_f32 v6,v32,v33\nv_cvt_pk_bf16_f32 v7,v34,v35\n"
"ds_write_b128 v241,v[4:7] offset:2048\nv_mul_f32 v60,v249,v60\nv_mul_f32 v61,v249,v61\nv_mul_f32 v62,v249,v62\nv_mul_f32 v63,v249,v63\nv_cvt_pk_bf16_f32 v60,v60,v61\nv_cvt_pk_bf16_f32 v61,v62,v63\nv_mul_f32 v88,v249,v88\n"
"v_mul_f32 v89,v249,v89\nv_mul_f32 v90,v249,v90\nv_mul_f32 v91,v249,v91\nv_cvt_pk_bf16_f32 v62,v88,v89\nv_cvt_pk_bf16_f32 v63,v90,v91\nds_write_b128 v241,v[60:63] offset:3072\nv_pk_mul_f32 v[8:9],v[8:9],v[250:251] op_sel:[0,0] op_sel_hi:[1,0]\nv_pk_mul_f32 v[10:11],v[10:11],v[250:251] op_sel:[0,0] op_sel_hi:[1,0]\n"
"v_cvt_pk_bf16_f32 v8,v8,v9\nv_cvt_pk_bf16_f32 v9,v10,v11\nv_pk_mul_f32 v[36:37],v[36:37],v[250:251] op_sel:[0,0] op_sel_hi:[1,0]\nv_pk_mul_f32 v[38:39],v[38:39],v[250:251] op_sel:[0,0] op_sel_hi:[1,0]\nv_cvt_pk_bf16_f32 v10,v36,v37\nv_cvt_pk_bf16_f32 v11,v38,v39\nds_write_b128 v241,v[8:11] offset:4096\nv_pk_mul_f32 v[64:65],v[64:65],v[250:251] op_sel:[0,0] op_sel_hi:[1,0]\n"
"v_pk_mul_f32 v[66:67],v[66:67],v[250:251] op_sel:[0,0] op_sel_hi:[1,0]\nv_cvt_pk_bf16_f32 v64,v64,v65\nv_cvt_pk_bf16_f32 v65,v66,v67\nv_pk_mul_f32 v[92:93],v[92:93],v[250:251] op_sel:[0,0] op_sel_hi:[1,0]\nv_pk_mul_f32 v[94:95],v[94:95],v[250:251] op_sel:[0,0] op_sel_hi:[1,0]\nv_cvt_pk_bf16_f32 v66,v92,v93\nv_cvt_pk_bf16_f32 v67,v94,v95\nds_write_b128 v241,v[64:67] offset:5120\n"
"v_pk_mul_f32 v[12:13],v[12:13],v[250:251] op_sel:[0,1] op_sel_hi:[1,1]\nv_pk_mul_f32 v[14:15],v[14:15],v[250:251] op_sel:[0,1] op_sel_hi:[1,1]\nv_cvt_pk_bf16_f32 v12,v12,v13\nv_cvt_pk_bf16_f32 v13,v14,v15\nv_pk_mul_f32 v[40:41],v[40:41],v[250:251] op_sel:[0,1] op_sel_hi:[1,1]\nv_pk_mul_f32 v[42:43],v[42:43],v[250:251] op_sel:[0,1] op_sel_hi:[1,1]\nv_cvt_pk_bf16_f32 v14,v40,v41\nv_cvt_pk_bf16_f32 v15,v42,v43\n"
"ds_write_b128 v241,v[12:15] offset:6144\nv_pk_mul_f32 v[68:69],v[68:69],v[250:251] op_sel:[0,1] op_sel_hi:[1,1]\nv_pk_mul_f32 v[70:71],v[70:71],v[250:251] op_sel:[0,1] op_sel_hi:[1,1]\nv_cvt_pk_bf16_f32 v68,v68,v69\nv_cvt_pk_bf16_f32 v69,v70,v71\nv_pk_mul_f32 v[96:97],v[96:97],v[250:251] op_sel:[0,1] op_sel_hi:[1,1]\nv_pk_mul_f32 v[98:99],v[98:99],v[250:251] op_sel:[0,1] op_sel_hi:[1,1]\nv_cvt_pk_bf16_f32 v70,v96,v97\n"
"v_cvt_pk_bf16_f32 v71,v98,v99\nds_write_b128 v241,v[68:71] offset:7168\nv_pk_mul_f32 v[16:17],v[16:17],v[252:253] op_sel:[0,0] op_sel_hi:[1,0]\nv_pk_mul_f32 v[18:19],v[18:19],v[252:253] op_sel:[0,0] op_sel_hi:[1,0]\nv_cvt_pk_bf16_f32 v16,v16,v17\nv_cvt_pk_bf16_f32 v17,v18,v19\nv_pk_mul_f32 v[44:45],v[44:45],v[252:253] op_sel:[0,0] op_sel_hi:[1,0]\nv_pk_mul_f32 v[46:47],v[46:47],v[252:253] op_sel:[0,0] op_sel_hi:[1,0]\n"
"v_cvt_pk_bf16_f32 v18,v44,v45\nv_cvt_pk_bf16_f32 v19,v46,v47\nds_write_b128 v241,v[16:19] offset:8192\nv_pk_mul_f32 v[72:73],v[72:73],v[252:253] op_sel:[0,0] op_sel_hi:[1,0]\nv_pk_mul_f32 v[74:75],v[74:75],v[252:253] op_sel:[0,0] op_sel_hi:[1,0]\nv_cvt_pk_bf16_f32 v72,v72,v73\nv_cvt_pk_bf16_f32 v73,v74,v75\nv_pk_mul_f32 v[100:101],v[100:101],v[252:253] op_sel:[0,0] op_sel_hi:[1,0]\n"
"v_pk_mul_f32 v[102:103],v[102:103],v[252:253] op_sel:[0,0] op_sel_hi:[1,0]\nv_cvt_pk_bf16_f32 v74,v100,v101\nv_cvt_pk_bf16_f32 v75,v102,v103\nds_write_b128 v241,v[72:75] offset:9216\nv_pk_mul_f32 v[20:21],v[20:21],v[252:253] op_sel:[0,1] op_sel_hi:[1,1]\nv_pk_mul_f32 v[22:23],v[22:23],v[252:253] op_sel:[0,1] op_sel_hi:[1,1]\nv_cvt_pk_bf16_f32 v20,v20,v21\nv_cvt_pk_bf16_f32 v21,v22,v23\n"
"v_pk_mul_f32 v[48:49],v[48:49],v[252:253] op_sel:[0,1] op_sel_hi:[1,1]\nv_pk_mul_f32 v[50:51],v[50:51],v[252:253] op_sel:[0,1] op_sel_hi:[1,1]\nv_cvt_pk_bf16_f32 v22,v48,v49\nv_cvt_pk_bf16_f32 v23,v50,v51\nds_write_b128 v241,v[20:23] offset:10240\nv_pk_mul_f32 v[76:77],v[76:77],v[252:253] op_sel:[0,1] op_sel_hi:[1,1]\nv_pk_mul_f32 v[78:79],v[78:79],v[252:253] op_sel:[0,1] op_sel_hi:[1,1]\nv_cvt_pk_bf16_f32 v76,v76,v77\n"
"v_cvt_pk_bf16_f32 v77,v78,v79\nv_pk_mul_f32 v[104:105],v[104:105],v[252:253] op_sel:[0,1] op_sel_hi:[1,1]\nv_pk_mul_f32 v[106:107],v[106:107],v[252:253] op_sel:[0,1] op_sel_hi:[1,1]\nv_cvt_pk_bf16_f32 v78,v104,v105\nv_cvt_pk_bf16_f32 v79,v106,v107\nds_write_b128 v241,v[76:79] offset:11264\nv_pk_mul_f32 v[24:25],v[24:25],v[254:255] op_sel:[0,0] op_sel_hi:[1,0]\nv_pk_mul_f32 v[26:27],v[26:27],v[254:255] op_sel:[0,0] op_sel_hi:[1,0]\n"
"v_cvt_pk_bf16_f32 v24,v24,v25\nv_cvt_pk_bf16_f32 v25,v26,v27\nv_pk_mul_f32 v[52:53],v[52:53],v[254:255] op_sel:[0,0] op_sel_hi:[1,0]\nv_pk_mul_f32 v[54:55],v[54:55],v[254:255] op_sel:[0,0] op_sel_hi:[1,0]\nv_cvt_pk_bf16_f32 v26,v52,v53\nv_cvt_pk_bf16_f32 v27,v54,v55\nds_write_b128 v241,v[24:27] offset:12288\nv_pk_mul_f32 v[80:81],v[80:81],v[254:255] op_sel:[0,0] op_sel_hi:[1,0]\n"
"v_pk_mul_f32 v[82:83],v[82:83],v[254:255] op_sel:[0,0] op_sel_hi:[1,0]\nv_cvt_pk_bf16_f32 v80,v80,v81\nv_cvt_pk_bf16_f32 v81,v82,v83\nv_pk_mul_f32 v[108:109],v[108:109],v[254:255] op_sel:[0,0] op_sel_hi:[1,0]\nv_pk_mul_f32 v[110:111],v[110:111],v[254:255] op_sel:[0,0] op_sel_hi:[1,0]\nv_cvt_pk_bf16_f32 v82,v108,v109\nv_cvt_pk_bf16_f32 v83,v110,v111\nds_write_b128 v241,v[80:83] offset:13312\n"
"s_waitcnt lgkmcnt(0)\ns_barrier\nds_read_b128 a[228:231],v239 offset:0\nds_read_b128 a[232:235],v239 offset:2048\nds_read_b128 a[236:239],v239 offset:4096\nds_read_b128 a[240:243],v239 offset:6144\nds_read_b128 a[244:247],v239 offset:8192\nds_read_b128 a[248:251],v239 offset:10240\n"
"ds_read_b128 a[252:255],v239 offset:12288\ns_waitcnt lgkmcnt(6)\nv_mfma_f32_16x16x32_bf16 a[0:3],a[196:199],a[228:231],0\nbuffer_load_dwordx4 v226,s[56:59],s73 offen offset:0 lds\nv_lshl_add_u32 v252,s70,11,v239\nv_mfma_f32_16x16x32_bf16 a[28:31],a[200:203],a[228:231],0\ns_waitcnt vmcnt(17)\nds_read_b64_tr_b16 a[212:213],v231 offset:20480\n"
"v_mfma_f32_16x16x32_bf16 a[56:59],a[204:207],a[228:231],0\nds_read_b64_tr_b16 a[214:215],v231 offset:21504\nv_lshl_add_u32 v253,s70,11,v237\nv_mfma_f32_16x16x32_bf16 a[84:87],a[208:211],a[228:231],0\nds_read_b128 a[228:231],v239 offset:1024\ns_waitcnt lgkmcnt(8)\nv_mfma_f32_16x16x32_bf16 a[4:7],a[196:199],a[232:235],0\ns_mov_b32 s74,185344\n"
"buffer_load_dwordx4 v226,s[56:59],s74 offen offset:1024 lds\nv_mfma_f32_16x16x32_bf16 a[32:35],a[200:203],a[232:235],0\nds_read_b64_tr_b16 a[216:217],v232 offset:20480\nv_lshl_add_u32 v254,s70,11,v238\nv_mfma_f32_16x16x32_bf16 a[60:63],a[204:207],a[232:235],0\ns_mov_b32 s75,177152\nv_mfma_f32_16x16x32_bf16 a[88:91],a[208:211],a[232:235],0\nds_read_b128 a[232:235],v239 offset:3072\n"
"s_waitcnt lgkmcnt(9)\nv_mfma_f32_16x16x32_bf16 a[8:11],a[196:199],a[236:239],0\nbuffer_load_dwordx4 v226,s[56:59],s75 offen offset:2048 lds\ns_mov_b32 s72,190464\nv_mfma_f32_16x16x32_bf16 a[36:39],a[200:203],a[236:239],0\nds_read_b64_tr_b16 a[218:219],v232 offset:21504\nv_mfma_f32_16x16x32_bf16 a[64:67],a[204:207],a[236:239],0\nv_mfma_f32_16x16x32_bf16 a[92:95],a[208:211],a[236:239],0\n"
"ds_read_b128 a[236:239],v239 offset:5120\ns_waitcnt lgkmcnt(9)\nv_mfma_f32_16x16x32_bf16 a[12:15],a[196:199],a[240:243],0\nbuffer_load_dwordx4 v226,s[56:59],s72 offen offset:3072 lds\nv_mfma_f32_16x16x32_bf16 a[40:43],a[200:203],a[240:243],0\nds_read_b64_tr_b16 a[220:221],v233 offset:20480\nv_mfma_f32_16x16x32_bf16 a[68:71],a[204:207],a[240:243],0\nds_read_b64_tr_b16 a[222:223],v233 offset:21504\n"
"v_mfma_f32_16x16x32_bf16 a[96:99],a[208:211],a[240:243],0\nds_read_b128 a[240:243],v239 offset:7168\nv_mfma_f32_16x16x32_bf16 a[16:19],a[196:199],a[244:247],0\nds_read_b64_tr_b16 a[224:225],v234 offset:20480\nv_mfma_f32_16x16x32_bf16 a[44:47],a[200:203],a[244:247],0\nv_mfma_f32_16x16x32_bf16 a[72:75],a[204:207],a[244:247],0\nv_mfma_f32_16x16x32_bf16 a[100:103],a[208:211],a[244:247],0\nds_read_b128 a[244:247],v239 offset:9216\n"
"s_waitcnt lgkmcnt(12)\nv_mfma_f32_16x16x32_bf16 a[20:23],a[196:199],a[248:251],0\nds_read_b64_tr_b16 a[226:227],v234 offset:21504\ns_add_u32 m0,s64,20480\nv_mfma_f32_16x16x32_bf16 a[48:51],a[200:203],a[248:251],0\ns_mov_b32 s73,200704\nv_mfma_f32_16x16x32_bf16 a[76:79],a[204:207],a[248:251],0\nv_mfma_f32_16x16x32_bf16 a[104:107],a[208:211],a[248:251],0\n"
"ds_read_b128 a[248:251],v239 offset:11264\nv_mfma_f32_16x16x32_bf16 a[24:27],a[196:199],a[252:255],0\nv_mfma_f32_16x16x32_bf16 a[52:55],a[200:203],a[252:255],0\nv_mfma_f32_16x16x32_bf16 a[80:83],a[204:207],a[252:255],0\nv_mfma_f32_16x16x32_bf16 a[108:111],a[208:211],a[252:255],0\nds_read_b128 a[252:255],v239 offset:13312\ns_waitcnt lgkmcnt(2)\nv_mfma_f32_16x16x32_bf16 a[0:3],a[212:215],a[228:231],a[0:3]\n"
"buffer_load_dwordx4 v226,s[56:59],s73 offen offset:0 lds\ns_mov_b32 s74,214016\nv_mfma_f32_16x16x32_bf16 a[28:31],a[216:219],a[228:231],a[28:31]\ns_waitcnt vmcnt(17)\nds_read_b64_tr_b16 a[196:197],v231 offset:0\nv_mfma_f32_16x16x32_bf16 a[56:59],a[220:223],a[228:231],a[56:59]\nds_read_b64_tr_b16 a[198:199],v231 offset:1024\nv_mfma_f32_16x16x32_bf16 a[84:87],a[224:227],a[228:231],a[84:87]\n"
"ds_read_b128 a[228:231],v239 offset:14336\nv_mfma_f32_16x16x32_bf16 a[4:7],a[212:215],a[232:235],a[4:7]\nbuffer_load_dwordx4 v226,s[56:59],s74 offen offset:1024 lds\ns_mov_b32 s75,205824\nv_mfma_f32_16x16x32_bf16 a[32:35],a[216:219],a[232:235],a[32:35]\nds_read_b64_tr_b16 a[200:201],v232 offset:0\nv_mfma_f32_16x16x32_bf16 a[60:63],a[220:223],a[232:235],a[60:63]\nv_mfma_f32_16x16x32_bf16 a[88:91],a[224:227],a[232:235],a[88:91]\n"
"ds_read_b128 a[232:235],v239 offset:16384\nv_mfma_f32_16x16x32_bf16 a[8:11],a[212:215],a[236:239],a[8:11]\nbuffer_load_dwordx4 v226,s[56:59],s75 offen offset:2048 lds\ns_mov_b32 s72,219136\nv_mfma_f32_16x16x32_bf16 a[36:39],a[216:219],a[236:239],a[36:39]\nds_read_b64_tr_b16 a[202:203],v232 offset:1024\nv_mfma_f32_16x16x32_bf16 a[64:67],a[220:223],a[236:239],a[64:67]\nv_mfma_f32_16x16x32_bf16 a[92:95],a[224:227],a[236:239],a[92:95]\n"
"ds_read_b128 a[236:239],v239 offset:18432\nv_mfma_f32_16x16x32_bf16 a[12:15],a[212:215],a[240:243],a[12:15]\nbuffer_load_dwordx4 v226,s[56:59],s72 offen offset:3072 lds\nv_mfma_f32_16x16x32_bf16 a[40:43],a[216:219],a[240:243],a[40:43]\nds_read_b64_tr_b16 a[204:205],v233 offset:0\nv_mfma_f32_16x16x32_bf16 a[68:71],a[220:223],a[240:243],a[68:71]\nds_read_b64_tr_b16 a[206:207],v233 offset:1024\nv_mfma_f32_16x16x32_bf16 a[96:99],a[224:227],a[240:243],a[96:99]\n"
"ds_read_b128 a[240:243],v239 offset:20480\nv_mfma_f32_16x16x32_bf16 a[16:19],a[212:215],a[244:247],a[16:19]\nds_read_b64_tr_b16 a[208:209],v234 offset:0\nv_mfma_f32_16x16x32_bf16 a[44:47],a[216:219],a[244:247],a[44:47]\nv_mfma_f32_16x16x32_bf16 a[72:75],a[220:223],a[244:247],a[72:75]\nv_mfma_f32_16x16x32_bf16 a[100:103],a[224:227],a[244:247],a[100:103]\nds_read_b128 a[244:247],v239 offset:22528\ns_waitcnt lgkmcnt(12)\n"
"v_mfma_f32_16x16x32_bf16 a[20:23],a[212:215],a[248:251],a[20:23]\nds_read_b64_tr_b16 a[210:211],v234 offset:1024\nv_add_u32 v226,s68,v226\nv_mfma_f32_16x16x32_bf16 a[48:51],a[216:219],a[248:251],a[48:51]\ns_add_u32 m0,s64,0\ns_mov_b32 s73,0\nv_mfma_f32_16x16x32_bf16 a[76:79],a[220:223],a[248:251],a[76:79]\nv_mfma_f32_16x16x32_bf16 a[104:107],a[224:227],a[248:251],a[104:107]\n"
"ds_read_b128 a[248:251],v239 offset:24576\nv_mfma_f32_16x16x32_bf16 a[24:27],a[212:215],a[252:255],a[24:27]\nv_mfma_f32_16x16x32_bf16 a[52:55],a[216:219],a[252:255],a[52:55]\nv_mfma_f32_16x16x32_bf16 a[80:83],a[220:223],a[252:255],a[80:83]\nv_mfma_f32_16x16x32_bf16 a[108:111],a[224:227],a[252:255],a[108:111]\nds_read_b128 a[252:255],v239 offset:26624\ns_waitcnt lgkmcnt(2)\nv_mfma_f32_16x16x32_bf16 a[0:3],a[196:199],a[228:231],a[0:3]\n"
"buffer_load_dwordx4 v226,s[56:59],s73 offen offset:0 lds\ns_mov_b32 s74,13312\nv_mfma_f32_16x16x32_bf16 a[28:31],a[200:203],a[228:231],a[28:31]\ns_waitcnt vmcnt(17)\nds_read_b64_tr_b16 a[212:213],v231 offset:4096\nv_mfma_f32_16x16x32_bf16 a[56:59],a[204:207],a[228:231],a[56:59]\nds_read_b64_tr_b16 a[214:215],v231 offset:5120\nv_mfma_f32_16x16x32_bf16 a[84:87],a[208:211],a[228:231],a[84:87]\n"
"ds_read_b128 a[228:231],v239 offset:15360\nv_mfma_f32_16x16x32_bf16 a[4:7],a[196:199],a[232:235],a[4:7]\nbuffer_load_dwordx4 v226,s[56:59],s74 offen offset:1024 lds\ns_mov_b32 s75,5120\nv_mfma_f32_16x16x32_bf16 a[32:35],a[200:203],a[232:235],a[32:35]\nds_read_b64_tr_b16 a[216:217],v232 offset:4096\nv_mfma_f32_16x16x32_bf16 a[60:63],a[204:207],a[232:235],a[60:63]\nv_mfma_f32_16x16x32_bf16 a[88:91],a[208:211],a[232:235],a[88:91]\n"
"ds_read_b128 a[232:235],v239 offset:17408\nv_mfma_f32_16x16x32_bf16 a[8:11],a[196:199],a[236:239],a[8:11]\nbuffer_load_dwordx4 v226,s[56:59],s75 offen offset:2048 lds\ns_mov_b32 s72,18432\nv_mfma_f32_16x16x32_bf16 a[36:39],a[200:203],a[236:239],a[36:39]\nds_read_b64_tr_b16 a[218:219],v232 offset:5120\nv_mfma_f32_16x16x32_bf16 a[64:67],a[204:207],a[236:239],a[64:67]\nv_mfma_f32_16x16x32_bf16 a[92:95],a[208:211],a[236:239],a[92:95]\n"
"ds_read_b128 a[236:239],v239 offset:19456\nv_mfma_f32_16x16x32_bf16 a[12:15],a[196:199],a[240:243],a[12:15]\nbuffer_load_dwordx4 v226,s[56:59],s72 offen offset:3072 lds\nv_mfma_f32_16x16x32_bf16 a[40:43],a[200:203],a[240:243],a[40:43]\nds_read_b64_tr_b16 a[220:221],v233 offset:4096\nv_mfma_f32_16x16x32_bf16 a[68:71],a[204:207],a[240:243],a[68:71]\nds_read_b64_tr_b16 a[222:223],v233 offset:5120\nv_mfma_f32_16x16x32_bf16 a[96:99],a[208:211],a[240:243],a[96:99]\n"
"ds_read_b128 a[240:243],v239 offset:21504\nv_mfma_f32_16x16x32_bf16 a[16:19],a[196:199],a[244:247],a[16:19]\nds_read_b64_tr_b16 a[224:225],v234 offset:4096\nv_mfma_f32_16x16x32_bf16 a[44:47],a[200:203],a[244:247],a[44:47]\nv_mfma_f32_16x16x32_bf16 a[72:75],a[204:207],a[244:247],a[72:75]\nv_mfma_f32_16x16x32_bf16 a[100:103],a[208:211],a[244:247],a[100:103]\nds_read_b128 a[244:247],v239 offset:23552\ns_waitcnt lgkmcnt(12)\n"
"v_mfma_f32_16x16x32_bf16 a[20:23],a[196:199],a[248:251],a[20:23]\nds_read_b64_tr_b16 a[226:227],v234 offset:5120\ns_add_u32 m0,s64,4096\nv_mfma_f32_16x16x32_bf16 a[48:51],a[200:203],a[248:251],a[48:51]\ns_mov_b32 s73,28672\nv_mfma_f32_16x16x32_bf16 a[76:79],a[204:207],a[248:251],a[76:79]\nv_mfma_f32_16x16x32_bf16 a[104:107],a[208:211],a[248:251],a[104:107]\nds_read_b128 a[248:251],v239 offset:25600\n"
"v_mfma_f32_16x16x32_bf16 a[24:27],a[196:199],a[252:255],a[24:27]\nv_mfma_f32_16x16x32_bf16 a[52:55],a[200:203],a[252:255],a[52:55]\nv_mfma_f32_16x16x32_bf16 a[80:83],a[204:207],a[252:255],a[80:83]\nv_mfma_f32_16x16x32_bf16 a[108:111],a[208:211],a[252:255],a[108:111]\nds_read_b128 a[252:255],v239 offset:27648\ns_waitcnt lgkmcnt(2)\nv_mfma_f32_16x16x32_bf16 a[0:3],a[212:215],a[228:231],a[0:3]\nbuffer_load_dwordx4 v226,s[56:59],s73 offen offset:0 lds\n"
"s_mov_b32 s74,41984\nv_mfma_f32_16x16x32_bf16 a[28:31],a[216:219],a[228:231],a[28:31]\ns_waitcnt vmcnt(17)\nds_read_b64_tr_b16 a[196:197],v231 offset:8192\nv_mfma_f32_16x16x32_bf16 a[56:59],a[220:223],a[228:231],a[56:59]\nds_read_b64_tr_b16 a[198:199],v231 offset:9216\nv_mfma_f32_16x16x32_bf16 a[84:87],a[224:227],a[228:231],a[84:87]\nds_read_b128 a[228:231],v239 offset:28672\n"
"v_mfma_f32_16x16x32_bf16 a[4:7],a[212:215],a[232:235],a[4:7]\nbuffer_load_dwordx4 v226,s[56:59],s74 offen offset:1024 lds\ns_mov_b32 s75,33792\nv_mfma_f32_16x16x32_bf16 a[32:35],a[216:219],a[232:235],a[32:35]\nds_read_b64_tr_b16 a[200:201],v232 offset:8192\nv_mfma_f32_16x16x32_bf16 a[60:63],a[220:223],a[232:235],a[60:63]\nv_mfma_f32_16x16x32_bf16 a[88:91],a[224:227],a[232:235],a[88:91]\nds_read_b128 a[232:235],v239 offset:30720\n"
"v_mfma_f32_16x16x32_bf16 a[8:11],a[212:215],a[236:239],a[8:11]\nbuffer_load_dwordx4 v226,s[56:59],s75 offen offset:2048 lds\ns_mov_b32 s72,47104\nv_mfma_f32_16x16x32_bf16 a[36:39],a[216:219],a[236:239],a[36:39]\nds_read_b64_tr_b16 a[202:203],v232 offset:9216\nv_mfma_f32_16x16x32_bf16 a[64:67],a[220:223],a[236:239],a[64:67]\nv_mfma_f32_16x16x32_bf16 a[92:95],a[224:227],a[236:239],a[92:95]\nds_read_b128 a[236:239],v239 offset:32768\n"
"v_mfma_f32_16x16x32_bf16 a[12:15],a[212:215],a[240:243],a[12:15]\nbuffer_load_dwordx4 v226,s[56:59],s72 offen offset:3072 lds\nv_mfma_f32_16x16x32_bf16 a[40:43],a[216:219],a[240:243],a[40:43]\nds_read_b64_tr_b16 a[204:205],v233 offset:8192\nv_mfma_f32_16x16x32_bf16 a[68:71],a[220:223],a[240:243],a[68:71]\nds_read_b64_tr_b16 a[206:207],v233 offset:9216\nv_mfma_f32_16x16x32_bf16 a[96:99],a[224:227],a[240:243],a[96:99]\nds_read_b128 a[240:243],v239 offset:34816\n"
"v_mfma_f32_16x16x32_bf16 a[16:19],a[212:215],a[244:247],a[16:19]\nds_read_b64_tr_b16 a[208:209],v234 offset:8192\nv_mfma_f32_16x16x32_bf16 a[44:47],a[216:219],a[244:247],a[44:47]\nv_mfma_f32_16x16x32_bf16 a[72:75],a[220:223],a[244:247],a[72:75]\nv_mfma_f32_16x16x32_bf16 a[100:103],a[224:227],a[244:247],a[100:103]\nds_read_b128 a[244:247],v239 offset:36864\ns_waitcnt lgkmcnt(12)\nv_mfma_f32_16x16x32_bf16 a[20:23],a[212:215],a[248:251],a[20:23]\n"
"ds_read_b64_tr_b16 a[210:211],v234 offset:9216\ns_add_u32 m0,s64,8192\nv_mfma_f32_16x16x32_bf16 a[48:51],a[216:219],a[248:251],a[48:51]\ns_mov_b32 s73,57344\nv_mfma_f32_16x16x32_bf16 a[76:79],a[220:223],a[248:251],a[76:79]\nv_mfma_f32_16x16x32_bf16 a[104:107],a[224:227],a[248:251],a[104:107]\nds_read_b128 a[248:251],v239 offset:38912\nv_mfma_f32_16x16x32_bf16 a[24:27],a[212:215],a[252:255],a[24:27]\n"
"v_mfma_f32_16x16x32_bf16 a[52:55],a[216:219],a[252:255],a[52:55]\nv_mfma_f32_16x16x32_bf16 a[80:83],a[220:223],a[252:255],a[80:83]\nv_mfma_f32_16x16x32_bf16 a[108:111],a[224:227],a[252:255],a[108:111]\nds_read_b128 a[252:255],v239 offset:40960\ns_waitcnt lgkmcnt(2)\nv_mfma_f32_16x16x32_bf16 a[0:3],a[196:199],a[228:231],a[0:3]\nbuffer_load_dwordx4 v226,s[56:59],s73 offen offset:0 lds\ns_mov_b32 s74,70656\n"
"v_mfma_f32_16x16x32_bf16 a[28:31],a[200:203],a[228:231],a[28:31]\ns_waitcnt vmcnt(17)\nds_read_b64_tr_b16 a[212:213],v231 offset:12288\nv_mfma_f32_16x16x32_bf16 a[56:59],a[204:207],a[228:231],a[56:59]\nds_read_b64_tr_b16 a[214:215],v231 offset:13312\nv_mfma_f32_16x16x32_bf16 a[84:87],a[208:211],a[228:231],a[84:87]\nds_read_b128 a[228:231],v239 offset:29696\nv_mfma_f32_16x16x32_bf16 a[4:7],a[196:199],a[232:235],a[4:7]\n"
"buffer_load_dwordx4 v226,s[56:59],s74 offen offset:1024 lds\ns_mov_b32 s75,62464\nv_mfma_f32_16x16x32_bf16 a[32:35],a[200:203],a[232:235],a[32:35]\nds_read_b64_tr_b16 a[216:217],v232 offset:12288\nv_mfma_f32_16x16x32_bf16 a[60:63],a[204:207],a[232:235],a[60:63]\nv_mfma_f32_16x16x32_bf16 a[88:91],a[208:211],a[232:235],a[88:91]\nds_read_b128 a[232:235],v239 offset:31744\nv_mfma_f32_16x16x32_bf16 a[8:11],a[196:199],a[236:239],a[8:11]\n"
"buffer_load_dwordx4 v226,s[56:59],s75 offen offset:2048 lds\ns_mov_b32 s72,75776\nv_mfma_f32_16x16x32_bf16 a[36:39],a[200:203],a[236:239],a[36:39]\nds_read_b64_tr_b16 a[218:219],v232 offset:13312\nv_mfma_f32_16x16x32_bf16 a[64:67],a[204:207],a[236:239],a[64:67]\nv_mfma_f32_16x16x32_bf16 a[92:95],a[208:211],a[236:239],a[92:95]\nds_read_b128 a[236:239],v239 offset:33792\nv_mfma_f32_16x16x32_bf16 a[12:15],a[196:199],a[240:243],a[12:15]\n"
"buffer_load_dwordx4 v226,s[56:59],s72 offen offset:3072 lds\nv_mfma_f32_16x16x32_bf16 a[40:43],a[200:203],a[240:243],a[40:43]\nds_read_b64_tr_b16 a[220:221],v233 offset:12288\nv_mfma_f32_16x16x32_bf16 a[68:71],a[204:207],a[240:243],a[68:71]\nds_read_b64_tr_b16 a[222:223],v233 offset:13312\nv_mfma_f32_16x16x32_bf16 a[96:99],a[208:211],a[240:243],a[96:99]\nds_read_b128 a[240:243],v239 offset:35840\nv_mfma_f32_16x16x32_bf16 a[16:19],a[196:199],a[244:247],a[16:19]\n"
"ds_read_b64_tr_b16 a[224:225],v234 offset:12288\nv_mfma_f32_16x16x32_bf16 a[44:47],a[200:203],a[244:247],a[44:47]\nv_mfma_f32_16x16x32_bf16 a[72:75],a[204:207],a[244:247],a[72:75]\nv_mfma_f32_16x16x32_bf16 a[100:103],a[208:211],a[244:247],a[100:103]\nds_read_b128 a[244:247],v239 offset:37888\ns_waitcnt lgkmcnt(12)\nv_mfma_f32_16x16x32_bf16 a[20:23],a[196:199],a[248:251],a[20:23]\nds_read_b64_tr_b16 a[226:227],v234 offset:13312\n"
"s_add_u32 m0,s64,12288\nv_mfma_f32_16x16x32_bf16 a[48:51],a[200:203],a[248:251],a[48:51]\ns_mov_b32 s73,86016\nv_mfma_f32_16x16x32_bf16 a[76:79],a[204:207],a[248:251],a[76:79]\nv_mfma_f32_16x16x32_bf16 a[104:107],a[208:211],a[248:251],a[104:107]\nds_read_b128 a[248:251],v239 offset:39936\nv_mfma_f32_16x16x32_bf16 a[24:27],a[196:199],a[252:255],a[24:27]\nv_mfma_f32_16x16x32_bf16 a[52:55],a[200:203],a[252:255],a[52:55]\n"
"v_mfma_f32_16x16x32_bf16 a[80:83],a[204:207],a[252:255],a[80:83]\nv_mfma_f32_16x16x32_bf16 a[108:111],a[208:211],a[252:255],a[108:111]\nds_read_b128 a[252:255],v239 offset:41984\ns_waitcnt lgkmcnt(2)\nv_mfma_f32_16x16x32_bf16 a[0:3],a[212:215],a[228:231],a[0:3]\nbuffer_load_dwordx4 v226,s[56:59],s73 offen offset:0 lds\ns_mov_b32 s74,99328\nv_mfma_f32_16x16x32_bf16 a[28:31],a[216:219],a[228:231],a[28:31]\n"
"s_waitcnt vmcnt(17)\nds_read_b64_tr_b16 a[196:197],v231 offset:16384\nv_mfma_f32_16x16x32_bf16 a[56:59],a[220:223],a[228:231],a[56:59]\nds_read_b64_tr_b16 a[198:199],v231 offset:17408\nv_mfma_f32_16x16x32_bf16 a[84:87],a[224:227],a[228:231],a[84:87]\nds_read_b128 a[228:231],v239 offset:43008\nv_mfma_f32_16x16x32_bf16 a[4:7],a[212:215],a[232:235],a[4:7]\nbuffer_load_dwordx4 v226,s[56:59],s74 offen offset:1024 lds\n"
"s_mov_b32 s75,91136\nv_mfma_f32_16x16x32_bf16 a[32:35],a[216:219],a[232:235],a[32:35]\nds_read_b64_tr_b16 a[200:201],v232 offset:16384\nv_mfma_f32_16x16x32_bf16 a[60:63],a[220:223],a[232:235],a[60:63]\nv_mfma_f32_16x16x32_bf16 a[88:91],a[224:227],a[232:235],a[88:91]\nds_read_b128 a[232:235],v239 offset:45056\nv_mfma_f32_16x16x32_bf16 a[8:11],a[212:215],a[236:239],a[8:11]\nbuffer_load_dwordx4 v226,s[56:59],s75 offen offset:2048 lds\n"
"s_mov_b32 s72,104448\nv_mfma_f32_16x16x32_bf16 a[36:39],a[216:219],a[236:239],a[36:39]\nds_read_b64_tr_b16 a[202:203],v232 offset:17408\nv_mfma_f32_16x16x32_bf16 a[64:67],a[220:223],a[236:239],a[64:67]\nv_mfma_f32_16x16x32_bf16 a[92:95],a[224:227],a[236:239],a[92:95]\nds_read_b128 a[236:239],v239 offset:47104\nv_mfma_f32_16x16x32_bf16 a[12:15],a[212:215],a[240:243],a[12:15]\nbuffer_load_dwordx4 v226,s[56:59],s72 offen offset:3072 lds\n"
"v_mfma_f32_16x16x32_bf16 a[40:43],a[216:219],a[240:243],a[40:43]\nds_read_b64_tr_b16 a[204:205],v233 offset:16384\nv_mfma_f32_16x16x32_bf16 a[68:71],a[220:223],a[240:243],a[68:71]\nds_read_b64_tr_b16 a[206:207],v233 offset:17408\nv_mfma_f32_16x16x32_bf16 a[96:99],a[224:227],a[240:243],a[96:99]\nds_read_b128 a[240:243],v239 offset:49152\nv_mfma_f32_16x16x32_bf16 a[16:19],a[212:215],a[244:247],a[16:19]\nds_read_b64_tr_b16 a[208:209],v234 offset:16384\n"
"v_mfma_f32_16x16x32_bf16 a[44:47],a[216:219],a[244:247],a[44:47]\nv_mfma_f32_16x16x32_bf16 a[72:75],a[220:223],a[244:247],a[72:75]\nv_mfma_f32_16x16x32_bf16 a[100:103],a[224:227],a[244:247],a[100:103]\nds_read_b128 a[244:247],v239 offset:51200\ns_waitcnt lgkmcnt(12)\nv_mfma_f32_16x16x32_bf16 a[20:23],a[212:215],a[248:251],a[20:23]\nds_read_b64_tr_b16 a[210:211],v234 offset:17408\ns_add_u32 m0,s64,16384\n"
"v_mfma_f32_16x16x32_bf16 a[48:51],a[216:219],a[248:251],a[48:51]\ns_mov_b32 s73,114688\nv_mfma_f32_16x16x32_bf16 a[76:79],a[220:223],a[248:251],a[76:79]\nv_mfma_f32_16x16x32_bf16 a[104:107],a[224:227],a[248:251],a[104:107]\nds_read_b128 a[248:251],v239 offset:53248\nv_mfma_f32_16x16x32_bf16 a[24:27],a[212:215],a[252:255],a[24:27]\nv_mfma_f32_16x16x32_bf16 a[52:55],a[216:219],a[252:255],a[52:55]\nv_mfma_f32_16x16x32_bf16 a[80:83],a[220:223],a[252:255],a[80:83]\n"
"v_mfma_f32_16x16x32_bf16 a[108:111],a[224:227],a[252:255],a[108:111]\nds_read_b128 a[252:255],v239 offset:55296\ns_waitcnt lgkmcnt(2)\nv_mfma_f32_16x16x32_bf16 a[0:3],a[196:199],a[228:231],a[0:3]\nbuffer_load_dwordx4 v226,s[56:59],s73 offen offset:0 lds\ns_mov_b32 s74,128000\nv_mfma_f32_16x16x32_bf16 a[28:31],a[200:203],a[228:231],a[28:31]\ns_waitcnt vmcnt(17)\n"
"ds_read_b64_tr_b16 a[212:213],v231 offset:20480\nv_mfma_f32_16x16x32_bf16 a[56:59],a[204:207],a[228:231],a[56:59]\nds_read_b64_tr_b16 a[214:215],v231 offset:21504\nv_mfma_f32_16x16x32_bf16 a[84:87],a[208:211],a[228:231],a[84:87]\nds_read_b128 a[228:231],v239 offset:44032\nv_mfma_f32_16x16x32_bf16 a[4:7],a[196:199],a[232:235],a[4:7]\nbuffer_load_dwordx4 v226,s[56:59],s74 offen offset:1024 lds\ns_mov_b32 s75,119808\n"
"v_mfma_f32_16x16x32_bf16 a[32:35],a[200:203],a[232:235],a[32:35]\nds_read_b64_tr_b16 a[216:217],v232 offset:20480\nv_mfma_f32_16x16x32_bf16 a[60:63],a[204:207],a[232:235],a[60:63]\nv_mfma_f32_16x16x32_bf16 a[88:91],a[208:211],a[232:235],a[88:91]\nds_read_b128 a[232:235],v239 offset:46080\nv_mfma_f32_16x16x32_bf16 a[8:11],a[196:199],a[236:239],a[8:11]\nbuffer_load_dwordx4 v226,s[56:59],s75 offen offset:2048 lds\ns_mov_b32 s72,133120\n"
"v_mfma_f32_16x16x32_bf16 a[36:39],a[200:203],a[236:239],a[36:39]\nds_read_b64_tr_b16 a[218:219],v232 offset:21504\nv_mfma_f32_16x16x32_bf16 a[64:67],a[204:207],a[236:239],a[64:67]\nv_mfma_f32_16x16x32_bf16 a[92:95],a[208:211],a[236:239],a[92:95]\nds_read_b128 a[236:239],v239 offset:48128\nv_mfma_f32_16x16x32_bf16 a[12:15],a[196:199],a[240:243],a[12:15]\nbuffer_load_dwordx4 v226,s[56:59],s72 offen offset:3072 lds\nv_mfma_f32_16x16x32_bf16 a[40:43],a[200:203],a[240:243],a[40:43]\n"
"ds_read_b64_tr_b16 a[220:221],v233 offset:20480\nv_mfma_f32_16x16x32_bf16 a[68:71],a[204:207],a[240:243],a[68:71]\nds_read_b64_tr_b16 a[222:223],v233 offset:21504\nv_mfma_f32_16x16x32_bf16 a[96:99],a[208:211],a[240:243],a[96:99]\nds_read_b128 a[240:243],v239 offset:50176\nv_mfma_f32_16x16x32_bf16 a[16:19],a[196:199],a[244:247],a[16:19]\nds_read_b64_tr_b16 a[224:225],v234 offset:20480\nv_mfma_f32_16x16x32_bf16 a[44:47],a[200:203],a[244:247],a[44:47]\n"
"v_mfma_f32_16x16x32_bf16 a[72:75],a[204:207],a[244:247],a[72:75]\nv_mfma_f32_16x16x32_bf16 a[100:103],a[208:211],a[244:247],a[100:103]\nds_read_b128 a[244:247],v239 offset:52224\ns_waitcnt lgkmcnt(12)\nv_mfma_f32_16x16x32_bf16 a[20:23],a[196:199],a[248:251],a[20:23]\nds_read_b64_tr_b16 a[226:227],v234 offset:21504\ns_add_u32 m0,s64,20480\nv_mfma_f32_16x16x32_bf16 a[48:51],a[200:203],a[248:251],a[48:51]\n"
"s_mov_b32 s73,143360\nv_mfma_f32_16x16x32_bf16 a[76:79],a[204:207],a[248:251],a[76:79]\nv_mfma_f32_16x16x32_bf16 a[104:107],a[208:211],a[248:251],a[104:107]\nds_read_b128 a[248:251],v239 offset:54272\nv_mfma_f32_16x16x32_bf16 a[24:27],a[196:199],a[252:255],a[24:27]\nv_mfma_f32_16x16x32_bf16 a[52:55],a[200:203],a[252:255],a[52:55]\nv_mfma_f32_16x16x32_bf16 a[80:83],a[204:207],a[252:255],a[80:83]\nv_mfma_f32_16x16x32_bf16 a[108:111],a[208:211],a[252:255],a[108:111]\n"
"ds_read_b128 a[252:255],v239 offset:56320\ns_waitcnt lgkmcnt(2)\nv_mfma_f32_16x16x32_bf16 a[0:3],a[212:215],a[228:231],a[0:3]\nbuffer_load_dwordx4 v226,s[56:59],s73 offen offset:0 lds\ns_mov_b32 s74,156672\nv_mfma_f32_16x16x32_bf16 a[28:31],a[216:219],a[228:231],a[28:31]\ns_waitcnt vmcnt(17)\nds_read_b64_tr_b16 a[196:197],v231 offset:0\n"
"v_mfma_f32_16x16x32_bf16 a[56:59],a[220:223],a[228:231],a[56:59]\nv_mfma_f32_16x16x32_bf16 a[84:87],a[224:227],a[228:231],a[84:87]\nds_read_b128 a[228:231],v239 offset:0\nv_mfma_f32_16x16x32_bf16 a[4:7],a[212:215],a[232:235],a[4:7]\nbuffer_load_dwordx4 v226,s[56:59],s74 offen offset:1024 lds\ns_mov_b32 s75,148480\nv_mfma_f32_16x16x32_bf16 a[32:35],a[216:219],a[232:235],a[32:35]\nds_read_b64_tr_b16 a[198:199],v231 offset:1024\n"
"v_mfma_f32_16x16x32_bf16 a[60:63],a[220:223],a[232:235],a[60:63]\nv_mfma_f32_16x16x32_bf16 a[88:91],a[224:227],a[232:235],a[88:91]\nds_read_b128 a[232:235],v239 offset:2048\nv_mfma_f32_16x16x32_bf16 a[8:11],a[212:215],a[236:239],a[8:11]\nbuffer_load_dwordx4 v226,s[56:59],s75 offen offset:2048 lds\ns_mov_b32 s72,161792\nv_mfma_f32_16x16x32_bf16 a[36:39],a[216:219],a[236:239],a[36:39]\nds_read_b64_tr_b16 a[200:201],v232 offset:0\n"
"v_mfma_f32_16x16x32_bf16 a[64:67],a[220:223],a[236:239],a[64:67]\nv_mfma_f32_16x16x32_bf16 a[92:95],a[224:227],a[236:239],a[92:95]\nds_read_b128 a[236:239],v239 offset:4096\nv_mfma_f32_16x16x32_bf16 a[12:15],a[212:215],a[240:243],a[12:15]\nbuffer_load_dwordx4 v226,s[56:59],s72 offen offset:3072 lds\nv_mfma_f32_16x16x32_bf16 a[40:43],a[216:219],a[240:243],a[40:43]\nds_read_b64_tr_b16 a[202:203],v232 offset:1024\nv_mfma_f32_16x16x32_bf16 a[68:71],a[220:223],a[240:243],a[68:71]\n"
"v_mfma_f32_16x16x32_bf16 a[96:99],a[224:227],a[240:243],a[96:99]\nds_read_b128 a[240:243],v239 offset:6144\nv_mfma_f32_16x16x32_bf16 a[16:19],a[212:215],a[244:247],a[16:19]\nds_read_b64_tr_b16 a[204:205],v233 offset:0\nv_mfma_f32_16x16x32_bf16 a[44:47],a[216:219],a[244:247],a[44:47]\nv_mfma_f32_16x16x32_bf16 a[72:75],a[220:223],a[244:247],a[72:75]\nv_mfma_f32_16x16x32_bf16 a[100:103],a[224:227],a[244:247],a[100:103]\nds_read_b128 a[244:247],v239 offset:8192\n"
"s_waitcnt lgkmcnt(10)\nv_mfma_f32_16x16x32_bf16 a[20:23],a[212:215],a[248:251],a[20:23]\nds_read_b64_tr_b16 a[206:207],v233 offset:1024\ns_add_u32 m0,s64,0\nv_mfma_f32_16x16x32_bf16 a[48:51],a[216:219],a[248:251],a[48:51]\ns_mov_b32 s73,172032\nv_mfma_f32_16x16x32_bf16 a[76:79],a[220:223],a[248:251],a[76:79]\nv_mfma_f32_16x16x32_bf16 a[104:107],a[224:227],a[248:251],a[104:107]\n"
"ds_read_b128 a[248:251],v239 offset:10240\nv_mfma_f32_16x16x32_bf16 a[24:27],a[212:215],a[252:255],a[24:27]\nv_mfma_f32_16x16x32_bf16 a[52:55],a[216:219],a[252:255],a[52:55]\nv_mfma_f32_16x16x32_bf16 a[80:83],a[220:223],a[252:255],a[80:83]\nv_mfma_f32_16x16x32_bf16 a[108:111],a[224:227],a[252:255],a[108:111]\nds_read_b128 a[252:255],v239 offset:12288\ns_waitcnt lgkmcnt(2)\nv_mfma_f32_16x16x32_bf16 a[112:115],a[196:199],a[228:231],0\n"
"buffer_load_dwordx4 v226,s[56:59],s73 offen offset:0 lds\ns_mov_b32 s74,185344\nv_mfma_f32_16x16x32_bf16 a[140:143],a[200:203],a[228:231],0\ns_waitcnt vmcnt(17)\nds_read_b64_tr_b16 a[212:213],v231 offset:4096\nv_mfma_f32_16x16x32_bf16 a[168:171],a[204:207],a[228:231],0\nds_read_b128 a[228:231],v239 offset:1024\nv_mfma_f32_16x16x32_bf16 a[116:119],a[196:199],a[232:235],0\n"
"buffer_load_dwordx4 v226,s[56:59],s74 offen offset:1024 lds\ns_mov_b32 s75,177152\nv_mfma_f32_16x16x32_bf16 a[144:147],a[200:203],a[232:235],0\nds_read_b64_tr_b16 a[214:215],v231 offset:5120\nv_mfma_f32_16x16x32_bf16 a[172:175],a[204:207],a[232:235],0\nds_read_b128 a[232:235],v239 offset:3072\nv_mfma_f32_16x16x32_bf16 a[120:123],a[196:199],a[236:239],0\nbuffer_load_dwordx4 v226,s[56:59],s75 offen offset:2048 lds\n"
"s_mov_b32 s72,190464\nv_mfma_f32_16x16x32_bf16 a[148:151],a[200:203],a[236:239],0\nds_read_b64_tr_b16 a[216:217],v232 offset:4096\nv_mfma_f32_16x16x32_bf16 a[176:179],a[204:207],a[236:239],0\nds_read_b128 a[236:239],v239 offset:5120\nv_mfma_f32_16x16x32_bf16 a[124:127],a[196:199],a[240:243],0\nbuffer_load_dwordx4 v226,s[56:59],s72 offen offset:3072 lds\nv_mfma_f32_16x16x32_bf16 a[152:155],a[200:203],a[240:243],0\n"
"ds_read_b64_tr_b16 a[218:219],v232 offset:5120\nv_mfma_f32_16x16x32_bf16 a[180:183],a[204:207],a[240:243],0\nds_read_b128 a[240:243],v239 offset:7168\nv_mfma_f32_16x16x32_bf16 a[128:131],a[196:199],a[244:247],0\nds_read_b64_tr_b16 a[220:221],v233 offset:4096\nv_mfma_f32_16x16x32_bf16 a[156:159],a[200:203],a[244:247],0\nv_mfma_f32_16x16x32_bf16 a[184:187],a[204:207],a[244:247],0\nds_read_b128 a[244:247],v239 offset:9216\n"
"s_waitcnt lgkmcnt(10)\nv_mfma_f32_16x16x32_bf16 a[132:135],a[196:199],a[248:251],0\nds_read_b64_tr_b16 a[222:223],v233 offset:5120\ns_add_u32 m0,s64,4096\nv_mfma_f32_16x16x32_bf16 a[160:163],a[200:203],a[248:251],0\ns_mov_b32 s73,200704\nv_mfma_f32_16x16x32_bf16 a[188:191],a[204:207],a[248:251],0\nds_read_b128 a[248:251],v239 offset:11264\n"
"v_mfma_f32_16x16x32_bf16 a[136:139],a[196:199],a[252:255],0\nv_mfma_f32_16x16x32_bf16 a[164:167],a[200:203],a[252:255],0\nv_mfma_f32_16x16x32_bf16 a[192:195],a[204:207],a[252:255],0\nds_read_b128 a[252:255],v239 offset:13312\ns_waitcnt lgkmcnt(2)\nv_mfma_f32_16x16x32_bf16 a[112:115],a[212:215],a[228:231],a[112:115]\nbuffer_load_dwordx4 v226,s[56:59],s73 offen offset:0 lds\ns_mov_b32 s74,214016\n"
"v_mfma_f32_16x16x32_bf16 a[140:143],a[216:219],a[228:231],a[140:143]\ns_waitcnt vmcnt(17)\nds_read_b64_tr_b16 a[196:197],v231 offset:8192\nv_mfma_f32_16x16x32_bf16 a[168:171],a[220:223],a[228:231],a[168:171]\ns_waitcnt lgkmcnt(1)\ns_barrier\nds_read_b128 a[228:231],v239 offset:14336\nv_mfma_f32_16x16x32_bf16 a[116:119],a[212:215],a[232:235],a[116:119]\n"
"buffer_load_dwordx4 v226,s[56:59],s74 offen offset:1024 lds\ns_add_u32 m0,s65,0\nv_mfma_f32_16x16x32_bf16 a[144:147],a[216:219],a[232:235],a[144:147]\nds_read_b64_tr_b16 a[198:199],v231 offset:9216\ns_mov_b32 s75,0\nv_mfma_f32_16x16x32_bf16 a[172:175],a[220:223],a[232:235],a[172:175]\nds_read_b128 a[232:235],v239 offset:16384\nv_mfma_f32_16x16x32_bf16 a[120:123],a[212:215],a[236:239],a[120:123]\n"
"buffer_load_dwordx4 v229,s[40:43],s75 offen offset:0 lds\ns_mov_b32 s72,6144\nv_mfma_f32_16x16x32_bf16 a[148:151],a[216:219],a[236:239],a[148:151]\nds_read_b64_tr_b16 a[200:201],v232 offset:8192\nv_mfma_f32_16x16x32_bf16 a[176:179],a[220:223],a[236:239],a[176:179]\nds_read_b128 a[236:239],v239 offset:18432\nv_mfma_f32_16x16x32_bf16 a[124:127],a[212:215],a[240:243],a[124:127]\nbuffer_load_dwordx4 v230,s[40:43],s72 offen offset:1024 lds\n"
"s_add_u32 m0,s66,0\nv_mfma_f32_16x16x32_bf16 a[152:155],a[216:219],a[240:243],a[152:155]\nds_read_b64_tr_b16 a[202:203],v232 offset:9216\ns_add_u32 s73,s67,0\nv_mfma_f32_16x16x32_bf16 a[180:183],a[220:223],a[240:243],a[180:183]\nds_read_b128 a[240:243],v239 offset:20480\nv_mfma_f32_16x16x32_bf16 a[128:131],a[212:215],a[244:247],a[128:131]\nbuffer_load_dwordx4 v229,s[40:43],s73 offen offset:0 lds\n"
"s_add_u32 s74,s67,6144\nv_mfma_f32_16x16x32_bf16 a[156:159],a[216:219],a[244:247],a[156:159]\nds_read_b64_tr_b16 a[204:205],v233 offset:8192\nv_mfma_f32_16x16x32_bf16 a[184:187],a[220:223],a[244:247],a[184:187]\nds_read_b128 a[244:247],v239 offset:22528\nv_mfma_f32_16x16x32_bf16 a[132:135],a[212:215],a[248:251],a[132:135]\nbuffer_load_dwordx4 v230,s[40:43],s74 offen offset:1024 lds\ns_add_u32 m0,s64,4096\n"
"v_mfma_f32_16x16x32_bf16 a[160:163],a[216:219],a[248:251],a[160:163]\nds_read_b64_tr_b16 a[206:207],v233 offset:9216\ns_mov_b32 s75,205824\nv_mfma_f32_16x16x32_bf16 a[188:191],a[220:223],a[248:251],a[188:191]\nds_read_b128 a[248:251],v239 offset:24576\nv_mfma_f32_16x16x32_bf16 a[136:139],a[212:215],a[252:255],a[136:139]\nbuffer_load_dwordx4 v226,s[56:59],s75 offen offset:2048 lds\ns_mov_b32 s72,219136\n"
"v_mfma_f32_16x16x32_bf16 a[164:167],a[216:219],a[252:255],a[164:167]\nv_mfma_f32_16x16x32_bf16 a[192:195],a[220:223],a[252:255],a[192:195]\nds_read_b128 a[252:255],v239 offset:26624\ns_waitcnt lgkmcnt(2)\nv_mfma_f32_16x16x32_bf16 a[112:115],a[196:199],a[228:231],a[112:115]\nbuffer_load_dwordx4 v226,s[56:59],s72 offen offset:3072 lds\ns_add_u32 m0,s64,8192\nv_mfma_f32_16x16x32_bf16 a[140:143],a[200:203],a[228:231],a[140:143]\n"
"s_waitcnt vmcnt(20)\nds_read_b64_tr_b16 a[212:213],v231 offset:12288\nv_mfma_f32_16x16x32_bf16 a[168:171],a[204:207],a[228:231],a[168:171]\nds_read_b128 a[228:231],v239 offset:15360\ns_mov_b32 s73,512\nv_mfma_f32_16x16x32_bf16 a[116:119],a[196:199],a[232:235],a[116:119]\nbuffer_load_dwordx4 v224,s[44:47],s73 offen offset:0 lds\ns_mov_b32 s74,32256\n"
"v_mfma_f32_16x16x32_bf16 a[144:147],a[200:203],a[232:235],a[144:147]\nds_read_b64_tr_b16 a[214:215],v231 offset:13312\nv_mfma_f32_16x16x32_bf16 a[172:175],a[204:207],a[232:235],a[172:175]\nds_read_b128 a[232:235],v239 offset:17408\nv_mfma_f32_16x16x32_bf16 a[120:123],a[196:199],a[236:239],a[120:123]\nbuffer_load_dwordx4 v224,s[44:47],s74 offen offset:1024 lds\ns_mov_b32 s75,129536\nv_mfma_f32_16x16x32_bf16 a[148:151],a[200:203],a[236:239],a[148:151]\n"
"ds_read_b64_tr_b16 a[216:217],v232 offset:12288\nv_mfma_f32_16x16x32_bf16 a[176:179],a[204:207],a[236:239],a[176:179]\nds_read_b128 a[236:239],v239 offset:19456\nv_mfma_f32_16x16x32_bf16 a[124:127],a[196:199],a[240:243],a[124:127]\nbuffer_load_dwordx4 v224,s[44:47],s75 offen offset:2048 lds\ns_mov_b32 s72,161280\nv_mfma_f32_16x16x32_bf16 a[152:155],a[200:203],a[240:243],a[152:155]\nds_read_b64_tr_b16 a[218:219],v232 offset:13312\n"
"v_mfma_f32_16x16x32_bf16 a[180:183],a[204:207],a[240:243],a[180:183]\nds_read_b128 a[240:243],v239 offset:21504\nv_mfma_f32_16x16x32_bf16 a[128:131],a[196:199],a[244:247],a[128:131]\nbuffer_load_dwordx4 v224,s[44:47],s72 offen offset:3072 lds\nv_mfma_f32_16x16x32_bf16 a[156:159],a[200:203],a[244:247],a[156:159]\nds_read_b64_tr_b16 a[220:221],v233 offset:12288\nv_mfma_f32_16x16x32_bf16 a[184:187],a[204:207],a[244:247],a[184:187]\nds_read_b128 a[244:247],v239 offset:23552\n"
"s_waitcnt lgkmcnt(10)\nv_mfma_f32_16x16x32_bf16 a[132:135],a[196:199],a[248:251],a[132:135]\nds_read_b64_tr_b16 a[222:223],v233 offset:13312\ns_add_u32 m0,s64,12288\nv_mfma_f32_16x16x32_bf16 a[160:163],a[200:203],a[248:251],a[160:163]\ns_mov_b32 s73,262656\nv_mfma_f32_16x16x32_bf16 a[188:191],a[204:207],a[248:251],a[188:191]\nds_read_b128 a[248:251],v239 offset:25600\n"
"v_mfma_f32_16x16x32_bf16 a[136:139],a[196:199],a[252:255],a[136:139]\nv_mfma_f32_16x16x32_bf16 a[164:167],a[200:203],a[252:255],a[164:167]\nv_mfma_f32_16x16x32_bf16 a[192:195],a[204:207],a[252:255],a[192:195]\nds_read_b128 a[252:255],v239 offset:27648\ns_waitcnt lgkmcnt(2)\nv_mfma_f32_16x16x32_bf16 a[112:115],a[212:215],a[228:231],a[112:115]\nbuffer_load_dwordx4 v224,s[44:47],s73 offen offset:0 lds\ns_mov_b32 s74,294400\n"
"v_mfma_f32_16x16x32_bf16 a[140:143],a[216:219],a[228:231],a[140:143]\ns_waitcnt vmcnt(21)\nds_read_b64_tr_b16 a[196:197],v231 offset:16384\nv_mfma_f32_16x16x32_bf16 a[168:171],a[220:223],a[228:231],a[168:171]\ns_waitcnt lgkmcnt(1)\ns_barrier\nds_read_b128 a[228:231],v239 offset:28672\nv_mfma_f32_16x16x32_bf16 a[116:119],a[212:215],a[232:235],a[116:119]\n"
"buffer_load_dwordx4 v224,s[44:47],s74 offen offset:1024 lds\ns_add_u32 m0,s65,14336\nv_mfma_f32_16x16x32_bf16 a[144:147],a[216:219],a[232:235],a[144:147]\nds_read_b64_tr_b16 a[198:199],v231 offset:17408\ns_mov_b32 s75,128\nv_mfma_f32_16x16x32_bf16 a[172:175],a[220:223],a[232:235],a[172:175]\nds_read_b128 a[232:235],v239 offset:30720\nv_mfma_f32_16x16x32_bf16 a[120:123],a[212:215],a[236:239],a[120:123]\n"
"buffer_load_dwordx4 v229,s[40:43],s75 offen offset:0 lds\ns_mov_b32 s72,6272\nv_mfma_f32_16x16x32_bf16 a[148:151],a[216:219],a[236:239],a[148:151]\nds_read_b64_tr_b16 a[200:201],v232 offset:16384\nv_mfma_f32_16x16x32_bf16 a[176:179],a[220:223],a[236:239],a[176:179]\nds_read_b128 a[236:239],v239 offset:32768\nv_mfma_f32_16x16x32_bf16 a[124:127],a[212:215],a[240:243],a[124:127]\nbuffer_load_dwordx4 v230,s[40:43],s72 offen offset:1024 lds\n"
"s_add_u32 m0,s66,14336\nv_mfma_f32_16x16x32_bf16 a[152:155],a[216:219],a[240:243],a[152:155]\nds_read_b64_tr_b16 a[202:203],v232 offset:17408\ns_add_u32 s73,s67,128\nv_mfma_f32_16x16x32_bf16 a[180:183],a[220:223],a[240:243],a[180:183]\nds_read_b128 a[240:243],v239 offset:34816\nv_mfma_f32_16x16x32_bf16 a[128:131],a[212:215],a[244:247],a[128:131]\nbuffer_load_dwordx4 v229,s[40:43],s73 offen offset:0 lds\n"
"s_add_u32 s74,s67,6272\nv_mfma_f32_16x16x32_bf16 a[156:159],a[216:219],a[244:247],a[156:159]\nds_read_b64_tr_b16 a[204:205],v233 offset:16384\nv_mfma_f32_16x16x32_bf16 a[184:187],a[220:223],a[244:247],a[184:187]\nds_read_b128 a[244:247],v239 offset:36864\nv_mfma_f32_16x16x32_bf16 a[132:135],a[212:215],a[248:251],a[132:135]\nbuffer_load_dwordx4 v230,s[40:43],s74 offen offset:1024 lds\ns_add_u32 m0,s64,12288\n"
"v_mfma_f32_16x16x32_bf16 a[160:163],a[216:219],a[248:251],a[160:163]\nds_read_b64_tr_b16 a[206:207],v233 offset:17408\ns_mov_b32 s75,391680\nv_mfma_f32_16x16x32_bf16 a[188:191],a[220:223],a[248:251],a[188:191]\nds_read_b128 a[248:251],v239 offset:38912\nv_mfma_f32_16x16x32_bf16 a[136:139],a[212:215],a[252:255],a[136:139]\nbuffer_load_dwordx4 v224,s[44:47],s75 offen offset:2048 lds\ns_mov_b32 s72,423424\n"
"v_mfma_f32_16x16x32_bf16 a[164:167],a[216:219],a[252:255],a[164:167]\nv_mfma_f32_16x16x32_bf16 a[192:195],a[220:223],a[252:255],a[192:195]\nds_read_b128 a[252:255],v239 offset:40960\ns_waitcnt lgkmcnt(2)\nv_mfma_f32_16x16x32_bf16 a[112:115],a[196:199],a[228:231],a[112:115]\nbuffer_load_dwordx4 v224,s[44:47],s72 offen offset:3072 lds\ns_add_u32 m0,s64,16384\nv_mfma_f32_16x16x32_bf16 a[140:143],a[200:203],a[228:231],a[140:143]\n"
"s_waitcnt vmcnt(24)\nds_read_b64_tr_b16 a[212:213],v231 offset:20480\nv_mfma_f32_16x16x32_bf16 a[168:171],a[204:207],a[228:231],a[168:171]\nds_read_b128 a[228:231],v239 offset:29696\ns_mov_b32 s73,524800\nv_mfma_f32_16x16x32_bf16 a[116:119],a[196:199],a[232:235],a[116:119]\nbuffer_load_dwordx4 v224,s[44:47],s73 offen offset:0 lds\ns_mov_b32 s74,556544\n"
"v_mfma_f32_16x16x32_bf16 a[144:147],a[200:203],a[232:235],a[144:147]\nds_read_b64_tr_b16 a[214:215],v231 offset:21504\nv_mfma_f32_16x16x32_bf16 a[172:175],a[204:207],a[232:235],a[172:175]\nds_read_b128 a[232:235],v239 offset:31744\nv_mfma_f32_16x16x32_bf16 a[120:123],a[196:199],a[236:239],a[120:123]\nbuffer_load_dwordx4 v224,s[44:47],s74 offen offset:1024 lds\ns_mov_b32 s75,653824\nv_mfma_f32_16x16x32_bf16 a[148:151],a[200:203],a[236:239],a[148:151]\n"
"ds_read_b64_tr_b16 a[216:217],v232 offset:20480\nv_mfma_f32_16x16x32_bf16 a[176:179],a[204:207],a[236:239],a[176:179]\nds_read_b128 a[236:239],v239 offset:33792\nv_mfma_f32_16x16x32_bf16 a[124:127],a[196:199],a[240:243],a[124:127]\nbuffer_load_dwordx4 v224,s[44:47],s75 offen offset:2048 lds\ns_mov_b32 s72,685568\nv_mfma_f32_16x16x32_bf16 a[152:155],a[200:203],a[240:243],a[152:155]\nds_read_b64_tr_b16 a[218:219],v232 offset:21504\n"
"v_mfma_f32_16x16x32_bf16 a[180:183],a[204:207],a[240:243],a[180:183]\nds_read_b128 a[240:243],v239 offset:35840\nv_mfma_f32_16x16x32_bf16 a[128:131],a[196:199],a[244:247],a[128:131]\nbuffer_load_dwordx4 v224,s[44:47],s72 offen offset:3072 lds\nv_mfma_f32_16x16x32_bf16 a[156:159],a[200:203],a[244:247],a[156:159]\nds_read_b64_tr_b16 a[220:221],v233 offset:20480\nv_mfma_f32_16x16x32_bf16 a[184:187],a[204:207],a[244:247],a[184:187]\nds_read_b128 a[244:247],v239 offset:37888\n"
"s_waitcnt lgkmcnt(10)\nv_mfma_f32_16x16x32_bf16 a[132:135],a[196:199],a[248:251],a[132:135]\nds_read_b64_tr_b16 a[222:223],v233 offset:21504\ns_add_u32 m0,s64,20480\nv_mfma_f32_16x16x32_bf16 a[160:163],a[200:203],a[248:251],a[160:163]\ns_mov_b32 s73,786944\nv_mfma_f32_16x16x32_bf16 a[188:191],a[204:207],a[248:251],a[188:191]\nds_read_b128 a[248:251],v239 offset:39936\n"
"v_mfma_f32_16x16x32_bf16 a[136:139],a[196:199],a[252:255],a[136:139]\nv_mfma_f32_16x16x32_bf16 a[164:167],a[200:203],a[252:255],a[164:167]\nv_mfma_f32_16x16x32_bf16 a[192:195],a[204:207],a[252:255],a[192:195]\nds_read_b128 a[252:255],v239 offset:41984\ns_waitcnt lgkmcnt(2)\nv_mfma_f32_16x16x32_bf16 a[112:115],a[212:215],a[228:231],a[112:115]\nbuffer_load_dwordx4 v224,s[44:47],s73 offen offset:0 lds\ns_mov_b32 s74,818688\n"
"v_mfma_f32_16x16x32_bf16 a[140:143],a[216:219],a[228:231],a[140:143]\ns_waitcnt vmcnt(25)\nds_read_b64_tr_b16 a[196:197],v231 offset:0\nv_mfma_f32_16x16x32_bf16 a[168:171],a[220:223],a[228:231],a[168:171]\ns_waitcnt lgkmcnt(1)\ns_barrier\nds_read_b128 a[228:231],v239 offset:43008\nv_mfma_f32_16x16x32_bf16 a[116:119],a[212:215],a[232:235],a[116:119]\n"
"buffer_load_dwordx4 v224,s[44:47],s74 offen offset:1024 lds\ns_add_u32 m0,s65,28672\nv_mfma_f32_16x16x32_bf16 a[144:147],a[216:219],a[232:235],a[144:147]\nds_read_b64_tr_b16 a[198:199],v231 offset:1024\ns_mov_b32 s75,256\nv_mfma_f32_16x16x32_bf16 a[172:175],a[220:223],a[232:235],a[172:175]\nds_read_b128 a[232:235],v239 offset:45056\nv_mfma_f32_16x16x32_bf16 a[120:123],a[212:215],a[236:239],a[120:123]\n"
"buffer_load_dwordx4 v229,s[40:43],s75 offen offset:0 lds\ns_mov_b32 s72,6400\nv_mfma_f32_16x16x32_bf16 a[148:151],a[216:219],a[236:239],a[148:151]\nds_read_b64_tr_b16 a[200:201],v232 offset:0\nv_mfma_f32_16x16x32_bf16 a[176:179],a[220:223],a[236:239],a[176:179]\nds_read_b128 a[236:239],v239 offset:47104\nv_mfma_f32_16x16x32_bf16 a[124:127],a[212:215],a[240:243],a[124:127]\nbuffer_load_dwordx4 v230,s[40:43],s72 offen offset:1024 lds\n"
"s_add_u32 m0,s66,28672\nv_mfma_f32_16x16x32_bf16 a[152:155],a[216:219],a[240:243],a[152:155]\nds_read_b64_tr_b16 a[202:203],v232 offset:1024\ns_add_u32 s73,s67,256\nv_mfma_f32_16x16x32_bf16 a[180:183],a[220:223],a[240:243],a[180:183]\nds_read_b128 a[240:243],v239 offset:49152\nv_mfma_f32_16x16x32_bf16 a[128:131],a[212:215],a[244:247],a[128:131]\nbuffer_load_dwordx4 v229,s[40:43],s73 offen offset:0 lds\n"
"s_add_u32 s74,s67,6400\nv_mfma_f32_16x16x32_bf16 a[156:159],a[216:219],a[244:247],a[156:159]\nds_read_b64_tr_b16 a[204:205],v233 offset:0\nv_mfma_f32_16x16x32_bf16 a[184:187],a[220:223],a[244:247],a[184:187]\nds_read_b128 a[244:247],v239 offset:51200\nv_mfma_f32_16x16x32_bf16 a[132:135],a[212:215],a[248:251],a[132:135]\nbuffer_load_dwordx4 v230,s[40:43],s74 offen offset:1024 lds\ns_add_u32 m0,s64,20480\n"
"v_mfma_f32_16x16x32_bf16 a[160:163],a[216:219],a[248:251],a[160:163]\nds_read_b64_tr_b16 a[206:207],v233 offset:1024\ns_mov_b32 s75,915968\nv_mfma_f32_16x16x32_bf16 a[188:191],a[220:223],a[248:251],a[188:191]\nds_read_b128 a[248:251],v239 offset:53248\nv_mfma_f32_16x16x32_bf16 a[136:139],a[212:215],a[252:255],a[136:139]\nbuffer_load_dwordx4 v224,s[44:47],s75 offen offset:2048 lds\ns_mov_b32 s72,947712\n"
"v_mfma_f32_16x16x32_bf16 a[164:167],a[216:219],a[252:255],a[164:167]\nv_mfma_f32_16x16x32_bf16 a[192:195],a[220:223],a[252:255],a[192:195]\nds_read_b128 a[252:255],v239 offset:55296\ns_waitcnt lgkmcnt(2)\nv_mfma_f32_16x16x32_bf16 a[112:115],a[196:199],a[228:231],a[112:115]\nbuffer_load_dwordx4 v224,s[44:47],s72 offen offset:3072 lds\ns_add_u32 m0,s64,0\nv_mfma_f32_16x16x32_bf16 a[140:143],a[200:203],a[228:231],a[140:143]\n"
"s_waitcnt vmcnt(24)\nds_read_b64_tr_b16 a[212:213],v231 offset:4096\nv_mfma_f32_16x16x32_bf16 a[168:171],a[204:207],a[228:231],a[168:171]\nds_read_b128 a[228:231],v239 offset:44032\ns_mov_b32 s73,1049088\nv_mfma_f32_16x16x32_bf16 a[116:119],a[196:199],a[232:235],a[116:119]\nbuffer_load_dwordx4 v224,s[44:47],s73 offen offset:0 lds\ns_mov_b32 s74,1080832\n"
"v_mfma_f32_16x16x32_bf16 a[144:147],a[200:203],a[232:235],a[144:147]\nds_read_b64_tr_b16 a[214:215],v231 offset:5120\nv_mfma_f32_16x16x32_bf16 a[172:175],a[204:207],a[232:235],a[172:175]\nds_read_b128 a[232:235],v239 offset:46080\nv_mfma_f32_16x16x32_bf16 a[120:123],a[196:199],a[236:239],a[120:123]\nbuffer_load_dwordx4 v224,s[44:47],s74 offen offset:1024 lds\ns_mov_b32 s75,1178112\nv_mfma_f32_16x16x32_bf16 a[148:151],a[200:203],a[236:239],a[148:151]\n"
"ds_read_b64_tr_b16 a[216:217],v232 offset:4096\nv_mfma_f32_16x16x32_bf16 a[176:179],a[204:207],a[236:239],a[176:179]\nds_read_b128 a[236:239],v239 offset:48128\nv_mfma_f32_16x16x32_bf16 a[124:127],a[196:199],a[240:243],a[124:127]\nbuffer_load_dwordx4 v224,s[44:47],s75 offen offset:2048 lds\ns_mov_b32 s72,1209856\nv_mfma_f32_16x16x32_bf16 a[152:155],a[200:203],a[240:243],a[152:155]\nds_read_b64_tr_b16 a[218:219],v232 offset:5120\n"
"v_mfma_f32_16x16x32_bf16 a[180:183],a[204:207],a[240:243],a[180:183]\nds_read_b128 a[240:243],v239 offset:50176\nv_mfma_f32_16x16x32_bf16 a[128:131],a[196:199],a[244:247],a[128:131]\nbuffer_load_dwordx4 v224,s[44:47],s72 offen offset:3072 lds\nv_mfma_f32_16x16x32_bf16 a[156:159],a[200:203],a[244:247],a[156:159]\nds_read_b64_tr_b16 a[220:221],v233 offset:4096\nv_mfma_f32_16x16x32_bf16 a[184:187],a[204:207],a[244:247],a[184:187]\nds_read_b128 a[244:247],v239 offset:52224\n"
"s_waitcnt lgkmcnt(10)\nv_mfma_f32_16x16x32_bf16 a[132:135],a[196:199],a[248:251],a[132:135]\nds_read_b64_tr_b16 a[222:223],v233 offset:5120\ns_add_u32 m0,s64,4096\nv_mfma_f32_16x16x32_bf16 a[160:163],a[200:203],a[248:251],a[160:163]\ns_mov_b32 s73,1311232\nv_mfma_f32_16x16x32_bf16 a[188:191],a[204:207],a[248:251],a[188:191]\nds_read_b128 a[248:251],v239 offset:54272\n"
"v_mfma_f32_16x16x32_bf16 a[136:139],a[196:199],a[252:255],a[136:139]\nv_mfma_f32_16x16x32_bf16 a[164:167],a[200:203],a[252:255],a[164:167]\nv_mfma_f32_16x16x32_bf16 a[192:195],a[204:207],a[252:255],a[192:195]\nds_read_b128 a[252:255],v239 offset:56320\ns_waitcnt lgkmcnt(2)\nv_mfma_f32_16x16x32_bf16 a[112:115],a[212:215],a[228:231],a[112:115]\ns_waitcnt vmcnt(24)\nds_read_b64_tr_b16 a[196:197],v231 offset:8192\n"
"v_mfma_f32_16x16x32_bf16 a[140:143],a[216:219],a[228:231],a[140:143]\nds_read_b64_tr_b16 a[198:199],v231 offset:9216\nv_mfma_f32_16x16x32_bf16 a[168:171],a[220:223],a[228:231],a[168:171]\ns_waitcnt lgkmcnt(2)\ns_barrier\nds_read_b128 a[228:231],v237 offset:0\nv_mfma_f32_16x16x32_bf16 a[116:119],a[212:215],a[232:235],a[116:119]\nbuffer_load_dwordx4 v224,s[44:47],s73 offen offset:0 lds\n"
"s_add_u32 m0,s65,43008\nv_mfma_f32_16x16x32_bf16 a[144:147],a[216:219],a[232:235],a[144:147]\nds_read_b64_tr_b16 a[200:201],v232 offset:8192\ns_mov_b32 s74,384\nv_mfma_f32_16x16x32_bf16 a[172:175],a[220:223],a[232:235],a[172:175]\nds_read_b128 a[232:235],v237 offset:2048\nv_mfma_f32_16x16x32_bf16 a[120:123],a[212:215],a[236:239],a[120:123]\nbuffer_load_dwordx4 v229,s[40:43],s74 offen offset:0 lds\n"
"s_mov_b32 s75,6528\nv_mfma_f32_16x16x32_bf16 a[148:151],a[216:219],a[236:239],a[148:151]\nds_read_b64_tr_b16 a[202:203],v232 offset:9216\nv_mfma_f32_16x16x32_bf16 a[176:179],a[220:223],a[236:239],a[176:179]\nds_read_b128 a[236:239],v237 offset:4096\nv_mfma_f32_16x16x32_bf16 a[124:127],a[212:215],a[240:243],a[124:127]\nds_read_b64_tr_b16 a[204:205],v233 offset:8192\nv_mfma_f32_16x16x32_bf16 a[152:155],a[216:219],a[240:243],a[152:155]\n"
"ds_read_b64_tr_b16 a[206:207],v233 offset:9216\nv_mfma_f32_16x16x32_bf16 a[180:183],a[220:223],a[240:243],a[180:183]\nds_read_b128 a[240:243],v237 offset:6144\nv_mfma_f32_16x16x32_bf16 a[128:131],a[212:215],a[244:247],a[128:131]\nbuffer_load_dwordx4 v230,s[40:43],s75 offen offset:1024 lds\ns_add_u32 m0,s66,43008\nv_mfma_f32_16x16x32_bf16 a[156:159],a[216:219],a[244:247],a[156:159]\nds_read_b64_tr_b16 a[208:209],v234 offset:8192\n"
"s_add_u32 s72,s67,384\nv_mfma_f32_16x16x32_bf16 a[184:187],a[220:223],a[244:247],a[184:187]\nds_read_b128 a[244:247],v237 offset:8192\nv_mfma_f32_16x16x32_bf16 a[132:135],a[212:215],a[248:251],a[132:135]\nbuffer_load_dwordx4 v229,s[40:43],s72 offen offset:0 lds\ns_add_u32 s73,s67,6528\nv_mfma_f32_16x16x32_bf16 a[160:163],a[216:219],a[248:251],a[160:163]\nds_read_b64_tr_b16 a[210:211],v234 offset:9216\n"
"v_mfma_f32_16x16x32_bf16 a[188:191],a[220:223],a[248:251],a[188:191]\nds_read_b128 a[248:251],v237 offset:10240\nv_mfma_f32_16x16x32_bf16 a[136:139],a[212:215],a[252:255],a[136:139]\nbuffer_load_dwordx4 v230,s[40:43],s73 offen offset:1024 lds\ns_add_u32 m0,s64,4096\nv_mfma_f32_16x16x32_bf16 a[164:167],a[216:219],a[252:255],a[164:167]\ns_mov_b32 s74,1342976\nv_mfma_f32_16x16x32_bf16 a[192:195],a[220:223],a[252:255],a[192:195]\n"
"ds_read_b128 a[252:255],v237 offset:12288\ns_waitcnt lgkmcnt(2)\nv_mfma_f32_16x16x32_bf16 v[0:3],a[196:199],a[228:231],0\nbuffer_load_dwordx4 v224,s[44:47],s74 offen offset:1024 lds\ns_mov_b32 s75,1440256\nv_mfma_f32_16x16x32_bf16 v[28:31],a[200:203],a[228:231],0\ns_waitcnt vmcnt(22)\nds_read_b64_tr_b16 a[212:213],v231 offset:12288\n"
"v_mfma_f32_16x16x32_bf16 v[56:59],a[204:207],a[228:231],0\nds_read_b64_tr_b16 a[214:215],v231 offset:13312\nv_mfma_f32_16x16x32_bf16 v[84:87],a[208:211],a[228:231],0\nds_read_b128 a[228:231],v238 offset:0\nv_mfma_f32_16x16x32_bf16 v[4:7],a[196:199],a[232:235],0\nbuffer_load_dwordx4 v224,s[44:47],s75 offen offset:2048 lds\ns_mov_b32 s72,1472000\nv_mfma_f32_16x16x32_bf16 v[32:35],a[200:203],a[232:235],0\n"
"ds_read_b64_tr_b16 a[216:217],v232 offset:12288\nv_mfma_f32_16x16x32_bf16 v[60:63],a[204:207],a[232:235],0\nv_mfma_f32_16x16x32_bf16 v[88:91],a[208:211],a[232:235],0\nds_read_b128 a[232:235],v238 offset:2048\nv_mfma_f32_16x16x32_bf16 v[8:11],a[196:199],a[236:239],0\nbuffer_load_dwordx4 v224,s[44:47],s72 offen offset:3072 lds\ns_add_u32 m0,s64,8192\nv_mfma_f32_16x16x32_bf16 v[36:39],a[200:203],a[236:239],0\n"
"ds_read_b64_tr_b16 a[218:219],v232 offset:13312\ns_mov_b32 s73,1573376\nv_mfma_f32_16x16x32_bf16 v[64:67],a[204:207],a[236:239],0\nv_mfma_f32_16x16x32_bf16 v[92:95],a[208:211],a[236:239],0\nds_read_b128 a[236:239],v238 offset:4096\nv_mfma_f32_16x16x32_bf16 v[12:15],a[196:199],a[240:243],0\nbuffer_load_dwordx4 v224,s[44:47],s73 offen offset:0 lds\ns_mov_b32 s74,1605120\n"
"v_mfma_f32_16x16x32_bf16 v[40:43],a[200:203],a[240:243],0\nds_read_b64_tr_b16 a[220:221],v233 offset:12288\nv_mfma_f32_16x16x32_bf16 v[68:71],a[204:207],a[240:243],0\nds_read_b64_tr_b16 a[222:223],v233 offset:13312\nv_mfma_f32_16x16x32_bf16 v[96:99],a[208:211],a[240:243],0\nds_read_b128 a[240:243],v238 offset:6144\nv_mfma_f32_16x16x32_bf16 v[16:19],a[196:199],a[244:247],0\nbuffer_load_dwordx4 v224,s[44:47],s74 offen offset:1024 lds\n"
"s_mov_b32 s75,1702400\nv_mfma_f32_16x16x32_bf16 v[44:47],a[200:203],a[244:247],0\nds_read_b64_tr_b16 a[224:225],v234 offset:12288\nv_mfma_f32_16x16x32_bf16 v[72:75],a[204:207],a[244:247],0\nv_mfma_f32_16x16x32_bf16 v[100:103],a[208:211],a[244:247],0\nds_read_b128 a[244:247],v238 offset:8192\ns_waitcnt lgkmcnt(12)\nv_mfma_f32_16x16x32_bf16 v[20:23],a[196:199],a[248:251],0\n"
"buffer_load_dwordx4 v224,s[44:47],s75 offen offset:2048 lds\ns_mov_b32 s72,1734144\nv_mfma_f32_16x16x32_bf16 v[48:51],a[200:203],a[248:251],0\nds_read_b64_tr_b16 a[226:227],v234 offset:13312\nv_mfma_f32_16x16x32_bf16 v[76:79],a[204:207],a[248:251],0\nv_mfma_f32_16x16x32_bf16 v[104:107],a[208:211],a[248:251],0\nds_read_b128 a[248:251],v238 offset:10240\nv_mfma_f32_16x16x32_bf16 v[24:27],a[196:199],a[252:255],0\n"
"buffer_load_dwordx4 v224,s[44:47],s72 offen offset:3072 lds\ns_add_u32 m0,s64,12288\nv_mfma_f32_16x16x32_bf16 v[52:55],a[200:203],a[252:255],0\ns_mov_b32 s73,1835520\nv_mfma_f32_16x16x32_bf16 v[80:83],a[204:207],a[252:255],0\nv_mfma_f32_16x16x32_bf16 v[108:111],a[208:211],a[252:255],0\nds_read_b128 a[252:255],v238 offset:12288\ns_waitcnt lgkmcnt(2)\n"
"v_mfma_f32_16x16x32_bf16 v[0:3],a[212:215],a[228:231],v[0:3]\nbuffer_load_dwordx4 v224,s[44:47],s73 offen offset:0 lds\nv_mov_b32 v250,0xff800000\nv_mfma_f32_16x16x32_bf16 v[28:31],a[216:219],a[228:231],v[28:31]\ns_waitcnt vmcnt(25)\nds_read_b64_tr_b16 a[196:197],v231 offset:16384\nv_mfma_f32_16x16x32_bf16 v[56:59],a[220:223],a[228:231],v[56:59]\nds_read_b64_tr_b16 a[198:199],v231 offset:17408\n"
"v_lshrrev_b32 v248,2,v239\nv_mfma_f32_16x16x32_bf16 v[84:87],a[224:227],a[228:231],v[84:87]\ns_waitcnt lgkmcnt(2)\ns_barrier\nds_read_b128 a[228:231],v237 offset:14336\nv_mfma_f32_16x16x32_bf16 v[4:7],a[212:215],a[232:235],v[4:7]\ns_mov_b32 s74,1867264\nbuffer_load_dwordx4 v224,s[44:47],s74 offen offset:1024 lds\n"
"v_mfma_f32_16x16x32_bf16 v[32:35],a[216:219],a[232:235],v[32:35]\nds_read_b64_tr_b16 a[200:201],v232 offset:16384\nv_lshrrev_b32 v242,9,v239\nv_mfma_f32_16x16x32_bf16 v[60:63],a[220:223],a[232:235],v[60:63]\ns_add_u32 m0,s65,0\nv_and_b32 v241,0xf0,v239\nv_lshl_or_b32 v241,v242,8,v241\nv_mfma_f32_16x16x32_bf16 v[88:91],a[224:227],a[232:235],v[88:91]\n"
"ds_read_b128 a[232:235],v237 offset:16384\nv_bfe_u32 v242,v239,8,1\nv_mfma_f32_16x16x32_bf16 v[8:11],a[212:215],a[236:239],v[8:11]\ns_mov_b32 s75,512\nbuffer_load_dwordx4 v229,s[40:43],s75 offen offset:0 lds\nv_mfma_f32_16x16x32_bf16 v[36:39],a[216:219],a[236:239],v[36:39]\nds_read_b64_tr_b16 a[202:203],v232 offset:17408\nv_lshl_or_b32 v241,v242,3,v241\n"
"v_mfma_f32_16x16x32_bf16 v[64:67],a[220:223],a[236:239],v[64:67]\ns_mov_b32 s72,6656\nv_mov_b32 v242,14336\nv_mad_u32_u24 v241,v242,s70,v241\nv_mfma_f32_16x16x32_bf16 v[92:95],a[224:227],a[236:239],v[92:95]\nds_read_b128 a[236:239],v237 offset:18432\nv_mfma_f32_16x16x32_bf16 v[12:15],a[212:215],a[240:243],v[12:15]\nbuffer_load_dwordx4 v230,s[40:43],s72 offen offset:1024 lds\n"
"s_add_u32 m0,s66,0\nv_mfma_f32_16x16x32_bf16 v[40:43],a[216:219],a[240:243],v[40:43]\nds_read_b64_tr_b16 a[204:205],v233 offset:16384\ns_add_u32 s73,s67,512\nv_mfma_f32_16x16x32_bf16 v[68:71],a[220:223],a[240:243],v[68:71]\nds_read_b64_tr_b16 a[206:207],v233 offset:17408\nv_mfma_f32_16x16x32_bf16 v[96:99],a[224:227],a[240:243],v[96:99]\nds_read_b128 a[240:243],v237 offset:20480\n"
"v_mfma_f32_16x16x32_bf16 v[16:19],a[212:215],a[244:247],v[16:19]\nbuffer_load_dwordx4 v229,s[40:43],s73 offen offset:0 lds\ns_add_u32 s74,s67,6656\nv_mfma_f32_16x16x32_bf16 v[44:47],a[216:219],a[244:247],v[44:47]\nds_read_b64_tr_b16 a[208:209],v234 offset:16384\nv_mfma_f32_16x16x32_bf16 v[72:75],a[220:223],a[244:247],v[72:75]\nv_mfma_f32_16x16x32_bf16 v[100:103],a[224:227],a[244:247],v[100:103]\nds_read_b128 a[244:247],v237 offset:22528\n"
"v_mfma_f32_16x16x32_bf16 v[20:23],a[212:215],a[248:251],v[20:23]\nbuffer_load_dwordx4 v230,s[40:43],s74 offen offset:1024 lds\ns_add_u32 m0,s64,12288\nv_mfma_f32_16x16x32_bf16 v[48:51],a[216:219],a[248:251],v[48:51]\nds_read_b64_tr_b16 a[210:211],v234 offset:17408\ns_mov_b32 s75,1964544\nv_mfma_f32_16x16x32_bf16 v[76:79],a[220:223],a[248:251],v[76:79]\nv_mfma_f32_16x16x32_bf16 v[104:107],a[224:227],a[248:251],v[104:107]\n"
"ds_read_b128 a[248:251],v237 offset:24576\nv_mfma_f32_16x16x32_bf16 v[24:27],a[212:215],a[252:255],v[24:27]\nbuffer_load_dwordx4 v224,s[44:47],s75 offen offset:2048 lds\ns_mov_b32 s72,1996288\nv_mfma_f32_16x16x32_bf16 v[52:55],a[216:219],a[252:255],v[52:55]\nv_mfma_f32_16x16x32_bf16 v[80:83],a[220:223],a[252:255],v[80:83]\nv_mfma_f32_16x16x32_bf16 v[108:111],a[224:227],a[252:255],v[108:111]\nds_read_b128 a[252:255],v237 offset:26624\n"
"s_waitcnt lgkmcnt(2)\nv_mfma_f32_16x16x32_bf16 v[0:3],a[196:199],a[228:231],v[0:3]\nbuffer_load_dwordx4 v224,s[44:47],s72 offen offset:3072 lds\ns_add_u32 m0,s64,16384\nv_mfma_f32_16x16x32_bf16 v[28:31],a[200:203],a[228:231],v[28:31]\ns_waitcnt vmcnt(24)\nds_read_b64_tr_b16 a[212:213],v231 offset:20480\nv_mfma_f32_16x16x32_bf16 v[56:59],a[204:207],a[228:231],v[56:59]\n"
"ds_read_b64_tr_b16 a[214:215],v231 offset:21504\ns_mov_b32 s73,2097664\nv_mfma_f32_16x16x32_bf16 v[84:87],a[208:211],a[228:231],v[84:87]\nds_read_b128 a[228:231],v238 offset:14336\nv_mfma_f32_16x16x32_bf16 v[4:7],a[196:199],a[232:235],v[4:7]\nbuffer_load_dwordx4 v224,s[44:47],s73 offen offset:0 lds\ns_mov_b32 s74,2129408\nv_mfma_f32_16x16x32_bf16 v[32:35],a[200:203],a[232:235],v[32:35]\n"
"ds_read_b64_tr_b16 a[216:217],v232 offset:20480\nv_mfma_f32_16x16x32_bf16 v[60:63],a[204:207],a[232:235],v[60:63]\nv_mfma_f32_16x16x32_bf16 v[88:91],a[208:211],a[232:235],v[88:91]\nds_read_b128 a[232:235],v238 offset:16384\nv_mfma_f32_16x16x32_bf16 v[8:11],a[196:199],a[236:239],v[8:11]\nbuffer_load_dwordx4 v224,s[44:47],s74 offen offset:1024 lds\ns_mov_b32 s75,2226688\nv_mfma_f32_16x16x32_bf16 v[36:39],a[200:203],a[236:239],v[36:39]\n"
"ds_read_b64_tr_b16 a[218:219],v232 offset:21504\nv_mfma_f32_16x16x32_bf16 v[64:67],a[204:207],a[236:239],v[64:67]\nv_mfma_f32_16x16x32_bf16 v[92:95],a[208:211],a[236:239],v[92:95]\nds_read_b128 a[236:239],v238 offset:18432\nv_mfma_f32_16x16x32_bf16 v[12:15],a[196:199],a[240:243],v[12:15]\nbuffer_load_dwordx4 v224,s[44:47],s75 offen offset:2048 lds\ns_mov_b32 s72,2258432\nv_mfma_f32_16x16x32_bf16 v[40:43],a[200:203],a[240:243],v[40:43]\n"
"ds_read_b64_tr_b16 a[220:221],v233 offset:20480\nv_mfma_f32_16x16x32_bf16 v[68:71],a[204:207],a[240:243],v[68:71]\nds_read_b64_tr_b16 a[222:223],v233 offset:21504\nv_mfma_f32_16x16x32_bf16 v[96:99],a[208:211],a[240:243],v[96:99]\nds_read_b128 a[240:243],v238 offset:20480\nv_mfma_f32_16x16x32_bf16 v[16:19],a[196:199],a[244:247],v[16:19]\nbuffer_load_dwordx4 v224,s[44:47],s72 offen offset:3072 lds\nv_mfma_f32_16x16x32_bf16 v[44:47],a[200:203],a[244:247],v[44:47]\n"
"ds_read_b64_tr_b16 a[224:225],v234 offset:20480\nv_mfma_f32_16x16x32_bf16 v[72:75],a[204:207],a[244:247],v[72:75]\nv_mfma_f32_16x16x32_bf16 v[100:103],a[208:211],a[244:247],v[100:103]\nds_read_b128 a[244:247],v238 offset:22528\ns_waitcnt lgkmcnt(12)\nv_mfma_f32_16x16x32_bf16 v[20:23],a[196:199],a[248:251],v[20:23]\nds_read_b64_tr_b16 a[226:227],v234 offset:21504\ns_add_u32 m0,s64,20480\n"
"v_mfma_f32_16x16x32_bf16 v[48:51],a[200:203],a[248:251],v[48:51]\ns_mov_b32 s73,2359808\nv_mfma_f32_16x16x32_bf16 v[76:79],a[204:207],a[248:251],v[76:79]\nv_mfma_f32_16x16x32_bf16 v[104:107],a[208:211],a[248:251],v[104:107]\nds_read_b128 a[248:251],v238 offset:24576\nv_mfma_f32_16x16x32_bf16 v[24:27],a[196:199],a[252:255],v[24:27]\nv_mfma_f32_16x16x32_bf16 v[52:55],a[200:203],a[252:255],v[52:55]\nv_mfma_f32_16x16x32_bf16 v[80:83],a[204:207],a[252:255],v[80:83]\n"
"v_mfma_f32_16x16x32_bf16 v[108:111],a[208:211],a[252:255],v[108:111]\nds_read_b128 a[252:255],v238 offset:26624\ns_waitcnt lgkmcnt(2)\nv_mfma_f32_16x16x32_bf16 v[0:3],a[212:215],a[228:231],v[0:3]\nbuffer_load_dwordx4 v224,s[44:47],s73 offen offset:0 lds\ns_mov_b32 s74,2391552\nv_mfma_f32_16x16x32_bf16 v[28:31],a[216:219],a[228:231],v[28:31]\ns_waitcnt vmcnt(25)\n"
"ds_read_b64_tr_b16 a[196:197],v231 offset:0\nv_mfma_f32_16x16x32_bf16 v[56:59],a[220:223],a[228:231],v[56:59]\nds_read_b64_tr_b16 a[198:199],v231 offset:1024\nv_mfma_f32_16x16x32_bf16 v[84:87],a[224:227],a[228:231],v[84:87]\ns_waitcnt lgkmcnt(2)\ns_barrier\nds_read_b128 a[228:231],v237 offset:28672\nv_mfma_f32_16x16x32_bf16 v[4:7],a[212:215],a[232:235],v[4:7]\n"
"buffer_load_dwordx4 v224,s[44:47],s74 offen offset:1024 lds\ns_add_u32 m0,s65,14336\nv_mfma_f32_16x16x32_bf16 v[32:35],a[216:219],a[232:235],v[32:35]\nds_read_b64_tr_b16 a[200:201],v232 offset:0\ns_mov_b32 s75,640\nv_mfma_f32_16x16x32_bf16 v[60:63],a[220:223],a[232:235],v[60:63]\nv_mfma_f32_16x16x32_bf16 v[88:91],a[224:227],a[232:235],v[88:91]\nds_read_b128 a[232:235],v237 offset:30720\n"
"v_mfma_f32_16x16x32_bf16 v[8:11],a[212:215],a[236:239],v[8:11]\nbuffer_load_dwordx4 v229,s[40:43],s75 offen offset:0 lds\ns_mov_b32 s72,6784\nv_mfma_f32_16x16x32_bf16 v[36:39],a[216:219],a[236:239],v[36:39]\nds_read_b64_tr_b16 a[202:203],v232 offset:1024\nv_mfma_f32_16x16x32_bf16 v[64:67],a[220:223],a[236:239],v[64:67]\nv_mfma_f32_16x16x32_bf16 v[92:95],a[224:227],a[236:239],v[92:95]\nds_read_b128 a[236:239],v237 offset:32768\n"
"v_mfma_f32_16x16x32_bf16 v[12:15],a[212:215],a[240:243],v[12:15]\nbuffer_load_dwordx4 v230,s[40:43],s72 offen offset:1024 lds\ns_add_u32 m0,s66,14336\nv_mfma_f32_16x16x32_bf16 v[40:43],a[216:219],a[240:243],v[40:43]\nds_read_b64_tr_b16 a[204:205],v233 offset:0\ns_add_u32 s73,s67,640\nv_mfma_f32_16x16x32_bf16 v[68:71],a[220:223],a[240:243],v[68:71]\nds_read_b64_tr_b16 a[206:207],v233 offset:1024\n"
"v_mfma_f32_16x16x32_bf16 v[96:99],a[224:227],a[240:243],v[96:99]\nds_read_b128 a[240:243],v237 offset:34816\nv_mfma_f32_16x16x32_bf16 v[16:19],a[212:215],a[244:247],v[16:19]\nbuffer_load_dwordx4 v229,s[40:43],s73 offen offset:0 lds\ns_add_u32 s74,s67,6784\nv_mfma_f32_16x16x32_bf16 v[44:47],a[216:219],a[244:247],v[44:47]\nds_read_b64_tr_b16 a[208:209],v234 offset:0\nv_mfma_f32_16x16x32_bf16 v[72:75],a[220:223],a[244:247],v[72:75]\n"
"v_mfma_f32_16x16x32_bf16 v[100:103],a[224:227],a[244:247],v[100:103]\nds_read_b128 a[244:247],v237 offset:36864\nv_mfma_f32_16x16x32_bf16 v[20:23],a[212:215],a[248:251],v[20:23]\nbuffer_load_dwordx4 v230,s[40:43],s74 offen offset:1024 lds\ns_add_u32 m0,s64,20480\nv_mfma_f32_16x16x32_bf16 v[48:51],a[216:219],a[248:251],v[48:51]\nds_read_b64_tr_b16 a[210:211],v234 offset:1024\ns_mov_b32 s75,2488832\n"
"v_mfma_f32_16x16x32_bf16 v[76:79],a[220:223],a[248:251],v[76:79]\nv_mfma_f32_16x16x32_bf16 v[104:107],a[224:227],a[248:251],v[104:107]\nds_read_b128 a[248:251],v237 offset:38912\nv_mfma_f32_16x16x32_bf16 v[24:27],a[212:215],a[252:255],v[24:27]\nbuffer_load_dwordx4 v224,s[44:47],s75 offen offset:2048 lds\ns_mov_b32 s72,2520576\nv_mfma_f32_16x16x32_bf16 v[52:55],a[216:219],a[252:255],v[52:55]\nv_mfma_f32_16x16x32_bf16 v[80:83],a[220:223],a[252:255],v[80:83]\n"
"v_mfma_f32_16x16x32_bf16 v[108:111],a[224:227],a[252:255],v[108:111]\nds_read_b128 a[252:255],v237 offset:40960\ns_waitcnt lgkmcnt(2)\nv_mfma_f32_16x16x32_bf16 v[0:3],a[196:199],a[228:231],v[0:3]\nbuffer_load_dwordx4 v224,s[44:47],s72 offen offset:3072 lds\ns_add_u32 m0,s64,0\nv_mfma_f32_16x16x32_bf16 v[28:31],a[200:203],a[228:231],v[28:31]\ns_waitcnt vmcnt(24)\n"
"ds_read_b64_tr_b16 a[212:213],v231 offset:4096\nv_mfma_f32_16x16x32_bf16 v[56:59],a[204:207],a[228:231],v[56:59]\nds_read_b64_tr_b16 a[214:215],v231 offset:5120\ns_mov_b32 s73,2621952\nv_mfma_f32_16x16x32_bf16 v[84:87],a[208:211],a[228:231],v[84:87]\nds_read_b128 a[228:231],v238 offset:28672\nv_mfma_f32_16x16x32_bf16 v[4:7],a[196:199],a[232:235],v[4:7]\nbuffer_load_dwordx4 v224,s[44:47],s73 offen offset:0 lds\n"
"s_mov_b32 s74,2653696\nv_mfma_f32_16x16x32_bf16 v[32:35],a[200:203],a[232:235],v[32:35]\nds_read_b64_tr_b16 a[216:217],v232 offset:4096\nv_mfma_f32_16x16x32_bf16 v[60:63],a[204:207],a[232:235],v[60:63]\nv_mfma_f32_16x16x32_bf16 v[88:91],a[208:211],a[232:235],v[88:91]\nds_read_b128 a[232:235],v238 offset:30720\nv_mfma_f32_16x16x32_bf16 v[8:11],a[196:199],a[236:239],v[8:11]\nbuffer_load_dwordx4 v224,s[44:47],s74 offen offset:1024 lds\n"
"s_mov_b32 s75,2750976\nv_mfma_f32_16x16x32_bf16 v[36:39],a[200:203],a[236:239],v[36:39]\nds_read_b64_tr_b16 a[218:219],v232 offset:5120\nv_mfma_f32_16x16x32_bf16 v[64:67],a[204:207],a[236:239],v[64:67]\nv_mfma_f32_16x16x32_bf16 v[92:95],a[208:211],a[236:239],v[92:95]\nds_read_b128 a[236:239],v238 offset:32768\nv_mfma_f32_16x16x32_bf16 v[12:15],a[196:199],a[240:243],v[12:15]\nbuffer_load_dwordx4 v224,s[44:47],s75 offen offset:2048 lds\n"
"s_mov_b32 s72,2782720\nv_mfma_f32_16x16x32_bf16 v[40:43],a[200:203],a[240:243],v[40:43]\nds_read_b64_tr_b16 a[220:221],v233 offset:4096\nv_mfma_f32_16x16x32_bf16 v[68:71],a[204:207],a[240:243],v[68:71]\nds_read_b64_tr_b16 a[222:223],v233 offset:5120\nv_mfma_f32_16x16x32_bf16 v[96:99],a[208:211],a[240:243],v[96:99]\nds_read_b128 a[240:243],v238 offset:34816\nv_mfma_f32_16x16x32_bf16 v[16:19],a[196:199],a[244:247],v[16:19]\n"
"buffer_load_dwordx4 v224,s[44:47],s72 offen offset:3072 lds\nv_mfma_f32_16x16x32_bf16 v[44:47],a[200:203],a[244:247],v[44:47]\nds_read_b64_tr_b16 a[224:225],v234 offset:4096\nv_mfma_f32_16x16x32_bf16 v[72:75],a[204:207],a[244:247],v[72:75]\nv_mfma_f32_16x16x32_bf16 v[100:103],a[208:211],a[244:247],v[100:103]\nds_read_b128 a[244:247],v238 offset:36864\ns_waitcnt lgkmcnt(12)\nv_mfma_f32_16x16x32_bf16 v[20:23],a[196:199],a[248:251],v[20:23]\n"
"ds_read_b64_tr_b16 a[226:227],v234 offset:5120\ns_add_u32 m0,s64,4096\nv_mfma_f32_16x16x32_bf16 v[48:51],a[200:203],a[248:251],v[48:51]\ns_mov_b32 s73,2884096\nv_mfma_f32_16x16x32_bf16 v[76:79],a[204:207],a[248:251],v[76:79]\nv_mfma_f32_16x16x32_bf16 v[104:107],a[208:211],a[248:251],v[104:107]\nds_read_b128 a[248:251],v238 offset:38912\nv_mfma_f32_16x16x32_bf16 v[24:27],a[196:199],a[252:255],v[24:27]\n"
"v_mfma_f32_16x16x32_bf16 v[52:55],a[200:203],a[252:255],v[52:55]\nv_mfma_f32_16x16x32_bf16 v[80:83],a[204:207],a[252:255],v[80:83]\nv_mfma_f32_16x16x32_bf16 v[108:111],a[208:211],a[252:255],v[108:111]\nds_read_b128 a[252:255],v238 offset:40960\ns_waitcnt lgkmcnt(2)\nv_mfma_f32_16x16x32_bf16 v[0:3],a[212:215],a[228:231],v[0:3]\nbuffer_load_dwordx4 v224,s[44:47],s73 offen offset:0 lds\ns_mov_b32 s74,2915840\n"
"v_mfma_f32_16x16x32_bf16 v[28:31],a[216:219],a[228:231],v[28:31]\ns_waitcnt vmcnt(25)\nds_read_b64_tr_b16 a[196:197],v231 offset:8192\nv_mfma_f32_16x16x32_bf16 v[56:59],a[220:223],a[228:231],v[56:59]\nds_read_b64_tr_b16 a[198:199],v231 offset:9216\nv_mfma_f32_16x16x32_bf16 v[84:87],a[224:227],a[228:231],v[84:87]\ns_waitcnt lgkmcnt(2)\ns_barrier\n"
"ds_read_b128 a[228:231],v237 offset:43008\nv_mfma_f32_16x16x32_bf16 v[4:7],a[212:215],a[232:235],v[4:7]\nbuffer_load_dwordx4 v224,s[44:47],s74 offen offset:1024 lds\ns_add_u32 m0,s65,28672\nv_mfma_f32_16x16x32_bf16 v[32:35],a[216:219],a[232:235],v[32:35]\nds_read_b64_tr_b16 a[200:201],v232 offset:8192\ns_mov_b32 s75,768\nv_mfma_f32_16x16x32_bf16 v[60:63],a[220:223],a[232:235],v[60:63]\n"
"v_mfma_f32_16x16x32_bf16 v[88:91],a[224:227],a[232:235],v[88:91]\nds_read_b128 a[232:235],v237 offset:45056\nv_mfma_f32_16x16x32_bf16 v[8:11],a[212:215],a[236:239],v[8:11]\nbuffer_load_dwordx4 v229,s[40:43],s75 offen offset:0 lds\ns_mov_b32 s72,6912\nv_mfma_f32_16x16x32_bf16 v[36:39],a[216:219],a[236:239],v[36:39]\nds_read_b64_tr_b16 a[202:203],v232 offset:9216\nv_mfma_f32_16x16x32_bf16 v[64:67],a[220:223],a[236:239],v[64:67]\n"
"v_mfma_f32_16x16x32_bf16 v[92:95],a[224:227],a[236:239],v[92:95]\nds_read_b128 a[236:239],v237 offset:47104\nv_mfma_f32_16x16x32_bf16 v[12:15],a[212:215],a[240:243],v[12:15]\nbuffer_load_dwordx4 v230,s[40:43],s72 offen offset:1024 lds\ns_add_u32 m0,s66,28672\nv_mfma_f32_16x16x32_bf16 v[40:43],a[216:219],a[240:243],v[40:43]\nds_read_b64_tr_b16 a[204:205],v233 offset:8192\ns_add_u32 s73,s67,768\n"
"v_mfma_f32_16x16x32_bf16 v[68:71],a[220:223],a[240:243],v[68:71]\nds_read_b64_tr_b16 a[206:207],v233 offset:9216\nv_mfma_f32_16x16x32_bf16 v[96:99],a[224:227],a[240:243],v[96:99]\nds_read_b128 a[240:243],v237 offset:49152\nv_mfma_f32_16x16x32_bf16 v[16:19],a[212:215],a[244:247],v[16:19]\nbuffer_load_dwordx4 v229,s[40:43],s73 offen offset:0 lds\ns_add_u32 s74,s67,6912\nv_mfma_f32_16x16x32_bf16 v[44:47],a[216:219],a[244:247],v[44:47]\n"
"ds_read_b64_tr_b16 a[208:209],v234 offset:8192\nv_mfma_f32_16x16x32_bf16 v[72:75],a[220:223],a[244:247],v[72:75]\nv_mfma_f32_16x16x32_bf16 v[100:103],a[224:227],a[244:247],v[100:103]\nds_read_b128 a[244:247],v237 offset:51200\nv_mfma_f32_16x16x32_bf16 v[20:23],a[212:215],a[248:251],v[20:23]\nbuffer_load_dwordx4 v230,s[40:43],s74 offen offset:1024 lds\ns_add_u32 m0,s64,4096\nv_mfma_f32_16x16x32_bf16 v[48:51],a[216:219],a[248:251],v[48:51]\n"
"ds_read_b64_tr_b16 a[210:211],v234 offset:9216\ns_mov_b32 s75,3013120\nv_mfma_f32_16x16x32_bf16 v[76:79],a[220:223],a[248:251],v[76:79]\nv_mfma_f32_16x16x32_bf16 v[104:107],a[224:227],a[248:251],v[104:107]\nds_read_b128 a[248:251],v237 offset:53248\nv_mfma_f32_16x16x32_bf16 v[24:27],a[212:215],a[252:255],v[24:27]\nbuffer_load_dwordx4 v224,s[44:47],s75 offen offset:2048 lds\ns_mov_b32 s72,3044864\n"
"v_mfma_f32_16x16x32_bf16 v[52:55],a[216:219],a[252:255],v[52:55]\nv_mfma_f32_16x16x32_bf16 v[80:83],a[220:223],a[252:255],v[80:83]\nv_mfma_f32_16x16x32_bf16 v[108:111],a[224:227],a[252:255],v[108:111]\nds_read_b128 a[252:255],v237 offset:55296\ns_waitcnt lgkmcnt(2)\nv_mfma_f32_16x16x32_bf16 v[0:3],a[196:199],a[228:231],v[0:3]\nbuffer_load_dwordx4 v224,s[44:47],s72 offen offset:3072 lds\ns_add_u32 m0,s64,8192\n"
"v_mfma_f32_16x16x32_bf16 v[28:31],a[200:203],a[228:231],v[28:31]\ns_waitcnt vmcnt(24)\nds_read_b64_tr_b16 a[212:213],v231 offset:12288\nv_mfma_f32_16x16x32_bf16 v[56:59],a[204:207],a[228:231],v[56:59]\nds_read_b64_tr_b16 a[214:215],v231 offset:13312\ns_mov_b32 s73,3146240\nv_mfma_f32_16x16x32_bf16 v[84:87],a[208:211],a[228:231],v[84:87]\nds_read_b128 a[228:231],v238 offset:43008\n"
"v_mfma_f32_16x16x32_bf16 v[4:7],a[196:199],a[232:235],v[4:7]\nbuffer_load_dwordx4 v224,s[44:47],s73 offen offset:0 lds\ns_mov_b32 s74,3177984\nv_mfma_f32_16x16x32_bf16 v[32:35],a[200:203],a[232:235],v[32:35]\nds_read_b64_tr_b16 a[216:217],v232 offset:12288\nv_mfma_f32_16x16x32_bf16 v[60:63],a[204:207],a[232:235],v[60:63]\nv_mfma_f32_16x16x32_bf16 v[88:91],a[208:211],a[232:235],v[88:91]\nds_read_b128 a[232:235],v238 offset:45056\n"
"v_mfma_f32_16x16x32_bf16 v[8:11],a[196:199],a[236:239],v[8:11]\nbuffer_load_dwordx4 v224,s[44:47],s74 offen offset:1024 lds\ns_mov_b32 s75,3275264\nv_mfma_f32_16x16x32_bf16 v[36:39],a[200:203],a[236:239],v[36:39]\nds_read_b64_tr_b16 a[218:219],v232 offset:13312\nv_mfma_f32_16x16x32_bf16 v[64:67],a[204:207],a[236:239],v[64:67]\nv_mfma_f32_16x16x32_bf16 v[92:95],a[208:211],a[236:239],v[92:95]\nds_read_b128 a[236:239],v238 offset:47104\n"
"v_mfma_f32_16x16x32_bf16 v[12:15],a[196:199],a[240:243],v[12:15]\nbuffer_load_dwordx4 v224,s[44:47],s75 offen offset:2048 lds\ns_mov_b32 s72,3307008\nv_mfma_f32_16x16x32_bf16 v[40:43],a[200:203],a[240:243],v[40:43]\nds_read_b64_tr_b16 a[220:221],v233 offset:12288\nv_mfma_f32_16x16x32_bf16 v[68:71],a[204:207],a[240:243],v[68:71]\nds_read_b64_tr_b16 a[222:223],v233 offset:13312\nv_mfma_f32_16x16x32_bf16 v[96:99],a[208:211],a[240:243],v[96:99]\n"
"ds_read_b128 a[240:243],v238 offset:49152\nv_mfma_f32_16x16x32_bf16 v[16:19],a[196:199],a[244:247],v[16:19]\nbuffer_load_dwordx4 v224,s[44:47],s72 offen offset:3072 lds\nv_mfma_f32_16x16x32_bf16 v[44:47],a[200:203],a[244:247],v[44:47]\nds_read_b64_tr_b16 a[224:225],v234 offset:12288\nv_mfma_f32_16x16x32_bf16 v[72:75],a[204:207],a[244:247],v[72:75]\nv_mfma_f32_16x16x32_bf16 v[100:103],a[208:211],a[244:247],v[100:103]\nds_read_b128 a[244:247],v238 offset:51200\n"
"s_waitcnt lgkmcnt(12)\nv_mfma_f32_16x16x32_bf16 v[20:23],a[196:199],a[248:251],v[20:23]\nds_read_b64_tr_b16 a[226:227],v234 offset:13312\ns_add_u32 m0,s64,12288\nv_mfma_f32_16x16x32_bf16 v[48:51],a[200:203],a[248:251],v[48:51]\ns_mov_b32 s73,3408384\nv_mfma_f32_16x16x32_bf16 v[76:79],a[204:207],a[248:251],v[76:79]\nv_mfma_f32_16x16x32_bf16 v[104:107],a[208:211],a[248:251],v[104:107]\n"
"ds_read_b128 a[248:251],v238 offset:53248\nv_mfma_f32_16x16x32_bf16 v[24:27],a[196:199],a[252:255],v[24:27]\nv_mfma_f32_16x16x32_bf16 v[52:55],a[200:203],a[252:255],v[52:55]\nv_mfma_f32_16x16x32_bf16 v[80:83],a[204:207],a[252:255],v[80:83]\nv_mfma_f32_16x16x32_bf16 v[108:111],a[208:211],a[252:255],v[108:111]\nds_read_b128 a[252:255],v238 offset:55296\ns_waitcnt lgkmcnt(2)\nv_mfma_f32_16x16x32_bf16 v[0:3],a[212:215],a[228:231],v[0:3]\n"
"buffer_load_dwordx4 v224,s[44:47],s73 offen offset:0 lds\ns_mov_b32 s74,3440128\nv_mfma_f32_16x16x32_bf16 v[28:31],a[216:219],a[228:231],v[28:31]\ns_waitcnt vmcnt(25)\nds_read_b64_tr_b16 a[196:197],v231 offset:16384\nv_mfma_f32_16x16x32_bf16 v[56:59],a[220:223],a[228:231],v[56:59]\nds_read_b64_tr_b16 a[198:199],v231 offset:17408\nv_mfma_f32_16x16x32_bf16 v[84:87],a[224:227],a[228:231],v[84:87]\n"
"s_waitcnt lgkmcnt(2)\ns_barrier\nds_read_b128 a[228:231],v237 offset:0\nv_mfma_f32_16x16x32_bf16 v[4:7],a[212:215],a[232:235],v[4:7]\nbuffer_load_dwordx4 v224,s[44:47],s74 offen offset:1024 lds\ns_mov_b32 s75,3537408\nv_mfma_f32_16x16x32_bf16 v[32:35],a[216:219],a[232:235],v[32:35]\nds_read_b64_tr_b16 a[200:201],v232 offset:16384\n"
"v_mfma_f32_16x16x32_bf16 v[60:63],a[220:223],a[232:235],v[60:63]\nv_mfma_f32_16x16x32_bf16 v[88:91],a[224:227],a[232:235],v[88:91]\nds_read_b128 a[232:235],v237 offset:2048\nv_mfma_f32_16x16x32_bf16 v[8:11],a[212:215],a[236:239],v[8:11]\nbuffer_load_dwordx4 v224,s[44:47],s75 offen offset:2048 lds\ns_mov_b32 s72,3569152\nv_mfma_f32_16x16x32_bf16 v[36:39],a[216:219],a[236:239],v[36:39]\nds_read_b64_tr_b16 a[202:203],v232 offset:17408\n"
"v_mfma_f32_16x16x32_bf16 v[64:67],a[220:223],a[236:239],v[64:67]\nv_mfma_f32_16x16x32_bf16 v[92:95],a[224:227],a[236:239],v[92:95]\nds_read_b128 a[236:239],v237 offset:4096\nv_mfma_f32_16x16x32_bf16 v[12:15],a[212:215],a[240:243],v[12:15]\nbuffer_load_dwordx4 v224,s[44:47],s72 offen offset:3072 lds\nv_mfma_f32_16x16x32_bf16 v[40:43],a[216:219],a[240:243],v[40:43]\nds_read_b64_tr_b16 a[204:205],v233 offset:16384\nv_mfma_f32_16x16x32_bf16 v[68:71],a[220:223],a[240:243],v[68:71]\n"
"ds_read_b64_tr_b16 a[206:207],v233 offset:17408\nv_mfma_f32_16x16x32_bf16 v[96:99],a[224:227],a[240:243],v[96:99]\nds_read_b128 a[240:243],v237 offset:6144\nv_mfma_f32_16x16x32_bf16 v[16:19],a[212:215],a[244:247],v[16:19]\nds_read_b64_tr_b16 a[208:209],v234 offset:16384\nv_mfma_f32_16x16x32_bf16 v[44:47],a[216:219],a[244:247],v[44:47]\nv_mfma_f32_16x16x32_bf16 v[72:75],a[220:223],a[244:247],v[72:75]\nv_mfma_f32_16x16x32_bf16 v[100:103],a[224:227],a[244:247],v[100:103]\n"
"ds_read_b128 a[244:247],v237 offset:8192\nv_mfma_f32_16x16x32_bf16 v[20:23],a[212:215],a[248:251],v[20:23]\nds_read_b64_tr_b16 a[210:211],v234 offset:17408\ns_add_u32 m0,s64,16384\nv_mfma_f32_16x16x32_bf16 v[48:51],a[216:219],a[248:251],v[48:51]\ns_mov_b32 s73,262144\nv_mfma_f32_16x16x32_bf16 v[76:79],a[220:223],a[248:251],v[76:79]\nv_mfma_f32_16x16x32_bf16 v[104:107],a[224:227],a[248:251],v[104:107]\n"
"ds_read_b128 a[248:251],v237 offset:10240\nv_mfma_f32_16x16x32_bf16 v[24:27],a[212:215],a[252:255],v[24:27]\nv_mfma_f32_16x16x32_bf16 v[52:55],a[216:219],a[252:255],v[52:55]\nv_mfma_f32_16x16x32_bf16 v[80:83],a[220:223],a[252:255],v[80:83]\nv_mfma_f32_16x16x32_bf16 v[108:111],a[224:227],a[252:255],v[108:111]\nds_read_b128 a[252:255],v237 offset:12288\ns_waitcnt lgkmcnt(2)\nv_mfma_f32_16x16x32_bf16 v[0:3],a[196:199],a[228:231],v[0:3]\n"
"buffer_load_dwordx4 v227,s[48:51],s73 offen offset:0 nt lds\ns_mov_b32 s74,269312\nv_mfma_f32_16x16x32_bf16 v[28:31],a[200:203],a[228:231],v[28:31]\ns_waitcnt vmcnt(21)\nds_read_b64_tr_b16 a[212:213],v231 offset:20480\nv_mfma_f32_16x16x32_bf16 v[56:59],a[204:207],a[228:231],v[56:59]\nds_read_b64_tr_b16 a[214:215],v231 offset:21504\nv_mfma_f32_16x16x32_bf16 v[84:87],a[208:211],a[228:231],v[84:87]\n"
"ds_read_b128 a[228:231],v238 offset:0\nv_mfma_f32_16x16x32_bf16 v[4:7],a[196:199],a[232:235],v[4:7]\nbuffer_load_dwordx4 v228,s[48:51],s74 offen offset:1024 nt lds\ns_mov_b32 s75,292864\nv_mfma_f32_16x16x32_bf16 v[32:35],a[200:203],a[232:235],v[32:35]\nds_read_b64_tr_b16 a[216:217],v232 offset:20480\nv_mfma_f32_16x16x32_bf16 v[60:63],a[204:207],a[232:235],v[60:63]\nv_mfma_f32_16x16x32_bf16 v[88:91],a[208:211],a[232:235],v[88:91]\n"
"ds_read_b128 a[232:235],v238 offset:2048\nv_mfma_f32_16x16x32_bf16 v[8:11],a[196:199],a[236:239],v[8:11]\nbuffer_load_dwordx4 v227,s[48:51],s75 offen offset:2048 nt lds\ns_mov_b32 s72,300032\nv_mfma_f32_16x16x32_bf16 v[36:39],a[200:203],a[236:239],v[36:39]\nds_read_b64_tr_b16 a[218:219],v232 offset:21504\nv_mfma_f32_16x16x32_bf16 v[64:67],a[204:207],a[236:239],v[64:67]\nv_mfma_f32_16x16x32_bf16 v[92:95],a[208:211],a[236:239],v[92:95]\n"
"ds_read_b128 a[236:239],v238 offset:4096\nv_mfma_f32_16x16x32_bf16 v[12:15],a[196:199],a[240:243],v[12:15]\nbuffer_load_dwordx4 v228,s[48:51],s72 offen offset:3072 nt lds\nv_mfma_f32_16x16x32_bf16 v[40:43],a[200:203],a[240:243],v[40:43]\nds_read_b64_tr_b16 a[220:221],v233 offset:20480\nv_mfma_f32_16x16x32_bf16 v[68:71],a[204:207],a[240:243],v[68:71]\nds_read_b64_tr_b16 a[222:223],v233 offset:21504\nv_mfma_f32_16x16x32_bf16 v[96:99],a[208:211],a[240:243],v[96:99]\n"
"ds_read_b128 a[240:243],v238 offset:6144\nv_mfma_f32_16x16x32_bf16 v[16:19],a[196:199],a[244:247],v[16:19]\nds_read_b64_tr_b16 a[224:225],v234 offset:20480\nv_mfma_f32_16x16x32_bf16 v[44:47],a[200:203],a[244:247],v[44:47]\nv_mfma_f32_16x16x32_bf16 v[72:75],a[204:207],a[244:247],v[72:75]\nv_mfma_f32_16x16x32_bf16 v[100:103],a[208:211],a[244:247],v[100:103]\nds_read_b128 a[244:247],v238 offset:8192\ns_waitcnt lgkmcnt(12)\n"
"v_mfma_f32_16x16x32_bf16 v[20:23],a[196:199],a[248:251],v[20:23]\nds_read_b64_tr_b16 a[226:227],v234 offset:21504\ns_add_u32 m0,s64,20480\nv_mfma_f32_16x16x32_bf16 v[48:51],a[200:203],a[248:251],v[48:51]\ns_mov_b32 s73,327680\nv_mfma_f32_16x16x32_bf16 v[76:79],a[204:207],a[248:251],v[76:79]\nv_mfma_f32_16x16x32_bf16 v[104:107],a[208:211],a[248:251],v[104:107]\nds_read_b128 a[248:251],v238 offset:10240\n"
"v_mfma_f32_16x16x32_bf16 v[24:27],a[196:199],a[252:255],v[24:27]\nv_mfma_f32_16x16x32_bf16 v[52:55],a[200:203],a[252:255],v[52:55]\nv_mfma_f32_16x16x32_bf16 v[80:83],a[204:207],a[252:255],v[80:83]\nv_mfma_f32_16x16x32_bf16 v[108:111],a[208:211],a[252:255],v[108:111]\nds_read_b128 a[252:255],v238 offset:12288\ns_waitcnt lgkmcnt(2)\nv_mfma_f32_16x16x32_bf16 v[0:3],a[212:215],a[228:231],v[0:3]\nbuffer_load_dwordx4 v227,s[48:51],s73 offen offset:0 nt lds\n"
"s_mov_b32 s74,334848\nv_mfma_f32_16x16x32_bf16 v[28:31],a[216:219],a[228:231],v[28:31]\ns_waitcnt vmcnt(21)\nds_read_b64_tr_b16 a[196:197],v231 offset:0\nv_mfma_f32_16x16x32_bf16 v[56:59],a[220:223],a[228:231],v[56:59]\nds_read_b64_tr_b16 a[198:199],v231 offset:1024\nv_mfma_f32_16x16x32_bf16 v[84:87],a[224:227],a[228:231],v[84:87]\ns_waitcnt lgkmcnt(2)\n"
"s_barrier\nds_read_b128 a[228:231],v237 offset:14336\nv_mfma_f32_16x16x32_bf16 v[4:7],a[212:215],a[232:235],v[4:7]\nbuffer_load_dwordx4 v228,s[48:51],s74 offen offset:1024 nt lds\ns_mov_b32 s75,358400\nv_mfma_f32_16x16x32_bf16 v[32:35],a[216:219],a[232:235],v[32:35]\nds_read_b64_tr_b16 a[200:201],v232 offset:0\nv_mfma_f32_16x16x32_bf16 v[60:63],a[220:223],a[232:235],v[60:63]\n"
"v_mfma_f32_16x16x32_bf16 v[88:91],a[224:227],a[232:235],v[88:91]\nds_read_b128 a[232:235],v237 offset:16384\nv_mfma_f32_16x16x32_bf16 v[8:11],a[212:215],a[236:239],v[8:11]\nbuffer_load_dwordx4 v227,s[48:51],s75 offen offset:2048 nt lds\ns_mov_b32 s72,365568\nv_mfma_f32_16x16x32_bf16 v[36:39],a[216:219],a[236:239],v[36:39]\nds_read_b64_tr_b16 a[202:203],v232 offset:1024\nv_mfma_f32_16x16x32_bf16 v[64:67],a[220:223],a[236:239],v[64:67]\n"
"v_mfma_f32_16x16x32_bf16 v[92:95],a[224:227],a[236:239],v[92:95]\nds_read_b128 a[236:239],v237 offset:18432\nv_mfma_f32_16x16x32_bf16 v[12:15],a[212:215],a[240:243],v[12:15]\nbuffer_load_dwordx4 v228,s[48:51],s72 offen offset:3072 nt lds\nv_mfma_f32_16x16x32_bf16 v[40:43],a[216:219],a[240:243],v[40:43]\nds_read_b64_tr_b16 a[204:205],v233 offset:0\nv_mfma_f32_16x16x32_bf16 v[68:71],a[220:223],a[240:243],v[68:71]\nds_read_b64_tr_b16 a[206:207],v233 offset:1024\n"
"v_mfma_f32_16x16x32_bf16 v[96:99],a[224:227],a[240:243],v[96:99]\nds_read_b128 a[240:243],v237 offset:20480\nv_mfma_f32_16x16x32_bf16 v[16:19],a[212:215],a[244:247],v[16:19]\nds_read_b64_tr_b16 a[208:209],v234 offset:0\nv_mfma_f32_16x16x32_bf16 v[44:47],a[216:219],a[244:247],v[44:47]\nv_mfma_f32_16x16x32_bf16 v[72:75],a[220:223],a[244:247],v[72:75]\nv_mfma_f32_16x16x32_bf16 v[100:103],a[224:227],a[244:247],v[100:103]\nds_read_b128 a[244:247],v237 offset:22528\n"
"v_mfma_f32_16x16x32_bf16 v[20:23],a[212:215],a[248:251],v[20:23]\nds_read_b64_tr_b16 a[210:211],v234 offset:1024\ns_add_u32 m0,s64,0\nv_mfma_f32_16x16x32_bf16 v[48:51],a[216:219],a[248:251],v[48:51]\ns_mov_b32 s73,262272\nv_mfma_f32_16x16x32_bf16 v[76:79],a[220:223],a[248:251],v[76:79]\nv_mfma_f32_16x16x32_bf16 v[104:107],a[224:227],a[248:251],v[104:107]\nds_read_b128 a[248:251],v237 offset:24576\n"
"v_mfma_f32_16x16x32_bf16 v[24:27],a[212:215],a[252:255],v[24:27]\nv_mfma_f32_16x16x32_bf16 v[52:55],a[216:219],a[252:255],v[52:55]\nv_mfma_f32_16x16x32_bf16 v[80:83],a[220:223],a[252:255],v[80:83]\nv_mfma_f32_16x16x32_bf16 v[108:111],a[224:227],a[252:255],v[108:111]\nds_read_b128 a[252:255],v237 offset:26624\ns_waitcnt lgkmcnt(2)\nv_mfma_f32_16x16x32_bf16 v[0:3],a[196:199],a[228:231],v[0:3]\nbuffer_load_dwordx4 v227,s[48:51],s73 offen offset:0 nt lds\n"
"s_mov_b32 s74,269440\nv_mfma_f32_16x16x32_bf16 v[28:31],a[200:203],a[228:231],v[28:31]\ns_waitcnt vmcnt(17)\nds_read_b64_tr_b16 a[212:213],v231 offset:4096\nv_mfma_f32_16x16x32_bf16 v[56:59],a[204:207],a[228:231],v[56:59]\nds_read_b64_tr_b16 a[214:215],v231 offset:5120\nv_mfma_f32_16x16x32_bf16 v[84:87],a[208:211],a[228:231],v[84:87]\nds_read_b128 a[228:231],v238 offset:14336\n"
"v_mfma_f32_16x16x32_bf16 v[4:7],a[196:199],a[232:235],v[4:7]\nbuffer_load_dwordx4 v228,s[48:51],s74 offen offset:1024 nt lds\ns_mov_b32 s75,292992\nv_mfma_f32_16x16x32_bf16 v[32:35],a[200:203],a[232:235],v[32:35]\nds_read_b64_tr_b16 a[216:217],v232 offset:4096\nv_mfma_f32_16x16x32_bf16 v[60:63],a[204:207],a[232:235],v[60:63]\nv_mfma_f32_16x16x32_bf16 v[88:91],a[208:211],a[232:235],v[88:91]\nds_read_b128 a[232:235],v238 offset:16384\n"
"v_mfma_f32_16x16x32_bf16 v[8:11],a[196:199],a[236:239],v[8:11]\nbuffer_load_dwordx4 v227,s[48:51],s75 offen offset:2048 nt lds\ns_mov_b32 s72,300160\nv_mfma_f32_16x16x32_bf16 v[36:39],a[200:203],a[236:239],v[36:39]\nds_read_b64_tr_b16 a[218:219],v232 offset:5120\nv_mfma_f32_16x16x32_bf16 v[64:67],a[204:207],a[236:239],v[64:67]\nv_mfma_f32_16x16x32_bf16 v[92:95],a[208:211],a[236:239],v[92:95]\nds_read_b128 a[236:239],v238 offset:18432\n"
"v_mfma_f32_16x16x32_bf16 v[12:15],a[196:199],a[240:243],v[12:15]\nbuffer_load_dwordx4 v228,s[48:51],s72 offen offset:3072 nt lds\nv_mfma_f32_16x16x32_bf16 v[40:43],a[200:203],a[240:243],v[40:43]\nds_read_b64_tr_b16 a[220:221],v233 offset:4096\nv_mfma_f32_16x16x32_bf16 v[68:71],a[204:207],a[240:243],v[68:71]\nds_read_b64_tr_b16 a[222:223],v233 offset:5120\nv_mfma_f32_16x16x32_bf16 v[96:99],a[208:211],a[240:243],v[96:99]\nds_read_b128 a[240:243],v238 offset:20480\n"
"v_mfma_f32_16x16x32_bf16 v[16:19],a[196:199],a[244:247],v[16:19]\nds_read_b64_tr_b16 a[224:225],v234 offset:4096\nv_mfma_f32_16x16x32_bf16 v[44:47],a[200:203],a[244:247],v[44:47]\nv_mfma_f32_16x16x32_bf16 v[72:75],a[204:207],a[244:247],v[72:75]\nv_mfma_f32_16x16x32_bf16 v[100:103],a[208:211],a[244:247],v[100:103]\nds_read_b128 a[244:247],v238 offset:22528\ns_waitcnt lgkmcnt(12)\nv_mfma_f32_16x16x32_bf16 v[20:23],a[196:199],a[248:251],v[20:23]\n"
"ds_read_b64_tr_b16 a[226:227],v234 offset:5120\ns_add_u32 m0,s64,4096\nv_mfma_f32_16x16x32_bf16 v[48:51],a[200:203],a[248:251],v[48:51]\ns_mov_b32 s73,327808\nv_mfma_f32_16x16x32_bf16 v[76:79],a[204:207],a[248:251],v[76:79]\nv_mfma_f32_16x16x32_bf16 v[104:107],a[208:211],a[248:251],v[104:107]\nds_read_b128 a[248:251],v238 offset:24576\nv_mfma_f32_16x16x32_bf16 v[24:27],a[196:199],a[252:255],v[24:27]\n"
"v_mfma_f32_16x16x32_bf16 v[52:55],a[200:203],a[252:255],v[52:55]\nv_mfma_f32_16x16x32_bf16 v[80:83],a[204:207],a[252:255],v[80:83]\nv_mfma_f32_16x16x32_bf16 v[108:111],a[208:211],a[252:255],v[108:111]\nds_read_b128 a[252:255],v238 offset:26624\ns_waitcnt lgkmcnt(2)\nv_mfma_f32_16x16x32_bf16 v[0:3],a[212:215],a[228:231],v[0:3]\nbuffer_load_dwordx4 v227,s[48:51],s73 offen offset:0 nt lds\ns_mov_b32 s74,334976\n"
"v_mfma_f32_16x16x32_bf16 v[28:31],a[216:219],a[228:231],v[28:31]\ns_waitcnt vmcnt(17)\nds_read_b64_tr_b16 a[196:197],v231 offset:8192\nv_mfma_f32_16x16x32_bf16 v[56:59],a[220:223],a[228:231],v[56:59]\nds_read_b64_tr_b16 a[198:199],v231 offset:9216\nv_mfma_f32_16x16x32_bf16 v[84:87],a[224:227],a[228:231],v[84:87]\ns_waitcnt lgkmcnt(2)\ns_barrier\n"
"ds_read_b128 a[228:231],v237 offset:28672\nv_mfma_f32_16x16x32_bf16 v[4:7],a[212:215],a[232:235],v[4:7]\nbuffer_load_dwordx4 v228,s[48:51],s74 offen offset:1024 nt lds\ns_mov_b32 s75,358528\nv_mfma_f32_16x16x32_bf16 v[32:35],a[216:219],a[232:235],v[32:35]\nds_read_b64_tr_b16 a[200:201],v232 offset:8192\nv_mfma_f32_16x16x32_bf16 v[60:63],a[220:223],a[232:235],v[60:63]\nv_mfma_f32_16x16x32_bf16 v[88:91],a[224:227],a[232:235],v[88:91]\n"
"ds_read_b128 a[232:235],v237 offset:30720\nv_mfma_f32_16x16x32_bf16 v[8:11],a[212:215],a[236:239],v[8:11]\nbuffer_load_dwordx4 v227,s[48:51],s75 offen offset:2048 nt lds\ns_mov_b32 s72,365696\nv_mfma_f32_16x16x32_bf16 v[36:39],a[216:219],a[236:239],v[36:39]\nds_read_b64_tr_b16 a[202:203],v232 offset:9216\nv_mfma_f32_16x16x32_bf16 v[64:67],a[220:223],a[236:239],v[64:67]\nv_mfma_f32_16x16x32_bf16 v[92:95],a[224:227],a[236:239],v[92:95]\n"
"ds_read_b128 a[236:239],v237 offset:32768\nv_mfma_f32_16x16x32_bf16 v[12:15],a[212:215],a[240:243],v[12:15]\nbuffer_load_dwordx4 v228,s[48:51],s72 offen offset:3072 nt lds\nv_mfma_f32_16x16x32_bf16 v[40:43],a[216:219],a[240:243],v[40:43]\nds_read_b64_tr_b16 a[204:205],v233 offset:8192\nv_mfma_f32_16x16x32_bf16 v[68:71],a[220:223],a[240:243],v[68:71]\nds_read_b64_tr_b16 a[206:207],v233 offset:9216\nv_mfma_f32_16x16x32_bf16 v[96:99],a[224:227],a[240:243],v[96:99]\n"
"ds_read_b128 a[240:243],v237 offset:34816\nv_mfma_f32_16x16x32_bf16 v[16:19],a[212:215],a[244:247],v[16:19]\nds_read_b64_tr_b16 a[208:209],v234 offset:8192\nv_mfma_f32_16x16x32_bf16 v[44:47],a[216:219],a[244:247],v[44:47]\nv_mfma_f32_16x16x32_bf16 v[72:75],a[220:223],a[244:247],v[72:75]\nv_mfma_f32_16x16x32_bf16 v[100:103],a[224:227],a[244:247],v[100:103]\nds_read_b128 a[244:247],v237 offset:36864\nv_mfma_f32_16x16x32_bf16 v[20:23],a[212:215],a[248:251],v[20:23]\n"
"ds_read_b64_tr_b16 a[210:211],v234 offset:9216\ns_add_u32 m0,s64,8192\nv_mfma_f32_16x16x32_bf16 v[48:51],a[216:219],a[248:251],v[48:51]\ns_mov_b32 s73,262400\nv_mfma_f32_16x16x32_bf16 v[76:79],a[220:223],a[248:251],v[76:79]\nv_mfma_f32_16x16x32_bf16 v[104:107],a[224:227],a[248:251],v[104:107]\nds_read_b128 a[248:251],v237 offset:38912\nv_mfma_f32_16x16x32_bf16 v[24:27],a[212:215],a[252:255],v[24:27]\n"
"v_mfma_f32_16x16x32_bf16 v[52:55],a[216:219],a[252:255],v[52:55]\nv_mfma_f32_16x16x32_bf16 v[80:83],a[220:223],a[252:255],v[80:83]\nv_mfma_f32_16x16x32_bf16 v[108:111],a[224:227],a[252:255],v[108:111]\nds_read_b128 a[252:255],v237 offset:40960\ns_waitcnt lgkmcnt(2)\nv_mfma_f32_16x16x32_bf16 v[0:3],a[196:199],a[228:231],v[0:3]\nbuffer_load_dwordx4 v227,s[48:51],s73 offen offset:0 nt lds\ns_mov_b32 s74,269568\n"
"v_mfma_f32_16x16x32_bf16 v[28:31],a[200:203],a[228:231],v[28:31]\ns_waitcnt vmcnt(17)\nds_read_b64_tr_b16 a[212:213],v231 offset:12288\nv_mfma_f32_16x16x32_bf16 v[56:59],a[204:207],a[228:231],v[56:59]\nds_read_b64_tr_b16 a[214:215],v231 offset:13312\nv_mfma_f32_16x16x32_bf16 v[84:87],a[208:211],a[228:231],v[84:87]\nds_read_b128 a[228:231],v238 offset:28672\nv_mfma_f32_16x16x32_bf16 v[4:7],a[196:199],a[232:235],v[4:7]\n"
"buffer_load_dwordx4 v228,s[48:51],s74 offen offset:1024 nt lds\ns_mov_b32 s75,293120\nv_mfma_f32_16x16x32_bf16 v[32:35],a[200:203],a[232:235],v[32:35]\nds_read_b64_tr_b16 a[216:217],v232 offset:12288\nv_mfma_f32_16x16x32_bf16 v[60:63],a[204:207],a[232:235],v[60:63]\nv_mfma_f32_16x16x32_bf16 v[88:91],a[208:211],a[232:235],v[88:91]\nds_read_b128 a[232:235],v238 offset:30720\nv_mfma_f32_16x16x32_bf16 v[8:11],a[196:199],a[236:239],v[8:11]\n"
"buffer_load_dwordx4 v227,s[48:51],s75 offen offset:2048 nt lds\ns_mov_b32 s72,300288\nv_mfma_f32_16x16x32_bf16 v[36:39],a[200:203],a[236:239],v[36:39]\nds_read_b64_tr_b16 a[218:219],v232 offset:13312\nv_mfma_f32_16x16x32_bf16 v[64:67],a[204:207],a[236:239],v[64:67]\nv_mfma_f32_16x16x32_bf16 v[92:95],a[208:211],a[236:239],v[92:95]\nds_read_b128 a[236:239],v238 offset:32768\nv_mfma_f32_16x16x32_bf16 v[12:15],a[196:199],a[240:243],v[12:15]\n"
"buffer_load_dwordx4 v228,s[48:51],s72 offen offset:3072 nt lds\nv_mfma_f32_16x16x32_bf16 v[40:43],a[200:203],a[240:243],v[40:43]\nds_read_b64_tr_b16 a[220:221],v233 offset:12288\nv_mfma_f32_16x16x32_bf16 v[68:71],a[204:207],a[240:243],v[68:71]\nds_read_b64_tr_b16 a[222:223],v233 offset:13312\nv_mfma_f32_16x16x32_bf16 v[96:99],a[208:211],a[240:243],v[96:99]\nds_read_b128 a[240:243],v238 offset:34816\nv_mfma_f32_16x16x32_bf16 v[16:19],a[196:199],a[244:247],v[16:19]\n"
"ds_read_b64_tr_b16 a[224:225],v234 offset:12288\nv_mfma_f32_16x16x32_bf16 v[44:47],a[200:203],a[244:247],v[44:47]\nv_mfma_f32_16x16x32_bf16 v[72:75],a[204:207],a[244:247],v[72:75]\nv_mfma_f32_16x16x32_bf16 v[100:103],a[208:211],a[244:247],v[100:103]\nds_read_b128 a[244:247],v238 offset:36864\ns_waitcnt lgkmcnt(12)\nv_mfma_f32_16x16x32_bf16 v[20:23],a[196:199],a[248:251],v[20:23]\nds_read_b64_tr_b16 a[226:227],v234 offset:13312\n"
"s_add_u32 m0,s64,12288\nv_mfma_f32_16x16x32_bf16 v[48:51],a[200:203],a[248:251],v[48:51]\ns_mov_b32 s73,327936\nv_mfma_f32_16x16x32_bf16 v[76:79],a[204:207],a[248:251],v[76:79]\nv_mfma_f32_16x16x32_bf16 v[104:107],a[208:211],a[248:251],v[104:107]\nds_read_b128 a[248:251],v238 offset:38912\nv_mfma_f32_16x16x32_bf16 v[24:27],a[196:199],a[252:255],v[24:27]\nv_mfma_f32_16x16x32_bf16 v[52:55],a[200:203],a[252:255],v[52:55]\n"
"v_mfma_f32_16x16x32_bf16 v[80:83],a[204:207],a[252:255],v[80:83]\nv_mfma_f32_16x16x32_bf16 v[108:111],a[208:211],a[252:255],v[108:111]\nds_read_b128 a[252:255],v238 offset:40960\ns_waitcnt lgkmcnt(2)\nv_mfma_f32_16x16x32_bf16 v[0:3],a[212:215],a[228:231],v[0:3]\nbuffer_load_dwordx4 v227,s[48:51],s73 offen offset:0 nt lds\ns_mov_b32 s74,335104\nv_mfma_f32_16x16x32_bf16 v[28:31],a[216:219],a[228:231],v[28:31]\n"
"s_waitcnt vmcnt(17)\nds_read_b128 a[196:199],v235 offset:16384\nv_mfma_f32_16x16x32_bf16 v[56:59],a[220:223],a[228:231],v[56:59]\nv_mfma_f32_16x16x32_bf16 v[84:87],a[224:227],a[228:231],v[84:87]\ns_waitcnt lgkmcnt(0)\ns_barrier\nv_mfma_f32_16x16x32_bf16 v[4:7],a[212:215],a[232:235],v[4:7]\nbuffer_load_dwordx4 v228,s[48:51],s74 offen offset:1024 nt lds\n"
"s_mov_b32 s75,358656\nv_mfma_f32_16x16x32_bf16 v[32:35],a[216:219],a[232:235],v[32:35]\nds_read_b128 a[200:203],v235 offset:18432\nv_mfma_f32_16x16x32_bf16 v[60:63],a[220:223],a[232:235],v[60:63]\nds_write_b32 v248,v250 offset:57344\nv_mfma_f32_16x16x32_bf16 v[88:91],a[224:227],a[232:235],v[88:91]\nv_mfma_f32_16x16x32_bf16 v[8:11],a[212:215],a[236:239],v[8:11]\nbuffer_load_dwordx4 v227,s[48:51],s75 offen offset:2048 nt lds\n"
"v_cvt_pk_bf16_f32 v0,v0,v1\nv_mfma_f32_16x16x32_bf16 v[36:39],a[216:219],a[236:239],v[36:39]\ns_mov_b32 s72,365824\nds_write_b32 v248,v250 offset:57600\nv_mfma_f32_16x16x32_bf16 v[64:67],a[220:223],a[236:239],v[64:67]\nv_cvt_pk_bf16_f32 v1,v2,v3\nv_cvt_pk_bf16_f32 v28,v28,v29\nv_mfma_f32_16x16x32_bf16 v[92:95],a[224:227],a[236:239],v[92:95]\n"
"ds_write_b64 v241,v[0:1] offset:0\nv_mfma_f32_16x16x32_bf16 v[12:15],a[212:215],a[240:243],v[12:15]\nbuffer_load_dwordx4 v228,s[48:51],s72 offen offset:3072 nt lds\nv_cvt_pk_bf16_f32 v29,v30,v31\nv_mfma_f32_16x16x32_bf16 v[40:43],a[216:219],a[240:243],v[40:43]\ns_waitcnt vmcnt(16)\nds_read_b128 a[204:207],v235 offset:20480\nv_mfma_f32_16x16x32_bf16 v[68:71],a[220:223],a[240:243],v[68:71]\n"
"ds_write_b64 v241,v[28:29] offset:512\nv_mfma_f32_16x16x32_bf16 v[96:99],a[224:227],a[240:243],v[96:99]\nv_cvt_pk_bf16_f32 v56,v56,v57\nv_cvt_pk_bf16_f32 v57,v58,v59\nv_mfma_f32_16x16x32_bf16 v[16:19],a[212:215],a[244:247],v[16:19]\nds_read_b128 a[208:211],v235 offset:22528\nv_cvt_pk_bf16_f32 v84,v84,v85\nv_mfma_f32_16x16x32_bf16 v[44:47],a[216:219],a[244:247],v[44:47]\n"
"ds_write_b64 v241,v[56:57] offset:1024\nv_mfma_f32_16x16x32_bf16 v[72:75],a[220:223],a[244:247],v[72:75]\nv_cvt_pk_bf16_f32 v85,v86,v87\nv_cvt_pk_bf16_f32 v4,v4,v5\nv_mfma_f32_16x16x32_bf16 v[100:103],a[224:227],a[244:247],v[100:103]\nds_write_b64 v241,v[84:85] offset:1536\nv_mfma_f32_16x16x32_bf16 v[20:23],a[212:215],a[248:251],v[20:23]\nv_cvt_pk_bf16_f32 v5,v6,v7\n"
"v_cvt_pk_bf16_f32 v32,v32,v33\nv_mfma_f32_16x16x32_bf16 v[48:51],a[216:219],a[248:251],v[48:51]\nds_write_b64 v241,v[4:5] offset:2048\nv_mfma_f32_16x16x32_bf16 v[76:79],a[220:223],a[248:251],v[76:79]\nv_cvt_pk_bf16_f32 v33,v34,v35\nv_cvt_pk_bf16_f32 v60,v60,v61\nv_mfma_f32_16x16x32_bf16 v[104:107],a[224:227],a[248:251],v[104:107]\nds_write_b64 v241,v[32:33] offset:2560\n"
"v_mfma_f32_16x16x32_bf16 v[24:27],a[212:215],a[252:255],v[24:27]\nv_cvt_pk_bf16_f32 v61,v62,v63\nv_cvt_pk_bf16_f32 v88,v88,v89\nv_mfma_f32_16x16x32_bf16 v[52:55],a[216:219],a[252:255],v[52:55]\nds_write_b64 v241,v[60:61] offset:3072\nv_mfma_f32_16x16x32_bf16 v[80:83],a[220:223],a[252:255],v[80:83]\nv_cvt_pk_bf16_f32 v89,v90,v91\nv_cvt_pk_bf16_f32 v8,v8,v9\n"
"v_mfma_f32_16x16x32_bf16 v[108:111],a[224:227],a[252:255],v[108:111]\nds_write_b64 v241,v[88:89] offset:3584\nv_cvt_pk_bf16_f32 v9,v10,v11\nds_write_b64 v241,v[8:9] offset:4096\nv_cvt_pk_bf16_f32 v36,v36,v37\nv_cvt_pk_bf16_f32 v37,v38,v39\nds_write_b64 v241,v[36:37] offset:4608\nv_cvt_pk_bf16_f32 v64,v64,v65\n"
"v_cvt_pk_bf16_f32 v65,v66,v67\nds_write_b64 v241,v[64:65] offset:5120\nv_cvt_pk_bf16_f32 v92,v92,v93\nv_cvt_pk_bf16_f32 v93,v94,v95\nds_write_b64 v241,v[92:93] offset:5632\nv_cvt_pk_bf16_f32 v12,v12,v13\nv_cvt_pk_bf16_f32 v13,v14,v15\nds_write_b64 v241,v[12:13] offset:6144\n"
"v_cvt_pk_bf16_f32 v40,v40,v41\nv_cvt_pk_bf16_f32 v41,v42,v43\nds_write_b64 v241,v[40:41] offset:6656\nv_cvt_pk_bf16_f32 v68,v68,v69\nv_cvt_pk_bf16_f32 v69,v70,v71\nds_write_b64 v241,v[68:69] offset:7168\nv_cvt_pk_bf16_f32 v96,v96,v97\nv_cvt_pk_bf16_f32 v97,v98,v99\n"
"ds_write_b64 v241,v[96:97] offset:7680\nv_cvt_pk_bf16_f32 v16,v16,v17\nv_cvt_pk_bf16_f32 v17,v18,v19\nds_write_b64 v241,v[16:17] offset:8192\nv_cvt_pk_bf16_f32 v44,v44,v45\nv_cvt_pk_bf16_f32 v45,v46,v47\nds_write_b64 v241,v[44:45] offset:8704\nv_cvt_pk_bf16_f32 v72,v72,v73\n"
"v_cvt_pk_bf16_f32 v73,v74,v75\nds_write_b64 v241,v[72:73] offset:9216\nv_cvt_pk_bf16_f32 v100,v100,v101\nv_cvt_pk_bf16_f32 v101,v102,v103\nds_write_b64 v241,v[100:101] offset:9728\nv_cvt_pk_bf16_f32 v20,v20,v21\nv_cvt_pk_bf16_f32 v21,v22,v23\nds_write_b64 v241,v[20:21] offset:10240\n"
"v_cvt_pk_bf16_f32 v48,v48,v49\nv_cvt_pk_bf16_f32 v49,v50,v51\nds_write_b64 v241,v[48:49] offset:10752\nv_cvt_pk_bf16_f32 v76,v76,v77\nv_cvt_pk_bf16_f32 v77,v78,v79\nds_write_b64 v241,v[76:77] offset:11264\nv_cvt_pk_bf16_f32 v104,v104,v105\nv_cvt_pk_bf16_f32 v105,v106,v107\n"
"ds_write_b64 v241,v[104:105] offset:11776\nv_cvt_pk_bf16_f32 v24,v24,v25\nv_cvt_pk_bf16_f32 v25,v26,v27\nds_write_b64 v241,v[24:25] offset:12288\nv_cvt_pk_bf16_f32 v52,v52,v53\nv_cvt_pk_bf16_f32 v53,v54,v55\nds_write_b64 v241,v[52:53] offset:12800\nv_cvt_pk_bf16_f32 v80,v80,v81\n"
"v_cvt_pk_bf16_f32 v81,v82,v83\nds_write_b64 v241,v[80:81] offset:13312\nv_cvt_pk_bf16_f32 v108,v108,v109\nv_cvt_pk_bf16_f32 v109,v110,v111\nds_write_b64 v241,v[108:109] offset:13824\ns_waitcnt lgkmcnt(0)\ns_barrier\nds_read_b128 a[228:231],v239 offset:0\n"
"ds_read_b128 a[232:235],v239 offset:2048\nds_read_b128 a[236:239],v239 offset:4096\nds_read_b128 a[240:243],v239 offset:6144\nds_read_b128 a[244:247],v239 offset:8192\nds_read_b128 a[248:251],v239 offset:10240\nds_read_b128 a[252:255],v239 offset:12288\ns_waitcnt lgkmcnt(6)\nv_mfma_f32_16x16x32_bf16 v[0:3],a[196:199],a[228:231],0\n"
"ds_read_b128 a[212:215],v236 offset:16384\nv_mfma_f32_16x16x32_bf16 v[28:31],a[200:203],a[228:231],0\nv_mfma_f32_16x16x32_bf16 v[56:59],a[204:207],a[228:231],0\nv_mfma_f32_16x16x32_bf16 v[84:87],a[208:211],a[228:231],0\nds_read_b128 a[228:231],v239 offset:1024\ns_waitcnt lgkmcnt(7)\nv_mfma_f32_16x16x32_bf16 v[4:7],a[196:199],a[232:235],0\nds_read_b128 a[216:219],v236 offset:18432\n"
"s_add_u32 m0,s64,16384\nv_mfma_f32_16x16x32_bf16 v[32:35],a[200:203],a[232:235],0\ns_mov_b32 s73,262528\nv_mfma_f32_16x16x32_bf16 v[60:63],a[204:207],a[232:235],0\nv_mfma_f32_16x16x32_bf16 v[88:91],a[208:211],a[232:235],0\nds_read_b128 a[232:235],v239 offset:3072\ns_waitcnt lgkmcnt(8)\nv_mfma_f32_16x16x32_bf16 v[8:11],a[196:199],a[236:239],0\n"
"v_mfma_f32_16x16x32_bf16 v[36:39],a[200:203],a[236:239],0\nv_mfma_f32_16x16x32_bf16 v[64:67],a[204:207],a[236:239],0\nv_mfma_f32_16x16x32_bf16 v[92:95],a[208:211],a[236:239],0\nds_read_b128 a[236:239],v239 offset:5120\ns_waitcnt lgkmcnt(7)\nv_mfma_f32_16x16x32_bf16 v[12:15],a[196:199],a[240:243],0\nds_read_b128 a[220:223],v236 offset:20480\nv_mfma_f32_16x16x32_bf16 v[40:43],a[200:203],a[240:243],0\n"
"v_mfma_f32_16x16x32_bf16 v[68:71],a[204:207],a[240:243],0\nv_mfma_f32_16x16x32_bf16 v[96:99],a[208:211],a[240:243],0\nds_read_b128 a[240:243],v239 offset:7168\nv_mfma_f32_16x16x32_bf16 v[16:19],a[196:199],a[244:247],0\nds_read_b128 a[224:227],v236 offset:22528\nv_mfma_f32_16x16x32_bf16 v[44:47],a[200:203],a[244:247],0\nv_mfma_f32_16x16x32_bf16 v[72:75],a[204:207],a[244:247],0\nv_mfma_f32_16x16x32_bf16 v[100:103],a[208:211],a[244:247],0\n"
"ds_read_b128 a[244:247],v239 offset:9216\ns_waitcnt lgkmcnt(9)\nv_mfma_f32_16x16x32_bf16 v[20:23],a[196:199],a[248:251],0\nv_mfma_f32_16x16x32_bf16 v[48:51],a[200:203],a[248:251],0\nv_mfma_f32_16x16x32_bf16 v[76:79],a[204:207],a[248:251],0\nv_mfma_f32_16x16x32_bf16 v[104:107],a[208:211],a[248:251],0\nds_read_b128 a[248:251],v239 offset:11264\nv_mfma_f32_16x16x32_bf16 v[24:27],a[196:199],a[252:255],0\n"
"v_mfma_f32_16x16x32_bf16 v[52:55],a[200:203],a[252:255],0\nv_mfma_f32_16x16x32_bf16 v[80:83],a[204:207],a[252:255],0\nv_mfma_f32_16x16x32_bf16 v[108:111],a[208:211],a[252:255],0\nds_read_b128 a[252:255],v239 offset:13312\ns_waitcnt lgkmcnt(3)\nv_mfma_f32_16x16x32_bf16 v[0:3],a[212:215],a[228:231],v[0:3]\nbuffer_load_dwordx4 v227,s[48:51],s73 offen offset:0 nt lds\ns_mov_b32 s74,269696\n"
"v_mfma_f32_16x16x32_bf16 v[28:31],a[216:219],a[228:231],v[28:31]\ns_waitcnt vmcnt(13)\nds_read_b128 a[196:199],v235 offset:0\nv_mfma_f32_16x16x32_bf16 v[56:59],a[220:223],a[228:231],v[56:59]\nv_mfma_f32_16x16x32_bf16 v[84:87],a[224:227],a[228:231],v[84:87]\nds_read_b128 a[228:231],v239 offset:14336\nv_mfma_f32_16x16x32_bf16 v[4:7],a[212:215],a[232:235],v[4:7]\nbuffer_load_dwordx4 v228,s[48:51],s74 offen offset:1024 nt lds\n"
"s_mov_b32 s75,293248\nv_mfma_f32_16x16x32_bf16 v[32:35],a[216:219],a[232:235],v[32:35]\nds_read_b128 a[200:203],v235 offset:2048\nv_mfma_f32_16x16x32_bf16 v[60:63],a[220:223],a[232:235],v[60:63]\nv_mfma_f32_16x16x32_bf16 v[88:91],a[224:227],a[232:235],v[88:91]\nds_read_b128 a[232:235],v239 offset:16384\nv_mfma_f32_16x16x32_bf16 v[8:11],a[212:215],a[236:239],v[8:11]\nbuffer_load_dwordx4 v227,s[48:51],s75 offen offset:2048 nt lds\n"
"s_mov_b32 s72,300416\nv_mfma_f32_16x16x32_bf16 v[36:39],a[216:219],a[236:239],v[36:39]\nv_mfma_f32_16x16x32_bf16 v[64:67],a[220:223],a[236:239],v[64:67]\nv_mfma_f32_16x16x32_bf16 v[92:95],a[224:227],a[236:239],v[92:95]\nds_read_b128 a[236:239],v239 offset:18432\ns_waitcnt lgkmcnt(7)\nv_mfma_f32_16x16x32_bf16 v[12:15],a[212:215],a[240:243],v[12:15]\nbuffer_load_dwordx4 v228,s[48:51],s72 offen offset:3072 nt lds\n"
"s_add_u32 m0,s64,20480\nv_mfma_f32_16x16x32_bf16 v[40:43],a[216:219],a[240:243],v[40:43]\ns_waitcnt vmcnt(12)\nds_read_b128 a[204:207],v235 offset:4096\nv_mfma_f32_16x16x32_bf16 v[68:71],a[220:223],a[240:243],v[68:71]\ns_mov_b32 s73,328064\nv_mfma_f32_16x16x32_bf16 v[96:99],a[224:227],a[240:243],v[96:99]\nds_read_b128 a[240:243],v239 offset:20480\n"
"v_mfma_f32_16x16x32_bf16 v[16:19],a[212:215],a[244:247],v[16:19]\nbuffer_load_dwordx4 v227,s[48:51],s73 offen offset:0 nt lds\ns_mov_b32 s74,335232\nv_mfma_f32_16x16x32_bf16 v[44:47],a[216:219],a[244:247],v[44:47]\nds_read_b128 a[208:211],v235 offset:6144\nv_mfma_f32_16x16x32_bf16 v[72:75],a[220:223],a[244:247],v[72:75]\nv_mfma_f32_16x16x32_bf16 v[100:103],a[224:227],a[244:247],v[100:103]\nds_read_b128 a[244:247],v239 offset:22528\n"
"s_waitcnt lgkmcnt(9)\nv_mfma_f32_16x16x32_bf16 v[20:23],a[212:215],a[248:251],v[20:23]\nbuffer_load_dwordx4 v228,s[48:51],s74 offen offset:1024 nt lds\ns_mov_b32 s75,358784\nv_mfma_f32_16x16x32_bf16 v[48:51],a[216:219],a[248:251],v[48:51]\nv_mfma_f32_16x16x32_bf16 v[76:79],a[220:223],a[248:251],v[76:79]\nv_mfma_f32_16x16x32_bf16 v[104:107],a[224:227],a[248:251],v[104:107]\nds_read_b128 a[248:251],v239 offset:24576\n"
"v_mfma_f32_16x16x32_bf16 v[24:27],a[212:215],a[252:255],v[24:27]\nbuffer_load_dwordx4 v227,s[48:51],s75 offen offset:2048 nt lds\ns_mov_b32 s72,365952\nv_mfma_f32_16x16x32_bf16 v[52:55],a[216:219],a[252:255],v[52:55]\nv_mfma_f32_16x16x32_bf16 v[80:83],a[220:223],a[252:255],v[80:83]\nv_mfma_f32_16x16x32_bf16 v[108:111],a[224:227],a[252:255],v[108:111]\nds_read_b128 a[252:255],v239 offset:26624\ns_waitcnt lgkmcnt(3)\n"
"v_mfma_f32_16x16x32_bf16 v[0:3],a[196:199],a[228:231],v[0:3]\nbuffer_load_dwordx4 v228,s[48:51],s72 offen offset:3072 nt lds\nv_mfma_f32_16x16x32_bf16 v[28:31],a[200:203],a[228:231],v[28:31]\nds_read_b128 a[212:215],v236 offset:0\nv_mfma_f32_16x16x32_bf16 v[56:59],a[204:207],a[228:231],v[56:59]\nv_mfma_f32_16x16x32_bf16 v[84:87],a[208:211],a[228:231],v[84:87]\nds_read_b128 a[228:231],v239 offset:15360\nv_mfma_f32_16x16x32_bf16 v[4:7],a[196:199],a[232:235],v[4:7]\n"
"ds_read_b128 a[216:219],v236 offset:2048\ns_add_u32 m0,s64,0\nv_mfma_f32_16x16x32_bf16 v[32:35],a[200:203],a[232:235],v[32:35]\ns_mov_b32 s73,393216\nv_mfma_f32_16x16x32_bf16 v[60:63],a[204:207],a[232:235],v[60:63]\nv_mfma_f32_16x16x32_bf16 v[88:91],a[208:211],a[232:235],v[88:91]\nds_read_b128 a[232:235],v239 offset:17408\nv_mfma_f32_16x16x32_bf16 v[8:11],a[196:199],a[236:239],v[8:11]\n"
"v_mfma_f32_16x16x32_bf16 v[36:39],a[200:203],a[236:239],v[36:39]\nv_mfma_f32_16x16x32_bf16 v[64:67],a[204:207],a[236:239],v[64:67]\nv_mfma_f32_16x16x32_bf16 v[92:95],a[208:211],a[236:239],v[92:95]\nds_read_b128 a[236:239],v239 offset:19456\ns_waitcnt lgkmcnt(7)\nv_mfma_f32_16x16x32_bf16 v[12:15],a[196:199],a[240:243],v[12:15]\nds_read_b128 a[220:223],v236 offset:4096\nv_mfma_f32_16x16x32_bf16 v[40:43],a[200:203],a[240:243],v[40:43]\n"
"v_mfma_f32_16x16x32_bf16 v[68:71],a[204:207],a[240:243],v[68:71]\nv_mfma_f32_16x16x32_bf16 v[96:99],a[208:211],a[240:243],v[96:99]\nds_read_b128 a[240:243],v239 offset:21504\nv_mfma_f32_16x16x32_bf16 v[16:19],a[196:199],a[244:247],v[16:19]\nds_read_b128 a[224:227],v236 offset:6144\nv_mfma_f32_16x16x32_bf16 v[44:47],a[200:203],a[244:247],v[44:47]\nv_mfma_f32_16x16x32_bf16 v[72:75],a[204:207],a[244:247],v[72:75]\nv_mfma_f32_16x16x32_bf16 v[100:103],a[208:211],a[244:247],v[100:103]\n"
"ds_read_b128 a[244:247],v239 offset:23552\ns_waitcnt lgkmcnt(9)\nv_mfma_f32_16x16x32_bf16 v[20:23],a[196:199],a[248:251],v[20:23]\nv_mfma_f32_16x16x32_bf16 v[48:51],a[200:203],a[248:251],v[48:51]\nv_mfma_f32_16x16x32_bf16 v[76:79],a[204:207],a[248:251],v[76:79]\nv_mfma_f32_16x16x32_bf16 v[104:107],a[208:211],a[248:251],v[104:107]\nds_read_b128 a[248:251],v239 offset:25600\nv_mfma_f32_16x16x32_bf16 v[24:27],a[196:199],a[252:255],v[24:27]\n"
"v_mfma_f32_16x16x32_bf16 v[52:55],a[200:203],a[252:255],v[52:55]\nv_mfma_f32_16x16x32_bf16 v[80:83],a[204:207],a[252:255],v[80:83]\nv_mfma_f32_16x16x32_bf16 v[108:111],a[208:211],a[252:255],v[108:111]\nds_read_b128 a[252:255],v239 offset:27648\ns_waitcnt lgkmcnt(3)\nv_mfma_f32_16x16x32_bf16 v[0:3],a[212:215],a[228:231],v[0:3]\nbuffer_load_dwordx4 v227,s[48:51],s73 offen offset:0 nt lds\ns_mov_b32 s74,400384\n"
"v_mfma_f32_16x16x32_bf16 v[28:31],a[216:219],a[228:231],v[28:31]\ns_waitcnt vmcnt(13)\nds_read_b128 a[196:199],v235 offset:8192\nv_mfma_f32_16x16x32_bf16 v[56:59],a[220:223],a[228:231],v[56:59]\nv_mfma_f32_16x16x32_bf16 v[84:87],a[224:227],a[228:231],v[84:87]\nds_read_b128 a[228:231],v239 offset:28672\nv_mfma_f32_16x16x32_bf16 v[4:7],a[212:215],a[232:235],v[4:7]\nbuffer_load_dwordx4 v228,s[48:51],s74 offen offset:1024 nt lds\n"
"s_mov_b32 s75,423936\nv_mfma_f32_16x16x32_bf16 v[32:35],a[216:219],a[232:235],v[32:35]\nds_read_b128 a[200:203],v235 offset:10240\nv_mfma_f32_16x16x32_bf16 v[60:63],a[220:223],a[232:235],v[60:63]\nv_mfma_f32_16x16x32_bf16 v[88:91],a[224:227],a[232:235],v[88:91]\nds_read_b128 a[232:235],v239 offset:30720\nv_mfma_f32_16x16x32_bf16 v[8:11],a[212:215],a[236:239],v[8:11]\nbuffer_load_dwordx4 v227,s[48:51],s75 offen offset:2048 nt lds\n"
"s_mov_b32 s72,431104\nv_mfma_f32_16x16x32_bf16 v[36:39],a[216:219],a[236:239],v[36:39]\nv_mfma_f32_16x16x32_bf16 v[64:67],a[220:223],a[236:239],v[64:67]\nv_mfma_f32_16x16x32_bf16 v[92:95],a[224:227],a[236:239],v[92:95]\nds_read_b128 a[236:239],v239 offset:32768\ns_waitcnt lgkmcnt(7)\nv_mfma_f32_16x16x32_bf16 v[12:15],a[212:215],a[240:243],v[12:15]\nbuffer_load_dwordx4 v228,s[48:51],s72 offen offset:3072 nt lds\n"
"s_add_u32 m0,s64,4096\nv_mfma_f32_16x16x32_bf16 v[40:43],a[216:219],a[240:243],v[40:43]\ns_waitcnt vmcnt(12)\nds_read_b128 a[204:207],v235 offset:12288\nv_mfma_f32_16x16x32_bf16 v[68:71],a[220:223],a[240:243],v[68:71]\ns_mov_b32 s73,458752\nv_mfma_f32_16x16x32_bf16 v[96:99],a[224:227],a[240:243],v[96:99]\nds_read_b128 a[240:243],v239 offset:34816\n"
"v_mfma_f32_16x16x32_bf16 v[16:19],a[212:215],a[244:247],v[16:19]\nbuffer_load_dwordx4 v227,s[48:51],s73 offen offset:0 nt lds\ns_mov_b32 s74,465920\nv_mfma_f32_16x16x32_bf16 v[44:47],a[216:219],a[244:247],v[44:47]\nds_read_b128 a[208:211],v235 offset:14336\nv_mfma_f32_16x16x32_bf16 v[72:75],a[220:223],a[244:247],v[72:75]\nv_mfma_f32_16x16x32_bf16 v[100:103],a[224:227],a[244:247],v[100:103]\nds_read_b128 a[244:247],v239 offset:36864\n"
"s_waitcnt lgkmcnt(9)\nv_mfma_f32_16x16x32_bf16 v[20:23],a[212:215],a[248:251],v[20:23]\nbuffer_load_dwordx4 v228,s[48:51],s74 offen offset:1024 nt lds\ns_mov_b32 s75,489472\nv_mfma_f32_16x16x32_bf16 v[48:51],a[216:219],a[248:251],v[48:51]\nv_mfma_f32_16x16x32_bf16 v[76:79],a[220:223],a[248:251],v[76:79]\nv_mfma_f32_16x16x32_bf16 v[104:107],a[224:227],a[248:251],v[104:107]\nds_read_b128 a[248:251],v239 offset:38912\n"
"v_mfma_f32_16x16x32_bf16 v[24:27],a[212:215],a[252:255],v[24:27]\nbuffer_load_dwordx4 v227,s[48:51],s75 offen offset:2048 nt lds\ns_mov_b32 s72,496640\nv_mfma_f32_16x16x32_bf16 v[52:55],a[216:219],a[252:255],v[52:55]\nv_mfma_f32_16x16x32_bf16 v[80:83],a[220:223],a[252:255],v[80:83]\nv_mfma_f32_16x16x32_bf16 v[108:111],a[224:227],a[252:255],v[108:111]\nds_read_b128 a[252:255],v239 offset:40960\ns_waitcnt lgkmcnt(3)\n"
"v_mfma_f32_16x16x32_bf16 v[0:3],a[196:199],a[228:231],v[0:3]\nbuffer_load_dwordx4 v228,s[48:51],s72 offen offset:3072 nt lds\nv_mfma_f32_16x16x32_bf16 v[28:31],a[200:203],a[228:231],v[28:31]\nds_read_b128 a[212:215],v236 offset:8192\nv_mfma_f32_16x16x32_bf16 v[56:59],a[204:207],a[228:231],v[56:59]\nv_mfma_f32_16x16x32_bf16 v[84:87],a[208:211],a[228:231],v[84:87]\nds_read_b128 a[228:231],v239 offset:29696\nv_mfma_f32_16x16x32_bf16 v[4:7],a[196:199],a[232:235],v[4:7]\n"
"ds_read_b128 a[216:219],v236 offset:10240\ns_add_u32 m0,s64,8192\nv_mfma_f32_16x16x32_bf16 v[32:35],a[200:203],a[232:235],v[32:35]\ns_mov_b32 s73,393344\nv_mfma_f32_16x16x32_bf16 v[60:63],a[204:207],a[232:235],v[60:63]\nv_mfma_f32_16x16x32_bf16 v[88:91],a[208:211],a[232:235],v[88:91]\nds_read_b128 a[232:235],v239 offset:31744\nv_mfma_f32_16x16x32_bf16 v[8:11],a[196:199],a[236:239],v[8:11]\n"
"v_mfma_f32_16x16x32_bf16 v[36:39],a[200:203],a[236:239],v[36:39]\nv_mfma_f32_16x16x32_bf16 v[64:67],a[204:207],a[236:239],v[64:67]\nv_mfma_f32_16x16x32_bf16 v[92:95],a[208:211],a[236:239],v[92:95]\nds_read_b128 a[236:239],v239 offset:33792\ns_waitcnt lgkmcnt(7)\nv_mfma_f32_16x16x32_bf16 v[12:15],a[196:199],a[240:243],v[12:15]\nds_read_b128 a[220:223],v236 offset:12288\nv_mfma_f32_16x16x32_bf16 v[40:43],a[200:203],a[240:243],v[40:43]\n"
"v_mfma_f32_16x16x32_bf16 v[68:71],a[204:207],a[240:243],v[68:71]\nv_mfma_f32_16x16x32_bf16 v[96:99],a[208:211],a[240:243],v[96:99]\nds_read_b128 a[240:243],v239 offset:35840\nv_mfma_f32_16x16x32_bf16 v[16:19],a[196:199],a[244:247],v[16:19]\nds_read_b128 a[224:227],v236 offset:14336\nv_mfma_f32_16x16x32_bf16 v[44:47],a[200:203],a[244:247],v[44:47]\nv_mfma_f32_16x16x32_bf16 v[72:75],a[204:207],a[244:247],v[72:75]\nv_mfma_f32_16x16x32_bf16 v[100:103],a[208:211],a[244:247],v[100:103]\n"
"ds_read_b128 a[244:247],v239 offset:37888\ns_waitcnt lgkmcnt(9)\nv_mfma_f32_16x16x32_bf16 v[20:23],a[196:199],a[248:251],v[20:23]\nv_mfma_f32_16x16x32_bf16 v[48:51],a[200:203],a[248:251],v[48:51]\nv_mfma_f32_16x16x32_bf16 v[76:79],a[204:207],a[248:251],v[76:79]\nv_mfma_f32_16x16x32_bf16 v[104:107],a[208:211],a[248:251],v[104:107]\nds_read_b128 a[248:251],v239 offset:39936\nv_mfma_f32_16x16x32_bf16 v[24:27],a[196:199],a[252:255],v[24:27]\n"
"v_mfma_f32_16x16x32_bf16 v[52:55],a[200:203],a[252:255],v[52:55]\nv_mfma_f32_16x16x32_bf16 v[80:83],a[204:207],a[252:255],v[80:83]\nv_mfma_f32_16x16x32_bf16 v[108:111],a[208:211],a[252:255],v[108:111]\nds_read_b128 a[252:255],v239 offset:41984\ns_waitcnt lgkmcnt(3)\nv_mfma_f32_16x16x32_bf16 v[0:3],a[212:215],a[228:231],v[0:3]\nbuffer_load_dwordx4 v227,s[48:51],s73 offen offset:0 nt lds\ns_mov_b32 s74,400512\n"
"v_mfma_f32_16x16x32_bf16 v[28:31],a[216:219],a[228:231],v[28:31]\ns_waitcnt vmcnt(13)\nds_read_b128 a[196:199],v235 offset:16384\nv_mfma_f32_16x16x32_bf16 v[56:59],a[220:223],a[228:231],v[56:59]\nv_mfma_f32_16x16x32_bf16 v[84:87],a[224:227],a[228:231],v[84:87]\nds_read_b128 a[228:231],v239 offset:43008\nv_mfma_f32_16x16x32_bf16 v[4:7],a[212:215],a[232:235],v[4:7]\nbuffer_load_dwordx4 v228,s[48:51],s74 offen offset:1024 nt lds\n"
"s_mov_b32 s75,424064\nv_mfma_f32_16x16x32_bf16 v[32:35],a[216:219],a[232:235],v[32:35]\nds_read_b128 a[200:203],v235 offset:18432\nv_mfma_f32_16x16x32_bf16 v[60:63],a[220:223],a[232:235],v[60:63]\nv_mfma_f32_16x16x32_bf16 v[88:91],a[224:227],a[232:235],v[88:91]\nds_read_b128 a[232:235],v239 offset:45056\nv_mfma_f32_16x16x32_bf16 v[8:11],a[212:215],a[236:239],v[8:11]\nbuffer_load_dwordx4 v227,s[48:51],s75 offen offset:2048 nt lds\n"
"s_mov_b32 s72,431232\nv_mfma_f32_16x16x32_bf16 v[36:39],a[216:219],a[236:239],v[36:39]\nv_mfma_f32_16x16x32_bf16 v[64:67],a[220:223],a[236:239],v[64:67]\nv_mfma_f32_16x16x32_bf16 v[92:95],a[224:227],a[236:239],v[92:95]\nds_read_b128 a[236:239],v239 offset:47104\ns_waitcnt lgkmcnt(7)\nv_mfma_f32_16x16x32_bf16 v[12:15],a[212:215],a[240:243],v[12:15]\nbuffer_load_dwordx4 v228,s[48:51],s72 offen offset:3072 nt lds\n"
"s_add_u32 m0,s64,12288\nv_mfma_f32_16x16x32_bf16 v[40:43],a[216:219],a[240:243],v[40:43]\ns_waitcnt vmcnt(12)\nds_read_b128 a[204:207],v235 offset:20480\nv_mfma_f32_16x16x32_bf16 v[68:71],a[220:223],a[240:243],v[68:71]\ns_mov_b32 s73,458880\nv_mfma_f32_16x16x32_bf16 v[96:99],a[224:227],a[240:243],v[96:99]\nds_read_b128 a[240:243],v239 offset:49152\n"
"v_mfma_f32_16x16x32_bf16 v[16:19],a[212:215],a[244:247],v[16:19]\nbuffer_load_dwordx4 v227,s[48:51],s73 offen offset:0 nt lds\ns_mov_b32 s74,466048\nv_mfma_f32_16x16x32_bf16 v[44:47],a[216:219],a[244:247],v[44:47]\nds_read_b128 a[208:211],v235 offset:22528\nv_mfma_f32_16x16x32_bf16 v[72:75],a[220:223],a[244:247],v[72:75]\nv_mfma_f32_16x16x32_bf16 v[100:103],a[224:227],a[244:247],v[100:103]\nds_read_b128 a[244:247],v239 offset:51200\n"
"s_waitcnt lgkmcnt(9)\nv_mfma_f32_16x16x32_bf16 v[20:23],a[212:215],a[248:251],v[20:23]\nbuffer_load_dwordx4 v228,s[48:51],s74 offen offset:1024 nt lds\ns_mov_b32 s75,489600\nv_mfma_f32_16x16x32_bf16 v[48:51],a[216:219],a[248:251],v[48:51]\nv_mfma_f32_16x16x32_bf16 v[76:79],a[220:223],a[248:251],v[76:79]\nv_mfma_f32_16x16x32_bf16 v[104:107],a[224:227],a[248:251],v[104:107]\nds_read_b128 a[248:251],v239 offset:53248\n"
"v_mfma_f32_16x16x32_bf16 v[24:27],a[212:215],a[252:255],v[24:27]\nbuffer_load_dwordx4 v227,s[48:51],s75 offen offset:2048 nt lds\ns_mov_b32 s72,496768\nv_mfma_f32_16x16x32_bf16 v[52:55],a[216:219],a[252:255],v[52:55]\nv_mfma_f32_16x16x32_bf16 v[80:83],a[220:223],a[252:255],v[80:83]\nv_mfma_f32_16x16x32_bf16 v[108:111],a[224:227],a[252:255],v[108:111]\nds_read_b128 a[252:255],v239 offset:55296\ns_waitcnt lgkmcnt(3)\n"
"v_mfma_f32_16x16x32_bf16 v[0:3],a[196:199],a[228:231],v[0:3]\nbuffer_load_dwordx4 v228,s[48:51],s72 offen offset:3072 nt lds\nv_mfma_f32_16x16x32_bf16 v[28:31],a[200:203],a[228:231],v[28:31]\nds_read_b128 a[212:215],v236 offset:16384\nv_mfma_f32_16x16x32_bf16 v[56:59],a[204:207],a[228:231],v[56:59]\nv_mfma_f32_16x16x32_bf16 v[84:87],a[208:211],a[228:231],v[84:87]\nds_read_b128 a[228:231],v239 offset:44032\nv_mfma_f32_16x16x32_bf16 v[4:7],a[196:199],a[232:235],v[4:7]\n"
"ds_read_b128 a[216:219],v236 offset:18432\ns_add_u32 m0,s64,16384\nv_mfma_f32_16x16x32_bf16 v[32:35],a[200:203],a[232:235],v[32:35]\ns_mov_b32 s73,393472\nv_mfma_f32_16x16x32_bf16 v[60:63],a[204:207],a[232:235],v[60:63]\nv_mfma_f32_16x16x32_bf16 v[88:91],a[208:211],a[232:235],v[88:91]\nds_read_b128 a[232:235],v239 offset:46080\nv_mfma_f32_16x16x32_bf16 v[8:11],a[196:199],a[236:239],v[8:11]\n"
"v_mfma_f32_16x16x32_bf16 v[36:39],a[200:203],a[236:239],v[36:39]\nv_mfma_f32_16x16x32_bf16 v[64:67],a[204:207],a[236:239],v[64:67]\nv_mfma_f32_16x16x32_bf16 v[92:95],a[208:211],a[236:239],v[92:95]\nds_read_b128 a[236:239],v239 offset:48128\ns_waitcnt lgkmcnt(7)\nv_mfma_f32_16x16x32_bf16 v[12:15],a[196:199],a[240:243],v[12:15]\nds_read_b128 a[220:223],v236 offset:20480\nv_mfma_f32_16x16x32_bf16 v[40:43],a[200:203],a[240:243],v[40:43]\n"
"v_mfma_f32_16x16x32_bf16 v[68:71],a[204:207],a[240:243],v[68:71]\nv_mfma_f32_16x16x32_bf16 v[96:99],a[208:211],a[240:243],v[96:99]\nds_read_b128 a[240:243],v239 offset:50176\nv_mfma_f32_16x16x32_bf16 v[16:19],a[196:199],a[244:247],v[16:19]\nds_read_b128 a[224:227],v236 offset:22528\nv_mfma_f32_16x16x32_bf16 v[44:47],a[200:203],a[244:247],v[44:47]\nv_mfma_f32_16x16x32_bf16 v[72:75],a[204:207],a[244:247],v[72:75]\nv_mfma_f32_16x16x32_bf16 v[100:103],a[208:211],a[244:247],v[100:103]\n"
"ds_read_b128 a[244:247],v239 offset:52224\ns_waitcnt lgkmcnt(9)\nv_mfma_f32_16x16x32_bf16 v[20:23],a[196:199],a[248:251],v[20:23]\nv_mfma_f32_16x16x32_bf16 v[48:51],a[200:203],a[248:251],v[48:51]\nv_mfma_f32_16x16x32_bf16 v[76:79],a[204:207],a[248:251],v[76:79]\nv_mfma_f32_16x16x32_bf16 v[104:107],a[208:211],a[248:251],v[104:107]\nds_read_b128 a[248:251],v239 offset:54272\nv_mfma_f32_16x16x32_bf16 v[24:27],a[196:199],a[252:255],v[24:27]\n"
"v_mfma_f32_16x16x32_bf16 v[52:55],a[200:203],a[252:255],v[52:55]\nv_mfma_f32_16x16x32_bf16 v[80:83],a[204:207],a[252:255],v[80:83]\nv_mfma_f32_16x16x32_bf16 v[108:111],a[208:211],a[252:255],v[108:111]\nds_read_b128 a[252:255],v239 offset:56320\ns_waitcnt lgkmcnt(3)\nv_mfma_f32_16x16x32_bf16 v[0:3],a[212:215],a[228:231],v[0:3]\nbuffer_load_dwordx4 v227,s[48:51],s73 offen offset:0 nt lds\nv_bfe_u32 v255,v239,2,6\n"
"v_mfma_f32_16x16x32_bf16 v[28:31],a[216:219],a[228:231],v[28:31]\ns_waitcnt vmcnt(13)\nds_read_b128 a[196:199],v235 offset:0\nv_mfma_f32_16x16x32_bf16 v[56:59],a[220:223],a[228:231],v[56:59]\ns_mov_b32 s74,400640\nv_mfma_f32_16x16x32_bf16 v[84:87],a[224:227],a[228:231],v[84:87]\nds_read_b128 a[228:231],v239 offset:0\nv_mfma_f32_16x16x32_bf16 v[4:7],a[212:215],a[232:235],v[4:7]\n"
"buffer_load_dwordx4 v228,s[48:51],s74 offen offset:1024 nt lds\ns_mov_b32 s75,424192\nv_mfma_f32_16x16x32_bf16 v[32:35],a[216:219],a[232:235],v[32:35]\nds_read_b128 a[200:203],v235 offset:2048\nv_mfma_f32_16x16x32_bf16 v[60:63],a[220:223],a[232:235],v[60:63]\nv_mfma_f32_16x16x32_bf16 v[88:91],a[224:227],a[232:235],v[88:91]\nds_read_b128 a[232:235],v239 offset:2048\nv_mfma_f32_16x16x32_bf16 v[8:11],a[212:215],a[236:239],v[8:11]\n"
"buffer_load_dwordx4 v227,s[48:51],s75 offen offset:2048 nt lds\nv_max3_f32 v241,v0,v1,v0\nv_mfma_f32_16x16x32_bf16 v[36:39],a[216:219],a[236:239],v[36:39]\ns_mov_b32 s72,431360\nv_max3_f32 v241,v2,v3,v241\nv_max3_f32 v241,v28,v29,v241\nv_max3_f32 v241,v30,v31,v241\nv_mfma_f32_16x16x32_bf16 v[64:67],a[220:223],a[236:239],v[64:67]\n"
"v_max3_f32 v241,v56,v57,v241\nv_max3_f32 v241,v58,v59,v241\nv_mfma_f32_16x16x32_bf16 v[92:95],a[224:227],a[236:239],v[92:95]\nds_read_b128 a[236:239],v239 offset:4096\ns_waitcnt lgkmcnt(7)\nv_max3_f32 v241,v84,v85,v241\nv_mfma_f32_16x16x32_bf16 v[12:15],a[212:215],a[240:243],v[12:15]\nbuffer_load_dwordx4 v228,s[48:51],s72 offen offset:3072 nt lds\n"
"v_max3_f32 v241,v86,v87,v241\nv_mfma_f32_16x16x32_bf16 v[40:43],a[216:219],a[240:243],v[40:43]\ns_waitcnt vmcnt(12)\nds_read_b128 a[204:207],v235 offset:4096\nv_mul_f32 v241,s69,v241\nv_mfma_f32_16x16x32_bf16 v[68:71],a[220:223],a[240:243],v[68:71]\ns_add_u32 m0,s64,20480\nds_max_f32 v255,v241 offset:57344\n"
"v_mfma_f32_16x16x32_bf16 v[96:99],a[224:227],a[240:243],v[96:99]\nds_read_b128 a[240:243],v239 offset:6144\nv_max3_f32 v242,v4,v5,v4\nv_max3_f32 v242,v6,v7,v242\nv_mfma_f32_16x16x32_bf16 v[16:19],a[212:215],a[244:247],v[16:19]\ns_mov_b32 s73,459008\nbuffer_load_dwordx4 v227,s[48:51],s73 offen offset:0 nt lds\nv_mfma_f32_16x16x32_bf16 v[44:47],a[216:219],a[244:247],v[44:47]\n"
"ds_read_b128 a[208:211],v235 offset:6144\nv_max3_f32 v242,v32,v33,v242\nv_max3_f32 v242,v34,v35,v242\nv_mfma_f32_16x16x32_bf16 v[72:75],a[220:223],a[244:247],v[72:75]\ns_mov_b32 s74,466176\nv_max3_f32 v242,v60,v61,v242\nv_max3_f32 v242,v62,v63,v242\nv_mfma_f32_16x16x32_bf16 v[100:103],a[224:227],a[244:247],v[100:103]\n"
"ds_read_b128 a[244:247],v239 offset:8192\ns_waitcnt lgkmcnt(10)\nv_max3_f32 v242,v88,v89,v242\nv_mfma_f32_16x16x32_bf16 v[20:23],a[212:215],a[248:251],v[20:23]\nbuffer_load_dwordx4 v228,s[48:51],s74 offen offset:1024 nt lds\nv_max3_f32 v242,v90,v91,v242\nv_mfma_f32_16x16x32_bf16 v[48:51],a[216:219],a[248:251],v[48:51]\ns_mov_b32 s75,489728\n"
"v_mul_f32 v242,s69,v242\nv_max3_f32 v243,v8,v9,v8\nv_max3_f32 v243,v10,v11,v243\nv_mfma_f32_16x16x32_bf16 v[76:79],a[220:223],a[248:251],v[76:79]\nds_max_f32 v255,v242 offset:57408\nv_mfma_f32_16x16x32_bf16 v[104:107],a[224:227],a[248:251],v[104:107]\nds_read_b128 a[248:251],v239 offset:10240\nv_max3_f32 v243,v36,v37,v243\n"
"v_max3_f32 v243,v38,v39,v243\nv_mfma_f32_16x16x32_bf16 v[24:27],a[212:215],a[252:255],v[24:27]\nbuffer_load_dwordx4 v227,s[48:51],s75 offen offset:2048 nt lds\nv_max3_f32 v243,v64,v65,v243\nv_mfma_f32_16x16x32_bf16 v[52:55],a[216:219],a[252:255],v[52:55]\ns_mov_b32 s72,496896\nv_max3_f32 v243,v66,v67,v243\nv_max3_f32 v243,v92,v93,v243\n"
"v_max3_f32 v243,v94,v95,v243\nv_mfma_f32_16x16x32_bf16 v[80:83],a[220:223],a[252:255],v[80:83]\nv_mul_f32 v243,s69,v243\nv_max3_f32 v244,v12,v13,v12\nv_mfma_f32_16x16x32_bf16 v[108:111],a[224:227],a[252:255],v[108:111]\nds_read_b128 a[252:255],v239 offset:12288\ns_waitcnt lgkmcnt(4)\nv_max3_f32 v244,v14,v15,v244\n"
"v_mfma_f32_16x16x32_bf16 v[112:115],a[196:199],a[228:231],0\nbuffer_load_dwordx4 v228,s[48:51],s72 offen offset:3072 nt lds\nv_max3_f32 v244,v40,v41,v244\nv_mfma_f32_16x16x32_bf16 v[140:143],a[200:203],a[228:231],0\nds_read_b128 a[212:215],v236 offset:0\nv_max3_f32 v244,v42,v43,v244\nv_max3_f32 v244,v68,v69,v244\nv_mfma_f32_16x16x32_bf16 v[168:171],a[204:207],a[228:231],0\n"
"ds_max_f32 v255,v243 offset:57472\nv_mfma_f32_16x16x32_bf16 v[196:199],a[208:211],a[228:231],0\nds_read_b128 a[228:231],v239 offset:1024\nv_max3_f32 v244,v70,v71,v244\nv_max3_f32 v244,v96,v97,v244\nv_mfma_f32_16x16x32_bf16 v[116:119],a[196:199],a[232:235],0\nds_read_b128 a[216:219],v236 offset:2048\nv_max3_f32 v244,v98,v99,v244\n"
"v_mfma_f32_16x16x32_bf16 v[144:147],a[200:203],a[232:235],0\ns_add_u32 m0,s64,0\nv_mul_f32 v244,s69,v244\nv_max3_f32 v245,v16,v17,v16\nv_max3_f32 v245,v18,v19,v245\nv_mfma_f32_16x16x32_bf16 v[172:175],a[204:207],a[232:235],0\ns_mov_b32 s73,393600\nds_max_f32 v255,v244 offset:57536\n"
"v_mfma_f32_16x16x32_bf16 v[200:203],a[208:211],a[232:235],0\nds_read_b128 a[232:235],v239 offset:3072\nv_max3_f32 v245,v44,v45,v245\nv_max3_f32 v245,v46,v47,v245\nv_mfma_f32_16x16x32_bf16 v[120:123],a[196:199],a[236:239],0\nv_max3_f32 v245,v72,v73,v245\nv_max3_f32 v245,v74,v75,v245\nv_mfma_f32_16x16x32_bf16 v[148:151],a[200:203],a[236:239],0\n"
"v_max3_f32 v245,v100,v101,v245\nv_max3_f32 v245,v102,v103,v245\nv_mul_f32 v245,s69,v245\nv_mfma_f32_16x16x32_bf16 v[176:179],a[204:207],a[236:239],0\nds_max_f32 v255,v245 offset:57600\nv_mfma_f32_16x16x32_bf16 v[204:207],a[208:211],a[236:239],0\nds_read_b128 a[236:239],v239 offset:5120\ns_waitcnt lgkmcnt(11)\n"
"v_max3_f32 v246,v20,v21,v20\nv_mfma_f32_16x16x32_bf16 v[124:127],a[196:199],a[240:243],0\nds_read_b128 a[220:223],v236 offset:4096\nv_max3_f32 v246,v22,v23,v246\nv_mfma_f32_16x16x32_bf16 v[152:155],a[200:203],a[240:243],0\nv_max3_f32 v246,v48,v49,v246\nv_max3_f32 v246,v50,v51,v246\nv_max3_f32 v246,v76,v77,v246\n"
"v_mfma_f32_16x16x32_bf16 v[180:183],a[204:207],a[240:243],0\nv_max3_f32 v246,v78,v79,v246\nv_max3_f32 v246,v104,v105,v246\nv_mfma_f32_16x16x32_bf16 v[208:211],a[208:211],a[240:243],0\nds_read_b128 a[240:243],v239 offset:7168\nv_max3_f32 v246,v106,v107,v246\nv_mul_f32 v246,s69,v246\nv_mfma_f32_16x16x32_bf16 v[128:131],a[196:199],a[244:247],0\n"
"ds_read_b128 a[224:227],v236 offset:6144\nv_max3_f32 v247,v24,v25,v24\nv_mfma_f32_16x16x32_bf16 v[156:159],a[200:203],a[244:247],0\nds_max_f32 v255,v246 offset:57664\nv_mfma_f32_16x16x32_bf16 v[184:187],a[204:207],a[244:247],0\nv_max3_f32 v247,v26,v27,v247\nv_max3_f32 v247,v52,v53,v247\nv_mfma_f32_16x16x32_bf16 v[212:215],a[208:211],a[244:247],0\n"
"ds_read_b128 a[244:247],v239 offset:9216\ns_waitcnt lgkmcnt(13)\nv_max3_f32 v247,v54,v55,v247\nv_mfma_f32_16x16x32_bf16 v[132:135],a[196:199],a[248:251],0\nv_max3_f32 v247,v80,v81,v247\nv_max3_f32 v247,v82,v83,v247\nv_mfma_f32_16x16x32_bf16 v[160:163],a[200:203],a[248:251],0\nv_max3_f32 v247,v108,v109,v247\n"
"v_max3_f32 v247,v110,v111,v247\nv_mul_f32 v247,s69,v247\nv_mfma_f32_16x16x32_bf16 v[188:191],a[204:207],a[248:251],0\nds_max_f32 v255,v247 offset:57728\nv_mfma_f32_16x16x32_bf16 v[216:219],a[208:211],a[248:251],0\nds_read_b128 a[248:251],v239 offset:11264\nv_mfma_f32_16x16x32_bf16 v[136:139],a[196:199],a[252:255],0\nv_mfma_f32_16x16x32_bf16 v[164:167],a[200:203],a[252:255],0\n"
"v_mfma_f32_16x16x32_bf16 v[192:195],a[204:207],a[252:255],0\nv_mfma_f32_16x16x32_bf16 v[220:223],a[208:211],a[252:255],0\nds_read_b128 a[252:255],v239 offset:13312\ns_waitcnt lgkmcnt(5)\ns_waitcnt lgkmcnt(1)\nv_mfma_f32_16x16x32_bf16 v[112:115],a[212:215],a[228:231],v[112:115]\nbuffer_load_dwordx4 v227,s[48:51],s73 offen offset:0 nt lds\ns_barrier\n"
"v_mfma_f32_16x16x32_bf16 v[140:143],a[216:219],a[228:231],v[140:143]\ns_waitcnt vmcnt(13)\nds_read_b128 a[196:199],v235 offset:8192\nv_mfma_f32_16x16x32_bf16 v[168:171],a[220:223],a[228:231],v[168:171]\ns_mov_b32 s74,400768\nds_read_b32 v241,v255 offset:57344\nv_mfma_f32_16x16x32_bf16 v[196:199],a[224:227],a[228:231],v[196:199]\nds_read_b128 a[228:231],v239 offset:14336\n"
"ds_read_b32 v242,v255 offset:57408\nv_mfma_f32_16x16x32_bf16 v[116:119],a[212:215],a[232:235],v[116:119]\nbuffer_load_dwordx4 v228,s[48:51],s74 offen offset:1024 nt lds\ns_mov_b32 s75,424320\nv_mfma_f32_16x16x32_bf16 v[144:147],a[216:219],a[232:235],v[144:147]\nds_read_b128 a[200:203],v235 offset:10240\nds_read_b32 v243,v255 offset:57472\nv_mfma_f32_16x16x32_bf16 v[172:175],a[220:223],a[232:235],v[172:175]\n"
"ds_read_b32 v244,v255 offset:57536\nv_mfma_f32_16x16x32_bf16 v[200:203],a[224:227],a[232:235],v[200:203]\nds_read_b128 a[232:235],v239 offset:16384\nds_read_b32 v245,v255 offset:57600\nv_mfma_f32_16x16x32_bf16 v[120:123],a[212:215],a[236:239],v[120:123]\nbuffer_load_dwordx4 v227,s[48:51],s75 offen offset:2048 nt lds\ns_mov_b32 s72,431488\nv_mfma_f32_16x16x32_bf16 v[148:151],a[216:219],a[236:239],v[148:151]\n"
"ds_read_b32 v246,v255 offset:57664\nds_read_b32 v247,v255 offset:57728\nv_mfma_f32_16x16x32_bf16 v[176:179],a[220:223],a[236:239],v[176:179]\nv_mfma_f32_16x16x32_bf16 v[204:207],a[224:227],a[236:239],v[204:207]\nds_read_b128 a[236:239],v239 offset:18432\nv_mfma_f32_16x16x32_bf16 v[124:127],a[212:215],a[240:243],v[124:127]\nbuffer_load_dwordx4 v228,s[48:51],s72 offen offset:3072 nt lds\ns_add_u32 m0,s64,4096\n"
"v_mfma_f32_16x16x32_bf16 v[152:155],a[216:219],a[240:243],v[152:155]\ns_waitcnt vmcnt(12)\nds_read_b128 a[204:207],v235 offset:12288\ns_waitcnt lgkmcnt(2)\nv_mfma_f32_16x16x32_bf16 v[180:183],a[220:223],a[240:243],v[180:183]\ns_mov_b32 s73,459136\nv_fma_f32 v0,v0,s69,-v241\nv_fma_f32 v1,v1,s69,-v241\n"
"v_mfma_f32_16x16x32_bf16 v[208:211],a[224:227],a[240:243],v[208:211]\nds_read_b128 a[240:243],v239 offset:20480\nv_fma_f32 v2,v2,s69,-v241\nv_fma_f32 v3,v3,s69,-v241\nv_mfma_f32_16x16x32_bf16 v[128:131],a[212:215],a[244:247],v[128:131]\nbuffer_load_dwordx4 v227,s[48:51],s73 offen offset:0 nt lds\nv_fma_f32 v4,v4,s69,-v242\nv_mfma_f32_16x16x32_bf16 v[156:159],a[216:219],a[244:247],v[156:159]\n"
"ds_read_b128 a[208:211],v235 offset:14336\nv_exp_f32 v0,v0\nv_mfma_f32_16x16x32_bf16 v[184:187],a[220:223],a[244:247],v[184:187]\ns_mov_b32 s74,466304\nv_exp_f32 v1,v1\nv_mfma_f32_16x16x32_bf16 v[212:215],a[224:227],a[244:247],v[212:215]\nds_read_b128 a[244:247],v239 offset:22528\nv_exp_f32 v2,v2\n"
"v_mfma_f32_16x16x32_bf16 v[132:135],a[212:215],a[248:251],v[132:135]\nbuffer_load_dwordx4 v228,s[48:51],s74 offen offset:1024 nt lds\nv_cvt_pk_bf16_f32 v0,v0,v1\nv_mfma_f32_16x16x32_bf16 v[160:163],a[216:219],a[248:251],v[160:163]\ns_mov_b32 s75,489856\nv_exp_f32 v3,v3\nv_fma_f32 v5,v5,s69,-v242\nv_mfma_f32_16x16x32_bf16 v[188:191],a[220:223],a[248:251],v[188:191]\n"
"v_cvt_pk_bf16_f32 v1,v2,v3\nv_fma_f32 v6,v6,s69,-v242\nv_mfma_f32_16x16x32_bf16 v[216:219],a[224:227],a[248:251],v[216:219]\nds_read_b128 a[248:251],v239 offset:24576\nv_fma_f32 v7,v7,s69,-v242\nv_fma_f32 v8,v8,s69,-v243\nv_mfma_f32_16x16x32_bf16 v[136:139],a[212:215],a[252:255],v[136:139]\nbuffer_load_dwordx4 v227,s[48:51],s75 offen offset:2048 nt lds\n"
"v_fma_f32 v9,v9,s69,-v243\nv_mfma_f32_16x16x32_bf16 v[164:167],a[216:219],a[252:255],v[164:167]\ns_mov_b32 s72,497024\nv_exp_f32 v4,v4\nv_fma_f32 v10,v10,s69,-v243\nv_mfma_f32_16x16x32_bf16 v[192:195],a[220:223],a[252:255],v[192:195]\nv_exp_f32 v5,v5\nv_mfma_f32_16x16x32_bf16 v[220:223],a[224:227],a[252:255],v[220:223]\n"
"ds_read_b128 a[252:255],v239 offset:26624\ns_waitcnt lgkmcnt(3)\nv_cvt_pk_bf16_f32 v4,v4,v5\nv_mfma_f32_16x16x32_bf16 v[112:115],a[196:199],a[228:231],v[112:115]\nbuffer_load_dwordx4 v228,s[48:51],s72 offen offset:3072 nt lds\nv_fma_f32 v11,v11,s69,-v243\nv_mfma_f32_16x16x32_bf16 v[140:143],a[200:203],a[228:231],v[140:143]\nds_read_b128 a[212:215],v236 offset:8192\n"
"v_exp_f32 v6,v6\nv_mfma_f32_16x16x32_bf16 v[168:171],a[204:207],a[228:231],v[168:171]\nv_exp_f32 v7,v7\nv_mfma_f32_16x16x32_bf16 v[196:199],a[208:211],a[228:231],v[196:199]\nds_read_b128 a[228:231],v239 offset:15360\nv_cvt_pk_bf16_f32 v5,v6,v7\nv_fma_f32 v12,v12,s69,-v244\nv_mfma_f32_16x16x32_bf16 v[116:119],a[196:199],a[232:235],v[116:119]\n"
"ds_read_b128 a[216:219],v236 offset:10240\nv_fma_f32 v13,v13,s69,-v244\nv_mfma_f32_16x16x32_bf16 v[144:147],a[200:203],a[232:235],v[144:147]\ns_add_u32 m0,s64,8192\nv_exp_f32 v8,v8\nv_fma_f32 v14,v14,s69,-v244\nv_mfma_f32_16x16x32_bf16 v[172:175],a[204:207],a[232:235],v[172:175]\ns_mov_b32 s73,262144\n"
"v_exp_f32 v9,v9\nv_mfma_f32_16x16x32_bf16 v[200:203],a[208:211],a[232:235],v[200:203]\nds_read_b128 a[232:235],v239 offset:17408\nv_exp_f32 v10,v10\nv_mfma_f32_16x16x32_bf16 v[120:123],a[196:199],a[236:239],v[120:123]\nv_exp_f32 v11,v11\nv_mfma_f32_16x16x32_bf16 v[148:151],a[200:203],a[236:239],v[148:151]\nv_cvt_pk_bf16_f32 v8,v8,v9\n"
"v_cvt_pk_bf16_f32 v9,v10,v11\nv_fma_f32 v15,v15,s69,-v244\nv_mfma_f32_16x16x32_bf16 v[176:179],a[204:207],a[236:239],v[176:179]\nv_exp_f32 v12,v12\nv_mfma_f32_16x16x32_bf16 v[204:207],a[208:211],a[236:239],v[204:207]\nds_read_b128 a[236:239],v239 offset:19456\ns_waitcnt lgkmcnt(7)\nv_fma_f32 v16,v16,s69,-v245\n"
"v_mfma_f32_16x16x32_bf16 v[124:127],a[196:199],a[240:243],v[124:127]\nds_read_b128 a[220:223],v236 offset:12288\nv_fma_f32 v17,v17,s69,-v245\nv_mfma_f32_16x16x32_bf16 v[152:155],a[200:203],a[240:243],v[152:155]\nv_exp_f32 v13,v13\nv_fma_f32 v18,v18,s69,-v245\nv_mfma_f32_16x16x32_bf16 v[180:183],a[204:207],a[240:243],v[180:183]\nv_exp_f32 v14,v14\n"
"v_mfma_f32_16x16x32_bf16 v[208:211],a[208:211],a[240:243],v[208:211]\nds_read_b128 a[240:243],v239 offset:21504\nv_exp_f32 v15,v15\nv_mfma_f32_16x16x32_bf16 v[128:131],a[196:199],a[244:247],v[128:131]\nds_read_b128 a[224:227],v236 offset:14336\nv_cvt_pk_bf16_f32 v12,v12,v13\nv_mfma_f32_16x16x32_bf16 v[156:159],a[200:203],a[244:247],v[156:159]\nv_cvt_pk_bf16_f32 v13,v14,v15\n"
"v_fma_f32 v19,v19,s69,-v245\nv_fma_f32 v20,v20,s69,-v246\nv_mfma_f32_16x16x32_bf16 v[184:187],a[204:207],a[244:247],v[184:187]\nv_exp_f32 v16,v16\nv_mfma_f32_16x16x32_bf16 v[212:215],a[208:211],a[244:247],v[212:215]\nds_read_b128 a[244:247],v239 offset:23552\ns_waitcnt lgkmcnt(9)\nv_fma_f32 v21,v21,s69,-v246\n"
"v_mfma_f32_16x16x32_bf16 v[132:135],a[196:199],a[248:251],v[132:135]\nv_exp_f32 v17,v17\nv_mfma_f32_16x16x32_bf16 v[160:163],a[200:203],a[248:251],v[160:163]\nv_exp_f32 v18,v18\nv_cvt_pk_bf16_f32 v16,v16,v17\nv_mfma_f32_16x16x32_bf16 v[188:191],a[204:207],a[248:251],v[188:191]\nv_exp_f32 v19,v19\nv_mfma_f32_16x16x32_bf16 v[216:219],a[208:211],a[248:251],v[216:219]\n"
"ds_read_b128 a[248:251],v239 offset:25600\nv_cvt_pk_bf16_f32 v17,v18,v19\nv_fma_f32 v22,v22,s69,-v246\nv_mfma_f32_16x16x32_bf16 v[136:139],a[196:199],a[252:255],v[136:139]\nv_fma_f32 v23,v23,s69,-v246\nv_fma_f32 v24,v24,s69,-v247\nv_mfma_f32_16x16x32_bf16 v[164:167],a[200:203],a[252:255],v[164:167]\nv_exp_f32 v20,v20\n"
"v_fma_f32 v25,v25,s69,-v247\nv_mfma_f32_16x16x32_bf16 v[192:195],a[204:207],a[252:255],v[192:195]\nv_exp_f32 v21,v21\nv_mfma_f32_16x16x32_bf16 v[220:223],a[208:211],a[252:255],v[220:223]\nds_read_b128 a[252:255],v239 offset:27648\ns_waitcnt lgkmcnt(3)\nv_cvt_pk_bf16_f32 v20,v20,v21\nv_mfma_f32_16x16x32_bf16 v[112:115],a[212:215],a[228:231],v[112:115]\n"
"buffer_load_dwordx4 v225,s[52:55],s73 offen offset:0 nt lds\nv_fma_f32 v26,v26,s69,-v247\nv_mfma_f32_16x16x32_bf16 v[140:143],a[216:219],a[228:231],v[140:143]\ns_waitcnt vmcnt(13)\nds_read_b128 a[196:199],v235 offset:16384\nv_fma_f32 v27,v27,s69,-v247\nv_mfma_f32_16x16x32_bf16 v[168:171],a[220:223],a[228:231],v[168:171]\ns_mov_b32 s74,263168\n"
"v_exp_f32 v22,v22\nv_mfma_f32_16x16x32_bf16 v[196:199],a[224:227],a[228:231],v[196:199]\nds_read_b128 a[228:231],v239 offset:28672\nv_exp_f32 v23,v23\nv_mfma_f32_16x16x32_bf16 v[116:119],a[212:215],a[232:235],v[116:119]\nbuffer_load_dwordx4 v225,s[52:55],s74 offen offset:1024 nt lds\nv_cvt_pk_bf16_f32 v21,v22,v23\nv_mfma_f32_16x16x32_bf16 v[144:147],a[216:219],a[232:235],v[144:147]\n"
"ds_read_b128 a[200:203],v235 offset:18432\nv_exp_f32 v24,v24\nv_mfma_f32_16x16x32_bf16 v[172:175],a[220:223],a[232:235],v[172:175]\ns_mov_b32 s75,268288\nv_exp_f32 v25,v25\nv_mfma_f32_16x16x32_bf16 v[200:203],a[224:227],a[232:235],v[200:203]\nds_read_b128 a[232:235],v239 offset:30720\nv_exp_f32 v26,v26\n"
"v_mfma_f32_16x16x32_bf16 v[120:123],a[212:215],a[236:239],v[120:123]\nbuffer_load_dwordx4 v225,s[52:55],s75 offen offset:2048 nt lds\nv_cvt_pk_bf16_f32 v24,v24,v25\nv_mfma_f32_16x16x32_bf16 v[148:151],a[216:219],a[236:239],v[148:151]\ns_mov_b32 s72,269312\nv_exp_f32 v27,v27\nv_fma_f32 v28,v28,s69,-v241\nv_mfma_f32_16x16x32_bf16 v[176:179],a[220:223],a[236:239],v[176:179]\n"
"v_cvt_pk_bf16_f32 v25,v26,v27\nv_fma_f32 v29,v29,s69,-v241\nv_mfma_f32_16x16x32_bf16 v[204:207],a[224:227],a[236:239],v[204:207]\nds_read_b128 a[236:239],v239 offset:32768\ns_waitcnt lgkmcnt(7)\nv_fma_f32 v30,v30,s69,-v241\nv_mfma_f32_16x16x32_bf16 v[124:127],a[212:215],a[240:243],v[124:127]\nbuffer_load_dwordx4 v225,s[52:55],s72 offen offset:3072 nt lds\n"
"v_fma_f32 v31,v31,s69,-v241\nv_mfma_f32_16x16x32_bf16 v[152:155],a[216:219],a[240:243],v[152:155]\ns_waitcnt vmcnt(12)\nds_read_b128 a[204:207],v235 offset:20480\nv_fma_f32 v32,v32,s69,-v242\nv_mfma_f32_16x16x32_bf16 v[180:183],a[220:223],a[240:243],v[180:183]\ns_add_u32 m0,s64,12288\nv_exp_f32 v28,v28\n"
"v_mfma_f32_16x16x32_bf16 v[208:211],a[224:227],a[240:243],v[208:211]\nds_read_b128 a[240:243],v239 offset:34816\nv_exp_f32 v29,v29\nv_mfma_f32_16x16x32_bf16 v[128:131],a[212:215],a[244:247],v[128:131]\ns_mov_b32 s73,278528\nbuffer_load_dwordx4 v225,s[52:55],s73 offen offset:0 nt lds\nv_mfma_f32_16x16x32_bf16 v[156:159],a[216:219],a[244:247],v[156:159]\nds_read_b128 a[208:211],v235 offset:22528\n"
"v_exp_f32 v30,v30\nv_mfma_f32_16x16x32_bf16 v[184:187],a[220:223],a[244:247],v[184:187]\ns_mov_b32 s74,279552\nv_exp_f32 v31,v31\nv_mfma_f32_16x16x32_bf16 v[212:215],a[224:227],a[244:247],v[212:215]\nds_read_b128 a[244:247],v239 offset:36864\ns_waitcnt lgkmcnt(9)\nv_cvt_pk_bf16_f32 v28,v28,v29\n"
"v_mfma_f32_16x16x32_bf16 v[132:135],a[212:215],a[248:251],v[132:135]\nbuffer_load_dwordx4 v225,s[52:55],s74 offen offset:1024 nt lds\nv_cvt_pk_bf16_f32 v29,v30,v31\nv_mfma_f32_16x16x32_bf16 v[160:163],a[216:219],a[248:251],v[160:163]\ns_mov_b32 s75,284672\nv_fma_f32 v33,v33,s69,-v242\nv_fma_f32 v34,v34,s69,-v242\nv_fma_f32 v35,v35,s69,-v242\n"
"v_mfma_f32_16x16x32_bf16 v[188:191],a[220:223],a[248:251],v[188:191]\nv_exp_f32 v32,v32\nv_mfma_f32_16x16x32_bf16 v[216:219],a[224:227],a[248:251],v[216:219]\nds_read_b128 a[248:251],v239 offset:38912\nv_exp_f32 v33,v33\nv_mfma_f32_16x16x32_bf16 v[136:139],a[212:215],a[252:255],v[136:139]\nbuffer_load_dwordx4 v225,s[52:55],s75 offen offset:2048 nt lds\nv_cvt_pk_bf16_f32 v32,v32,v33\n"
"v_mfma_f32_16x16x32_bf16 v[164:167],a[216:219],a[252:255],v[164:167]\ns_mov_b32 s72,285696\nv_exp_f32 v34,v34\nv_fma_f32 v36,v36,s69,-v243\nv_mfma_f32_16x16x32_bf16 v[192:195],a[220:223],a[252:255],v[192:195]\nv_exp_f32 v35,v35\nv_mfma_f32_16x16x32_bf16 v[220:223],a[224:227],a[252:255],v[220:223]\nds_read_b128 a[252:255],v239 offset:40960\n"
"s_waitcnt lgkmcnt(3)\nv_cvt_pk_bf16_f32 v33,v34,v35\nv_mfma_f32_16x16x32_bf16 v[112:115],a[196:199],a[228:231],v[112:115]\nbuffer_load_dwordx4 v225,s[52:55],s72 offen offset:3072 nt lds\nv_fma_f32 v37,v37,s69,-v243\nv_mfma_f32_16x16x32_bf16 v[140:143],a[200:203],a[228:231],v[140:143]\nds_read_b128 a[212:215],v236 offset:16384\nv_fma_f32 v38,v38,s69,-v243\n"
"v_fma_f32 v39,v39,s69,-v243\nv_mfma_f32_16x16x32_bf16 v[168:171],a[204:207],a[228:231],v[168:171]\nv_exp_f32 v36,v36\nv_mfma_f32_16x16x32_bf16 v[196:199],a[208:211],a[228:231],v[196:199]\nds_read_b128 a[228:231],v239 offset:29696\nv_exp_f32 v37,v37\nv_mfma_f32_16x16x32_bf16 v[116:119],a[196:199],a[232:235],v[116:119]\nds_read_b128 a[216:219],v236 offset:18432\n"
"v_cvt_pk_bf16_f32 v36,v36,v37\nv_mfma_f32_16x16x32_bf16 v[144:147],a[200:203],a[232:235],v[144:147]\ns_add_u32 m0,s64,16384\nv_exp_f32 v38,v38\nv_fma_f32 v40,v40,s69,-v244\nv_mfma_f32_16x16x32_bf16 v[172:175],a[204:207],a[232:235],v[172:175]\ns_mov_b32 s73,294912\nv_exp_f32 v39,v39\n"
"v_mfma_f32_16x16x32_bf16 v[200:203],a[208:211],a[232:235],v[200:203]\nds_read_b128 a[232:235],v239 offset:31744\nv_cvt_pk_bf16_f32 v37,v38,v39\nv_fma_f32 v41,v41,s69,-v244\nv_mfma_f32_16x16x32_bf16 v[120:123],a[196:199],a[236:239],v[120:123]\nv_fma_f32 v42,v42,s69,-v244\nv_fma_f32 v43,v43,s69,-v244\nv_mfma_f32_16x16x32_bf16 v[148:151],a[200:203],a[236:239],v[148:151]\n"
"v_exp_f32 v40,v40\nv_fma_f32 v44,v44,s69,-v245\nv_mfma_f32_16x16x32_bf16 v[176:179],a[204:207],a[236:239],v[176:179]\nv_exp_f32 v41,v41\nv_mfma_f32_16x16x32_bf16 v[204:207],a[208:211],a[236:239],v[204:207]\nds_read_b128 a[236:239],v239 offset:33792\ns_waitcnt lgkmcnt(7)\nv_cvt_pk_bf16_f32 v40,v40,v41\n"
"v_mfma_f32_16x16x32_bf16 v[124:127],a[196:199],a[240:243],v[124:127]\nds_read_b128 a[220:223],v236 offset:20480\nv_fma_f32 v45,v45,s69,-v245\nv_mfma_f32_16x16x32_bf16 v[152:155],a[200:203],a[240:243],v[152:155]\nv_exp_f32 v42,v42\nv_fma_f32 v46,v46,s69,-v245\nv_mfma_f32_16x16x32_bf16 v[180:183],a[204:207],a[240:243],v[180:183]\nv_exp_f32 v43,v43\n"
"v_mfma_f32_16x16x32_bf16 v[208:211],a[208:211],a[240:243],v[208:211]\nds_read_b128 a[240:243],v239 offset:35840\nv_cvt_pk_bf16_f32 v41,v42,v43\nv_fma_f32 v47,v47,s69,-v245\nv_mfma_f32_16x16x32_bf16 v[128:131],a[196:199],a[244:247],v[128:131]\nds_read_b128 a[224:227],v236 offset:22528\nv_fma_f32 v48,v48,s69,-v246\nv_mfma_f32_16x16x32_bf16 v[156:159],a[200:203],a[244:247],v[156:159]\n"
"v_exp_f32 v44,v44\nv_fma_f32 v49,v49,s69,-v246\nv_mfma_f32_16x16x32_bf16 v[184:187],a[204:207],a[244:247],v[184:187]\nv_exp_f32 v45,v45\nv_mfma_f32_16x16x32_bf16 v[212:215],a[208:211],a[244:247],v[212:215]\nds_read_b128 a[244:247],v239 offset:37888\ns_waitcnt lgkmcnt(9)\nv_cvt_pk_bf16_f32 v44,v44,v45\n"
"v_mfma_f32_16x16x32_bf16 v[132:135],a[196:199],a[248:251],v[132:135]\nv_exp_f32 v46,v46\nv_mfma_f32_16x16x32_bf16 v[160:163],a[200:203],a[248:251],v[160:163]\nv_exp_f32 v47,v47\nv_fma_f32 v50,v50,s69,-v246\nv_mfma_f32_16x16x32_bf16 v[188:191],a[204:207],a[248:251],v[188:191]\nv_cvt_pk_bf16_f32 v45,v46,v47\nv_fma_f32 v51,v51,s69,-v246\n"
"v_mfma_f32_16x16x32_bf16 v[216:219],a[208:211],a[248:251],v[216:219]\nds_read_b128 a[248:251],v239 offset:39936\nv_exp_f32 v48,v48\nv_mfma_f32_16x16x32_bf16 v[136:139],a[196:199],a[252:255],v[136:139]\nv_exp_f32 v49,v49\nv_mfma_f32_16x16x32_bf16 v[164:167],a[200:203],a[252:255],v[164:167]\nv_exp_f32 v50,v50\nv_cvt_pk_bf16_f32 v48,v48,v49\n"
"v_mfma_f32_16x16x32_bf16 v[192:195],a[204:207],a[252:255],v[192:195]\nv_exp_f32 v51,v51\nv_mfma_f32_16x16x32_bf16 v[220:223],a[208:211],a[252:255],v[220:223]\nds_read_b128 a[252:255],v239 offset:41984\ns_waitcnt lgkmcnt(3)\nv_cvt_pk_bf16_f32 v49,v50,v51\nv_mfma_f32_16x16x32_bf16 v[112:115],a[212:215],a[228:231],v[112:115]\nbuffer_load_dwordx4 v225,s[52:55],s73 offen offset:0 nt lds\n"
"v_fma_f32 v52,v52,s69,-v247\nv_mfma_f32_16x16x32_bf16 v[140:143],a[216:219],a[228:231],v[140:143]\ns_waitcnt vmcnt(13)\nds_read_b128 a[196:199],v235 offset:0\nv_fma_f32 v53,v53,s69,-v247\nv_mfma_f32_16x16x32_bf16 v[168:171],a[220:223],a[228:231],v[168:171]\ns_mov_b32 s74,295936\nv_fma_f32 v54,v54,s69,-v247\n"
"v_fma_f32 v55,v55,s69,-v247\nv_mfma_f32_16x16x32_bf16 v[196:199],a[224:227],a[228:231],v[196:199]\nds_read_b128 a[228:231],v239 offset:43008\nv_exp_f32 v52,v52\nv_mfma_f32_16x16x32_bf16 v[116:119],a[212:215],a[232:235],v[116:119]\nbuffer_load_dwordx4 v225,s[52:55],s74 offen offset:1024 nt lds\nv_fma_f32 v56,v56,s69,-v241\nv_mfma_f32_16x16x32_bf16 v[144:147],a[216:219],a[232:235],v[144:147]\n"
"ds_read_b128 a[200:203],v235 offset:2048\nv_exp_f32 v53,v53\nv_mfma_f32_16x16x32_bf16 v[172:175],a[220:223],a[232:235],v[172:175]\ns_mov_b32 s75,301056\nv_exp_f32 v54,v54\nv_mfma_f32_16x16x32_bf16 v[200:203],a[224:227],a[232:235],v[200:203]\nds_read_b128 a[232:235],v239 offset:45056\nv_exp_f32 v55,v55\n"
"v_mfma_f32_16x16x32_bf16 v[120:123],a[212:215],a[236:239],v[120:123]\nbuffer_load_dwordx4 v225,s[52:55],s75 offen offset:2048 nt lds\nv_cvt_pk_bf16_f32 v52,v52,v53\nv_mfma_f32_16x16x32_bf16 v[148:151],a[216:219],a[236:239],v[148:151]\ns_mov_b32 s72,302080\nv_cvt_pk_bf16_f32 v53,v54,v55\nv_fma_f32 v57,v57,s69,-v241\nv_fma_f32 v58,v58,s69,-v241\n"
"v_mfma_f32_16x16x32_bf16 v[176:179],a[220:223],a[236:239],v[176:179]\nv_fma_f32 v59,v59,s69,-v241\nv_fma_f32 v60,v60,s69,-v242\nv_mfma_f32_16x16x32_bf16 v[204:207],a[224:227],a[236:239],v[204:207]\nds_read_b128 a[236:239],v239 offset:47104\ns_waitcnt lgkmcnt(7)\nv_fma_f32 v61,v61,s69,-v242\nv_mfma_f32_16x16x32_bf16 v[124:127],a[212:215],a[240:243],v[124:127]\n"
"buffer_load_dwordx4 v225,s[52:55],s72 offen offset:3072 nt lds\nv_fma_f32 v62,v62,s69,-v242\nv_mfma_f32_16x16x32_bf16 v[152:155],a[216:219],a[240:243],v[152:155]\ns_waitcnt vmcnt(12)\nds_read_b128 a[204:207],v235 offset:4096\nv_fma_f32 v63,v63,s69,-v242\nv_mfma_f32_16x16x32_bf16 v[180:183],a[220:223],a[240:243],v[180:183]\ns_add_u32 m0,s64,20480\n"
"v_exp_f32 v56,v56\nv_mfma_f32_16x16x32_bf16 v[208:211],a[224:227],a[240:243],v[208:211]\nds_read_b128 a[240:243],v239 offset:49152\nv_exp_f32 v57,v57\nv_mfma_f32_16x16x32_bf16 v[128:131],a[212:215],a[244:247],v[128:131]\ns_mov_b32 s73,311296\nbuffer_load_dwordx4 v225,s[52:55],s73 offen offset:0 nt lds\nv_mfma_f32_16x16x32_bf16 v[156:159],a[216:219],a[244:247],v[156:159]\n"
"ds_read_b128 a[208:211],v235 offset:6144\nv_exp_f32 v58,v58\nv_mfma_f32_16x16x32_bf16 v[184:187],a[220:223],a[244:247],v[184:187]\ns_mov_b32 s74,312320\nv_exp_f32 v59,v59\nv_mfma_f32_16x16x32_bf16 v[212:215],a[224:227],a[244:247],v[212:215]\nds_read_b128 a[244:247],v239 offset:51200\ns_waitcnt lgkmcnt(9)\n"
"v_cvt_pk_bf16_f32 v56,v56,v57\nv_mfma_f32_16x16x32_bf16 v[132:135],a[212:215],a[248:251],v[132:135]\nbuffer_load_dwordx4 v225,s[52:55],s74 offen offset:1024 nt lds\nv_cvt_pk_bf16_f32 v57,v58,v59\nv_mfma_f32_16x16x32_bf16 v[160:163],a[216:219],a[248:251],v[160:163]\ns_mov_b32 s75,317440\nv_exp_f32 v60,v60\nv_fma_f32 v64,v64,s69,-v243\n"
"v_mfma_f32_16x16x32_bf16 v[188:191],a[220:223],a[248:251],v[188:191]\nv_exp_f32 v61,v61\nv_mfma_f32_16x16x32_bf16 v[216:219],a[224:227],a[248:251],v[216:219]\nds_read_b128 a[248:251],v239 offset:53248\nv_exp_f32 v62,v62\nv_mfma_f32_16x16x32_bf16 v[136:139],a[212:215],a[252:255],v[136:139]\nbuffer_load_dwordx4 v225,s[52:55],s75 offen offset:2048 nt lds\nv_cvt_pk_bf16_f32 v60,v60,v61\n"
"v_mfma_f32_16x16x32_bf16 v[164:167],a[216:219],a[252:255],v[164:167]\ns_mov_b32 s72,318464\nv_exp_f32 v63,v63\nv_fma_f32 v65,v65,s69,-v243\nv_mfma_f32_16x16x32_bf16 v[192:195],a[220:223],a[252:255],v[192:195]\nv_cvt_pk_bf16_f32 v61,v62,v63\nv_fma_f32 v66,v66,s69,-v243\nv_mfma_f32_16x16x32_bf16 v[220:223],a[224:227],a[252:255],v[220:223]\n"
"ds_read_b128 a[252:255],v239 offset:55296\ns_waitcnt lgkmcnt(3)\nv_fma_f32 v67,v67,s69,-v243\nv_mfma_f32_16x16x32_bf16 v[112:115],a[196:199],a[228:231],v[112:115]\nbuffer_load_dwordx4 v225,s[52:55],s72 offen offset:3072 nt lds\nv_fma_f32 v68,v68,s69,-v244\nv_mfma_f32_16x16x32_bf16 v[140:143],a[200:203],a[228:231],v[140:143]\nds_read_b128 a[212:215],v236 offset:0\n"
"v_exp_f32 v64,v64\nv_mfma_f32_16x16x32_bf16 v[168:171],a[204:207],a[228:231],v[168:171]\nv_exp_f32 v65,v65\nv_mfma_f32_16x16x32_bf16 v[196:199],a[208:211],a[228:231],v[196:199]\nds_read_b128 a[228:231],v239 offset:44032\nv_exp_f32 v66,v66\nv_mfma_f32_16x16x32_bf16 v[116:119],a[196:199],a[232:235],v[116:119]\nds_read_b128 a[216:219],v236 offset:2048\n"
"v_cvt_pk_bf16_f32 v64,v64,v65\nv_mfma_f32_16x16x32_bf16 v[144:147],a[200:203],a[232:235],v[144:147]\ns_add_u32 m0,s64,0\nv_exp_f32 v67,v67\nv_fma_f32 v69,v69,s69,-v244\nv_mfma_f32_16x16x32_bf16 v[172:175],a[204:207],a[232:235],v[172:175]\ns_mov_b32 s73,327680\nv_cvt_pk_bf16_f32 v65,v66,v67\n"
"v_fma_f32 v70,v70,s69,-v244\nv_mfma_f32_16x16x32_bf16 v[200:203],a[208:211],a[232:235],v[200:203]\nds_read_b128 a[232:235],v239 offset:46080\nv_fma_f32 v71,v71,s69,-v244\nv_fma_f32 v72,v72,s69,-v245\nv_mfma_f32_16x16x32_bf16 v[120:123],a[196:199],a[236:239],v[120:123]\nv_exp_f32 v68,v68\nv_mfma_f32_16x16x32_bf16 v[148:151],a[200:203],a[236:239],v[148:151]\n"
"v_exp_f32 v69,v69\nv_fma_f32 v73,v73,s69,-v245\nv_mfma_f32_16x16x32_bf16 v[176:179],a[204:207],a[236:239],v[176:179]\nv_exp_f32 v70,v70\nv_mfma_f32_16x16x32_bf16 v[204:207],a[208:211],a[236:239],v[204:207]\nds_read_b128 a[236:239],v239 offset:48128\ns_waitcnt lgkmcnt(7)\nv_cvt_pk_bf16_f32 v68,v68,v69\n"
"v_mfma_f32_16x16x32_bf16 v[124:127],a[196:199],a[240:243],v[124:127]\nds_read_b128 a[220:223],v236 offset:4096\nv_fma_f32 v74,v74,s69,-v245\nv_mfma_f32_16x16x32_bf16 v[152:155],a[200:203],a[240:243],v[152:155]\nv_exp_f32 v71,v71\nv_fma_f32 v75,v75,s69,-v245\nv_mfma_f32_16x16x32_bf16 v[180:183],a[204:207],a[240:243],v[180:183]\nv_cvt_pk_bf16_f32 v69,v70,v71\n"
"v_fma_f32 v76,v76,s69,-v246\nv_mfma_f32_16x16x32_bf16 v[208:211],a[208:211],a[240:243],v[208:211]\nds_read_b128 a[240:243],v239 offset:50176\nv_exp_f32 v72,v72\nv_mfma_f32_16x16x32_bf16 v[128:131],a[196:199],a[244:247],v[128:131]\nds_read_b128 a[224:227],v236 offset:6144\nv_fma_f32 v77,v77,s69,-v246\nv_mfma_f32_16x16x32_bf16 v[156:159],a[200:203],a[244:247],v[156:159]\n"
"v_exp_f32 v73,v73\nv_fma_f32 v78,v78,s69,-v246\nv_mfma_f32_16x16x32_bf16 v[184:187],a[204:207],a[244:247],v[184:187]\nv_exp_f32 v74,v74\nv_mfma_f32_16x16x32_bf16 v[212:215],a[208:211],a[244:247],v[212:215]\nds_read_b128 a[244:247],v239 offset:52224\ns_waitcnt lgkmcnt(9)\nv_cvt_pk_bf16_f32 v72,v72,v73\n"
"v_mfma_f32_16x16x32_bf16 v[132:135],a[196:199],a[248:251],v[132:135]\nv_exp_f32 v75,v75\nv_mfma_f32_16x16x32_bf16 v[160:163],a[200:203],a[248:251],v[160:163]\nv_cvt_pk_bf16_f32 v73,v74,v75\nv_fma_f32 v79,v79,s69,-v246\nv_fma_f32 v80,v80,s69,-v247\nv_mfma_f32_16x16x32_bf16 v[188:191],a[204:207],a[248:251],v[188:191]\nv_exp_f32 v76,v76\n"
"v_mfma_f32_16x16x32_bf16 v[216:219],a[208:211],a[248:251],v[216:219]\nds_read_b128 a[248:251],v239 offset:54272\nv_exp_f32 v77,v77\nv_mfma_f32_16x16x32_bf16 v[136:139],a[196:199],a[252:255],v[136:139]\nv_exp_f32 v78,v78\nv_mfma_f32_16x16x32_bf16 v[164:167],a[200:203],a[252:255],v[164:167]\nv_exp_f32 v79,v79\nv_cvt_pk_bf16_f32 v76,v76,v77\n"
"v_mfma_f32_16x16x32_bf16 v[192:195],a[204:207],a[252:255],v[192:195]\nv_cvt_pk_bf16_f32 v77,v78,v79\nv_fma_f32 v81,v81,s69,-v247\nv_mfma_f32_16x16x32_bf16 v[220:223],a[208:211],a[252:255],v[220:223]\nds_read_b128 a[252:255],v239 offset:56320\ns_waitcnt lgkmcnt(3)\nv_fma_f32 v82,v82,s69,-v247\nv_mfma_f32_16x16x32_bf16 v[112:115],a[212:215],a[228:231],v[112:115]\n"
"buffer_load_dwordx4 v225,s[52:55],s73 offen offset:0 nt lds\nv_fma_f32 v83,v83,s69,-v247\nv_mfma_f32_16x16x32_bf16 v[140:143],a[216:219],a[228:231],v[140:143]\ns_waitcnt vmcnt(13)\nds_read_b64_tr_b16 a[196:197],v231 offset:8192\nv_fma_f32 v84,v84,s69,-v241\nv_mfma_f32_16x16x32_bf16 v[168:171],a[220:223],a[228:231],v[168:171]\nds_read_b64_tr_b16 a[198:199],v231 offset:9216\n"
"v_fma_f32 v85,v85,s69,-v241\nv_mfma_f32_16x16x32_bf16 v[196:199],a[224:227],a[228:231],v[196:199]\ns_waitcnt lgkmcnt(2)\ns_barrier\nv_exp_f32 v80,v80\nv_mfma_f32_16x16x32_bf16 v[116:119],a[212:215],a[232:235],v[116:119]\ns_mov_b32 s74,328704\nbuffer_load_dwordx4 v225,s[52:55],s74 offen offset:1024 nt lds\n"
"ds_write_b64 v240,v[0:1] offset:0\nv_mfma_f32_16x16x32_bf16 v[144:147],a[216:219],a[232:235],v[144:147]\nds_read_b64_tr_b16 a[200:201],v232 offset:8192\nv_exp_f32 v81,v81\nv_mfma_f32_16x16x32_bf16 v[172:175],a[220:223],a[232:235],v[172:175]\ns_mov_b32 s75,333824\nv_exp_f32 v82,v82\nv_mfma_f32_16x16x32_bf16 v[200:203],a[224:227],a[232:235],v[200:203]\n"
"v_exp_f32 v83,v83\nv_cvt_pk_bf16_f32 v80,v80,v81\nv_mfma_f32_16x16x32_bf16 v[120:123],a[212:215],a[236:239],v[120:123]\nbuffer_load_dwordx4 v225,s[52:55],s75 offen offset:2048 nt lds\nds_write_b64 v240,v[4:5] offset:2048\nv_mfma_f32_16x16x32_bf16 v[148:151],a[216:219],a[236:239],v[148:151]\nds_read_b64_tr_b16 a[202:203],v232 offset:9216\nv_cvt_pk_bf16_f32 v81,v82,v83\n"
"v_fma_f32 v86,v86,s69,-v241\nv_mfma_f32_16x16x32_bf16 v[176:179],a[220:223],a[236:239],v[176:179]\ns_mov_b32 s72,334848\nv_fma_f32 v87,v87,s69,-v241\nv_fma_f32 v88,v88,s69,-v242\nv_mfma_f32_16x16x32_bf16 v[204:207],a[224:227],a[236:239],v[204:207]\nv_exp_f32 v84,v84\nv_fma_f32 v89,v89,s69,-v242\n"
"v_mfma_f32_16x16x32_bf16 v[124:127],a[212:215],a[240:243],v[124:127]\nbuffer_load_dwordx4 v225,s[52:55],s72 offen offset:3072 nt lds\nds_write_b64 v240,v[8:9] offset:4096\nv_mfma_f32_16x16x32_bf16 v[152:155],a[216:219],a[240:243],v[152:155]\nds_read_b64_tr_b16 a[204:205],v233 offset:8192\nv_exp_f32 v85,v85\nv_mfma_f32_16x16x32_bf16 v[180:183],a[220:223],a[240:243],v[180:183]\nds_read_b64_tr_b16 a[206:207],v233 offset:9216\n"
"v_cvt_pk_bf16_f32 v84,v84,v85\nv_mfma_f32_16x16x32_bf16 v[208:211],a[224:227],a[240:243],v[208:211]\ns_add_u32 m0,s64,4096\nv_exp_f32 v86,v86\nv_fma_f32 v90,v90,s69,-v242\nv_mfma_f32_16x16x32_bf16 v[128:131],a[212:215],a[244:247],v[128:131]\ns_mov_b32 s73,344064\nbuffer_load_dwordx4 v225,s[52:55],s73 offen offset:0 nt lds\n"
"ds_write_b64 v240,v[12:13] offset:6144\nv_mfma_f32_16x16x32_bf16 v[156:159],a[216:219],a[244:247],v[156:159]\nds_read_b64_tr_b16 a[208:209],v234 offset:8192\nv_exp_f32 v87,v87\nv_mfma_f32_16x16x32_bf16 v[184:187],a[220:223],a[244:247],v[184:187]\ns_mov_b32 s74,345088\nv_cvt_pk_bf16_f32 v85,v86,v87\nv_fma_f32 v91,v91,s69,-v242\n"
"v_mfma_f32_16x16x32_bf16 v[212:215],a[224:227],a[244:247],v[212:215]\nv_exp_f32 v88,v88\nv_fma_f32 v92,v92,s69,-v243\nv_mfma_f32_16x16x32_bf16 v[132:135],a[212:215],a[248:251],v[132:135]\nbuffer_load_dwordx4 v225,s[52:55],s74 offen offset:1024 nt lds\nds_write_b64 v240,v[16:17] offset:8192\nv_mfma_f32_16x16x32_bf16 v[160:163],a[216:219],a[248:251],v[160:163]\nds_read_b64_tr_b16 a[210:211],v234 offset:9216\n"
"v_exp_f32 v89,v89\nv_mfma_f32_16x16x32_bf16 v[188:191],a[220:223],a[248:251],v[188:191]\ns_mov_b32 s75,350208\nv_exp_f32 v90,v90\nv_mfma_f32_16x16x32_bf16 v[216:219],a[224:227],a[248:251],v[216:219]\nv_exp_f32 v91,v91\nv_cvt_pk_bf16_f32 v88,v88,v89\nv_mfma_f32_16x16x32_bf16 v[136:139],a[212:215],a[252:255],v[136:139]\n"
"buffer_load_dwordx4 v225,s[52:55],s75 offen offset:2048 nt lds\nds_write_b64 v240,v[20:21] offset:10240\nv_mfma_f32_16x16x32_bf16 v[164:167],a[216:219],a[252:255],v[164:167]\ns_mov_b32 s72,351232\nv_cvt_pk_bf16_f32 v89,v90,v91\nv_fma_f32 v93,v93,s69,-v243\nv_fma_f32 v94,v94,s69,-v243\nv_mfma_f32_16x16x32_bf16 v[192:195],a[220:223],a[252:255],v[192:195]\n"
"v_fma_f32 v95,v95,s69,-v243\nv_fma_f32 v96,v96,s69,-v244\nv_mfma_f32_16x16x32_bf16 v[220:223],a[224:227],a[252:255],v[220:223]\nv_exp_f32 v92,v92\nv_fma_f32 v97,v97,s69,-v244\nv_exp_f32 v93,v93\nv_exp_f32 v94,v94\nv_exp_f32 v95,v95\n"
"v_cvt_pk_bf16_f32 v92,v92,v93\nv_cvt_pk_bf16_f32 v93,v94,v95\nv_fma_f32 v98,v98,s69,-v244\nv_fma_f32 v99,v99,s69,-v244\nv_exp_f32 v96,v96\nv_exp_f32 v97,v97\nv_exp_f32 v98,v98\nv_exp_f32 v99,v99\n"
"v_cvt_pk_bf16_f32 v96,v96,v97\nv_cvt_pk_bf16_f32 v97,v98,v99\nv_fma_f32 v100,v100,s69,-v245\nv_fma_f32 v101,v101,s69,-v245\nv_fma_f32 v102,v102,s69,-v245\nv_fma_f32 v103,v103,s69,-v245\nv_exp_f32 v100,v100\nv_exp_f32 v101,v101\n"
"v_exp_f32 v102,v102\nv_exp_f32 v103,v103\nv_cvt_pk_bf16_f32 v100,v100,v101\nv_cvt_pk_bf16_f32 v101,v102,v103\nv_fma_f32 v104,v104,s69,-v246\nv_fma_f32 v105,v105,s69,-v246\nv_fma_f32 v106,v106,s69,-v246\nv_fma_f32 v107,v107,s69,-v246\n"
"v_exp_f32 v104,v104\nv_exp_f32 v105,v105\nv_exp_f32 v106,v106\nv_exp_f32 v107,v107\nv_cvt_pk_bf16_f32 v104,v104,v105\nv_cvt_pk_bf16_f32 v105,v106,v107\nv_fma_f32 v108,v108,s69,-v247\nv_fma_f32 v109,v109,s69,-v247\n"
"v_fma_f32 v110,v110,s69,-v247\nv_fma_f32 v111,v111,s69,-v247\nv_exp_f32 v108,v108\nv_exp_f32 v109,v109\nv_exp_f32 v110,v110\nv_exp_f32 v111,v111\nv_cvt_pk_bf16_f32 v108,v108,v109\nv_cvt_pk_bf16_f32 v109,v110,v111\n"
"ds_write_b64 v240,v[24:25] offset:12288\nv_max3_f32 v248,v112,v113,v112\nv_max3_f32 v248,v114,v115,v248\nv_max3_f32 v248,v140,v141,v248\nv_max3_f32 v248,v142,v143,v248\nv_max3_f32 v248,v168,v169,v248\nv_max3_f32 v248,v170,v171,v248\nds_write_b64 v240,v[28:29] offset:14336\n"
"v_max3_f32 v248,v196,v197,v248\nv_max3_f32 v248,v198,v199,v248\nv_mul_f32 v248,s69,v248\nds_max_f32 v255,v248 offset:57344\nv_max3_f32 v249,v116,v117,v116\nv_max3_f32 v249,v118,v119,v249\nbuffer_load_dwordx4 v225,s[52:55],s72 offen offset:3072 nt lds\nds_write_b64 v240,v[32:33] offset:16384\n"
"v_max3_f32 v249,v144,v145,v249\nv_max3_f32 v249,v146,v147,v249\nv_max3_f32 v249,v172,v173,v249\nv_max3_f32 v249,v174,v175,v249\nv_max3_f32 v249,v200,v201,v249\nv_max3_f32 v249,v202,v203,v249\nds_write_b64 v240,v[36:37] offset:18432\nv_mul_f32 v249,s69,v249\n"
"ds_max_f32 v255,v249 offset:57408\nv_max3_f32 v250,v120,v121,v120\nv_max3_f32 v250,v122,v123,v250\nv_max3_f32 v250,v148,v149,v250\nv_max3_f32 v250,v150,v151,v250\nds_write_b64 v240,v[40:41] offset:20480\nv_max3_f32 v250,v176,v177,v250\nv_max3_f32 v250,v178,v179,v250\n"
"v_max3_f32 v250,v204,v205,v250\nv_max3_f32 v250,v206,v207,v250\nv_mul_f32 v250,s69,v250\nds_max_f32 v255,v250 offset:57472\nds_write_b64 v240,v[44:45] offset:22528\nv_max3_f32 v251,v124,v125,v124\nv_max3_f32 v251,v126,v127,v251\nv_max3_f32 v251,v152,v153,v251\n"
"v_max3_f32 v251,v154,v155,v251\nv_max3_f32 v251,v180,v181,v251\nv_max3_f32 v251,v182,v183,v251\nds_write_b64 v240,v[48:49] offset:24576\nv_max3_f32 v251,v208,v209,v251\nv_max3_f32 v251,v210,v211,v251\nv_mul_f32 v251,s69,v251\nds_max_f32 v255,v251 offset:57536\n"
"v_max3_f32 v252,v128,v129,v128\nv_max3_f32 v252,v130,v131,v252\nds_write_b64 v240,v[52:53] offset:26624\nv_max3_f32 v252,v156,v157,v252\nv_max3_f32 v252,v158,v159,v252\nv_max3_f32 v252,v184,v185,v252\nv_max3_f32 v252,v186,v187,v252\nv_max3_f32 v252,v212,v213,v252\n"
"v_max3_f32 v252,v214,v215,v252\nds_write_b64 v240,v[56:57] offset:28672\nv_mul_f32 v252,s69,v252\nds_max_f32 v255,v252 offset:57600\nv_max3_f32 v253,v132,v133,v132\nv_max3_f32 v253,v134,v135,v253\nv_max3_f32 v253,v160,v161,v253\nv_max3_f32 v253,v162,v163,v253\n"
"ds_write_b64 v240,v[60:61] offset:30720\nv_max3_f32 v253,v188,v189,v253\nv_max3_f32 v253,v190,v191,v253\nv_max3_f32 v253,v216,v217,v253\nv_max3_f32 v253,v218,v219,v253\nv_mul_f32 v253,s69,v253\nds_max_f32 v255,v253 offset:57664\nds_write_b64 v240,v[64:65] offset:32768\n"
"v_max3_f32 v254,v136,v137,v136\nv_max3_f32 v254,v138,v139,v254\nv_max3_f32 v254,v164,v165,v254\nv_max3_f32 v254,v166,v167,v254\nv_max3_f32 v254,v192,v193,v254\nv_max3_f32 v254,v194,v195,v254\nds_write_b64 v240,v[68:69] offset:34816\nv_max3_f32 v254,v220,v221,v254\n"
"v_max3_f32 v254,v222,v223,v254\nv_mul_f32 v254,s69,v254\nds_max_f32 v255,v254 offset:57728\nv_dot2_f32_bf16 v2,v0,s35,0\nv_dot2_f32_bf16 v2,v1,s35,v2\nds_write_b64 v240,v[72:73] offset:36864\nv_dot2_f32_bf16 v6,v4,s35,0\nv_dot2_f32_bf16 v6,v5,s35,v6\n"
"v_dot2_f32_bf16 v10,v8,s35,0\nv_dot2_f32_bf16 v10,v9,s35,v10\nv_dot2_f32_bf16 v14,v12,s35,0\nv_dot2_f32_bf16 v14,v13,s35,v14\nds_write_b64 v240,v[76:77] offset:38912\nv_dot2_f32_bf16 v18,v16,s35,0\nv_dot2_f32_bf16 v18,v17,s35,v18\nv_dot2_f32_bf16 v22,v20,s35,0\n"
"v_dot2_f32_bf16 v22,v21,s35,v22\nv_dot2_f32_bf16 v26,v24,s35,0\nv_dot2_f32_bf16 v26,v25,s35,v26\nds_write_b64 v240,v[80:81] offset:40960\nv_dot2_f32_bf16 v2,v28,s35,v2\nv_dot2_f32_bf16 v2,v29,s35,v2\nv_dot2_f32_bf16 v6,v32,s35,v6\nv_dot2_f32_bf16 v6,v33,s35,v6\n"
"v_dot2_f32_bf16 v10,v36,s35,v10\nv_dot2_f32_bf16 v10,v37,s35,v10\nds_write_b64 v240,v[84:85] offset:43008\nv_dot2_f32_bf16 v14,v40,s35,v14\nv_dot2_f32_bf16 v14,v41,s35,v14\nv_dot2_f32_bf16 v18,v44,s35,v18\nv_dot2_f32_bf16 v18,v45,s35,v18\nv_dot2_f32_bf16 v22,v48,s35,v22\n"
"v_dot2_f32_bf16 v22,v49,s35,v22\nds_write_b64 v240,v[88:89] offset:45056\nv_dot2_f32_bf16 v26,v52,s35,v26\nv_dot2_f32_bf16 v26,v53,s35,v26\nv_dot2_f32_bf16 v2,v56,s35,v2\nv_dot2_f32_bf16 v2,v57,s35,v2\nv_dot2_f32_bf16 v6,v60,s35,v6\nv_dot2_f32_bf16 v6,v61,s35,v6\n"
"ds_write_b64 v240,v[92:93] offset:47104\nv_dot2_f32_bf16 v10,v64,s35,v10\nv_dot2_f32_bf16 v10,v65,s35,v10\nv_dot2_f32_bf16 v14,v68,s35,v14\nv_dot2_f32_bf16 v14,v69,s35,v14\nv_dot2_f32_bf16 v18,v72,s35,v18\nv_dot2_f32_bf16 v18,v73,s35,v18\nds_write_b64 v240,v[96:97] offset:49152\n"
"v_dot2_f32_bf16 v22,v76,s35,v22\nv_dot2_f32_bf16 v22,v77,s35,v22\nv_dot2_f32_bf16 v26,v80,s35,v26\nv_dot2_f32_bf16 v26,v81,s35,v26\nv_dot2_f32_bf16 v2,v84,s35,v2\nv_dot2_f32_bf16 v2,v85,s35,v2\nds_write_b64 v240,v[100:101] offset:51200\nv_dot2_f32_bf16 v6,v88,s35,v6\n"
"v_dot2_f32_bf16 v6,v89,s35,v6\nv_dot2_f32_bf16 v10,v92,s35,v10\nv_dot2_f32_bf16 v10,v93,s35,v10\nv_dot2_f32_bf16 v14,v96,s35,v14\nv_dot2_f32_bf16 v14,v97,s35,v14\nds_write_b64 v240,v[104:105] offset:53248\nv_dot2_f32_bf16 v18,v100,s35,v18\nv_dot2_f32_bf16 v18,v101,s35,v18\n"
"v_dot2_f32_bf16 v22,v104,s35,v22\nv_dot2_f32_bf16 v22,v105,s35,v22\nv_dot2_f32_bf16 v26,v108,s35,v26\nv_dot2_f32_bf16 v26,v109,s35,v26\nds_write_b64 v240,v[108:109] offset:55296\nv_mov_b32 v3,0\nv_permlane32_swap_b32 v2,v6\nv_add_f32 v2,v2,v6\n"
"v_permlane32_swap_b32 v10,v14\nv_add_f32 v10,v10,v14\nv_permlane32_swap_b32 v18,v22\nv_add_f32 v18,v18,v22\nv_permlane32_swap_b32 v26,v3\nv_add_f32 v26,v26,v3\nv_bfe_u32 v6,v239,8,1\nv_lshrrev_b32 v14,9,v239\n"
"v_permlane16_swap_b32 v2,v10\nv_add_f32 v2,v2,v10\nv_permlane16_swap_b32 v18,v26\nv_add_f32 v18,v18,v26\nv_lshl_or_b32 v6,v6,1,v14\nv_and_b32 v14,0xf0,v239\nv_lshl_or_b32 v6,v6,8,v14\nv_lshl_add_u32 v6,s70,2,v6\n"
"ds_write_b32 v6,v2 offset:59392\nds_write_b32 v6,v18 offset:60416\ns_waitcnt lgkmcnt(0)\ns_barrier\nds_read_b128 a[228:231],v239 offset:0\nds_read_b128 a[232:235],v239 offset:2048\nds_read_b128 a[236:239],v239 offset:4096\nds_read_b128 a[240:243],v239 offset:6144\n"
"ds_read_b128 a[244:247],v239 offset:8192\nds_read_b128 a[248:251],v239 offset:10240\nds_read_b128 a[252:255],v239 offset:12288\ns_waitcnt lgkmcnt(6)\nv_mfma_f32_16x16x32_bf16 v[0:3],a[196:199],a[228:231],0\ns_waitcnt vmcnt(16)\nds_read_b64_tr_b16 a[212:213],v231 offset:12288\nv_mfma_f32_16x16x32_bf16 v[28:31],a[200:203],a[228:231],0\n"
"s_add_u32 m0,s64,8192\nds_read_b32 v248,v255 offset:57344\nv_mfma_f32_16x16x32_bf16 v[56:59],a[204:207],a[228:231],0\nds_read_b64_tr_b16 a[214:215],v231 offset:13312\ns_mov_b32 s73,360448\nv_mfma_f32_16x16x32_bf16 v[84:87],a[208:211],a[228:231],0\nds_read_b128 a[228:231],v239 offset:1024\ns_waitcnt lgkmcnt(9)\n"
"v_mfma_f32_16x16x32_bf16 v[4:7],a[196:199],a[232:235],0\nbuffer_load_dwordx4 v225,s[52:55],s73 offen offset:0 nt lds\ns_mov_b32 s74,361472\nv_mfma_f32_16x16x32_bf16 v[32:35],a[200:203],a[232:235],0\nds_read_b64_tr_b16 a[216:217],v232 offset:12288\nds_read_b32 v249,v255 offset:57408\nv_mfma_f32_16x16x32_bf16 v[60:63],a[204:207],a[232:235],0\nds_read_b32 v250,v255 offset:57472\n"
"v_mfma_f32_16x16x32_bf16 v[88:91],a[208:211],a[232:235],0\nds_read_b128 a[232:235],v239 offset:3072\ns_waitcnt lgkmcnt(12)\nv_mfma_f32_16x16x32_bf16 v[8:11],a[196:199],a[236:239],0\nbuffer_load_dwordx4 v225,s[52:55],s74 offen offset:1024 nt lds\ns_mov_b32 s75,366592\nv_mfma_f32_16x16x32_bf16 v[36:39],a[200:203],a[236:239],0\nds_read_b64_tr_b16 a[218:219],v232 offset:13312\n"
"ds_read_b32 v251,v255 offset:57536\nv_mfma_f32_16x16x32_bf16 v[64:67],a[204:207],a[236:239],0\nds_read_b32 v252,v255 offset:57600\nv_mfma_f32_16x16x32_bf16 v[92:95],a[208:211],a[236:239],0\nds_read_b128 a[236:239],v239 offset:5120\ns_waitcnt lgkmcnt(14)\nv_mfma_f32_16x16x32_bf16 v[12:15],a[196:199],a[240:243],0\nbuffer_load_dwordx4 v225,s[52:55],s75 offen offset:2048 nt lds\n"
"s_mov_b32 s72,367616\nv_mfma_f32_16x16x32_bf16 v[40:43],a[200:203],a[240:243],0\nds_read_b64_tr_b16 a[220:221],v233 offset:12288\nds_read_b32 v253,v255 offset:57664\nv_mfma_f32_16x16x32_bf16 v[68:71],a[204:207],a[240:243],0\nds_read_b64_tr_b16 a[222:223],v233 offset:13312\nv_mfma_f32_16x16x32_bf16 v[96:99],a[208:211],a[240:243],0\nds_read_b128 a[240:243],v239 offset:7168\n"
"ds_read_b32 v254,v255 offset:57728\nv_mfma_f32_16x16x32_bf16 v[16:19],a[196:199],a[244:247],0\nbuffer_load_dwordx4 v225,s[52:55],s72 offen offset:3072 nt lds\nv_mfma_f32_16x16x32_bf16 v[44:47],a[200:203],a[244:247],0\nds_read_b64_tr_b16 a[224:225],v234 offset:12288\nv_mfma_f32_16x16x32_bf16 v[72:75],a[204:207],a[244:247],0\nv_mfma_f32_16x16x32_bf16 v[100:103],a[208:211],a[244:247],0\nds_read_b128 a[244:247],v239 offset:9216\n"
"s_waitcnt lgkmcnt(15)\ns_waitcnt lgkmcnt(2)\nv_mfma_f32_16x16x32_bf16 v[20:23],a[196:199],a[248:251],0\nds_read_b64_tr_b16 a[226:227],v234 offset:13312\nv_sub_f32 v241,v241,v248\nv_mfma_f32_16x16x32_bf16 v[48:51],a[200:203],a[248:251],0\ns_add_u32 m0,s64,12288\nv_sub_f32 v242,v242,v249\n"
"v_sub_f32 v243,v243,v250\nv_sub_f32 v244,v244,v251\nv_mfma_f32_16x16x32_bf16 v[76:79],a[204:207],a[248:251],0\ns_mov_b32 s73,376832\nv_sub_f32 v245,v245,v252\nv_sub_f32 v246,v246,v253\nv_mfma_f32_16x16x32_bf16 v[104:107],a[208:211],a[248:251],0\nds_read_b128 a[248:251],v239 offset:11264\n"
"v_sub_f32 v247,v247,v254\nv_fma_f32 v112,v112,s69,-v248\nv_mfma_f32_16x16x32_bf16 v[24:27],a[196:199],a[252:255],0\nv_exp_f32 v241,v241\nv_mfma_f32_16x16x32_bf16 v[52:55],a[200:203],a[252:255],0\nv_exp_f32 v242,v242\nv_fma_f32 v113,v113,s69,-v248\nv_mfma_f32_16x16x32_bf16 v[80:83],a[204:207],a[252:255],0\n"
"v_exp_f32 v243,v243\nv_mfma_f32_16x16x32_bf16 v[108:111],a[208:211],a[252:255],0\nds_read_b128 a[252:255],v239 offset:13312\ns_waitcnt lgkmcnt(2)\nv_fma_f32 v114,v114,s69,-v248\nv_mfma_f32_16x16x32_bf16 v[0:3],a[212:215],a[228:231],v[0:3]\nbuffer_load_dwordx4 v225,s[52:55],s73 offen offset:0 nt lds\nv_fma_f32 v115,v115,s69,-v248\n"
"v_mfma_f32_16x16x32_bf16 v[28:31],a[216:219],a[228:231],v[28:31]\ns_waitcnt vmcnt(17)\nds_read_b64_tr_b16 a[196:197],v231 offset:16384\nv_fma_f32 v116,v116,s69,-v249\nv_mfma_f32_16x16x32_bf16 v[56:59],a[220:223],a[228:231],v[56:59]\nds_read_b64_tr_b16 a[198:199],v231 offset:17408\nv_fma_f32 v117,v117,s69,-v249\nv_mfma_f32_16x16x32_bf16 v[84:87],a[224:227],a[228:231],v[84:87]\n"
"s_waitcnt lgkmcnt(2)\ns_barrier\nds_read_b128 a[228:231],v239 offset:14336\nv_mfma_f32_16x16x32_bf16 v[4:7],a[212:215],a[232:235],v[4:7]\ns_mov_b32 s74,377856\nbuffer_load_dwordx4 v225,s[52:55],s74 offen offset:1024 nt lds\nv_mfma_f32_16x16x32_bf16 v[32:35],a[216:219],a[232:235],v[32:35]\nds_read_b64_tr_b16 a[200:201],v232 offset:16384\n"
"v_exp_f32 v244,v244\nv_mfma_f32_16x16x32_bf16 v[60:63],a[220:223],a[232:235],v[60:63]\ns_mov_b32 s75,382976\nv_exp_f32 v245,v245\nv_mfma_f32_16x16x32_bf16 v[88:91],a[224:227],a[232:235],v[88:91]\nds_read_b128 a[232:235],v239 offset:16384\nv_exp_f32 v246,v246\nv_mfma_f32_16x16x32_bf16 v[8:11],a[212:215],a[236:239],v[8:11]\n"
"buffer_load_dwordx4 v225,s[52:55],s75 offen offset:2048 nt lds\nv_fma_f32 v118,v118,s69,-v249\nv_mfma_f32_16x16x32_bf16 v[36:39],a[216:219],a[236:239],v[36:39]\nds_read_b64_tr_b16 a[202:203],v232 offset:17408\nv_exp_f32 v247,v247\nv_mfma_f32_16x16x32_bf16 v[64:67],a[220:223],a[236:239],v[64:67]\ns_mov_b32 s72,384000\nv_exp_f32 v112,v112\n"
"v_mfma_f32_16x16x32_bf16 v[92:95],a[224:227],a[236:239],v[92:95]\nds_read_b128 a[236:239],v239 offset:18432\nv_exp_f32 v113,v113\nv_mfma_f32_16x16x32_bf16 v[12:15],a[212:215],a[240:243],v[12:15]\nbuffer_load_dwordx4 v225,s[52:55],s72 offen offset:3072 nt lds\nv_add_f32 v255,v112,v113\nv_mfma_f32_16x16x32_bf16 v[40:43],a[216:219],a[240:243],v[40:43]\nds_read_b64_tr_b16 a[204:205],v233 offset:16384\n"
"v_exp_f32 v114,v114\nv_mfma_f32_16x16x32_bf16 v[68:71],a[220:223],a[240:243],v[68:71]\nds_read_b64_tr_b16 a[206:207],v233 offset:17408\nv_cvt_pk_bf16_f32 v112,v112,v113\nv_mfma_f32_16x16x32_bf16 v[96:99],a[224:227],a[240:243],v[96:99]\nds_read_b128 a[240:243],v239 offset:20480\nv_exp_f32 v115,v115\nv_mfma_f32_16x16x32_bf16 v[16:19],a[212:215],a[244:247],v[16:19]\n"
"ds_read_b64_tr_b16 a[208:209],v234 offset:16384\nv_cvt_pk_bf16_f32 v113,v114,v115\nv_mfma_f32_16x16x32_bf16 v[44:47],a[216:219],a[244:247],v[44:47]\nv_add_f32 v114,v114,v115\nv_add_f32 v114,v114,v255\nv_fma_f32 v119,v119,s69,-v249\nv_mfma_f32_16x16x32_bf16 v[72:75],a[220:223],a[244:247],v[72:75]\nv_exp_f32 v116,v116\n"
"v_mfma_f32_16x16x32_bf16 v[100:103],a[224:227],a[244:247],v[100:103]\nds_read_b128 a[244:247],v239 offset:22528\nv_exp_f32 v117,v117\nv_mfma_f32_16x16x32_bf16 v[20:23],a[212:215],a[248:251],v[20:23]\nds_read_b64_tr_b16 a[210:211],v234 offset:17408\nv_add_f32 v255,v116,v117\nv_mfma_f32_16x16x32_bf16 v[48:51],a[216:219],a[248:251],v[48:51]\ns_add_u32 m0,s64,16384\n"
"v_exp_f32 v118,v118\nv_cvt_pk_bf16_f32 v116,v116,v117\nv_mfma_f32_16x16x32_bf16 v[76:79],a[220:223],a[248:251],v[76:79]\ns_mov_b32 s73,393216\nv_exp_f32 v119,v119\nv_mfma_f32_16x16x32_bf16 v[104:107],a[224:227],a[248:251],v[104:107]\nds_read_b128 a[248:251],v239 offset:24576\nv_cvt_pk_bf16_f32 v117,v118,v119\n"
"v_add_f32 v118,v118,v119\nv_mfma_f32_16x16x32_bf16 v[24:27],a[212:215],a[252:255],v[24:27]\nv_add_f32 v118,v118,v255\nv_fma_f32 v120,v120,s69,-v250\nv_mfma_f32_16x16x32_bf16 v[52:55],a[216:219],a[252:255],v[52:55]\nv_fma_f32 v121,v121,s69,-v250\nv_fma_f32 v122,v122,s69,-v250\nv_fma_f32 v123,v123,s69,-v250\n"
"v_mfma_f32_16x16x32_bf16 v[80:83],a[220:223],a[252:255],v[80:83]\nv_exp_f32 v120,v120\nv_mfma_f32_16x16x32_bf16 v[108:111],a[224:227],a[252:255],v[108:111]\nds_read_b128 a[252:255],v239 offset:26624\ns_waitcnt lgkmcnt(2)\nv_fma_f32 v124,v124,s69,-v251\nv_mfma_f32_16x16x32_bf16 v[0:3],a[196:199],a[228:231],v[0:3]\nbuffer_load_dwordx4 v225,s[52:55],s73 offen offset:0 nt lds\n"
"v_fma_f32 v125,v125,s69,-v251\nv_mfma_f32_16x16x32_bf16 v[28:31],a[200:203],a[228:231],v[28:31]\ns_waitcnt vmcnt(17)\nds_read_b64_tr_b16 a[212:213],v231 offset:20480\nv_fma_f32 v126,v126,s69,-v251\nv_mfma_f32_16x16x32_bf16 v[56:59],a[204:207],a[228:231],v[56:59]\nds_read_b64_tr_b16 a[214:215],v231 offset:21504\nv_fma_f32 v127,v127,s69,-v251\n"
"v_mfma_f32_16x16x32_bf16 v[84:87],a[208:211],a[228:231],v[84:87]\nds_read_b128 a[228:231],v239 offset:15360\nv_exp_f32 v121,v121\nv_mfma_f32_16x16x32_bf16 v[4:7],a[196:199],a[232:235],v[4:7]\ns_mov_b32 s74,394240\nbuffer_load_dwordx4 v225,s[52:55],s74 offen offset:1024 nt lds\nv_mfma_f32_16x16x32_bf16 v[32:35],a[200:203],a[232:235],v[32:35]\nds_read_b64_tr_b16 a[216:217],v232 offset:20480\n"
"v_exp_f32 v122,v122\nv_mfma_f32_16x16x32_bf16 v[60:63],a[204:207],a[232:235],v[60:63]\ns_mov_b32 s75,399360\nv_exp_f32 v123,v123\nv_mfma_f32_16x16x32_bf16 v[88:91],a[208:211],a[232:235],v[88:91]\nds_read_b128 a[232:235],v239 offset:17408\nv_add_f32 v255,v120,v121\nv_cvt_pk_bf16_f32 v120,v120,v121\n"
"v_mfma_f32_16x16x32_bf16 v[8:11],a[196:199],a[236:239],v[8:11]\nbuffer_load_dwordx4 v225,s[52:55],s75 offen offset:2048 nt lds\nv_cvt_pk_bf16_f32 v121,v122,v123\nv_mfma_f32_16x16x32_bf16 v[36:39],a[200:203],a[236:239],v[36:39]\nds_read_b64_tr_b16 a[218:219],v232 offset:21504\nv_add_f32 v122,v122,v123\nv_add_f32 v122,v122,v255\nv_mfma_f32_16x16x32_bf16 v[64:67],a[204:207],a[236:239],v[64:67]\n"
"s_mov_b32 s72,400384\nv_exp_f32 v124,v124\nv_mfma_f32_16x16x32_bf16 v[92:95],a[208:211],a[236:239],v[92:95]\nds_read_b128 a[236:239],v239 offset:19456\nv_exp_f32 v125,v125\nv_mfma_f32_16x16x32_bf16 v[12:15],a[196:199],a[240:243],v[12:15]\nbuffer_load_dwordx4 v225,s[52:55],s72 offen offset:3072 nt lds\nv_add_f32 v255,v124,v125\n"
"v_mfma_f32_16x16x32_bf16 v[40:43],a[200:203],a[240:243],v[40:43]\nds_read_b64_tr_b16 a[220:221],v233 offset:20480\nv_exp_f32 v126,v126\nv_mfma_f32_16x16x32_bf16 v[68:71],a[204:207],a[240:243],v[68:71]\nds_read_b64_tr_b16 a[222:223],v233 offset:21504\nv_cvt_pk_bf16_f32 v124,v124,v125\nv_mfma_f32_16x16x32_bf16 v[96:99],a[208:211],a[240:243],v[96:99]\nds_read_b128 a[240:243],v239 offset:21504\n"
"v_exp_f32 v127,v127\nv_mfma_f32_16x16x32_bf16 v[16:19],a[196:199],a[244:247],v[16:19]\nds_read_b64_tr_b16 a[224:225],v234 offset:20480\nv_cvt_pk_bf16_f32 v125,v126,v127\nv_mfma_f32_16x16x32_bf16 v[44:47],a[200:203],a[244:247],v[44:47]\nv_add_f32 v126,v126,v127\nv_add_f32 v126,v126,v255\nv_fma_f32 v128,v128,s69,-v252\n"
"v_mfma_f32_16x16x32_bf16 v[72:75],a[204:207],a[244:247],v[72:75]\nv_fma_f32 v129,v129,s69,-v252\nv_fma_f32 v130,v130,s69,-v252\nv_mfma_f32_16x16x32_bf16 v[100:103],a[208:211],a[244:247],v[100:103]\nds_read_b128 a[244:247],v239 offset:23552\ns_waitcnt lgkmcnt(12)\nv_fma_f32 v131,v131,s69,-v252\nv_mfma_f32_16x16x32_bf16 v[20:23],a[196:199],a[248:251],v[20:23]\n"
"ds_read_b64_tr_b16 a[226:227],v234 offset:21504\nv_fma_f32 v132,v132,s69,-v253\nv_mfma_f32_16x16x32_bf16 v[48:51],a[200:203],a[248:251],v[48:51]\ns_add_u32 m0,s64,20480\nv_exp_f32 v128,v128\nv_fma_f32 v133,v133,s69,-v253\nv_mfma_f32_16x16x32_bf16 v[76:79],a[204:207],a[248:251],v[76:79]\ns_mov_b32 s73,409600\n"
"v_exp_f32 v129,v129\nv_mfma_f32_16x16x32_bf16 v[104:107],a[208:211],a[248:251],v[104:107]\nds_read_b128 a[248:251],v239 offset:25600\nv_exp_f32 v130,v130\nv_mfma_f32_16x16x32_bf16 v[24:27],a[196:199],a[252:255],v[24:27]\nv_exp_f32 v131,v131\nv_mfma_f32_16x16x32_bf16 v[52:55],a[200:203],a[252:255],v[52:55]\nv_add_f32 v255,v128,v129\n"
"v_cvt_pk_bf16_f32 v128,v128,v129\nv_cvt_pk_bf16_f32 v129,v130,v131\nv_mfma_f32_16x16x32_bf16 v[80:83],a[204:207],a[252:255],v[80:83]\nv_add_f32 v130,v130,v131\nv_add_f32 v130,v130,v255\nv_mfma_f32_16x16x32_bf16 v[108:111],a[208:211],a[252:255],v[108:111]\nds_read_b128 a[252:255],v239 offset:27648\ns_waitcnt lgkmcnt(2)\n"
"v_fma_f32 v134,v134,s69,-v253\nv_mfma_f32_16x16x32_bf16 v[0:3],a[212:215],a[228:231],v[0:3]\nbuffer_load_dwordx4 v225,s[52:55],s73 offen offset:0 nt lds\nv_fma_f32 v135,v135,s69,-v253\nv_mfma_f32_16x16x32_bf16 v[28:31],a[216:219],a[228:231],v[28:31]\ns_waitcnt vmcnt(17)\nds_read_b64_tr_b16 a[196:197],v231 offset:0\nv_fma_f32 v136,v136,s69,-v254\n"
"v_mfma_f32_16x16x32_bf16 v[56:59],a[220:223],a[228:231],v[56:59]\nds_read_b64_tr_b16 a[198:199],v231 offset:1024\nv_fma_f32 v137,v137,s69,-v254\nv_mfma_f32_16x16x32_bf16 v[84:87],a[224:227],a[228:231],v[84:87]\ns_waitcnt lgkmcnt(2)\ns_barrier\nds_read_b128 a[228:231],v239 offset:28672\nv_mfma_f32_16x16x32_bf16 v[4:7],a[212:215],a[232:235],v[4:7]\n"
"s_mov_b32 s74,410624\nbuffer_load_dwordx4 v225,s[52:55],s74 offen offset:1024 nt lds\nv_mfma_f32_16x16x32_bf16 v[32:35],a[216:219],a[232:235],v[32:35]\nds_read_b64_tr_b16 a[200:201],v232 offset:0\nv_exp_f32 v132,v132\nv_mfma_f32_16x16x32_bf16 v[60:63],a[220:223],a[232:235],v[60:63]\ns_mov_b32 s75,415744\nv_exp_f32 v133,v133\n"
"v_mfma_f32_16x16x32_bf16 v[88:91],a[224:227],a[232:235],v[88:91]\nds_read_b128 a[232:235],v239 offset:30720\nv_exp_f32 v134,v134\nv_mfma_f32_16x16x32_bf16 v[8:11],a[212:215],a[236:239],v[8:11]\nbuffer_load_dwordx4 v225,s[52:55],s75 offen offset:2048 nt lds\nv_add_f32 v255,v132,v133\nv_mfma_f32_16x16x32_bf16 v[36:39],a[216:219],a[236:239],v[36:39]\nds_read_b64_tr_b16 a[202:203],v232 offset:1024\n"
"v_exp_f32 v135,v135\nv_mfma_f32_16x16x32_bf16 v[64:67],a[220:223],a[236:239],v[64:67]\ns_mov_b32 s72,416768\nv_cvt_pk_bf16_f32 v132,v132,v133\nv_cvt_pk_bf16_f32 v133,v134,v135\nv_mfma_f32_16x16x32_bf16 v[92:95],a[224:227],a[236:239],v[92:95]\nds_read_b128 a[236:239],v239 offset:32768\nv_add_f32 v134,v134,v135\n"
"v_add_f32 v134,v134,v255\nv_mfma_f32_16x16x32_bf16 v[12:15],a[212:215],a[240:243],v[12:15]\nbuffer_load_dwordx4 v225,s[52:55],s72 offen offset:3072 nt lds\nv_fma_f32 v138,v138,s69,-v254\nv_mfma_f32_16x16x32_bf16 v[40:43],a[216:219],a[240:243],v[40:43]\nds_read_b64_tr_b16 a[204:205],v233 offset:0\nv_fma_f32 v139,v139,s69,-v254\nv_fma_f32 v140,v140,s69,-v248\n"
"v_mfma_f32_16x16x32_bf16 v[68:71],a[220:223],a[240:243],v[68:71]\nds_read_b64_tr_b16 a[206:207],v233 offset:1024\nv_fma_f32 v141,v141,s69,-v248\nv_mfma_f32_16x16x32_bf16 v[96:99],a[224:227],a[240:243],v[96:99]\nds_read_b128 a[240:243],v239 offset:34816\nv_exp_f32 v136,v136\nv_mfma_f32_16x16x32_bf16 v[16:19],a[212:215],a[244:247],v[16:19]\nds_read_b64_tr_b16 a[208:209],v234 offset:0\n"
"v_fma_f32 v142,v142,s69,-v248\nv_mfma_f32_16x16x32_bf16 v[44:47],a[216:219],a[244:247],v[44:47]\nv_exp_f32 v137,v137\nv_fma_f32 v143,v143,s69,-v248\nv_mfma_f32_16x16x32_bf16 v[72:75],a[220:223],a[244:247],v[72:75]\nv_exp_f32 v138,v138\nv_mfma_f32_16x16x32_bf16 v[100:103],a[224:227],a[244:247],v[100:103]\nds_read_b128 a[244:247],v239 offset:36864\n"
"v_exp_f32 v139,v139\nv_mfma_f32_16x16x32_bf16 v[20:23],a[212:215],a[248:251],v[20:23]\nds_read_b64_tr_b16 a[210:211],v234 offset:1024\nv_add_f32 v255,v136,v137\nv_mfma_f32_16x16x32_bf16 v[48:51],a[216:219],a[248:251],v[48:51]\ns_add_u32 m0,s64,0\nv_cvt_pk_bf16_f32 v136,v136,v137\nv_cvt_pk_bf16_f32 v137,v138,v139\n"
"v_add_f32 v138,v138,v139\nv_mfma_f32_16x16x32_bf16 v[76:79],a[220:223],a[248:251],v[76:79]\ns_mov_b32 s73,425984\nv_add_f32 v138,v138,v255\nv_fma_f32 v144,v144,s69,-v249\nv_mfma_f32_16x16x32_bf16 v[104:107],a[224:227],a[248:251],v[104:107]\nds_read_b128 a[248:251],v239 offset:38912\nv_exp_f32 v140,v140\n"
"v_mfma_f32_16x16x32_bf16 v[24:27],a[212:215],a[252:255],v[24:27]\nds_write_b64 v240,v[112:113] offset:0\nv_mfma_f32_16x16x32_bf16 v[52:55],a[216:219],a[252:255],v[52:55]\nv_fma_f32 v145,v145,s69,-v249\nv_fma_f32 v146,v146,s69,-v249\nv_fma_f32 v147,v147,s69,-v249\nv_mfma_f32_16x16x32_bf16 v[80:83],a[220:223],a[252:255],v[80:83]\nds_write_b64 v240,v[116:117] offset:2048\n"
"v_mfma_f32_16x16x32_bf16 v[108:111],a[224:227],a[252:255],v[108:111]\nds_read_b128 a[252:255],v239 offset:40960\ns_waitcnt lgkmcnt(4)\nv_fma_f32 v148,v148,s69,-v250\nv_mfma_f32_16x16x32_bf16 v[0:3],a[196:199],a[228:231],v[0:3]\nbuffer_load_dwordx4 v225,s[52:55],s73 offen offset:0 nt lds\nv_fma_f32 v149,v149,s69,-v250\nv_mfma_f32_16x16x32_bf16 v[28:31],a[200:203],a[228:231],v[28:31]\n"
"s_waitcnt vmcnt(17)\nds_read_b64_tr_b16 a[212:213],v231 offset:4096\nv_fma_f32 v150,v150,s69,-v250\nv_mfma_f32_16x16x32_bf16 v[56:59],a[204:207],a[228:231],v[56:59]\nds_read_b64_tr_b16 a[214:215],v231 offset:5120\nv_fma_f32 v151,v151,s69,-v250\nv_mfma_f32_16x16x32_bf16 v[84:87],a[208:211],a[228:231],v[84:87]\nds_read_b128 a[228:231],v239 offset:29696\n"
"v_exp_f32 v144,v144\nv_mfma_f32_16x16x32_bf16 v[4:7],a[196:199],a[232:235],v[4:7]\ns_mov_b32 s74,427008\nbuffer_load_dwordx4 v225,s[52:55],s74 offen offset:1024 nt lds\nv_mfma_f32_16x16x32_bf16 v[32:35],a[200:203],a[232:235],v[32:35]\nds_read_b64_tr_b16 a[216:217],v232 offset:4096\nv_exp_f32 v148,v148\nv_mfma_f32_16x16x32_bf16 v[60:63],a[204:207],a[232:235],v[60:63]\n"
"s_mov_b32 s75,432128\nds_write_b64 v240,v[120:121] offset:4096\nv_mfma_f32_16x16x32_bf16 v[88:91],a[208:211],a[232:235],v[88:91]\nds_read_b128 a[232:235],v239 offset:31744\nv_fma_f32 v152,v152,s69,-v251\nv_fma_f32 v153,v153,s69,-v251\nv_mfma_f32_16x16x32_bf16 v[8:11],a[196:199],a[236:239],v[8:11]\nbuffer_load_dwordx4 v225,s[52:55],s75 offen offset:2048 nt lds\n"
"v_fma_f32 v154,v154,s69,-v251\nv_mfma_f32_16x16x32_bf16 v[36:39],a[200:203],a[236:239],v[36:39]\nds_read_b64_tr_b16 a[218:219],v232 offset:5120\nv_fma_f32 v155,v155,s69,-v251\nv_fma_f32 v156,v156,s69,-v252\nv_mfma_f32_16x16x32_bf16 v[64:67],a[204:207],a[236:239],v[64:67]\ns_mov_b32 s72,433152\nds_write_b64 v240,v[124:125] offset:6144\n"
"v_mfma_f32_16x16x32_bf16 v[92:95],a[208:211],a[236:239],v[92:95]\nds_read_b128 a[236:239],v239 offset:33792\nv_exp_f32 v152,v152\nv_mfma_f32_16x16x32_bf16 v[12:15],a[196:199],a[240:243],v[12:15]\nbuffer_load_dwordx4 v225,s[52:55],s72 offen offset:3072 nt lds\nv_fma_f32 v157,v157,s69,-v252\nv_mfma_f32_16x16x32_bf16 v[40:43],a[200:203],a[240:243],v[40:43]\nds_read_b64_tr_b16 a[220:221],v233 offset:4096\n"
"v_fma_f32 v158,v158,s69,-v252\nv_fma_f32 v159,v159,s69,-v252\nv_mfma_f32_16x16x32_bf16 v[68:71],a[204:207],a[240:243],v[68:71]\nds_read_b64_tr_b16 a[222:223],v233 offset:5120\nv_fma_f32 v160,v160,s69,-v253\nv_mfma_f32_16x16x32_bf16 v[96:99],a[208:211],a[240:243],v[96:99]\nds_read_b128 a[240:243],v239 offset:35840\nv_exp_f32 v156,v156\n"
"v_mfma_f32_16x16x32_bf16 v[16:19],a[196:199],a[244:247],v[16:19]\nds_read_b64_tr_b16 a[224:225],v234 offset:4096\nv_fma_f32 v161,v161,s69,-v253\nv_mfma_f32_16x16x32_bf16 v[44:47],a[200:203],a[244:247],v[44:47]\nds_write_b64 v240,v[128:129] offset:8192\nv_mfma_f32_16x16x32_bf16 v[72:75],a[204:207],a[244:247],v[72:75]\nv_fma_f32 v162,v162,s69,-v253\nv_fma_f32 v163,v163,s69,-v253\n"
"v_mfma_f32_16x16x32_bf16 v[100:103],a[208:211],a[244:247],v[100:103]\nds_read_b128 a[244:247],v239 offset:37888\ns_waitcnt lgkmcnt(15)\nv_fma_f32 v164,v164,s69,-v254\nv_mfma_f32_16x16x32_bf16 v[20:23],a[196:199],a[248:251],v[20:23]\nds_read_b64_tr_b16 a[226:227],v234 offset:5120\nv_fma_f32 v165,v165,s69,-v254\nv_mfma_f32_16x16x32_bf16 v[48:51],a[200:203],a[248:251],v[48:51]\n"
"s_add_u32 m0,s64,4096\nds_write_b64 v240,v[132:133] offset:10240\nv_mfma_f32_16x16x32_bf16 v[76:79],a[204:207],a[248:251],v[76:79]\ns_mov_b32 s73,442368\nv_exp_f32 v160,v160\nv_mfma_f32_16x16x32_bf16 v[104:107],a[208:211],a[248:251],v[104:107]\nds_read_b128 a[248:251],v239 offset:39936\nv_fma_f32 v166,v166,s69,-v254\n"
"v_fma_f32 v167,v167,s69,-v254\nv_mfma_f32_16x16x32_bf16 v[24:27],a[196:199],a[252:255],v[24:27]\nds_write_b64 v240,v[136:137] offset:12288\nv_mfma_f32_16x16x32_bf16 v[52:55],a[200:203],a[252:255],v[52:55]\nv_exp_f32 v164,v164\nv_add_f32 v114,v140,v114\nv_mfma_f32_16x16x32_bf16 v[80:83],a[204:207],a[252:255],v[80:83]\nv_exp_f32 v141,v141\n"
"v_mfma_f32_16x16x32_bf16 v[108:111],a[208:211],a[252:255],v[108:111]\nds_read_b128 a[252:255],v239 offset:41984\ns_waitcnt lgkmcnt(4)\nv_add_f32 v114,v141,v114\nv_mfma_f32_16x16x32_bf16 v[0:3],a[212:215],a[228:231],v[0:3]\nbuffer_load_dwordx4 v225,s[52:55],s73 offen offset:0 nt lds\nv_cvt_pk_bf16_f32 v140,v140,v141\nv_mfma_f32_16x16x32_bf16 v[28:31],a[216:219],a[228:231],v[28:31]\n"
"s_waitcnt vmcnt(17)\nds_read_b64_tr_b16 a[196:197],v231 offset:8192\nv_add_f32 v118,v144,v118\nv_mfma_f32_16x16x32_bf16 v[56:59],a[220:223],a[228:231],v[56:59]\nds_read_b64_tr_b16 a[198:199],v231 offset:9216\nv_add_f32 v122,v148,v122\nv_mfma_f32_16x16x32_bf16 v[84:87],a[224:227],a[228:231],v[84:87]\ns_waitcnt lgkmcnt(2)\n"
"s_barrier\nds_read_b128 a[228:231],v239 offset:43008\nv_mfma_f32_16x16x32_bf16 v[4:7],a[212:215],a[232:235],v[4:7]\ns_mov_b32 s74,443392\nbuffer_load_dwordx4 v225,s[52:55],s74 offen offset:1024 nt lds\nv_mfma_f32_16x16x32_bf16 v[32:35],a[216:219],a[232:235],v[32:35]\nds_read_b64_tr_b16 a[200:201],v232 offset:8192\nv_exp_f32 v142,v142\n"
"v_mfma_f32_16x16x32_bf16 v[60:63],a[220:223],a[232:235],v[60:63]\ns_mov_b32 s75,448512\nv_exp_f32 v143,v143\nv_mfma_f32_16x16x32_bf16 v[88:91],a[224:227],a[232:235],v[88:91]\nds_read_b128 a[232:235],v239 offset:45056\nv_add_f32 v114,v142,v114\nv_cvt_pk_bf16_f32 v141,v142,v143\nv_mfma_f32_16x16x32_bf16 v[8:11],a[212:215],a[236:239],v[8:11]\n"
"buffer_load_dwordx4 v225,s[52:55],s75 offen offset:2048 nt lds\nv_add_f32 v114,v143,v114\nv_mfma_f32_16x16x32_bf16 v[36:39],a[216:219],a[236:239],v[36:39]\nds_read_b64_tr_b16 a[202:203],v232 offset:9216\nv_exp_f32 v145,v145\nv_mfma_f32_16x16x32_bf16 v[64:67],a[220:223],a[236:239],v[64:67]\ns_mov_b32 s72,449536\nv_exp_f32 v146,v146\n"
"v_mfma_f32_16x16x32_bf16 v[92:95],a[224:227],a[236:239],v[92:95]\nds_read_b128 a[236:239],v239 offset:47104\nv_exp_f32 v147,v147\nv_mfma_f32_16x16x32_bf16 v[12:15],a[212:215],a[240:243],v[12:15]\nbuffer_load_dwordx4 v225,s[52:55],s72 offen offset:3072 nt lds\nv_add_f32 v118,v145,v118\nv_mfma_f32_16x16x32_bf16 v[40:43],a[216:219],a[240:243],v[40:43]\nds_read_b64_tr_b16 a[204:205],v233 offset:8192\n"
"v_cvt_pk_bf16_f32 v144,v144,v145\nv_add_f32 v118,v146,v118\nv_mfma_f32_16x16x32_bf16 v[68:71],a[220:223],a[240:243],v[68:71]\nds_read_b64_tr_b16 a[206:207],v233 offset:9216\nv_cvt_pk_bf16_f32 v145,v146,v147\nv_mfma_f32_16x16x32_bf16 v[96:99],a[224:227],a[240:243],v[96:99]\nds_read_b128 a[240:243],v239 offset:49152\nv_add_f32 v118,v147,v118\n"
"v_add_f32 v126,v152,v126\nv_mfma_f32_16x16x32_bf16 v[16:19],a[212:215],a[244:247],v[16:19]\nds_read_b64_tr_b16 a[208:209],v234 offset:8192\nv_add_f32 v130,v156,v130\nv_mfma_f32_16x16x32_bf16 v[44:47],a[216:219],a[244:247],v[44:47]\nv_exp_f32 v149,v149\nv_mfma_f32_16x16x32_bf16 v[72:75],a[220:223],a[244:247],v[72:75]\nv_exp_f32 v150,v150\n"
"v_mfma_f32_16x16x32_bf16 v[100:103],a[224:227],a[244:247],v[100:103]\nds_read_b128 a[244:247],v239 offset:51200\nv_exp_f32 v151,v151\nv_mfma_f32_16x16x32_bf16 v[20:23],a[212:215],a[248:251],v[20:23]\nds_read_b64_tr_b16 a[210:211],v234 offset:9216\nv_add_f32 v122,v149,v122\nv_mfma_f32_16x16x32_bf16 v[48:51],a[216:219],a[248:251],v[48:51]\ns_add_u32 m0,s64,8192\n"
"v_cvt_pk_bf16_f32 v148,v148,v149\nv_add_f32 v122,v150,v122\nv_cvt_pk_bf16_f32 v149,v150,v151\nv_mfma_f32_16x16x32_bf16 v[76:79],a[220:223],a[248:251],v[76:79]\ns_mov_b32 s73,458752\nv_add_f32 v122,v151,v122\nv_add_f32 v134,v160,v134\nv_mfma_f32_16x16x32_bf16 v[104:107],a[224:227],a[248:251],v[104:107]\n"
"ds_read_b128 a[248:251],v239 offset:53248\nv_exp_f32 v153,v153\nv_mfma_f32_16x16x32_bf16 v[24:27],a[212:215],a[252:255],v[24:27]\nv_exp_f32 v154,v154\nv_mfma_f32_16x16x32_bf16 v[52:55],a[216:219],a[252:255],v[52:55]\nv_exp_f32 v155,v155\nv_add_f32 v126,v153,v126\nv_mfma_f32_16x16x32_bf16 v[80:83],a[220:223],a[252:255],v[80:83]\n"
"v_cvt_pk_bf16_f32 v152,v152,v153\nv_add_f32 v126,v154,v126\nv_mfma_f32_16x16x32_bf16 v[108:111],a[224:227],a[252:255],v[108:111]\nds_read_b128 a[252:255],v239 offset:55296\ns_waitcnt lgkmcnt(2)\nv_cvt_pk_bf16_f32 v153,v154,v155\nv_mfma_f32_16x16x32_bf16 v[0:3],a[196:199],a[228:231],v[0:3]\nbuffer_load_dwordx4 v225,s[52:55],s73 offen offset:0 nt lds\n"
"v_add_f32 v126,v155,v126\nv_mfma_f32_16x16x32_bf16 v[28:31],a[200:203],a[228:231],v[28:31]\ns_waitcnt vmcnt(17)\nds_read_b64_tr_b16 a[212:213],v231 offset:12288\nv_add_f32 v138,v164,v138\nv_mfma_f32_16x16x32_bf16 v[56:59],a[204:207],a[228:231],v[56:59]\nds_read_b64_tr_b16 a[214:215],v231 offset:13312\ns_mov_b32 s74,459776\n"
"v_mfma_f32_16x16x32_bf16 v[84:87],a[208:211],a[228:231],v[84:87]\nds_read_b128 a[228:231],v239 offset:44032\nv_exp_f32 v157,v157\nv_mfma_f32_16x16x32_bf16 v[4:7],a[196:199],a[232:235],v[4:7]\nbuffer_load_dwordx4 v225,s[52:55],s74 offen offset:1024 nt lds\nv_add_f32 v130,v157,v130\nv_mfma_f32_16x16x32_bf16 v[32:35],a[200:203],a[232:235],v[32:35]\nds_read_b64_tr_b16 a[216:217],v232 offset:12288\n"
"v_exp_f32 v158,v158\nv_mfma_f32_16x16x32_bf16 v[60:63],a[204:207],a[232:235],v[60:63]\ns_mov_b32 s75,464896\nv_exp_f32 v159,v159\nv_mfma_f32_16x16x32_bf16 v[88:91],a[208:211],a[232:235],v[88:91]\nds_read_b128 a[232:235],v239 offset:46080\nv_cvt_pk_bf16_f32 v156,v156,v157\nv_add_f32 v130,v158,v130\n"
"v_mfma_f32_16x16x32_bf16 v[8:11],a[196:199],a[236:239],v[8:11]\nbuffer_load_dwordx4 v225,s[52:55],s75 offen offset:2048 nt lds\nv_cvt_pk_bf16_f32 v157,v158,v159\nv_mfma_f32_16x16x32_bf16 v[36:39],a[200:203],a[236:239],v[36:39]\nds_read_b64_tr_b16 a[218:219],v232 offset:13312\nv_add_f32 v130,v159,v130\nv_fma_f32 v168,v168,s69,-v248\nv_mfma_f32_16x16x32_bf16 v[64:67],a[204:207],a[236:239],v[64:67]\n"
"s_mov_b32 s72,465920\nv_exp_f32 v161,v161\nv_mfma_f32_16x16x32_bf16 v[92:95],a[208:211],a[236:239],v[92:95]\nds_read_b128 a[236:239],v239 offset:48128\nv_exp_f32 v162,v162\nv_mfma_f32_16x16x32_bf16 v[12:15],a[196:199],a[240:243],v[12:15]\nbuffer_load_dwordx4 v225,s[52:55],s72 offen offset:3072 nt lds\nv_add_f32 v134,v161,v134\n"
"v_mfma_f32_16x16x32_bf16 v[40:43],a[200:203],a[240:243],v[40:43]\nds_read_b64_tr_b16 a[220:221],v233 offset:12288\nv_exp_f32 v163,v163\nv_mfma_f32_16x16x32_bf16 v[68:71],a[204:207],a[240:243],v[68:71]\nds_read_b64_tr_b16 a[222:223],v233 offset:13312\nv_cvt_pk_bf16_f32 v160,v160,v161\nv_mfma_f32_16x16x32_bf16 v[96:99],a[208:211],a[240:243],v[96:99]\nds_read_b128 a[240:243],v239 offset:50176\n"
"v_add_f32 v134,v162,v134\nv_cvt_pk_bf16_f32 v161,v162,v163\nv_mfma_f32_16x16x32_bf16 v[16:19],a[196:199],a[244:247],v[16:19]\nds_read_b64_tr_b16 a[224:225],v234 offset:12288\nv_add_f32 v134,v163,v134\nv_mfma_f32_16x16x32_bf16 v[44:47],a[200:203],a[244:247],v[44:47]\nv_exp_f32 v165,v165\nv_fma_f32 v169,v169,s69,-v248\n"
"v_mfma_f32_16x16x32_bf16 v[72:75],a[204:207],a[244:247],v[72:75]\nv_exp_f32 v166,v166\nv_mfma_f32_16x16x32_bf16 v[100:103],a[208:211],a[244:247],v[100:103]\nds_read_b128 a[244:247],v239 offset:52224\ns_waitcnt lgkmcnt(12)\nv_add_f32 v138,v165,v138\nv_mfma_f32_16x16x32_bf16 v[20:23],a[196:199],a[248:251],v[20:23]\nds_read_b64_tr_b16 a[226:227],v234 offset:13312\n"
"v_cvt_pk_bf16_f32 v164,v164,v165\nv_mfma_f32_16x16x32_bf16 v[48:51],a[200:203],a[248:251],v[48:51]\ns_add_u32 m0,s64,12288\nv_exp_f32 v167,v167\nv_add_f32 v138,v166,v138\nv_mfma_f32_16x16x32_bf16 v[76:79],a[204:207],a[248:251],v[76:79]\ns_mov_b32 s73,475136\nv_cvt_pk_bf16_f32 v165,v166,v167\n"
"v_add_f32 v138,v167,v138\nv_mfma_f32_16x16x32_bf16 v[104:107],a[208:211],a[248:251],v[104:107]\nds_read_b128 a[248:251],v239 offset:54272\nv_fma_f32 v170,v170,s69,-v248\nv_fma_f32 v171,v171,s69,-v248\nv_mfma_f32_16x16x32_bf16 v[24:27],a[196:199],a[252:255],v[24:27]\nds_write_b64 v240,v[140:141] offset:14336\nv_mfma_f32_16x16x32_bf16 v[52:55],a[200:203],a[252:255],v[52:55]\n"
"v_exp_f32 v168,v168\nv_fma_f32 v172,v172,s69,-v249\nv_mfma_f32_16x16x32_bf16 v[80:83],a[204:207],a[252:255],v[80:83]\nds_write_b64 v240,v[144:145] offset:16384\nv_mfma_f32_16x16x32_bf16 v[108:111],a[208:211],a[252:255],v[108:111]\nds_read_b128 a[252:255],v239 offset:56320\ns_waitcnt lgkmcnt(4)\nv_fma_f32 v173,v173,s69,-v249\n"
"v_mfma_f32_16x16x32_bf16 v[0:3],a[212:215],a[228:231],v[0:3]\nbuffer_load_dwordx4 v225,s[52:55],s73 offen offset:0 nt lds\nv_fma_f32 v174,v174,s69,-v249\nv_mfma_f32_16x16x32_bf16 v[28:31],a[216:219],a[228:231],v[28:31]\ns_waitcnt vmcnt(17)\nds_read_b64_tr_b16 a[196:197],v231 offset:16384\nv_fma_f32 v175,v175,s69,-v249\nv_mfma_f32_16x16x32_bf16 v[56:59],a[220:223],a[228:231],v[56:59]\n"
"ds_read_b64_tr_b16 a[198:199],v231 offset:17408\nv_fma_f32 v176,v176,s69,-v250\nv_mfma_f32_16x16x32_bf16 v[84:87],a[224:227],a[228:231],v[84:87]\ns_waitcnt lgkmcnt(2)\ns_barrier\nds_read_b128 a[228:231],v239 offset:0\nv_mfma_f32_16x16x32_bf16 v[4:7],a[212:215],a[232:235],v[4:7]\ns_mov_b32 s74,476160\n"
"buffer_load_dwordx4 v225,s[52:55],s74 offen offset:1024 nt lds\nv_mfma_f32_16x16x32_bf16 v[32:35],a[216:219],a[232:235],v[32:35]\nds_read_b64_tr_b16 a[200:201],v232 offset:16384\nv_exp_f32 v172,v172\nv_mfma_f32_16x16x32_bf16 v[60:63],a[220:223],a[232:235],v[60:63]\ns_mov_b32 s75,481280\nds_write_b64 v240,v[148:149] offset:18432\nv_mfma_f32_16x16x32_bf16 v[88:91],a[224:227],a[232:235],v[88:91]\n"
"ds_read_b128 a[232:235],v239 offset:2048\nv_fma_f32 v177,v177,s69,-v250\nv_fma_f32 v178,v178,s69,-v250\nv_mfma_f32_16x16x32_bf16 v[8:11],a[212:215],a[236:239],v[8:11]\nbuffer_load_dwordx4 v225,s[52:55],s75 offen offset:2048 nt lds\nv_fma_f32 v179,v179,s69,-v250\nv_mfma_f32_16x16x32_bf16 v[36:39],a[216:219],a[236:239],v[36:39]\nds_read_b64_tr_b16 a[202:203],v232 offset:17408\n"
"v_exp_f32 v176,v176\nv_mfma_f32_16x16x32_bf16 v[64:67],a[220:223],a[236:239],v[64:67]\ns_mov_b32 s72,482304\nds_write_b64 v240,v[152:153] offset:20480\nv_mfma_f32_16x16x32_bf16 v[92:95],a[224:227],a[236:239],v[92:95]\nds_read_b128 a[236:239],v239 offset:4096\nv_fma_f32 v180,v180,s69,-v251\nv_fma_f32 v181,v181,s69,-v251\n"
"v_mfma_f32_16x16x32_bf16 v[12:15],a[212:215],a[240:243],v[12:15]\nbuffer_load_dwordx4 v225,s[52:55],s72 offen offset:3072 nt lds\nv_mul_f32 v0,v241,v0\nv_mul_f32 v1,v241,v1\nv_mul_f32 v2,v241,v2\nv_mul_f32 v3,v241,v3\nv_mfma_f32_16x16x32_bf16 v[40:43],a[216:219],a[240:243],v[40:43]\nds_read_b64_tr_b16 a[204:205],v233 offset:16384\n"
"v_mul_f32 v28,v241,v28\nv_mul_f32 v29,v241,v29\nv_mul_f32 v30,v241,v30\nv_mul_f32 v31,v241,v31\nv_mfma_f32_16x16x32_bf16 v[68:71],a[220:223],a[240:243],v[68:71]\nds_read_b64_tr_b16 a[206:207],v233 offset:17408\nv_mul_f32 v56,v241,v56\nv_mul_f32 v57,v241,v57\n"
"v_mul_f32 v58,v241,v58\nv_mul_f32 v59,v241,v59\nv_mfma_f32_16x16x32_bf16 v[96:99],a[224:227],a[240:243],v[96:99]\nds_read_b128 a[240:243],v239 offset:6144\nv_mul_f32 v84,v241,v84\nv_mul_f32 v85,v241,v85\nv_mul_f32 v86,v241,v86\nv_mul_f32 v87,v241,v87\n"
"v_mfma_f32_16x16x32_bf16 v[16:19],a[212:215],a[244:247],v[16:19]\nds_read_b64_tr_b16 a[208:209],v234 offset:16384\nv_mul_f32 v4,v242,v4\nv_mul_f32 v5,v242,v5\nv_mul_f32 v6,v242,v6\nv_mul_f32 v7,v242,v7\nv_mfma_f32_16x16x32_bf16 v[44:47],a[216:219],a[244:247],v[44:47]\nv_mul_f32 v32,v242,v32\n"
"v_mul_f32 v33,v242,v33\nv_mul_f32 v34,v242,v34\nv_mul_f32 v35,v242,v35\nv_mfma_f32_16x16x32_bf16 v[72:75],a[220:223],a[244:247],v[72:75]\nv_mul_f32 v60,v242,v60\nv_mul_f32 v61,v242,v61\nv_mul_f32 v62,v242,v62\nv_mul_f32 v63,v242,v63\n"
"v_mfma_f32_16x16x32_bf16 v[100:103],a[224:227],a[244:247],v[100:103]\nds_read_b128 a[244:247],v239 offset:8192\nv_mul_f32 v88,v242,v88\nv_mul_f32 v89,v242,v89\nv_mul_f32 v90,v242,v90\nv_mul_f32 v91,v242,v91\nv_mfma_f32_16x16x32_bf16 v[20:23],a[212:215],a[248:251],v[20:23]\nds_read_b64_tr_b16 a[210:211],v234 offset:17408\n"
"v_mul_f32 v8,v243,v8\nv_mul_f32 v9,v243,v9\nv_mul_f32 v10,v243,v10\nv_mul_f32 v11,v243,v11\nv_mfma_f32_16x16x32_bf16 v[48:51],a[216:219],a[248:251],v[48:51]\ns_add_u32 m0,s64,16384\nv_mul_f32 v36,v243,v36\nv_mul_f32 v37,v243,v37\n"
"v_mul_f32 v38,v243,v38\nv_mul_f32 v39,v243,v39\nv_mfma_f32_16x16x32_bf16 v[76:79],a[220:223],a[248:251],v[76:79]\ns_mov_b32 s73,491520\nv_mul_f32 v64,v243,v64\nv_mul_f32 v65,v243,v65\nv_mul_f32 v66,v243,v66\nv_mul_f32 v67,v243,v67\n"
"v_mfma_f32_16x16x32_bf16 v[104:107],a[224:227],a[248:251],v[104:107]\nds_read_b128 a[248:251],v239 offset:10240\nv_mul_f32 v92,v243,v92\nv_mul_f32 v93,v243,v93\nv_mul_f32 v94,v243,v94\nv_mul_f32 v95,v243,v95\nv_mfma_f32_16x16x32_bf16 v[24:27],a[212:215],a[252:255],v[24:27]\nv_mul_f32 v12,v244,v12\n"
"v_mul_f32 v13,v244,v13\nv_mul_f32 v14,v244,v14\nv_mul_f32 v15,v244,v15\nv_mfma_f32_16x16x32_bf16 v[52:55],a[216:219],a[252:255],v[52:55]\nv_mul_f32 v40,v244,v40\nv_mul_f32 v41,v244,v41\nv_mul_f32 v42,v244,v42\nv_mul_f32 v43,v244,v43\n"
"v_mfma_f32_16x16x32_bf16 v[80:83],a[220:223],a[252:255],v[80:83]\nv_mul_f32 v68,v244,v68\nv_mul_f32 v69,v244,v69\nv_mul_f32 v70,v244,v70\nv_mul_f32 v71,v244,v71\nv_mfma_f32_16x16x32_bf16 v[108:111],a[224:227],a[252:255],v[108:111]\nds_read_b128 a[252:255],v239 offset:12288\ns_waitcnt lgkmcnt(2)\n"
"v_mul_f32 v96,v244,v96\nv_mul_f32 v97,v244,v97\nv_mul_f32 v98,v244,v98\nv_mul_f32 v99,v244,v99\nv_mfma_f32_16x16x32_bf16 v[0:3],a[196:199],a[228:231],v[0:3]\nbuffer_load_dwordx4 v225,s[52:55],s73 offen offset:0 nt lds\nv_mul_f32 v16,v245,v16\nv_mul_f32 v17,v245,v17\n"
"v_mul_f32 v18,v245,v18\nv_mul_f32 v19,v245,v19\nv_mfma_f32_16x16x32_bf16 v[28:31],a[200:203],a[228:231],v[28:31]\ns_waitcnt vmcnt(17)\nds_read_b64_tr_b16 a[212:213],v231 offset:20480\nv_mul_f32 v44,v245,v44\nv_mul_f32 v45,v245,v45\nv_mul_f32 v46,v245,v46\n"
"v_mul_f32 v47,v245,v47\nv_mfma_f32_16x16x32_bf16 v[56:59],a[204:207],a[228:231],v[56:59]\nds_read_b64_tr_b16 a[214:215],v231 offset:21504\nv_mul_f32 v72,v245,v72\nv_mul_f32 v73,v245,v73\nv_mul_f32 v74,v245,v74\nv_mul_f32 v75,v245,v75\nv_mfma_f32_16x16x32_bf16 v[84:87],a[208:211],a[228:231],v[84:87]\n"
"ds_read_b128 a[228:231],v239 offset:1024\nv_mul_f32 v100,v245,v100\nv_mul_f32 v101,v245,v101\nv_mul_f32 v102,v245,v102\nv_mul_f32 v103,v245,v103\nv_mfma_f32_16x16x32_bf16 v[4:7],a[196:199],a[232:235],v[4:7]\ns_mov_b32 s74,492544\nbuffer_load_dwordx4 v225,s[52:55],s74 offen offset:1024 nt lds\n"
"v_mul_f32 v20,v246,v20\nv_mul_f32 v21,v246,v21\nv_mul_f32 v22,v246,v22\nv_mul_f32 v23,v246,v23\nv_mfma_f32_16x16x32_bf16 v[32:35],a[200:203],a[232:235],v[32:35]\nds_read_b64_tr_b16 a[216:217],v232 offset:20480\nv_mul_f32 v48,v246,v48\nv_mul_f32 v49,v246,v49\n"
"v_mul_f32 v50,v246,v50\nv_mul_f32 v51,v246,v51\nv_mfma_f32_16x16x32_bf16 v[60:63],a[204:207],a[232:235],v[60:63]\ns_mov_b32 s75,497664\nv_mul_f32 v76,v246,v76\nv_mul_f32 v77,v246,v77\nv_mul_f32 v78,v246,v78\nv_mul_f32 v79,v246,v79\n"
"v_mfma_f32_16x16x32_bf16 v[88:91],a[208:211],a[232:235],v[88:91]\nds_read_b128 a[232:235],v239 offset:3072\nv_mul_f32 v104,v246,v104\nv_mul_f32 v105,v246,v105\nv_mul_f32 v106,v246,v106\nv_mul_f32 v107,v246,v107\nv_mfma_f32_16x16x32_bf16 v[8:11],a[196:199],a[236:239],v[8:11]\nbuffer_load_dwordx4 v225,s[52:55],s75 offen offset:2048 nt lds\n"
"v_mul_f32 v24,v247,v24\nv_mul_f32 v25,v247,v25\nv_mul_f32 v26,v247,v26\nv_mul_f32 v27,v247,v27\nv_mfma_f32_16x16x32_bf16 v[36:39],a[200:203],a[236:239],v[36:39]\nds_read_b64_tr_b16 a[218:219],v232 offset:21504\nv_mul_f32 v52,v247,v52\nv_mul_f32 v53,v247,v53\n"
"v_mul_f32 v54,v247,v54\nv_mul_f32 v55,v247,v55\nv_mfma_f32_16x16x32_bf16 v[64:67],a[204:207],a[236:239],v[64:67]\ns_mov_b32 s72,498688\nv_mul_f32 v80,v247,v80\nv_mul_f32 v81,v247,v81\nv_mul_f32 v82,v247,v82\nv_mul_f32 v83,v247,v83\n"
"v_mfma_f32_16x16x32_bf16 v[92:95],a[208:211],a[236:239],v[92:95]\nds_read_b128 a[236:239],v239 offset:5120\nv_mul_f32 v108,v247,v108\nv_mul_f32 v109,v247,v109\nv_mul_f32 v110,v247,v110\nv_mul_f32 v111,v247,v111\nv_mfma_f32_16x16x32_bf16 v[12:15],a[196:199],a[240:243],v[12:15]\nbuffer_load_dwordx4 v225,s[52:55],s72 offen offset:3072 nt lds\n"
"v_fma_f32 v182,v182,s69,-v251\nv_mfma_f32_16x16x32_bf16 v[40:43],a[200:203],a[240:243],v[40:43]\nds_read_b64_tr_b16 a[220:221],v233 offset:20480\nv_fma_f32 v183,v183,s69,-v251\nv_fma_f32 v184,v184,s69,-v252\nv_mfma_f32_16x16x32_bf16 v[68:71],a[204:207],a[240:243],v[68:71]\nds_read_b64_tr_b16 a[222:223],v233 offset:21504\nv_fma_f32 v185,v185,s69,-v252\n"
"v_mfma_f32_16x16x32_bf16 v[96:99],a[208:211],a[240:243],v[96:99]\nds_read_b128 a[240:243],v239 offset:7168\nv_exp_f32 v180,v180\nv_mfma_f32_16x16x32_bf16 v[16:19],a[196:199],a[244:247],v[16:19]\nds_read_b64_tr_b16 a[224:225],v234 offset:20480\nv_fma_f32 v186,v186,s69,-v252\nv_mfma_f32_16x16x32_bf16 v[44:47],a[200:203],a[244:247],v[44:47]\nds_write_b64 v240,v[156:157] offset:22528\n"
"v_mfma_f32_16x16x32_bf16 v[72:75],a[204:207],a[244:247],v[72:75]\nv_fma_f32 v187,v187,s69,-v252\nv_fma_f32 v188,v188,s69,-v253\nv_mfma_f32_16x16x32_bf16 v[100:103],a[208:211],a[244:247],v[100:103]\nds_read_b128 a[244:247],v239 offset:9216\ns_waitcnt lgkmcnt(13)\nv_fma_f32 v189,v189,s69,-v253\nv_mfma_f32_16x16x32_bf16 v[20:23],a[196:199],a[248:251],v[20:23]\n"
"ds_read_b64_tr_b16 a[226:227],v234 offset:21504\nv_fma_f32 v190,v190,s69,-v253\nv_mfma_f32_16x16x32_bf16 v[48:51],a[200:203],a[248:251],v[48:51]\ns_add_u32 m0,s64,20480\nv_exp_f32 v184,v184\nv_fma_f32 v191,v191,s69,-v253\nv_mfma_f32_16x16x32_bf16 v[76:79],a[204:207],a[248:251],v[76:79]\ns_mov_b32 s73,507904\n"
"ds_write_b64 v240,v[160:161] offset:24576\nv_mfma_f32_16x16x32_bf16 v[104:107],a[208:211],a[248:251],v[104:107]\nds_read_b128 a[248:251],v239 offset:11264\nv_exp_f32 v188,v188\nv_mfma_f32_16x16x32_bf16 v[24:27],a[196:199],a[252:255],v[24:27]\nds_write_b64 v240,v[164:165] offset:26624\nv_mfma_f32_16x16x32_bf16 v[52:55],a[200:203],a[252:255],v[52:55]\nv_fma_f32 v192,v192,s69,-v254\n"
"v_fma_f32 v193,v193,s69,-v254\nv_fma_f32 v194,v194,s69,-v254\nv_mfma_f32_16x16x32_bf16 v[80:83],a[204:207],a[252:255],v[80:83]\nv_fma_f32 v195,v195,s69,-v254\nv_mfma_f32_16x16x32_bf16 v[108:111],a[208:211],a[252:255],v[108:111]\nds_read_b128 a[252:255],v239 offset:13312\ns_waitcnt lgkmcnt(4)\nv_mfma_f32_16x16x32_bf16 v[0:3],a[212:215],a[228:231],v[0:3]\n"
"buffer_load_dwordx4 v225,s[52:55],s73 offen offset:0 nt lds\ns_mov_b32 s74,508928\nv_mfma_f32_16x16x32_bf16 v[28:31],a[216:219],a[228:231],v[28:31]\ns_waitcnt vmcnt(17)\nds_read_b64_tr_b16 a[196:197],v231 offset:0\nv_mfma_f32_16x16x32_bf16 v[56:59],a[220:223],a[228:231],v[56:59]\nds_read_b64_tr_b16 a[198:199],v231 offset:1024\nv_mfma_f32_16x16x32_bf16 v[84:87],a[224:227],a[228:231],v[84:87]\n"
"v_exp_f32 v192,v192\ns_waitcnt lgkmcnt(0)\ns_barrier\nds_read_b128 a[228:231],v239 offset:14336\nv_mfma_f32_16x16x32_bf16 v[4:7],a[212:215],a[232:235],v[4:7]\nbuffer_load_dwordx4 v225,s[52:55],s74 offen offset:1024 nt lds\nv_add_f32 v114,v168,v114\nv_mfma_f32_16x16x32_bf16 v[32:35],a[216:219],a[232:235],v[32:35]\n"
"ds_read_b64_tr_b16 a[200:201],v232 offset:0\nv_exp_f32 v169,v169\nv_mfma_f32_16x16x32_bf16 v[60:63],a[220:223],a[232:235],v[60:63]\ns_mov_b32 s75,514048\nv_exp_f32 v170,v170\nv_mfma_f32_16x16x32_bf16 v[88:91],a[224:227],a[232:235],v[88:91]\nds_read_b128 a[232:235],v239 offset:16384\nv_exp_f32 v171,v171\n"
"v_mfma_f32_16x16x32_bf16 v[8:11],a[212:215],a[236:239],v[8:11]\nbuffer_load_dwordx4 v225,s[52:55],s75 offen offset:2048 nt lds\nv_add_f32 v114,v169,v114\nv_mfma_f32_16x16x32_bf16 v[36:39],a[216:219],a[236:239],v[36:39]\nds_read_b64_tr_b16 a[202:203],v232 offset:1024\nv_cvt_pk_bf16_f32 v168,v168,v169\nv_add_f32 v114,v170,v114\nv_mfma_f32_16x16x32_bf16 v[64:67],a[220:223],a[236:239],v[64:67]\n"
"s_mov_b32 s72,515072\nv_cvt_pk_bf16_f32 v169,v170,v171\nv_add_f32 v114,v171,v114\nv_mfma_f32_16x16x32_bf16 v[92:95],a[224:227],a[236:239],v[92:95]\nds_read_b128 a[236:239],v239 offset:18432\nv_exp_f32 v173,v173\nv_mfma_f32_16x16x32_bf16 v[12:15],a[212:215],a[240:243],v[12:15]\nbuffer_load_dwordx4 v225,s[52:55],s72 offen offset:3072 nt lds\n"
"v_add_f32 v118,v172,v118\nv_mfma_f32_16x16x32_bf16 v[40:43],a[216:219],a[240:243],v[40:43]\nds_read_b64_tr_b16 a[204:205],v233 offset:0\nv_exp_f32 v174,v174\nv_mfma_f32_16x16x32_bf16 v[68:71],a[220:223],a[240:243],v[68:71]\nds_read_b64_tr_b16 a[206:207],v233 offset:1024\nv_add_f32 v118,v173,v118\nv_mfma_f32_16x16x32_bf16 v[96:99],a[224:227],a[240:243],v[96:99]\n"
"ds_read_b128 a[240:243],v239 offset:20480\nv_exp_f32 v175,v175\nv_mfma_f32_16x16x32_bf16 v[16:19],a[212:215],a[244:247],v[16:19]\nds_read_b64_tr_b16 a[208:209],v234 offset:0\nv_cvt_pk_bf16_f32 v172,v172,v173\nv_mfma_f32_16x16x32_bf16 v[44:47],a[216:219],a[244:247],v[44:47]\nv_add_f32 v118,v174,v118\nv_cvt_pk_bf16_f32 v173,v174,v175\n"
"v_add_f32 v118,v175,v118\nv_mfma_f32_16x16x32_bf16 v[72:75],a[220:223],a[244:247],v[72:75]\nv_exp_f32 v177,v177\nv_mfma_f32_16x16x32_bf16 v[100:103],a[224:227],a[244:247],v[100:103]\nds_read_b128 a[244:247],v239 offset:22528\nv_exp_f32 v178,v178\nv_mfma_f32_16x16x32_bf16 v[20:23],a[212:215],a[248:251],v[20:23]\nds_read_b64_tr_b16 a[210:211],v234 offset:1024\n"
"v_add_f32 v122,v176,v122\nv_mfma_f32_16x16x32_bf16 v[48:51],a[216:219],a[248:251],v[48:51]\nv_subrev_u32 v226,s68,v226\nv_exp_f32 v179,v179\nv_mfma_f32_16x16x32_bf16 v[76:79],a[220:223],a[248:251],v[76:79]\ns_add_u32 m0,s64,0\nv_add_f32 v122,v177,v122\nv_cvt_pk_bf16_f32 v176,v176,v177\n"
"v_mfma_f32_16x16x32_bf16 v[104:107],a[224:227],a[248:251],v[104:107]\nds_read_b128 a[248:251],v239 offset:24576\nv_add_f32 v122,v178,v122\nv_cvt_pk_bf16_f32 v177,v178,v179\nv_mfma_f32_16x16x32_bf16 v[24:27],a[212:215],a[252:255],v[24:27]\ns_mov_b32 s73,229376\nv_add_f32 v122,v179,v122\nv_add_f32 v126,v180,v126\n"
"v_mfma_f32_16x16x32_bf16 v[52:55],a[216:219],a[252:255],v[52:55]\nv_exp_f32 v181,v181\nv_add_f32 v130,v184,v130\nv_mfma_f32_16x16x32_bf16 v[80:83],a[220:223],a[252:255],v[80:83]\nv_exp_f32 v182,v182\nv_mfma_f32_16x16x32_bf16 v[108:111],a[224:227],a[252:255],v[108:111]\nds_read_b128 a[252:255],v239 offset:26624\ns_waitcnt lgkmcnt(2)\n"
"v_add_f32 v126,v181,v126\nv_mfma_f32_16x16x32_bf16 v[0:3],a[196:199],a[228:231],v[0:3]\nbuffer_load_dwordx4 v226,s[56:59],s73 offen offset:0 lds\nv_cvt_pk_bf16_f32 v180,v180,v181\nv_mfma_f32_16x16x32_bf16 v[28:31],a[200:203],a[228:231],v[28:31]\ns_waitcnt vmcnt(17)\nds_read_b64_tr_b16 a[212:213],v231 offset:4096\nv_add_f32 v126,v182,v126\n"
"v_mfma_f32_16x16x32_bf16 v[56:59],a[204:207],a[228:231],v[56:59]\nds_read_b64_tr_b16 a[214:215],v231 offset:5120\nv_add_f32 v134,v188,v134\nv_mfma_f32_16x16x32_bf16 v[84:87],a[208:211],a[228:231],v[84:87]\nds_read_b128 a[228:231],v239 offset:15360\nv_exp_f32 v183,v183\nv_mfma_f32_16x16x32_bf16 v[4:7],a[196:199],a[232:235],v[4:7]\ns_mov_b32 s74,242688\n"
"buffer_load_dwordx4 v226,s[56:59],s74 offen offset:1024 lds\nv_mfma_f32_16x16x32_bf16 v[32:35],a[200:203],a[232:235],v[32:35]\nds_read_b64_tr_b16 a[216:217],v232 offset:4096\nv_cvt_pk_bf16_f32 v181,v182,v183\nv_add_f32 v126,v183,v126\nv_mfma_f32_16x16x32_bf16 v[60:63],a[204:207],a[232:235],v[60:63]\ns_mov_b32 s75,234496\nv_exp_f32 v185,v185\n"
"v_mfma_f32_16x16x32_bf16 v[88:91],a[208:211],a[232:235],v[88:91]\nds_read_b128 a[232:235],v239 offset:17408\nv_exp_f32 v186,v186\nv_mfma_f32_16x16x32_bf16 v[8:11],a[196:199],a[236:239],v[8:11]\nbuffer_load_dwordx4 v226,s[56:59],s75 offen offset:2048 lds\nv_add_f32 v130,v185,v130\nv_mfma_f32_16x16x32_bf16 v[36:39],a[200:203],a[236:239],v[36:39]\nds_read_b64_tr_b16 a[218:219],v232 offset:5120\n"
"v_exp_f32 v187,v187\nv_mfma_f32_16x16x32_bf16 v[64:67],a[204:207],a[236:239],v[64:67]\ns_mov_b32 s72,247808\nv_cvt_pk_bf16_f32 v184,v184,v185\nv_add_f32 v130,v186,v130\nv_mfma_f32_16x16x32_bf16 v[92:95],a[208:211],a[236:239],v[92:95]\nds_read_b128 a[236:239],v239 offset:19456\nv_cvt_pk_bf16_f32 v185,v186,v187\n"
"v_add_f32 v130,v187,v130\nv_mfma_f32_16x16x32_bf16 v[12:15],a[196:199],a[240:243],v[12:15]\nbuffer_load_dwordx4 v226,s[56:59],s72 offen offset:3072 lds\nv_add_f32 v138,v192,v138\nv_mfma_f32_16x16x32_bf16 v[40:43],a[200:203],a[240:243],v[40:43]\nds_read_b64_tr_b16 a[220:221],v233 offset:4096\nv_exp_f32 v189,v189\nv_mfma_f32_16x16x32_bf16 v[68:71],a[204:207],a[240:243],v[68:71]\n"
"ds_read_b64_tr_b16 a[222:223],v233 offset:5120\nv_add_f32 v134,v189,v134\nv_mfma_f32_16x16x32_bf16 v[96:99],a[208:211],a[240:243],v[96:99]\nds_read_b128 a[240:243],v239 offset:21504\nv_exp_f32 v190,v190\nv_mfma_f32_16x16x32_bf16 v[16:19],a[196:199],a[244:247],v[16:19]\nds_read_b64_tr_b16 a[224:225],v234 offset:4096\nv_cvt_pk_bf16_f32 v188,v188,v189\n"
"v_mfma_f32_16x16x32_bf16 v[44:47],a[200:203],a[244:247],v[44:47]\nv_exp_f32 v191,v191\nv_add_f32 v134,v190,v134\nv_mfma_f32_16x16x32_bf16 v[72:75],a[204:207],a[244:247],v[72:75]\nv_cvt_pk_bf16_f32 v189,v190,v191\nv_add_f32 v134,v191,v134\nv_mfma_f32_16x16x32_bf16 v[100:103],a[208:211],a[244:247],v[100:103]\nds_read_b128 a[244:247],v239 offset:23552\n"
"s_waitcnt lgkmcnt(12)\nv_fma_f32 v196,v196,s69,-v248\nv_mfma_f32_16x16x32_bf16 v[20:23],a[196:199],a[248:251],v[20:23]\nds_read_b64_tr_b16 a[226:227],v234 offset:5120\nv_fma_f32 v197,v197,s69,-v248\nv_mfma_f32_16x16x32_bf16 v[48:51],a[200:203],a[248:251],v[48:51]\ns_add_u32 m0,s64,4096\nv_exp_f32 v193,v193\n"
"v_fma_f32 v198,v198,s69,-v248\nv_mfma_f32_16x16x32_bf16 v[76:79],a[204:207],a[248:251],v[76:79]\ns_mov_b32 s73,258048\nv_exp_f32 v194,v194\nv_mfma_f32_16x16x32_bf16 v[104:107],a[208:211],a[248:251],v[104:107]\nds_read_b128 a[248:251],v239 offset:25600\nv_exp_f32 v195,v195\nv_mfma_f32_16x16x32_bf16 v[24:27],a[196:199],a[252:255],v[24:27]\n"
"v_add_f32 v138,v193,v138\nv_cvt_pk_bf16_f32 v192,v192,v193\nv_mfma_f32_16x16x32_bf16 v[52:55],a[200:203],a[252:255],v[52:55]\nv_add_f32 v138,v194,v138\nv_cvt_pk_bf16_f32 v193,v194,v195\nv_add_f32 v138,v195,v138\nv_mfma_f32_16x16x32_bf16 v[80:83],a[204:207],a[252:255],v[80:83]\nds_write_b64 v240,v[168:169] offset:28672\n"
"v_mfma_f32_16x16x32_bf16 v[108:111],a[208:211],a[252:255],v[108:111]\nds_read_b128 a[252:255],v239 offset:27648\ns_waitcnt lgkmcnt(3)\nv_fma_f32 v199,v199,s69,-v248\nv_mfma_f32_16x16x32_bf16 v[0:3],a[212:215],a[228:231],v[0:3]\nbuffer_load_dwordx4 v226,s[56:59],s73 offen offset:0 lds\nv_fma_f32 v200,v200,s69,-v249\nv_mfma_f32_16x16x32_bf16 v[28:31],a[216:219],a[228:231],v[28:31]\n"
"s_waitcnt vmcnt(17)\nds_read_b64_tr_b16 a[196:197],v231 offset:8192\nv_fma_f32 v201,v201,s69,-v249\nv_mfma_f32_16x16x32_bf16 v[56:59],a[220:223],a[228:231],v[56:59]\nds_read_b64_tr_b16 a[198:199],v231 offset:9216\nv_fma_f32 v202,v202,s69,-v249\nv_mfma_f32_16x16x32_bf16 v[84:87],a[224:227],a[228:231],v[84:87]\nv_exp_f32 v196,v196\n"
"ds_write_b64 v240,v[172:173] offset:30720\nv_fma_f32 v203,v203,s69,-v249\nv_exp_f32 v200,v200\nds_write_b64 v240,v[176:177] offset:32768\nv_fma_f32 v204,v204,s69,-v250\nv_fma_f32 v205,v205,s69,-v250\nv_fma_f32 v206,v206,s69,-v250\nv_fma_f32 v207,v207,s69,-v250\n"
"v_exp_f32 v204,v204\nds_write_b64 v240,v[180:181] offset:34816\nv_fma_f32 v208,v208,s69,-v251\nv_fma_f32 v209,v209,s69,-v251\nv_fma_f32 v210,v210,s69,-v251\nv_fma_f32 v211,v211,s69,-v251\nv_exp_f32 v208,v208\nds_write_b64 v240,v[184:185] offset:36864\n"
"v_fma_f32 v212,v212,s69,-v252\nv_fma_f32 v213,v213,s69,-v252\nv_fma_f32 v214,v214,s69,-v252\nv_fma_f32 v215,v215,s69,-v252\nv_exp_f32 v212,v212\nds_write_b64 v240,v[188:189] offset:38912\nv_fma_f32 v216,v216,s69,-v253\nv_fma_f32 v217,v217,s69,-v253\n"
"v_fma_f32 v218,v218,s69,-v253\nv_fma_f32 v219,v219,s69,-v253\nv_exp_f32 v216,v216\nds_write_b64 v240,v[192:193] offset:40960\nv_fma_f32 v220,v220,s69,-v254\nv_fma_f32 v221,v221,s69,-v254\nv_fma_f32 v222,v222,s69,-v254\nv_fma_f32 v223,v223,s69,-v254\n"
"v_exp_f32 v220,v220\ns_waitcnt lgkmcnt(0)\ns_barrier\nds_read_b128 a[228:231],v239 offset:28672\nv_mfma_f32_16x16x32_bf16 v[4:7],a[212:215],a[232:235],v[4:7]\ns_mov_b32 s74,271360\nbuffer_load_dwordx4 v226,s[56:59],s74 offen offset:1024 lds\nv_mfma_f32_16x16x32_bf16 v[32:35],a[216:219],a[232:235],v[32:35]\n"
"ds_read_b64_tr_b16 a[200:201],v232 offset:8192\nv_exp_f32 v197,v197\nv_mfma_f32_16x16x32_bf16 v[60:63],a[220:223],a[232:235],v[60:63]\ns_mov_b32 s75,263168\nv_exp_f32 v198,v198\nv_mfma_f32_16x16x32_bf16 v[88:91],a[224:227],a[232:235],v[88:91]\nds_read_b128 a[232:235],v239 offset:30720\nv_exp_f32 v199,v199\n"
"v_mfma_f32_16x16x32_bf16 v[8:11],a[212:215],a[236:239],v[8:11]\nbuffer_load_dwordx4 v226,s[56:59],s75 offen offset:2048 lds\nv_add_f32 v114,v196,v114\nv_mfma_f32_16x16x32_bf16 v[36:39],a[216:219],a[236:239],v[36:39]\nds_read_b64_tr_b16 a[202:203],v232 offset:9216\nv_add_f32 v114,v197,v114\nv_cvt_pk_bf16_f32 v196,v196,v197\nv_mfma_f32_16x16x32_bf16 v[64:67],a[220:223],a[236:239],v[64:67]\n"
"s_mov_b32 s72,276480\nv_add_f32 v114,v198,v114\nv_cvt_pk_bf16_f32 v197,v198,v199\nv_mfma_f32_16x16x32_bf16 v[92:95],a[224:227],a[236:239],v[92:95]\nds_read_b128 a[236:239],v239 offset:32768\nv_add_f32 v114,v199,v114\nv_add_f32 v118,v200,v118\nv_mfma_f32_16x16x32_bf16 v[12:15],a[212:215],a[240:243],v[12:15]\n"
"buffer_load_dwordx4 v226,s[56:59],s72 offen offset:3072 lds\nv_add_f32 v122,v204,v122\nv_mfma_f32_16x16x32_bf16 v[40:43],a[216:219],a[240:243],v[40:43]\nds_read_b64_tr_b16 a[204:205],v233 offset:8192\nv_exp_f32 v201,v201\nv_mfma_f32_16x16x32_bf16 v[68:71],a[220:223],a[240:243],v[68:71]\nds_read_b64_tr_b16 a[206:207],v233 offset:9216\nv_add_f32 v118,v201,v118\n"
"v_mfma_f32_16x16x32_bf16 v[96:99],a[224:227],a[240:243],v[96:99]\nds_read_b128 a[240:243],v239 offset:34816\nv_exp_f32 v202,v202\nv_mfma_f32_16x16x32_bf16 v[16:19],a[212:215],a[244:247],v[16:19]\nds_read_b64_tr_b16 a[208:209],v234 offset:8192\nv_cvt_pk_bf16_f32 v200,v200,v201\nv_mfma_f32_16x16x32_bf16 v[44:47],a[216:219],a[244:247],v[44:47]\nv_exp_f32 v203,v203\n"
"v_add_f32 v118,v202,v118\nv_mfma_f32_16x16x32_bf16 v[72:75],a[220:223],a[244:247],v[72:75]\nv_cvt_pk_bf16_f32 v201,v202,v203\nv_add_f32 v118,v203,v118\nv_mfma_f32_16x16x32_bf16 v[100:103],a[224:227],a[244:247],v[100:103]\nds_read_b128 a[244:247],v239 offset:36864\nv_exp_f32 v205,v205\nv_mfma_f32_16x16x32_bf16 v[20:23],a[212:215],a[248:251],v[20:23]\n"
"ds_read_b64_tr_b16 a[210:211],v234 offset:9216\nv_add_f32 v122,v205,v122\nv_mfma_f32_16x16x32_bf16 v[48:51],a[216:219],a[248:251],v[48:51]\ns_add_u32 m0,s64,8192\nv_exp_f32 v206,v206\nv_cvt_pk_bf16_f32 v204,v204,v205\nv_mfma_f32_16x16x32_bf16 v[76:79],a[220:223],a[248:251],v[76:79]\ns_mov_b32 s73,286720\n"
"v_exp_f32 v207,v207\nv_mfma_f32_16x16x32_bf16 v[104:107],a[224:227],a[248:251],v[104:107]\nds_read_b128 a[248:251],v239 offset:38912\nv_add_f32 v122,v206,v122\nv_cvt_pk_bf16_f32 v205,v206,v207\nv_mfma_f32_16x16x32_bf16 v[24:27],a[212:215],a[252:255],v[24:27]\nv_add_f32 v122,v207,v122\nv_add_f32 v126,v208,v126\n"
"v_mfma_f32_16x16x32_bf16 v[52:55],a[216:219],a[252:255],v[52:55]\nv_exp_f32 v209,v209\nv_add_f32 v130,v212,v130\nv_mfma_f32_16x16x32_bf16 v[80:83],a[220:223],a[252:255],v[80:83]\nv_exp_f32 v210,v210\nv_mfma_f32_16x16x32_bf16 v[108:111],a[224:227],a[252:255],v[108:111]\nds_read_b128 a[252:255],v239 offset:40960\ns_waitcnt lgkmcnt(2)\n"
"v_add_f32 v126,v209,v126\nv_mfma_f32_16x16x32_bf16 v[0:3],a[196:199],a[228:231],v[0:3]\nbuffer_load_dwordx4 v226,s[56:59],s73 offen offset:0 lds\nv_cvt_pk_bf16_f32 v208,v208,v209\nv_mfma_f32_16x16x32_bf16 v[28:31],a[200:203],a[228:231],v[28:31]\ns_waitcnt vmcnt(17)\nds_read_b64_tr_b16 a[212:213],v231 offset:12288\nv_add_f32 v126,v210,v126\n"
"v_mfma_f32_16x16x32_bf16 v[56:59],a[204:207],a[228:231],v[56:59]\nds_read_b64_tr_b16 a[214:215],v231 offset:13312\nv_add_f32 v134,v216,v134\nv_mfma_f32_16x16x32_bf16 v[84:87],a[208:211],a[228:231],v[84:87]\nds_read_b128 a[228:231],v239 offset:29696\nv_exp_f32 v211,v211\nv_mfma_f32_16x16x32_bf16 v[4:7],a[196:199],a[232:235],v[4:7]\ns_mov_b32 s74,300032\n"
"buffer_load_dwordx4 v226,s[56:59],s74 offen offset:1024 lds\nv_mfma_f32_16x16x32_bf16 v[32:35],a[200:203],a[232:235],v[32:35]\nds_read_b64_tr_b16 a[216:217],v232 offset:12288\nv_cvt_pk_bf16_f32 v209,v210,v211\nv_add_f32 v126,v211,v126\nv_mfma_f32_16x16x32_bf16 v[60:63],a[204:207],a[232:235],v[60:63]\ns_mov_b32 s75,291840\nv_exp_f32 v213,v213\n"
"v_mfma_f32_16x16x32_bf16 v[88:91],a[208:211],a[232:235],v[88:91]\nds_read_b128 a[232:235],v239 offset:31744\nv_exp_f32 v214,v214\nv_mfma_f32_16x16x32_bf16 v[8:11],a[196:199],a[236:239],v[8:11]\nbuffer_load_dwordx4 v226,s[56:59],s75 offen offset:2048 lds\nv_add_f32 v130,v213,v130\nv_mfma_f32_16x16x32_bf16 v[36:39],a[200:203],a[236:239],v[36:39]\nds_read_b64_tr_b16 a[218:219],v232 offset:13312\n"
"v_exp_f32 v215,v215\nv_mfma_f32_16x16x32_bf16 v[64:67],a[204:207],a[236:239],v[64:67]\ns_mov_b32 s72,305152\nv_cvt_pk_bf16_f32 v212,v212,v213\nv_add_f32 v130,v214,v130\nv_mfma_f32_16x16x32_bf16 v[92:95],a[208:211],a[236:239],v[92:95]\nds_read_b128 a[236:239],v239 offset:33792\nv_cvt_pk_bf16_f32 v213,v214,v215\n"
"v_add_f32 v130,v215,v130\nv_mfma_f32_16x16x32_bf16 v[12:15],a[196:199],a[240:243],v[12:15]\nbuffer_load_dwordx4 v226,s[56:59],s72 offen offset:3072 lds\nv_add_f32 v138,v220,v138\nv_mfma_f32_16x16x32_bf16 v[40:43],a[200:203],a[240:243],v[40:43]\nds_read_b64_tr_b16 a[220:221],v233 offset:12288\nv_exp_f32 v217,v217\nv_mfma_f32_16x16x32_bf16 v[68:71],a[204:207],a[240:243],v[68:71]\n"
"ds_read_b64_tr_b16 a[222:223],v233 offset:13312\nv_add_f32 v134,v217,v134\nv_mfma_f32_16x16x32_bf16 v[96:99],a[208:211],a[240:243],v[96:99]\nds_read_b128 a[240:243],v239 offset:35840\nv_exp_f32 v218,v218\nv_mfma_f32_16x16x32_bf16 v[16:19],a[196:199],a[244:247],v[16:19]\nds_read_b64_tr_b16 a[224:225],v234 offset:12288\nv_cvt_pk_bf16_f32 v216,v216,v217\n"
"v_mfma_f32_16x16x32_bf16 v[44:47],a[200:203],a[244:247],v[44:47]\nv_exp_f32 v219,v219\nv_add_f32 v134,v218,v134\nv_mfma_f32_16x16x32_bf16 v[72:75],a[204:207],a[244:247],v[72:75]\nv_cvt_pk_bf16_f32 v217,v218,v219\nv_add_f32 v134,v219,v134\nv_mfma_f32_16x16x32_bf16 v[100:103],a[208:211],a[244:247],v[100:103]\nds_read_b128 a[244:247],v239 offset:37888\n"
"s_waitcnt lgkmcnt(12)\nv_mov_b32 v115,0\nv_mfma_f32_16x16x32_bf16 v[20:23],a[196:199],a[248:251],v[20:23]\nds_read_b64_tr_b16 a[226:227],v234 offset:13312\nv_permlane32_swap_b32 v114,v118\nv_mfma_f32_16x16x32_bf16 v[48:51],a[200:203],a[248:251],v[48:51]\ns_add_u32 m0,s64,12288\nv_exp_f32 v221,v221\n"
"v_add_f32 v114,v114,v118\nv_mfma_f32_16x16x32_bf16 v[76:79],a[204:207],a[248:251],v[76:79]\ns_mov_b32 s73,315392\nv_exp_f32 v222,v222\nv_mfma_f32_16x16x32_bf16 v[104:107],a[208:211],a[248:251],v[104:107]\nds_read_b128 a[248:251],v239 offset:39936\nv_exp_f32 v223,v223\nv_mfma_f32_16x16x32_bf16 v[24:27],a[196:199],a[252:255],v[24:27]\n"
"v_add_f32 v138,v221,v138\nv_cvt_pk_bf16_f32 v220,v220,v221\nv_mfma_f32_16x16x32_bf16 v[52:55],a[200:203],a[252:255],v[52:55]\nv_add_f32 v138,v222,v138\nv_cvt_pk_bf16_f32 v221,v222,v223\nv_add_f32 v138,v223,v138\nv_mfma_f32_16x16x32_bf16 v[80:83],a[204:207],a[252:255],v[80:83]\nds_write_b64 v240,v[196:197] offset:43008\n"
"v_mfma_f32_16x16x32_bf16 v[108:111],a[208:211],a[252:255],v[108:111]\nds_read_b128 a[252:255],v239 offset:41984\ns_waitcnt lgkmcnt(3)\nv_permlane32_swap_b32 v122,v126\nv_mfma_f32_16x16x32_bf16 v[0:3],a[212:215],a[228:231],v[0:3]\nbuffer_load_dwordx4 v226,s[56:59],s73 offen offset:0 lds\nv_add_f32 v122,v122,v126\nv_mfma_f32_16x16x32_bf16 v[28:31],a[216:219],a[228:231],v[28:31]\n"
"s_waitcnt vmcnt(17)\nds_read_b64_tr_b16 a[196:197],v231 offset:16384\nv_permlane32_swap_b32 v130,v134\nv_mfma_f32_16x16x32_bf16 v[56:59],a[220:223],a[228:231],v[56:59]\nds_read_b64_tr_b16 a[198:199],v231 offset:17408\nv_add_f32 v130,v130,v134\nv_mfma_f32_16x16x32_bf16 v[84:87],a[224:227],a[228:231],v[84:87]\nds_write_b64 v240,v[200:201] offset:45056\n"
"ds_write_b64 v240,v[204:205] offset:47104\nds_write_b64 v240,v[208:209] offset:49152\nds_write_b64 v240,v[212:213] offset:51200\nds_write_b64 v240,v[216:217] offset:53248\nds_write_b64 v240,v[220:221] offset:55296\nv_permlane32_swap_b32 v138,v115\nv_add_f32 v138,v138,v115\nv_bfe_u32 v118,v239,8,1\n"
"v_lshrrev_b32 v126,9,v239\nv_permlane16_swap_b32 v114,v122\nv_add_f32 v114,v114,v122\nv_permlane16_swap_b32 v130,v138\nv_add_f32 v130,v130,v138\nv_lshl_or_b32 v118,v118,1,v126\nv_and_b32 v126,0xf0,v239\nv_lshl_or_b32 v118,v118,8,v126\n"
"v_lshl_add_u32 v118,s70,2,v118\nds_write_b32 v118,v114 offset:61440\nds_write_b32 v118,v130 offset:62464\ns_waitcnt lgkmcnt(0)\ns_barrier\nds_read_b128 a[228:231],v239 offset:43008\nv_mfma_f32_16x16x32_bf16 v[4:7],a[212:215],a[232:235],v[4:7]\ns_mov_b32 s74,328704\n"
"buffer_load_dwordx4 v226,s[56:59],s74 offen offset:1024 lds\nv_mfma_f32_16x16x32_bf16 v[32:35],a[216:219],a[232:235],v[32:35]\nds_read_b64_tr_b16 a[200:201],v232 offset:16384\nv_and_b32 v255,0xf0,v239\nv_mfma_f32_16x16x32_bf16 v[60:63],a[220:223],a[232:235],v[60:63]\ns_mov_b32 s75,320512\nds_read_b128 v[140:143],v255 offset:59392\nv_mfma_f32_16x16x32_bf16 v[88:91],a[224:227],a[232:235],v[88:91]\n"
"ds_read_b128 a[232:235],v239 offset:45056\nds_read_b128 v[144:147],v255 offset:59648\nv_mfma_f32_16x16x32_bf16 v[8:11],a[212:215],a[236:239],v[8:11]\nbuffer_load_dwordx4 v226,s[56:59],s75 offen offset:2048 lds\ns_mov_b32 s72,333824\nv_mfma_f32_16x16x32_bf16 v[36:39],a[216:219],a[236:239],v[36:39]\nds_read_b64_tr_b16 a[202:203],v232 offset:17408\nds_read_b128 v[148:151],v255 offset:59904\n"
"v_mfma_f32_16x16x32_bf16 v[64:67],a[220:223],a[236:239],v[64:67]\nds_read_b128 v[152:155],v255 offset:60160\nv_mfma_f32_16x16x32_bf16 v[92:95],a[224:227],a[236:239],v[92:95]\nds_read_b128 a[236:239],v239 offset:47104\nds_read_b128 v[156:159],v255 offset:60416\nv_mfma_f32_16x16x32_bf16 v[12:15],a[212:215],a[240:243],v[12:15]\nbuffer_load_dwordx4 v226,s[56:59],s72 offen offset:3072 lds\nv_mfma_f32_16x16x32_bf16 v[40:43],a[216:219],a[240:243],v[40:43]\n"
"ds_read_b64_tr_b16 a[204:205],v233 offset:16384\nds_read_b128 v[160:163],v255 offset:60672\nv_mfma_f32_16x16x32_bf16 v[68:71],a[220:223],a[240:243],v[68:71]\nds_read_b64_tr_b16 a[206:207],v233 offset:17408\nv_mfma_f32_16x16x32_bf16 v[96:99],a[224:227],a[240:243],v[96:99]\nds_read_b128 a[240:243],v239 offset:49152\nds_read_b128 v[164:167],v255 offset:60928\nv_mfma_f32_16x16x32_bf16 v[16:19],a[212:215],a[244:247],v[16:19]\n"
"ds_read_b64_tr_b16 a[208:209],v234 offset:16384\nv_mfma_f32_16x16x32_bf16 v[44:47],a[216:219],a[244:247],v[44:47]\nds_read_b128 v[168:171],v255 offset:61440\nds_read_b128 v[172:175],v255 offset:61696\nv_mfma_f32_16x16x32_bf16 v[72:75],a[220:223],a[244:247],v[72:75]\nds_read_b128 v[176:179],v255 offset:61952\nv_mfma_f32_16x16x32_bf16 v[100:103],a[224:227],a[244:247],v[100:103]\nds_read_b128 a[244:247],v239 offset:51200\n"
"ds_read_b128 v[180:183],v255 offset:62208\nv_mfma_f32_16x16x32_bf16 v[20:23],a[212:215],a[248:251],v[20:23]\nds_read_b64_tr_b16 a[210:211],v234 offset:17408\ns_add_u32 m0,s64,16384\nv_mfma_f32_16x16x32_bf16 v[48:51],a[216:219],a[248:251],v[48:51]\ns_mov_b32 s73,344064\nds_read_b128 v[184:187],v255 offset:62464\nv_mfma_f32_16x16x32_bf16 v[76:79],a[220:223],a[248:251],v[76:79]\n"
"ds_read_b128 v[188:191],v255 offset:62720\nv_mfma_f32_16x16x32_bf16 v[104:107],a[224:227],a[248:251],v[104:107]\nds_read_b128 a[248:251],v239 offset:53248\nds_read_b128 v[192:195],v255 offset:62976\nv_mfma_f32_16x16x32_bf16 v[24:27],a[212:215],a[252:255],v[24:27]\nv_mfma_f32_16x16x32_bf16 v[52:55],a[216:219],a[252:255],v[52:55]\nv_mfma_f32_16x16x32_bf16 v[80:83],a[220:223],a[252:255],v[80:83]\nv_mfma_f32_16x16x32_bf16 v[108:111],a[224:227],a[252:255],v[108:111]\n"
"ds_read_b128 a[252:255],v239 offset:55296\ns_waitcnt lgkmcnt(5)\nv_mfma_f32_16x16x32_bf16 v[0:3],a[196:199],a[228:231],v[0:3]\nbuffer_load_dwordx4 v226,s[56:59],s73 offen offset:0 lds\ns_waitcnt lgkmcnt(1)\nv_mfma_f32_16x16x32_bf16 v[28:31],a[200:203],a[228:231],v[28:31]\ns_waitcnt vmcnt(17)\nds_read_b64_tr_b16 a[212:213],v231 offset:20480\n"
"v_add_f32 v140,v140,v141\nv_mfma_f32_16x16x32_bf16 v[56:59],a[204:207],a[228:231],v[56:59]\nds_read_b64_tr_b16 a[214:215],v231 offset:21504\nv_add_f32 v142,v142,v143\nv_mfma_f32_16x16x32_bf16 v[84:87],a[208:211],a[228:231],v[84:87]\nds_read_b128 a[228:231],v239 offset:44032\nv_add_f32 v140,v140,v142\nv_add_f32 v168,v168,v169\n"
"v_mfma_f32_16x16x32_bf16 v[4:7],a[196:199],a[232:235],v[4:7]\ns_mov_b32 s74,357376\nbuffer_load_dwordx4 v226,s[56:59],s74 offen offset:1024 lds\nv_mfma_f32_16x16x32_bf16 v[32:35],a[200:203],a[232:235],v[32:35]\nds_read_b64_tr_b16 a[216:217],v232 offset:20480\nv_add_f32 v170,v170,v171\nv_add_f32 v168,v168,v170\nv_mfma_f32_16x16x32_bf16 v[60:63],a[204:207],a[232:235],v[60:63]\n"
"s_mov_b32 s75,349184\nv_add_f32 v144,v144,v145\nv_add_f32 v146,v146,v147\nv_mfma_f32_16x16x32_bf16 v[88:91],a[208:211],a[232:235],v[88:91]\nds_read_b128 a[232:235],v239 offset:46080\nv_add_f32 v144,v144,v146\nv_add_f32 v172,v172,v173\nv_mfma_f32_16x16x32_bf16 v[8:11],a[196:199],a[236:239],v[8:11]\n"
"buffer_load_dwordx4 v226,s[56:59],s75 offen offset:2048 lds\nv_add_f32 v174,v174,v175\nv_mfma_f32_16x16x32_bf16 v[36:39],a[200:203],a[236:239],v[36:39]\nds_read_b64_tr_b16 a[218:219],v232 offset:21504\nv_add_f32 v172,v172,v174\nv_add_f32 v148,v148,v149\nv_mfma_f32_16x16x32_bf16 v[64:67],a[204:207],a[236:239],v[64:67]\ns_mov_b32 s72,362496\n"
"v_add_f32 v150,v150,v151\nv_add_f32 v148,v148,v150\nv_mfma_f32_16x16x32_bf16 v[92:95],a[208:211],a[236:239],v[92:95]\nds_read_b128 a[236:239],v239 offset:48128\nv_add_f32 v176,v176,v177\nv_add_f32 v178,v178,v179\nv_mfma_f32_16x16x32_bf16 v[12:15],a[196:199],a[240:243],v[12:15]\nbuffer_load_dwordx4 v226,s[56:59],s72 offen offset:3072 lds\n"
"v_add_f32 v176,v176,v178\nv_mfma_f32_16x16x32_bf16 v[40:43],a[200:203],a[240:243],v[40:43]\nds_read_b64_tr_b16 a[220:221],v233 offset:20480\nv_add_f32 v152,v152,v153\nv_add_f32 v154,v154,v155\nv_mfma_f32_16x16x32_bf16 v[68:71],a[204:207],a[240:243],v[68:71]\nds_read_b64_tr_b16 a[222:223],v233 offset:21504\nv_add_f32 v152,v152,v154\n"
"v_mfma_f32_16x16x32_bf16 v[96:99],a[208:211],a[240:243],v[96:99]\nds_read_b128 a[240:243],v239 offset:50176\nv_add_f32 v180,v180,v181\nv_add_f32 v182,v182,v183\nv_mfma_f32_16x16x32_bf16 v[16:19],a[196:199],a[244:247],v[16:19]\nds_read_b64_tr_b16 a[224:225],v234 offset:20480\nv_add_f32 v180,v180,v182\nv_mfma_f32_16x16x32_bf16 v[44:47],a[200:203],a[244:247],v[44:47]\n"
"v_add_f32 v156,v156,v157\nv_add_f32 v158,v158,v159\nv_add_f32 v156,v156,v158\nv_mfma_f32_16x16x32_bf16 v[72:75],a[204:207],a[244:247],v[72:75]\nv_add_f32 v184,v184,v185\nv_add_f32 v186,v186,v187\nv_mfma_f32_16x16x32_bf16 v[100:103],a[208:211],a[244:247],v[100:103]\nds_read_b128 a[244:247],v239 offset:52224\n"
"s_waitcnt lgkmcnt(12)\nv_add_f32 v184,v184,v186\nv_mfma_f32_16x16x32_bf16 v[20:23],a[196:199],a[248:251],v[20:23]\nds_read_b64_tr_b16 a[226:227],v234 offset:21504\nv_add_f32 v160,v160,v161\nv_mfma_f32_16x16x32_bf16 v[48:51],a[200:203],a[248:251],v[48:51]\ns_add_u32 m0,s64,20480\nv_add_f32 v162,v162,v163\n"
"v_add_f32 v160,v160,v162\nv_add_f32 v188,v188,v189\nv_mfma_f32_16x16x32_bf16 v[76:79],a[204:207],a[248:251],v[76:79]\ns_mov_b32 s73,372736\nv_add_f32 v190,v190,v191\nv_add_f32 v188,v188,v190\nv_mfma_f32_16x16x32_bf16 v[104:107],a[208:211],a[248:251],v[104:107]\nds_read_b128 a[248:251],v239 offset:54272\n"
"v_add_f32 v164,v164,v165\nv_add_f32 v166,v166,v167\nv_mfma_f32_16x16x32_bf16 v[24:27],a[196:199],a[252:255],v[24:27]\nv_add_f32 v164,v164,v166\nv_add_f32 v192,v192,v193\nv_mfma_f32_16x16x32_bf16 v[52:55],a[200:203],a[252:255],v[52:55]\nv_add_f32 v194,v194,v195\nv_add_f32 v192,v192,v194\n"
"v_fma_f32 v140,v140,v241,v168\nv_mfma_f32_16x16x32_bf16 v[80:83],a[204:207],a[252:255],v[80:83]\nv_fma_f32 v144,v144,v242,v172\nv_fma_f32 v148,v148,v243,v176\nv_mfma_f32_16x16x32_bf16 v[108:111],a[208:211],a[252:255],v[108:111]\nds_read_b128 a[252:255],v239 offset:56320\ns_waitcnt lgkmcnt(2)\nv_fma_f32 v152,v152,v244,v180\n"
"v_mfma_f32_16x16x32_bf16 v[0:3],a[212:215],a[228:231],v[0:3]\nbuffer_load_dwordx4 v226,s[56:59],s73 offen offset:0 lds\nv_fma_f32 v156,v156,v245,v184\nv_mfma_f32_16x16x32_bf16 v[28:31],a[216:219],a[228:231],v[28:31]\ns_waitcnt vmcnt(17)\nds_read_b64_tr_b16 a[196:197],v231 offset:0\nv_fma_f32 v160,v160,v246,v188\nv_mfma_f32_16x16x32_bf16 v[56:59],a[220:223],a[228:231],v[56:59]\n"
"ds_read_b64_tr_b16 a[198:199],v231 offset:1024\nv_fma_f32 v164,v164,v247,v192\nv_mfma_f32_16x16x32_bf16 v[84:87],a[224:227],a[228:231],v[84:87]\ns_waitcnt lgkmcnt(2)\ns_barrier\nv_rcp_f32 v248,v140\nv_mfma_f32_16x16x32_bf16 v[4:7],a[212:215],a[232:235],v[4:7]\ns_mov_b32 s74,386048\n"
"buffer_load_dwordx4 v226,s[56:59],s74 offen offset:1024 lds\nv_mfma_f32_16x16x32_bf16 v[32:35],a[216:219],a[232:235],v[32:35]\nds_read_b64_tr_b16 a[200:201],v232 offset:0\nv_rcp_f32 v249,v144\nv_mfma_f32_16x16x32_bf16 v[60:63],a[220:223],a[232:235],v[60:63]\ns_mov_b32 s75,377856\nv_rcp_f32 v250,v148\nv_mfma_f32_16x16x32_bf16 v[88:91],a[224:227],a[232:235],v[88:91]\n"
"v_rcp_f32 v251,v152\nv_mov_b32 v242,14336\nv_mfma_f32_16x16x32_bf16 v[8:11],a[212:215],a[236:239],v[8:11]\nbuffer_load_dwordx4 v226,s[56:59],s75 offen offset:2048 lds\nv_mad_u32_u24 v241,v242,s70,v239\nv_mfma_f32_16x16x32_bf16 v[36:39],a[216:219],a[236:239],v[36:39]\nds_read_b64_tr_b16 a[202:203],v232 offset:1024\nv_rcp_f32 v252,v156\n"
"v_mfma_f32_16x16x32_bf16 v[64:67],a[220:223],a[236:239],v[64:67]\ns_mov_b32 s72,391168\nv_rcp_f32 v253,v160\nv_mfma_f32_16x16x32_bf16 v[92:95],a[224:227],a[236:239],v[92:95]\nv_rcp_f32 v254,v164\nv_mul_f32 v0,v248,v0\nv_mfma_f32_16x16x32_bf16 v[12:15],a[212:215],a[240:243],v[12:15]\nbuffer_load_dwordx4 v226,s[56:59],s72 offen offset:3072 lds\n"
"v_mul_f32 v1,v248,v1\nv_mfma_f32_16x16x32_bf16 v[40:43],a[216:219],a[240:243],v[40:43]\nds_read_b64_tr_b16 a[204:205],v233 offset:0\nv_mul_f32 v2,v248,v2\nv_mul_f32 v3,v248,v3\nv_mfma_f32_16x16x32_bf16 v[68:71],a[220:223],a[240:243],v[68:71]\nds_read_b64_tr_b16 a[206:207],v233 offset:1024\nv_cvt_pk_bf16_f32 v0,v0,v1\n"
"v_mfma_f32_16x16x32_bf16 v[96:99],a[224:227],a[240:243],v[96:99]\nv_cvt_pk_bf16_f32 v1,v2,v3\nv_mul_f32 v28,v248,v28\nv_mul_f32 v29,v248,v29\nv_mfma_f32_16x16x32_bf16 v[16:19],a[212:215],a[244:247],v[16:19]\nds_read_b64_tr_b16 a[208:209],v234 offset:0\nv_mul_f32 v30,v248,v30\nv_mfma_f32_16x16x32_bf16 v[44:47],a[216:219],a[244:247],v[44:47]\n"
"v_mul_f32 v31,v248,v31\nv_cvt_pk_bf16_f32 v2,v28,v29\nv_cvt_pk_bf16_f32 v3,v30,v31\nv_mfma_f32_16x16x32_bf16 v[72:75],a[220:223],a[244:247],v[72:75]\nds_write_b128 v241,v[0:3] offset:0\nv_mfma_f32_16x16x32_bf16 v[100:103],a[224:227],a[244:247],v[100:103]\nv_mul_f32 v56,v248,v56\nv_mul_f32 v57,v248,v57\n"
"v_mul_f32 v58,v248,v58\nv_mfma_f32_16x16x32_bf16 v[20:23],a[212:215],a[248:251],v[20:23]\nds_read_b64_tr_b16 a[210:211],v234 offset:1024\nv_mul_f32 v59,v248,v59\nv_mfma_f32_16x16x32_bf16 v[48:51],a[216:219],a[248:251],v[48:51]\ns_add_u32 m0,s64,0\nv_cvt_pk_bf16_f32 v56,v56,v57\nv_cvt_pk_bf16_f32 v57,v58,v59\n"
"v_mul_f32 v84,v248,v84\nv_mfma_f32_16x16x32_bf16 v[76:79],a[220:223],a[248:251],v[76:79]\ns_mov_b32 s73,401408\nv_mul_f32 v85,v248,v85\nv_mul_f32 v86,v248,v86\nv_mfma_f32_16x16x32_bf16 v[104:107],a[224:227],a[248:251],v[104:107]\nv_mul_f32 v87,v248,v87\nv_cvt_pk_bf16_f32 v58,v84,v85\n"
"v_cvt_pk_bf16_f32 v59,v86,v87\nv_mfma_f32_16x16x32_bf16 v[24:27],a[212:215],a[252:255],v[24:27]\nds_write_b128 v241,v[56:59] offset:1024\nv_mfma_f32_16x16x32_bf16 v[52:55],a[216:219],a[252:255],v[52:55]\nv_mul_f32 v4,v249,v4\nv_mul_f32 v5,v249,v5\nv_mul_f32 v6,v249,v6\nv_mfma_f32_16x16x32_bf16 v[80:83],a[220:223],a[252:255],v[80:83]\n"
"v_mul_f32 v7,v249,v7\nv_cvt_pk_bf16_f32 v4,v4,v5\nv_mfma_f32_16x16x32_bf16 v[108:111],a[224:227],a[252:255],v[108:111]\nv_cvt_pk_bf16_f32 v5,v6,v7\nv_mul_f32 v32,v249,v32\nv_mul_f32 v33,v249,v33\nv_mul_f32 v34,v249,v34\nv_mul_f32 v35,v249,v35\n"
"v_cvt_pk_bf16_f32 v6,v32,v33\nv_cvt_pk_bf16_f32 v7,v34,v35\nds_write_b128 v241,v[4:7] offset:2048\nv_mul_f32 v60,v249,v60\nv_mul_f32 v61,v249,v61\nv_mul_f32 v62,v249,v62\nv_mul_f32 v63,v249,v63\nv_cvt_pk_bf16_f32 v60,v60,v61\n"
"v_cvt_pk_bf16_f32 v61,v62,v63\nv_mul_f32 v88,v249,v88\nv_mul_f32 v89,v249,v89\nv_mul_f32 v90,v249,v90\nv_mul_f32 v91,v249,v91\nv_cvt_pk_bf16_f32 v62,v88,v89\nv_cvt_pk_bf16_f32 v63,v90,v91\nds_write_b128 v241,v[60:63] offset:3072\n"
"v_pk_mul_f32 v[8:9],v[8:9],v[250:251] op_sel:[0,0] op_sel_hi:[1,0]\nv_pk_mul_f32 v[10:11],v[10:11],v[250:251] op_sel:[0,0] op_sel_hi:[1,0]\nv_cvt_pk_bf16_f32 v8,v8,v9\nv_cvt_pk_bf16_f32 v9,v10,v11\nv_pk_mul_f32 v[36:37],v[36:37],v[250:251] op_sel:[0,0] op_sel_hi:[1,0]\nv_pk_mul_f32 v[38:39],v[38:39],v[250:251] op_sel:[0,0] op_sel_hi:[1,0]\nv_cvt_pk_bf16_f32 v10,v36,v37\nv_cvt_pk_bf16_f32 v11,v38,v39\n"
"ds_write_b128 v241,v[8:11] offset:4096\nv_pk_mul_f32 v[64:65],v[64:65],v[250:251] op_sel:[0,0] op_sel_hi:[1,0]\nv_pk_mul_f32 v[66:67],v[66:67],v[250:251] op_sel:[0,0] op_sel_hi:[1,0]\nv_cvt_pk_bf16_f32 v64,v64,v65\nv_cvt_pk_bf16_f32 v65,v66,v67\nv_pk_mul_f32 v[92:93],v[92:93],v[250:251] op_sel:[0,0] op_sel_hi:[1,0]\nv_pk_mul_f32 v[94:95],v[94:95],v[250:251] op_sel:[0,0] op_sel_hi:[1,0]\nv_cvt_pk_bf16_f32 v66,v92,v93\n"
"v_cvt_pk_bf16_f32 v67,v94,v95\nds_write_b128 v241,v[64:67] offset:5120\nv_pk_mul_f32 v[12:13],v[12:13],v[250:251] op_sel:[0,1] op_sel_hi:[1,1]\nv_pk_mul_f32 v[14:15],v[14:15],v[250:251] op_sel:[0,1] op_sel_hi:[1,1]\nv_cvt_pk_bf16_f32 v12,v12,v13\nv_cvt_pk_bf16_f32 v13,v14,v15\nv_pk_mul_f32 v[40:41],v[40:41],v[250:251] op_sel:[0,1] op_sel_hi:[1,1]\nv_pk_mul_f32 v[42:43],v[42:43],v[250:251] op_sel:[0,1] op_sel_hi:[1,1]\n"
"v_cvt_pk_bf16_f32 v14,v40,v41\nv_cvt_pk_bf16_f32 v15,v42,v43\nds_write_b128 v241,v[12:15] offset:6144\nv_pk_mul_f32 v[68:69],v[68:69],v[250:251] op_sel:[0,1] op_sel_hi:[1,1]\nv_pk_mul_f32 v[70:71],v[70:71],v[250:251] op_sel:[0,1] op_sel_hi:[1,1]\nv_cvt_pk_bf16_f32 v68,v68,v69\nv_cvt_pk_bf16_f32 v69,v70,v71\nv_pk_mul_f32 v[96:97],v[96:97],v[250:251] op_sel:[0,1] op_sel_hi:[1,1]\n"
"v_pk_mul_f32 v[98:99],v[98:99],v[250:251] op_sel:[0,1] op_sel_hi:[1,1]\nv_cvt_pk_bf16_f32 v70,v96,v97\nv_cvt_pk_bf16_f32 v71,v98,v99\nds_write_b128 v241,v[68:71] offset:7168\nv_pk_mul_f32 v[16:17],v[16:17],v[252:253] op_sel:[0,0] op_sel_hi:[1,0]\nv_pk_mul_f32 v[18:19],v[18:19],v[252:253] op_sel:[0,0] op_sel_hi:[1,0]\nv_cvt_pk_bf16_f32 v16,v16,v17\nv_cvt_pk_bf16_f32 v17,v18,v19\n"
"v_pk_mul_f32 v[44:45],v[44:45],v[252:253] op_sel:[0,0] op_sel_hi:[1,0]\nv_pk_mul_f32 v[46:47],v[46:47],v[252:253] op_sel:[0,0] op_sel_hi:[1,0]\nv_cvt_pk_bf16_f32 v18,v44,v45\nv_cvt_pk_bf16_f32 v19,v46,v47\nds_write_b128 v241,v[16:19] offset:8192\nv_pk_mul_f32 v[72:73],v[72:73],v[252:253] op_sel:[0,0] op_sel_hi:[1,0]\nv_pk_mul_f32 v[74:75],v[74:75],v[252:253] op_sel:[0,0] op_sel_hi:[1,0]\nv_cvt_pk_bf16_f32 v72,v72,v73\n"
"v_cvt_pk_bf16_f32 v73,v74,v75\nv_pk_mul_f32 v[100:101],v[100:101],v[252:253] op_sel:[0,0] op_sel_hi:[1,0]\nv_pk_mul_f32 v[102:103],v[102:103],v[252:253] op_sel:[0,0] op_sel_hi:[1,0]\nv_cvt_pk_bf16_f32 v74,v100,v101\nv_cvt_pk_bf16_f32 v75,v102,v103\nds_write_b128 v241,v[72:75] offset:9216\nv_pk_mul_f32 v[20:21],v[20:21],v[252:253] op_sel:[0,1] op_sel_hi:[1,1]\nv_pk_mul_f32 v[22:23],v[22:23],v[252:253] op_sel:[0,1] op_sel_hi:[1,1]\n"
"v_cvt_pk_bf16_f32 v20,v20,v21\nv_cvt_pk_bf16_f32 v21,v22,v23\nv_pk_mul_f32 v[48:49],v[48:49],v[252:253] op_sel:[0,1] op_sel_hi:[1,1]\nv_pk_mul_f32 v[50:51],v[50:51],v[252:253] op_sel:[0,1] op_sel_hi:[1,1]\nv_cvt_pk_bf16_f32 v22,v48,v49\nv_cvt_pk_bf16_f32 v23,v50,v51\nds_write_b128 v241,v[20:23] offset:10240\nv_pk_mul_f32 v[76:77],v[76:77],v[252:253] op_sel:[0,1] op_sel_hi:[1,1]\n"
"v_pk_mul_f32 v[78:79],v[78:79],v[252:253] op_sel:[0,1] op_sel_hi:[1,1]\nv_cvt_pk_bf16_f32 v76,v76,v77\nv_cvt_pk_bf16_f32 v77,v78,v79\nv_pk_mul_f32 v[104:105],v[104:105],v[252:253] op_sel:[0,1] op_sel_hi:[1,1]\nv_pk_mul_f32 v[106:107],v[106:107],v[252:253] op_sel:[0,1] op_sel_hi:[1,1]\nv_cvt_pk_bf16_f32 v78,v104,v105\nv_cvt_pk_bf16_f32 v79,v106,v107\nds_write_b128 v241,v[76:79] offset:11264\n"
"v_pk_mul_f32 v[24:25],v[24:25],v[254:255] op_sel:[0,0] op_sel_hi:[1,0]\nv_pk_mul_f32 v[26:27],v[26:27],v[254:255] op_sel:[0,0] op_sel_hi:[1,0]\nv_cvt_pk_bf16_f32 v24,v24,v25\nv_cvt_pk_bf16_f32 v25,v26,v27\nv_pk_mul_f32 v[52:53],v[52:53],v[254:255] op_sel:[0,0] op_sel_hi:[1,0]\nv_pk_mul_f32 v[54:55],v[54:55],v[254:255] op_sel:[0,0] op_sel_hi:[1,0]\nv_cvt_pk_bf16_f32 v26,v52,v53\nv_cvt_pk_bf16_f32 v27,v54,v55\n"
"ds_write_b128 v241,v[24:27] offset:12288\nv_pk_mul_f32 v[80:81],v[80:81],v[254:255] op_sel:[0,0] op_sel_hi:[1,0]\nv_pk_mul_f32 v[82:83],v[82:83],v[254:255] op_sel:[0,0] op_sel_hi:[1,0]\nv_cvt_pk_bf16_f32 v80,v80,v81\nv_cvt_pk_bf16_f32 v81,v82,v83\nv_pk_mul_f32 v[108:109],v[108:109],v[254:255] op_sel:[0,0] op_sel_hi:[1,0]\nv_pk_mul_f32 v[110:111],v[110:111],v[254:255] op_sel:[0,0] op_sel_hi:[1,0]\nv_cvt_pk_bf16_f32 v82,v108,v109\n"
"v_cvt_pk_bf16_f32 v83,v110,v111\nds_write_b128 v241,v[80:83] offset:13312\ns_waitcnt lgkmcnt(0)\ns_barrier\nds_read_b128 a[228:231],v239 offset:0\nds_read_b128 a[232:235],v239 offset:2048\nds_read_b128 a[236:239],v239 offset:4096\nds_read_b128 a[240:243],v239 offset:6144\n"
"ds_read_b128 a[244:247],v239 offset:8192\nds_read_b128 a[248:251],v239 offset:10240\nds_read_b128 a[252:255],v239 offset:12288\ns_waitcnt lgkmcnt(6)\nv_mfma_f32_16x16x32_bf16 a[0:3],a[196:199],a[228:231],a[0:3]\nbuffer_load_dwordx4 v226,s[56:59],s73 offen offset:0 lds\ns_mov_b32 s74,414720\nv_mfma_f32_16x16x32_bf16 a[28:31],a[200:203],a[228:231],a[28:31]\n"
"s_waitcnt vmcnt(17)\nds_read_b64_tr_b16 a[212:213],v231 offset:4096\nv_mfma_f32_16x16x32_bf16 a[56:59],a[204:207],a[228:231],a[56:59]\nds_read_b64_tr_b16 a[214:215],v231 offset:5120\nv_mfma_f32_16x16x32_bf16 a[84:87],a[208:211],a[228:231],a[84:87]\nds_read_b128 a[228:231],v239 offset:1024\ns_waitcnt lgkmcnt(8)\nv_mfma_f32_16x16x32_bf16 a[4:7],a[196:199],a[232:235],a[4:7]\n"
"buffer_load_dwordx4 v226,s[56:59],s74 offen offset:1024 lds\ns_mov_b32 s75,406528\nv_mfma_f32_16x16x32_bf16 a[32:35],a[200:203],a[232:235],a[32:35]\nds_read_b64_tr_b16 a[216:217],v232 offset:4096\nv_mfma_f32_16x16x32_bf16 a[60:63],a[204:207],a[232:235],a[60:63]\nv_mfma_f32_16x16x32_bf16 a[88:91],a[208:211],a[232:235],a[88:91]\nds_read_b128 a[232:235],v239 offset:3072\ns_waitcnt lgkmcnt(9)\n"
"v_mfma_f32_16x16x32_bf16 a[8:11],a[196:199],a[236:239],a[8:11]\nbuffer_load_dwordx4 v226,s[56:59],s75 offen offset:2048 lds\ns_mov_b32 s72,419840\nv_mfma_f32_16x16x32_bf16 a[36:39],a[200:203],a[236:239],a[36:39]\nds_read_b64_tr_b16 a[218:219],v232 offset:5120\nv_mfma_f32_16x16x32_bf16 a[64:67],a[204:207],a[236:239],a[64:67]\nv_mfma_f32_16x16x32_bf16 a[92:95],a[208:211],a[236:239],a[92:95]\nds_read_b128 a[236:239],v239 offset:5120\n"
"s_waitcnt lgkmcnt(9)\nv_mfma_f32_16x16x32_bf16 a[12:15],a[196:199],a[240:243],a[12:15]\nbuffer_load_dwordx4 v226,s[56:59],s72 offen offset:3072 lds\nv_mfma_f32_16x16x32_bf16 a[40:43],a[200:203],a[240:243],a[40:43]\nds_read_b64_tr_b16 a[220:221],v233 offset:4096\nv_mfma_f32_16x16x32_bf16 a[68:71],a[204:207],a[240:243],a[68:71]\nds_read_b64_tr_b16 a[222:223],v233 offset:5120\nv_mfma_f32_16x16x32_bf16 a[96:99],a[208:211],a[240:243],a[96:99]\n"
"ds_read_b128 a[240:243],v239 offset:7168\nv_mfma_f32_16x16x32_bf16 a[16:19],a[196:199],a[244:247],a[16:19]\nds_read_b64_tr_b16 a[224:225],v234 offset:4096\nv_mfma_f32_16x16x32_bf16 a[44:47],a[200:203],a[244:247],a[44:47]\nv_mfma_f32_16x16x32_bf16 a[72:75],a[204:207],a[244:247],a[72:75]\nv_mfma_f32_16x16x32_bf16 a[100:103],a[208:211],a[244:247],a[100:103]\nds_read_b128 a[244:247],v239 offset:9216\ns_waitcnt lgkmcnt(12)\n"
"v_mfma_f32_16x16x32_bf16 a[20:23],a[196:199],a[248:251],a[20:23]\nds_read_b64_tr_b16 a[226:227],v234 offset:5120\ns_add_u32 m0,s64,4096\nv_mfma_f32_16x16x32_bf16 a[48:51],a[200:203],a[248:251],a[48:51]\ns_mov_b32 s73,430080\nv_mfma_f32_16x16x32_bf16 a[76:79],a[204:207],a[248:251],a[76:79]\nv_mfma_f32_16x16x32_bf16 a[104:107],a[208:211],a[248:251],a[104:107]\nds_read_b128 a[248:251],v239 offset:11264\n"
"v_mfma_f32_16x16x32_bf16 a[24:27],a[196:199],a[252:255],a[24:27]\nv_mfma_f32_16x16x32_bf16 a[52:55],a[200:203],a[252:255],a[52:55]\nv_mfma_f32_16x16x32_bf16 a[80:83],a[204:207],a[252:255],a[80:83]\nv_mfma_f32_16x16x32_bf16 a[108:111],a[208:211],a[252:255],a[108:111]\nds_read_b128 a[252:255],v239 offset:13312\ns_waitcnt lgkmcnt(2)\nv_mfma_f32_16x16x32_bf16 a[0:3],a[212:215],a[228:231],a[0:3]\nbuffer_load_dwordx4 v226,s[56:59],s73 offen offset:0 lds\n"
"s_mov_b32 s74,443392\nv_mfma_f32_16x16x32_bf16 a[28:31],a[216:219],a[228:231],a[28:31]\ns_waitcnt vmcnt(17)\nds_read_b64_tr_b16 a[196:197],v231 offset:8192\nv_mfma_f32_16x16x32_bf16 a[56:59],a[220:223],a[228:231],a[56:59]\nds_read_b64_tr_b16 a[198:199],v231 offset:9216\nv_mfma_f32_16x16x32_bf16 a[84:87],a[224:227],a[228:231],a[84:87]\nds_read_b128 a[228:231],v239 offset:14336\n"
"v_mfma_f32_16x16x32_bf16 a[4:7],a[212:215],a[232:235],a[4:7]\nbuffer_load_dwordx4 v226,s[56:59],s74 offen offset:1024 lds\ns_mov_b32 s75,435200\nv_mfma_f32_16x16x32_bf16 a[32:35],a[216:219],a[232:235],a[32:35]\nds_read_b64_tr_b16 a[200:201],v232 offset:8192\nv_mfma_f32_16x16x32_bf16 a[60:63],a[220:223],a[232:235],a[60:63]\nv_mfma_f32_16x16x32_bf16 a[88:91],a[224:227],a[232:235],a[88:91]\nds_read_b128 a[232:235],v239 offset:16384\n"
"v_mfma_f32_16x16x32_bf16 a[8:11],a[212:215],a[236:239],a[8:11]\nbuffer_load_dwordx4 v226,s[56:59],s75 offen offset:2048 lds\ns_mov_b32 s72,448512\nv_mfma_f32_16x16x32_bf16 a[36:39],a[216:219],a[236:239],a[36:39]\nds_read_b64_tr_b16 a[202:203],v232 offset:9216\nv_mfma_f32_16x16x32_bf16 a[64:67],a[220:223],a[236:239],a[64:67]\nv_mfma_f32_16x16x32_bf16 a[92:95],a[224:227],a[236:239],a[92:95]\nds_read_b128 a[236:239],v239 offset:18432\n"
"v_mfma_f32_16x16x32_bf16 a[12:15],a[212:215],a[240:243],a[12:15]\nbuffer_load_dwordx4 v226,s[56:59],s72 offen offset:3072 lds\nv_mfma_f32_16x16x32_bf16 a[40:43],a[216:219],a[240:243],a[40:43]\nds_read_b64_tr_b16 a[204:205],v233 offset:8192\nv_mfma_f32_16x16x32_bf16 a[68:71],a[220:223],a[240:243],a[68:71]\nds_read_b64_tr_b16 a[206:207],v233 offset:9216\nv_mfma_f32_16x16x32_bf16 a[96:99],a[224:227],a[240:243],a[96:99]\nds_read_b128 a[240:243],v239 offset:20480\n"
"v_mfma_f32_16x16x32_bf16 a[16:19],a[212:215],a[244:247],a[16:19]\nds_read_b64_tr_b16 a[208:209],v234 offset:8192\nv_mfma_f32_16x16x32_bf16 a[44:47],a[216:219],a[244:247],a[44:47]\nv_mfma_f32_16x16x32_bf16 a[72:75],a[220:223],a[244:247],a[72:75]\nv_mfma_f32_16x16x32_bf16 a[100:103],a[224:227],a[244:247],a[100:103]\nds_read_b128 a[244:247],v239 offset:22528\ns_waitcnt lgkmcnt(12)\nv_mfma_f32_16x16x32_bf16 a[20:23],a[212:215],a[248:251],a[20:23]\n"
"ds_read_b64_tr_b16 a[210:211],v234 offset:9216\nv_add_u32 v226,s68,v226\nv_mfma_f32_16x16x32_bf16 a[48:51],a[216:219],a[248:251],a[48:51]\ns_add_u32 m0,s64,8192\ns_mov_b32 s73,229376\nv_mfma_f32_16x16x32_bf16 a[76:79],a[220:223],a[248:251],a[76:79]\nv_mfma_f32_16x16x32_bf16 a[104:107],a[224:227],a[248:251],a[104:107]\nds_read_b128 a[248:251],v239 offset:24576\n"
"v_mfma_f32_16x16x32_bf16 a[24:27],a[212:215],a[252:255],a[24:27]\nv_mfma_f32_16x16x32_bf16 a[52:55],a[216:219],a[252:255],a[52:55]\nv_mfma_f32_16x16x32_bf16 a[80:83],a[220:223],a[252:255],a[80:83]\nv_mfma_f32_16x16x32_bf16 a[108:111],a[224:227],a[252:255],a[108:111]\nds_read_b128 a[252:255],v239 offset:26624\ns_waitcnt lgkmcnt(2)\nv_mfma_f32_16x16x32_bf16 a[0:3],a[196:199],a[228:231],a[0:3]\nbuffer_load_dwordx4 v226,s[56:59],s73 offen offset:0 lds\n"
"v_lshrrev_b32 v200,2,v239\nv_mfma_f32_16x16x32_bf16 a[28:31],a[200:203],a[228:231],a[28:31]\ns_waitcnt vmcnt(17)\nds_read_b64_tr_b16 a[212:213],v231 offset:12288\nv_mfma_f32_16x16x32_bf16 a[56:59],a[204:207],a[228:231],a[56:59]\nds_read_b64_tr_b16 a[214:215],v231 offset:13312\nv_lshlrev_b32 v201,3,v239\nv_mfma_f32_16x16x32_bf16 a[84:87],a[208:211],a[228:231],a[84:87]\n"
"ds_read_b128 a[228:231],v239 offset:15360\nv_lshl_add_u32 v200,%12,7,v200\nv_mfma_f32_16x16x32_bf16 a[4:7],a[196:199],a[232:235],a[4:7]\ns_mov_b32 s74,242688\nbuffer_load_dwordx4 v226,s[56:59],s74 offen offset:1024 lds\nv_mfma_f32_16x16x32_bf16 a[32:35],a[200:203],a[232:235],a[32:35]\nds_read_b64_tr_b16 a[216:217],v232 offset:12288\nv_lshl_add_u32 v201,%12,2,v201\n"
"v_mfma_f32_16x16x32_bf16 a[60:63],a[204:207],a[232:235],a[60:63]\ns_mov_b32 s75,234496\nv_lshrrev_b32 v202,7,v239\nv_add_u32 v202,s70,v202\nv_mfma_f32_16x16x32_bf16 a[88:91],a[208:211],a[232:235],a[88:91]\nds_read_b128 a[232:235],v239 offset:17408\nv_lshl_add_u32 v200,v202,16,v200\nv_mfma_f32_16x16x32_bf16 a[8:11],a[196:199],a[236:239],a[8:11]\n"
"buffer_load_dwordx4 v226,s[56:59],s75 offen offset:2048 lds\nv_lshl_add_u32 v201,v202,16,v201\nv_mfma_f32_16x16x32_bf16 a[36:39],a[200:203],a[236:239],a[36:39]\nds_read_b64_tr_b16 a[218:219],v232 offset:13312\ns_mov_b32 s72,247808\nv_mfma_f32_16x16x32_bf16 a[64:67],a[204:207],a[236:239],a[64:67]\nbuffer_load_dword v203,v200,s[96:99],0 offen sc1\nv_mfma_f32_16x16x32_bf16 a[92:95],a[208:211],a[236:239],a[92:95]\n"
"ds_read_b128 a[236:239],v239 offset:19456\nv_mfma_f32_16x16x32_bf16 a[12:15],a[196:199],a[240:243],a[12:15]\nbuffer_load_dwordx4 v226,s[56:59],s72 offen offset:3072 lds\nv_mfma_f32_16x16x32_bf16 a[40:43],a[200:203],a[240:243],a[40:43]\nds_read_b64_tr_b16 a[220:221],v233 offset:12288\nv_mfma_f32_16x16x32_bf16 a[68:71],a[204:207],a[240:243],a[68:71]\nds_read_b64_tr_b16 a[222:223],v233 offset:13312\nv_mfma_f32_16x16x32_bf16 a[96:99],a[208:211],a[240:243],a[96:99]\n"
"ds_read_b128 a[240:243],v239 offset:21504\nv_mfma_f32_16x16x32_bf16 a[16:19],a[196:199],a[244:247],a[16:19]\nds_read_b64_tr_b16 a[224:225],v234 offset:12288\nv_mfma_f32_16x16x32_bf16 a[44:47],a[200:203],a[244:247],a[44:47]\nbuffer_load_dword v204,v201,s[96:99],0 offen sc1\nv_mfma_f32_16x16x32_bf16 a[72:75],a[204:207],a[244:247],a[72:75]\nv_mfma_f32_16x16x32_bf16 a[100:103],a[208:211],a[244:247],a[100:103]\nds_read_b128 a[244:247],v239 offset:23552\n"
"s_waitcnt lgkmcnt(12)\nv_mfma_f32_16x16x32_bf16 a[20:23],a[196:199],a[248:251],a[20:23]\nds_read_b64_tr_b16 a[226:227],v234 offset:13312\ns_add_u32 m0,s64,12288\nv_mfma_f32_16x16x32_bf16 a[48:51],a[200:203],a[248:251],a[48:51]\ns_mov_b32 s73,258048\nv_mfma_f32_16x16x32_bf16 a[76:79],a[204:207],a[248:251],a[76:79]\nv_mfma_f32_16x16x32_bf16 a[104:107],a[208:211],a[248:251],a[104:107]\n"
"ds_read_b128 a[248:251],v239 offset:25600\nv_mfma_f32_16x16x32_bf16 a[24:27],a[196:199],a[252:255],a[24:27]\nv_mfma_f32_16x16x32_bf16 a[52:55],a[200:203],a[252:255],a[52:55]\nv_mfma_f32_16x16x32_bf16 a[80:83],a[204:207],a[252:255],a[80:83]\nv_mfma_f32_16x16x32_bf16 a[108:111],a[208:211],a[252:255],a[108:111]\nds_read_b128 a[252:255],v239 offset:27648\ns_waitcnt lgkmcnt(2)\nv_mfma_f32_16x16x32_bf16 a[0:3],a[212:215],a[228:231],a[0:3]\n"
"buffer_load_dwordx4 v226,s[56:59],s73 offen offset:0 lds\ns_mov_b32 s74,271360\nv_mfma_f32_16x16x32_bf16 a[28:31],a[216:219],a[228:231],a[28:31]\ns_waitcnt vmcnt(19)\nds_read_b64_tr_b16 a[196:197],v231 offset:16384\nv_mfma_f32_16x16x32_bf16 a[56:59],a[220:223],a[228:231],a[56:59]\nds_read_b64_tr_b16 a[198:199],v231 offset:17408\nv_mfma_f32_16x16x32_bf16 a[84:87],a[224:227],a[228:231],a[84:87]\n"
"ds_read_b128 a[228:231],v239 offset:28672\nv_mfma_f32_16x16x32_bf16 a[4:7],a[212:215],a[232:235],a[4:7]\nbuffer_load_dwordx4 v226,s[56:59],s74 offen offset:1024 lds\ns_mov_b32 s75,263168\nv_mfma_f32_16x16x32_bf16 a[32:35],a[216:219],a[232:235],a[32:35]\nds_read_b64_tr_b16 a[200:201],v232 offset:16384\nv_mfma_f32_16x16x32_bf16 a[60:63],a[220:223],a[232:235],a[60:63]\nv_mfma_f32_16x16x32_bf16 a[88:91],a[224:227],a[232:235],a[88:91]\n"
"ds_read_b128 a[232:235],v239 offset:30720\nv_mfma_f32_16x16x32_bf16 a[8:11],a[212:215],a[236:239],a[8:11]\nbuffer_load_dwordx4 v226,s[56:59],s75 offen offset:2048 lds\ns_mov_b32 s72,276480\nv_mfma_f32_16x16x32_bf16 a[36:39],a[216:219],a[236:239],a[36:39]\nds_read_b64_tr_b16 a[202:203],v232 offset:17408\nv_mfma_f32_16x16x32_bf16 a[64:67],a[220:223],a[236:239],a[64:67]\nv_mfma_f32_16x16x32_bf16 a[92:95],a[224:227],a[236:239],a[92:95]\n"
"ds_read_b128 a[236:239],v239 offset:32768\nv_mfma_f32_16x16x32_bf16 a[12:15],a[212:215],a[240:243],a[12:15]\nbuffer_load_dwordx4 v226,s[56:59],s72 offen offset:3072 lds\nv_mfma_f32_16x16x32_bf16 a[40:43],a[216:219],a[240:243],a[40:43]\nds_read_b64_tr_b16 a[204:205],v233 offset:16384\nv_mfma_f32_16x16x32_bf16 a[68:71],a[220:223],a[240:243],a[68:71]\nds_read_b64_tr_b16 a[206:207],v233 offset:17408\nv_mfma_f32_16x16x32_bf16 a[96:99],a[224:227],a[240:243],a[96:99]\n"
"ds_read_b128 a[240:243],v239 offset:34816\nv_mfma_f32_16x16x32_bf16 a[16:19],a[212:215],a[244:247],a[16:19]\nds_read_b64_tr_b16 a[208:209],v234 offset:16384\nv_mfma_f32_16x16x32_bf16 a[44:47],a[216:219],a[244:247],a[44:47]\nv_mfma_f32_16x16x32_bf16 a[72:75],a[220:223],a[244:247],a[72:75]\nv_mfma_f32_16x16x32_bf16 a[100:103],a[224:227],a[244:247],a[100:103]\nds_read_b128 a[244:247],v239 offset:36864\ns_waitcnt lgkmcnt(12)\n"
"v_mfma_f32_16x16x32_bf16 a[20:23],a[212:215],a[248:251],a[20:23]\nds_read_b64_tr_b16 a[210:211],v234 offset:17408\ns_add_u32 m0,s64,16384\nv_mfma_f32_16x16x32_bf16 a[48:51],a[216:219],a[248:251],a[48:51]\ns_mov_b32 s73,286720\nv_mfma_f32_16x16x32_bf16 a[76:79],a[220:223],a[248:251],a[76:79]\nv_mfma_f32_16x16x32_bf16 a[104:107],a[224:227],a[248:251],a[104:107]\nds_read_b128 a[248:251],v239 offset:38912\n"
"v_mfma_f32_16x16x32_bf16 a[24:27],a[212:215],a[252:255],a[24:27]\nv_mfma_f32_16x16x32_bf16 a[52:55],a[216:219],a[252:255],a[52:55]\nv_mfma_f32_16x16x32_bf16 a[80:83],a[220:223],a[252:255],a[80:83]\nv_mfma_f32_16x16x32_bf16 a[108:111],a[224:227],a[252:255],a[108:111]\nds_read_b128 a[252:255],v239 offset:40960\ns_waitcnt lgkmcnt(2)\nv_mfma_f32_16x16x32_bf16 a[0:3],a[196:199],a[228:231],a[0:3]\nbuffer_load_dwordx4 v226,s[56:59],s73 offen offset:0 lds\n"
"s_mov_b32 s74,300032\nv_mfma_f32_16x16x32_bf16 a[28:31],a[200:203],a[228:231],a[28:31]\ns_waitcnt vmcnt(19)\nds_read_b64_tr_b16 a[212:213],v231 offset:20480\nv_mfma_f32_16x16x32_bf16 a[56:59],a[204:207],a[228:231],a[56:59]\nds_read_b64_tr_b16 a[214:215],v231 offset:21504\nv_mfma_f32_16x16x32_bf16 a[84:87],a[208:211],a[228:231],a[84:87]\nds_read_b128 a[228:231],v239 offset:29696\n"
"v_mfma_f32_16x16x32_bf16 a[4:7],a[196:199],a[232:235],a[4:7]\nbuffer_load_dwordx4 v226,s[56:59],s74 offen offset:1024 lds\ns_mov_b32 s75,291840\nv_mfma_f32_16x16x32_bf16 a[32:35],a[200:203],a[232:235],a[32:35]\nds_read_b64_tr_b16 a[216:217],v232 offset:20480\nv_mfma_f32_16x16x32_bf16 a[60:63],a[204:207],a[232:235],a[60:63]\nv_mfma_f32_16x16x32_bf16 a[88:91],a[208:211],a[232:235],a[88:91]\nds_read_b128 a[232:235],v239 offset:31744\n"
"v_mfma_f32_16x16x32_bf16 a[8:11],a[196:199],a[236:239],a[8:11]\nbuffer_load_dwordx4 v226,s[56:59],s75 offen offset:2048 lds\ns_mov_b32 s72,305152\nv_mfma_f32_16x16x32_bf16 a[36:39],a[200:203],a[236:239],a[36:39]\nds_read_b64_tr_b16 a[218:219],v232 offset:21504\nv_mfma_f32_16x16x32_bf16 a[64:67],a[204:207],a[236:239],a[64:67]\nv_mfma_f32_16x16x32_bf16 a[92:95],a[208:211],a[236:239],a[92:95]\nds_read_b128 a[236:239],v239 offset:33792\n"
"v_mfma_f32_16x16x32_bf16 a[12:15],a[196:199],a[240:243],a[12:15]\nbuffer_load_dwordx4 v226,s[56:59],s72 offen offset:3072 lds\nv_mfma_f32_16x16x32_bf16 a[40:43],a[200:203],a[240:243],a[40:43]\nds_read_b64_tr_b16 a[220:221],v233 offset:20480\nv_mfma_f32_16x16x32_bf16 a[68:71],a[204:207],a[240:243],a[68:71]\nds_read_b64_tr_b16 a[222:223],v233 offset:21504\nv_mfma_f32_16x16x32_bf16 a[96:99],a[208:211],a[240:243],a[96:99]\nds_read_b128 a[240:243],v239 offset:35840\n"
"v_mfma_f32_16x16x32_bf16 a[16:19],a[196:199],a[244:247],a[16:19]\nds_read_b64_tr_b16 a[224:225],v234 offset:20480\nv_mfma_f32_16x16x32_bf16 a[44:47],a[200:203],a[244:247],a[44:47]\nv_mfma_f32_16x16x32_bf16 a[72:75],a[204:207],a[244:247],a[72:75]\nv_mfma_f32_16x16x32_bf16 a[100:103],a[208:211],a[244:247],a[100:103]\nds_read_b128 a[244:247],v239 offset:37888\ns_waitcnt lgkmcnt(12)\nv_mfma_f32_16x16x32_bf16 a[20:23],a[196:199],a[248:251],a[20:23]\n"
"ds_read_b64_tr_b16 a[226:227],v234 offset:21504\ns_add_u32 m0,s64,20480\nv_mfma_f32_16x16x32_bf16 a[48:51],a[200:203],a[248:251],a[48:51]\ns_mov_b32 s73,315392\nv_mfma_f32_16x16x32_bf16 a[76:79],a[204:207],a[248:251],a[76:79]\nv_mfma_f32_16x16x32_bf16 a[104:107],a[208:211],a[248:251],a[104:107]\nds_read_b128 a[248:251],v239 offset:39936\nv_mfma_f32_16x16x32_bf16 a[24:27],a[196:199],a[252:255],a[24:27]\n"
"v_mfma_f32_16x16x32_bf16 a[52:55],a[200:203],a[252:255],a[52:55]\nv_mfma_f32_16x16x32_bf16 a[80:83],a[204:207],a[252:255],a[80:83]\nv_mfma_f32_16x16x32_bf16 a[108:111],a[208:211],a[252:255],a[108:111]\nds_read_b128 a[252:255],v239 offset:41984\ns_waitcnt lgkmcnt(2)\nv_mfma_f32_16x16x32_bf16 a[0:3],a[212:215],a[228:231],a[0:3]\nbuffer_load_dwordx4 v226,s[56:59],s73 offen offset:0 lds\ns_mov_b32 s74,328704\n"
"v_mfma_f32_16x16x32_bf16 a[28:31],a[216:219],a[228:231],a[28:31]\ns_waitcnt vmcnt(19)\nds_read_b64_tr_b16 a[196:197],v231 offset:0\nv_mfma_f32_16x16x32_bf16 a[56:59],a[220:223],a[228:231],a[56:59]\nds_read_b64_tr_b16 a[198:199],v231 offset:1024\nv_mfma_f32_16x16x32_bf16 a[84:87],a[224:227],a[228:231],a[84:87]\nds_read_b128 a[228:231],v239 offset:43008\nv_mfma_f32_16x16x32_bf16 a[4:7],a[212:215],a[232:235],a[4:7]\n"
"buffer_load_dwordx4 v226,s[56:59],s74 offen offset:1024 lds\ns_mov_b32 s75,320512\nv_mfma_f32_16x16x32_bf16 a[32:35],a[216:219],a[232:235],a[32:35]\nds_read_b64_tr_b16 a[200:201],v232 offset:0\nv_mfma_f32_16x16x32_bf16 a[60:63],a[220:223],a[232:235],a[60:63]\nv_mfma_f32_16x16x32_bf16 a[88:91],a[224:227],a[232:235],a[88:91]\nds_read_b128 a[232:235],v239 offset:45056\nv_mfma_f32_16x16x32_bf16 a[8:11],a[212:215],a[236:239],a[8:11]\n"
"buffer_load_dwordx4 v226,s[56:59],s75 offen offset:2048 lds\ns_mov_b32 s72,333824\nv_mfma_f32_16x16x32_bf16 a[36:39],a[216:219],a[236:239],a[36:39]\nds_read_b64_tr_b16 a[202:203],v232 offset:1024\nv_mfma_f32_16x16x32_bf16 a[64:67],a[220:223],a[236:239],a[64:67]\nv_mfma_f32_16x16x32_bf16 a[92:95],a[224:227],a[236:239],a[92:95]\nds_read_b128 a[236:239],v239 offset:47104\nv_mfma_f32_16x16x32_bf16 a[12:15],a[212:215],a[240:243],a[12:15]\n"
"buffer_load_dwordx4 v226,s[56:59],s72 offen offset:3072 lds\nv_mfma_f32_16x16x32_bf16 a[40:43],a[216:219],a[240:243],a[40:43]\nds_read_b64_tr_b16 a[204:205],v233 offset:0\nv_mfma_f32_16x16x32_bf16 a[68:71],a[220:223],a[240:243],a[68:71]\nds_read_b64_tr_b16 a[206:207],v233 offset:1024\nv_mfma_f32_16x16x32_bf16 a[96:99],a[224:227],a[240:243],a[96:99]\nds_read_b128 a[240:243],v239 offset:49152\nv_mfma_f32_16x16x32_bf16 a[16:19],a[212:215],a[244:247],a[16:19]\n"
"ds_read_b64_tr_b16 a[208:209],v234 offset:0\nv_mfma_f32_16x16x32_bf16 a[44:47],a[216:219],a[244:247],a[44:47]\nv_mfma_f32_16x16x32_bf16 a[72:75],a[220:223],a[244:247],a[72:75]\nv_mfma_f32_16x16x32_bf16 a[100:103],a[224:227],a[244:247],a[100:103]\nds_read_b128 a[244:247],v239 offset:51200\ns_waitcnt lgkmcnt(12)\nv_mfma_f32_16x16x32_bf16 a[20:23],a[212:215],a[248:251],a[20:23]\nds_read_b64_tr_b16 a[210:211],v234 offset:1024\n"
"s_add_u32 m0,s64,0\nv_mfma_f32_16x16x32_bf16 a[48:51],a[216:219],a[248:251],a[48:51]\ns_mov_b32 s73,344064\nv_mfma_f32_16x16x32_bf16 a[76:79],a[220:223],a[248:251],a[76:79]\nv_mfma_f32_16x16x32_bf16 a[104:107],a[224:227],a[248:251],a[104:107]\nds_read_b128 a[248:251],v239 offset:53248\nv_mfma_f32_16x16x32_bf16 a[24:27],a[212:215],a[252:255],a[24:27]\nv_mfma_f32_16x16x32_bf16 a[52:55],a[216:219],a[252:255],a[52:55]\n"
"v_mfma_f32_16x16x32_bf16 a[80:83],a[220:223],a[252:255],a[80:83]\nv_mfma_f32_16x16x32_bf16 a[108:111],a[224:227],a[252:255],a[108:111]\nds_read_b128 a[252:255],v239 offset:55296\ns_waitcnt lgkmcnt(2)\nv_mfma_f32_16x16x32_bf16 a[0:3],a[196:199],a[228:231],a[0:3]\nbuffer_load_dwordx4 v226,s[56:59],s73 offen offset:0 lds\ns_mov_b32 s74,357376\nv_mfma_f32_16x16x32_bf16 a[28:31],a[200:203],a[228:231],a[28:31]\n"
"s_waitcnt vmcnt(19)\nds_read_b64_tr_b16 a[212:213],v231 offset:4096\nv_mfma_f32_16x16x32_bf16 a[56:59],a[204:207],a[228:231],a[56:59]\nds_read_b64_tr_b16 a[214:215],v231 offset:5120\nv_mfma_f32_16x16x32_bf16 a[84:87],a[208:211],a[228:231],a[84:87]\nds_read_b128 a[228:231],v239 offset:44032\nv_mfma_f32_16x16x32_bf16 a[4:7],a[196:199],a[232:235],a[4:7]\nbuffer_load_dwordx4 v226,s[56:59],s74 offen offset:1024 lds\n"
"s_mov_b32 s75,349184\nv_mfma_f32_16x16x32_bf16 a[32:35],a[200:203],a[232:235],a[32:35]\nds_read_b64_tr_b16 a[216:217],v232 offset:4096\nv_mfma_f32_16x16x32_bf16 a[60:63],a[204:207],a[232:235],a[60:63]\nv_mfma_f32_16x16x32_bf16 a[88:91],a[208:211],a[232:235],a[88:91]\nds_read_b128 a[232:235],v239 offset:46080\nv_mfma_f32_16x16x32_bf16 a[8:11],a[196:199],a[236:239],a[8:11]\nbuffer_load_dwordx4 v226,s[56:59],s75 offen offset:2048 lds\n"
"s_mov_b32 s72,362496\nv_mfma_f32_16x16x32_bf16 a[36:39],a[200:203],a[236:239],a[36:39]\nds_read_b64_tr_b16 a[218:219],v232 offset:5120\nv_mfma_f32_16x16x32_bf16 a[64:67],a[204:207],a[236:239],a[64:67]\nv_mfma_f32_16x16x32_bf16 a[92:95],a[208:211],a[236:239],a[92:95]\nds_read_b128 a[236:239],v239 offset:48128\nv_mfma_f32_16x16x32_bf16 a[12:15],a[196:199],a[240:243],a[12:15]\nbuffer_load_dwordx4 v226,s[56:59],s72 offen offset:3072 lds\n"
"v_mfma_f32_16x16x32_bf16 a[40:43],a[200:203],a[240:243],a[40:43]\nds_read_b64_tr_b16 a[220:221],v233 offset:4096\nv_mfma_f32_16x16x32_bf16 a[68:71],a[204:207],a[240:243],a[68:71]\nds_read_b64_tr_b16 a[222:223],v233 offset:5120\nv_mfma_f32_16x16x32_bf16 a[96:99],a[208:211],a[240:243],a[96:99]\nds_read_b128 a[240:243],v239 offset:50176\nv_mfma_f32_16x16x32_bf16 a[16:19],a[196:199],a[244:247],a[16:19]\nds_read_b64_tr_b16 a[224:225],v234 offset:4096\n"
"v_mfma_f32_16x16x32_bf16 a[44:47],a[200:203],a[244:247],a[44:47]\nv_mfma_f32_16x16x32_bf16 a[72:75],a[204:207],a[244:247],a[72:75]\nv_mfma_f32_16x16x32_bf16 a[100:103],a[208:211],a[244:247],a[100:103]\nds_read_b128 a[244:247],v239 offset:52224\ns_waitcnt lgkmcnt(12)\nv_mfma_f32_16x16x32_bf16 a[20:23],a[196:199],a[248:251],a[20:23]\nds_read_b64_tr_b16 a[226:227],v234 offset:5120\ns_add_u32 m0,s64,4096\n"
"v_mfma_f32_16x16x32_bf16 a[48:51],a[200:203],a[248:251],a[48:51]\ns_mov_b32 s73,372736\nv_mfma_f32_16x16x32_bf16 a[76:79],a[204:207],a[248:251],a[76:79]\nv_mfma_f32_16x16x32_bf16 a[104:107],a[208:211],a[248:251],a[104:107]\nds_read_b128 a[248:251],v239 offset:54272\nv_mfma_f32_16x16x32_bf16 a[24:27],a[196:199],a[252:255],a[24:27]\nv_mfma_f32_16x16x32_bf16 a[52:55],a[200:203],a[252:255],a[52:55]\nv_mfma_f32_16x16x32_bf16 a[80:83],a[204:207],a[252:255],a[80:83]\n"
"v_mfma_f32_16x16x32_bf16 a[108:111],a[208:211],a[252:255],a[108:111]\nds_read_b128 a[252:255],v239 offset:56320\ns_waitcnt lgkmcnt(2)\nv_mfma_f32_16x16x32_bf16 a[0:3],a[212:215],a[228:231],a[0:3]\nbuffer_load_dwordx4 v226,s[56:59],s73 offen offset:0 lds\ns_mov_b32 s74,386048\nv_mfma_f32_16x16x32_bf16 a[28:31],a[216:219],a[228:231],a[28:31]\ns_waitcnt vmcnt(18)\n"
"ds_read_b64_tr_b16 a[196:197],v231 offset:8192\nv_mfma_f32_16x16x32_bf16 a[56:59],a[220:223],a[228:231],a[56:59]\nv_mfma_f32_16x16x32_bf16 a[84:87],a[224:227],a[228:231],a[84:87]\nds_read_b128 a[228:231],v239 offset:0\nv_mfma_f32_16x16x32_bf16 a[4:7],a[212:215],a[232:235],a[4:7]\nbuffer_load_dwordx4 v226,s[56:59],s74 offen offset:1024 lds\ns_mov_b32 s75,377856\nv_mfma_f32_16x16x32_bf16 a[32:35],a[216:219],a[232:235],a[32:35]\n"
"ds_read_b64_tr_b16 a[198:199],v231 offset:9216\nv_mfma_f32_16x16x32_bf16 a[60:63],a[220:223],a[232:235],a[60:63]\nv_mfma_f32_16x16x32_bf16 a[88:91],a[224:227],a[232:235],a[88:91]\nds_read_b128 a[232:235],v239 offset:2048\nv_mfma_f32_16x16x32_bf16 a[8:11],a[212:215],a[236:239],a[8:11]\nbuffer_load_dwordx4 v226,s[56:59],s75 offen offset:2048 lds\ns_mov_b32 s72,391168\nv_mfma_f32_16x16x32_bf16 a[36:39],a[216:219],a[236:239],a[36:39]\n"
"ds_read_b64_tr_b16 a[200:201],v232 offset:8192\nv_mfma_f32_16x16x32_bf16 a[64:67],a[220:223],a[236:239],a[64:67]\nv_mfma_f32_16x16x32_bf16 a[92:95],a[224:227],a[236:239],a[92:95]\nds_read_b128 a[236:239],v239 offset:4096\nv_mfma_f32_16x16x32_bf16 a[12:15],a[212:215],a[240:243],a[12:15]\nbuffer_load_dwordx4 v226,s[56:59],s72 offen offset:3072 lds\nv_mfma_f32_16x16x32_bf16 a[40:43],a[216:219],a[240:243],a[40:43]\nds_read_b64_tr_b16 a[202:203],v232 offset:9216\n"
"v_mfma_f32_16x16x32_bf16 a[68:71],a[220:223],a[240:243],a[68:71]\nv_mfma_f32_16x16x32_bf16 a[96:99],a[224:227],a[240:243],a[96:99]\nds_read_b128 a[240:243],v239 offset:6144\nv_mfma_f32_16x16x32_bf16 a[16:19],a[212:215],a[244:247],a[16:19]\nds_read_b64_tr_b16 a[204:205],v233 offset:8192\nv_mfma_f32_16x16x32_bf16 a[44:47],a[216:219],a[244:247],a[44:47]\nv_mfma_f32_16x16x32_bf16 a[72:75],a[220:223],a[244:247],a[72:75]\nv_mfma_f32_16x16x32_bf16 a[100:103],a[224:227],a[244:247],a[100:103]\n"
"ds_read_b128 a[244:247],v239 offset:8192\ns_waitcnt lgkmcnt(10)\nv_mfma_f32_16x16x32_bf16 a[20:23],a[212:215],a[248:251],a[20:23]\nds_read_b64_tr_b16 a[206:207],v233 offset:9216\ns_add_u32 m0,s64,8192\nv_mfma_f32_16x16x32_bf16 a[48:51],a[216:219],a[248:251],a[48:51]\ns_mov_b32 s73,401408\nv_mfma_f32_16x16x32_bf16 a[76:79],a[220:223],a[248:251],a[76:79]\n"
"v_mfma_f32_16x16x32_bf16 a[104:107],a[224:227],a[248:251],a[104:107]\nds_read_b128 a[248:251],v239 offset:10240\nv_mfma_f32_16x16x32_bf16 a[24:27],a[212:215],a[252:255],a[24:27]\nv_mfma_f32_16x16x32_bf16 a[52:55],a[216:219],a[252:255],a[52:55]\nv_mfma_f32_16x16x32_bf16 a[80:83],a[220:223],a[252:255],a[80:83]\nv_mfma_f32_16x16x32_bf16 a[108:111],a[224:227],a[252:255],a[108:111]\nds_read_b128 a[252:255],v239 offset:12288\ns_waitcnt lgkmcnt(2)\n"
"v_mfma_f32_16x16x32_bf16 a[112:115],a[196:199],a[228:231],a[112:115]\nbuffer_load_dwordx4 v226,s[56:59],s73 offen offset:0 lds\nv_lshrrev_b32 v255,1,v239\nv_mfma_f32_16x16x32_bf16 a[140:143],a[200:203],a[228:231],a[140:143]\ns_waitcnt vmcnt(17)\nds_read_b64_tr_b16 a[212:213],v231 offset:12288\nv_mfma_f32_16x16x32_bf16 a[168:171],a[204:207],a[228:231],a[168:171]\nds_read_b128 a[228:231],v239 offset:1024\n"
"s_mov_b32 s74,414720\nv_mfma_f32_16x16x32_bf16 a[116:119],a[196:199],a[232:235],a[116:119]\nbuffer_load_dwordx4 v226,s[56:59],s74 offen offset:1024 lds\ns_mov_b32 s75,406528\nv_mfma_f32_16x16x32_bf16 a[144:147],a[200:203],a[232:235],a[144:147]\nds_read_b64_tr_b16 a[214:215],v231 offset:13312\nv_mfma_f32_16x16x32_bf16 a[172:175],a[204:207],a[232:235],a[172:175]\nds_read_b128 a[232:235],v239 offset:3072\n"
"v_mfma_f32_16x16x32_bf16 a[120:123],a[196:199],a[236:239],a[120:123]\nbuffer_load_dwordx4 v226,s[56:59],s75 offen offset:2048 lds\ns_mov_b32 s72,419840\nv_mfma_f32_16x16x32_bf16 a[148:151],a[200:203],a[236:239],a[148:151]\nds_read_b64_tr_b16 a[216:217],v232 offset:12288\nv_mfma_f32_16x16x32_bf16 a[176:179],a[204:207],a[236:239],a[176:179]\nds_read_b128 a[236:239],v239 offset:5120\nv_mfma_f32_16x16x32_bf16 a[124:127],a[196:199],a[240:243],a[124:127]\n"
"buffer_load_dwordx4 v226,s[56:59],s72 offen offset:3072 lds\nv_mfma_f32_16x16x32_bf16 a[152:155],a[200:203],a[240:243],a[152:155]\nds_read_b64_tr_b16 a[218:219],v232 offset:13312\nv_mfma_f32_16x16x32_bf16 a[180:183],a[204:207],a[240:243],a[180:183]\nds_read_b128 a[240:243],v239 offset:7168\nv_mfma_f32_16x16x32_bf16 a[128:131],a[196:199],a[244:247],a[128:131]\nds_read_b64_tr_b16 a[220:221],v233 offset:12288\nv_mfma_f32_16x16x32_bf16 a[156:159],a[200:203],a[244:247],a[156:159]\n"
"v_mfma_f32_16x16x32_bf16 a[184:187],a[204:207],a[244:247],a[184:187]\nds_read_b128 a[244:247],v239 offset:9216\ns_waitcnt lgkmcnt(10)\nv_mfma_f32_16x16x32_bf16 a[132:135],a[196:199],a[248:251],a[132:135]\nds_read_b64_tr_b16 a[222:223],v233 offset:13312\ns_add_u32 m0,s64,12288\nv_mfma_f32_16x16x32_bf16 a[160:163],a[200:203],a[248:251],a[160:163]\ns_mov_b32 s73,430080\n"
"v_mfma_f32_16x16x32_bf16 a[188:191],a[204:207],a[248:251],a[188:191]\nds_read_b128 a[248:251],v239 offset:11264\nv_mfma_f32_16x16x32_bf16 a[136:139],a[196:199],a[252:255],a[136:139]\nv_mfma_f32_16x16x32_bf16 a[164:167],a[200:203],a[252:255],a[164:167]\nv_mfma_f32_16x16x32_bf16 a[192:195],a[204:207],a[252:255],a[192:195]\nds_read_b128 a[252:255],v239 offset:13312\ns_waitcnt lgkmcnt(2)\nv_mfma_f32_16x16x32_bf16 a[112:115],a[212:215],a[228:231],a[112:115]\n"
"buffer_load_dwordx4 v226,s[56:59],s73 offen offset:0 lds\nv_accvgpr_read_b32 v0,a0\nv_mfma_f32_16x16x32_bf16 a[140:143],a[216:219],a[228:231],a[140:143]\ns_waitcnt vmcnt(17)\nds_read_b64_tr_b16 a[196:197],v231 offset:16384\nv_mfma_f32_16x16x32_bf16 a[168:171],a[220:223],a[228:231],a[168:171]\nds_read_b128 a[228:231],v239 offset:14336\nv_accvgpr_read_b32 v1,a1\n"
"v_mfma_f32_16x16x32_bf16 a[116:119],a[212:215],a[232:235],a[116:119]\ns_mov_b32 s74,443392\nbuffer_load_dwordx4 v226,s[56:59],s74 offen offset:1024 lds\nv_mfma_f32_16x16x32_bf16 a[144:147],a[216:219],a[232:235],a[144:147]\nds_read_b64_tr_b16 a[198:199],v231 offset:17408\nv_accvgpr_read_b32 v2,a2\nv_mfma_f32_16x16x32_bf16 a[172:175],a[220:223],a[232:235],a[172:175]\nds_read_b128 a[232:235],v239 offset:16384\n"
"v_accvgpr_read_b32 v3,a3\nv_mfma_f32_16x16x32_bf16 a[120:123],a[212:215],a[236:239],a[120:123]\ns_mov_b32 s75,435200\nbuffer_load_dwordx4 v226,s[56:59],s75 offen offset:2048 lds\nv_mfma_f32_16x16x32_bf16 a[148:151],a[216:219],a[236:239],a[148:151]\nds_read_b64_tr_b16 a[200:201],v232 offset:16384\nv_accvgpr_read_b32 v4,a28\nv_mfma_f32_16x16x32_bf16 a[176:179],a[220:223],a[236:239],a[176:179]\n"
"ds_read_b128 a[236:239],v239 offset:18432\nv_accvgpr_read_b32 v5,a29\nv_mfma_f32_16x16x32_bf16 a[124:127],a[212:215],a[240:243],a[124:127]\ns_mov_b32 s72,448512\nbuffer_load_dwordx4 v226,s[56:59],s72 offen offset:3072 lds\nv_mfma_f32_16x16x32_bf16 a[152:155],a[216:219],a[240:243],a[152:155]\nds_read_b64_tr_b16 a[202:203],v232 offset:17408\nv_accvgpr_read_b32 v6,a30\n"
"v_mfma_f32_16x16x32_bf16 a[180:183],a[220:223],a[240:243],a[180:183]\nds_read_b128 a[240:243],v239 offset:20480\nv_accvgpr_read_b32 v7,a31\nv_mfma_f32_16x16x32_bf16 a[128:131],a[212:215],a[244:247],a[128:131]\nds_read_b64_tr_b16 a[204:205],v233 offset:16384\nv_cvt_pk_bf16_f32 v0,v0,v1\nv_mfma_f32_16x16x32_bf16 a[156:159],a[216:219],a[244:247],a[156:159]\nv_cvt_pk_bf16_f32 v1,v2,v3\n"
"v_cvt_pk_bf16_f32 v2,v4,v5\nv_mfma_f32_16x16x32_bf16 a[184:187],a[220:223],a[244:247],a[184:187]\nds_read_b128 a[244:247],v239 offset:22528\ns_waitcnt lgkmcnt(10)\nv_mfma_f32_16x16x32_bf16 a[132:135],a[212:215],a[248:251],a[132:135]\nds_read_b64_tr_b16 a[206:207],v233 offset:17408\nv_cvt_pk_bf16_f32 v3,v6,v7\nv_mfma_f32_16x16x32_bf16 a[160:163],a[216:219],a[248:251],a[160:163]\n"
"s_add_u32 s71,s71,0\nbuffer_store_dwordx4 v[0:3],v239,s[60:63],s71 offen sc1\nv_mfma_f32_16x16x32_bf16 a[188:191],a[220:223],a[248:251],a[188:191]\nds_read_b128 a[248:251],v239 offset:24576\nv_accvgpr_read_b32 v8,a56\nv_mfma_f32_16x16x32_bf16 a[136:139],a[212:215],a[252:255],a[136:139]\nv_accvgpr_read_b32 v9,a57\nv_accvgpr_read_b32 v10,a58\n"
"v_mfma_f32_16x16x32_bf16 a[164:167],a[216:219],a[252:255],a[164:167]\nv_accvgpr_read_b32 v11,a59\nv_accvgpr_read_b32 v12,a84\nv_mfma_f32_16x16x32_bf16 a[192:195],a[220:223],a[252:255],a[192:195]\nds_read_b128 a[252:255],v239 offset:26624\ns_waitcnt lgkmcnt(2)\nv_mfma_f32_16x16x32_bf16 a[112:115],a[196:199],a[228:231],a[112:115]\ns_waitcnt vmcnt(17)\n"
"ds_read_b64_tr_b16 a[212:213],v231 offset:20480\nv_mfma_f32_16x16x32_bf16 a[140:143],a[200:203],a[228:231],a[140:143]\nv_accvgpr_read_b32 v13,a85\nv_accvgpr_read_b32 v14,a86\nv_mfma_f32_16x16x32_bf16 a[168:171],a[204:207],a[228:231],a[168:171]\nds_read_b128 a[228:231],v239 offset:15360\nv_accvgpr_read_b32 v15,a87\nv_mfma_f32_16x16x32_bf16 a[116:119],a[196:199],a[232:235],a[116:119]\n"
"ds_read_b64_tr_b16 a[214:215],v231 offset:21504\nv_cvt_pk_bf16_f32 v8,v8,v9\nv_mfma_f32_16x16x32_bf16 a[144:147],a[200:203],a[232:235],a[144:147]\nv_cvt_pk_bf16_f32 v9,v10,v11\nv_cvt_pk_bf16_f32 v10,v12,v13\nv_mfma_f32_16x16x32_bf16 a[172:175],a[204:207],a[232:235],a[172:175]\nds_read_b128 a[232:235],v239 offset:17408\nv_cvt_pk_bf16_f32 v11,v14,v15\n"
"v_mfma_f32_16x16x32_bf16 a[120:123],a[196:199],a[236:239],a[120:123]\nds_read_b64_tr_b16 a[216:217],v232 offset:20480\ns_add_u32 s71,s71,1024\nv_mfma_f32_16x16x32_bf16 a[148:151],a[200:203],a[236:239],a[148:151]\nbuffer_store_dwordx4 v[8:11],v239,s[60:63],s71 offen sc1\nv_mfma_f32_16x16x32_bf16 a[176:179],a[204:207],a[236:239],a[176:179]\nds_read_b128 a[236:239],v239 offset:19456\nv_accvgpr_read_b32 v16,a4\n"
"v_mfma_f32_16x16x32_bf16 a[124:127],a[196:199],a[240:243],a[124:127]\nds_read_b64_tr_b16 a[218:219],v232 offset:21504\nv_accvgpr_read_b32 v17,a5\nv_mfma_f32_16x16x32_bf16 a[152:155],a[200:203],a[240:243],a[152:155]\nv_accvgpr_read_b32 v18,a6\nv_accvgpr_read_b32 v19,a7\nv_mfma_f32_16x16x32_bf16 a[180:183],a[204:207],a[240:243],a[180:183]\nds_read_b128 a[240:243],v239 offset:21504\n"
"v_accvgpr_read_b32 v20,a32\nv_mfma_f32_16x16x32_bf16 a[128:131],a[196:199],a[244:247],a[128:131]\nds_read_b64_tr_b16 a[220:221],v233 offset:20480\nv_accvgpr_read_b32 v21,a33\nv_mfma_f32_16x16x32_bf16 a[156:159],a[200:203],a[244:247],a[156:159]\nv_accvgpr_read_b32 v22,a34\nv_accvgpr_read_b32 v23,a35\nv_mfma_f32_16x16x32_bf16 a[184:187],a[204:207],a[244:247],a[184:187]\n"
"ds_read_b128 a[244:247],v239 offset:23552\ns_waitcnt lgkmcnt(10)\nv_mfma_f32_16x16x32_bf16 a[132:135],a[196:199],a[248:251],a[132:135]\nds_read_b64_tr_b16 a[222:223],v233 offset:21504\nv_cvt_pk_bf16_f32 v16,v16,v17\nv_mfma_f32_16x16x32_bf16 a[160:163],a[200:203],a[248:251],a[160:163]\nv_cvt_pk_bf16_f32 v17,v18,v19\nv_cvt_pk_bf16_f32 v18,v20,v21\n"
"v_mfma_f32_16x16x32_bf16 a[188:191],a[204:207],a[248:251],a[188:191]\nds_read_b128 a[248:251],v239 offset:25600\nv_cvt_pk_bf16_f32 v19,v22,v23\nv_mfma_f32_16x16x32_bf16 a[136:139],a[196:199],a[252:255],a[136:139]\ns_add_u32 s71,s71,1024\nbuffer_store_dwordx4 v[16:19],v239,s[60:63],s71 offen sc1\nv_mfma_f32_16x16x32_bf16 a[164:167],a[200:203],a[252:255],a[164:167]\nv_accvgpr_read_b32 v24,a60\n"
"v_accvgpr_read_b32 v25,a61\nv_mfma_f32_16x16x32_bf16 a[192:195],a[204:207],a[252:255],a[192:195]\nds_read_b128 a[252:255],v239 offset:27648\ns_waitcnt lgkmcnt(2)\nv_mfma_f32_16x16x32_bf16 a[112:115],a[212:215],a[228:231],a[112:115]\ns_waitcnt vmcnt(15)\nds_read_b64_tr_b16 a[196:197],v231 offset:0\nv_mfma_f32_16x16x32_bf16 a[140:143],a[216:219],a[228:231],a[140:143]\n"
"v_accvgpr_read_b32 v26,a62\nv_accvgpr_read_b32 v27,a63\nv_mfma_f32_16x16x32_bf16 a[168:171],a[220:223],a[228:231],a[168:171]\nds_read_b128 a[228:231],v239 offset:28672\nv_accvgpr_read_b32 v28,a88\nv_mfma_f32_16x16x32_bf16 a[116:119],a[212:215],a[232:235],a[116:119]\nds_read_b64_tr_b16 a[198:199],v231 offset:1024\nv_accvgpr_read_b32 v29,a89\n"
"v_mfma_f32_16x16x32_bf16 a[144:147],a[216:219],a[232:235],a[144:147]\nv_accvgpr_read_b32 v30,a90\nv_accvgpr_read_b32 v31,a91\nv_mfma_f32_16x16x32_bf16 a[172:175],a[220:223],a[232:235],a[172:175]\nds_read_b128 a[232:235],v239 offset:30720\nv_cvt_pk_bf16_f32 v24,v24,v25\nv_mfma_f32_16x16x32_bf16 a[120:123],a[212:215],a[236:239],a[120:123]\nds_read_b64_tr_b16 a[200:201],v232 offset:0\n"
"v_cvt_pk_bf16_f32 v25,v26,v27\nv_mfma_f32_16x16x32_bf16 a[148:151],a[216:219],a[236:239],a[148:151]\nv_cvt_pk_bf16_f32 v26,v28,v29\nv_cvt_pk_bf16_f32 v27,v30,v31\nv_mfma_f32_16x16x32_bf16 a[176:179],a[220:223],a[236:239],a[176:179]\nds_read_b128 a[236:239],v239 offset:32768\ns_add_u32 s71,s71,1024\nv_mfma_f32_16x16x32_bf16 a[124:127],a[212:215],a[240:243],a[124:127]\n"
"ds_read_b64_tr_b16 a[202:203],v232 offset:1024\nv_accvgpr_read_b32 v32,a8\nv_mfma_f32_16x16x32_bf16 a[152:155],a[216:219],a[240:243],a[152:155]\nbuffer_store_dwordx4 v[24:27],v239,s[60:63],s71 offen sc1\nv_mfma_f32_16x16x32_bf16 a[180:183],a[220:223],a[240:243],a[180:183]\nds_read_b128 a[240:243],v239 offset:34816\nv_accvgpr_read_b32 v33,a9\nv_mfma_f32_16x16x32_bf16 a[128:131],a[212:215],a[244:247],a[128:131]\n"
"ds_read_b64_tr_b16 a[204:205],v233 offset:0\nv_accvgpr_read_b32 v34,a10\nv_mfma_f32_16x16x32_bf16 a[156:159],a[216:219],a[244:247],a[156:159]\nv_accvgpr_read_b32 v35,a11\nv_accvgpr_read_b32 v36,a36\nv_mfma_f32_16x16x32_bf16 a[184:187],a[220:223],a[244:247],a[184:187]\nds_read_b128 a[244:247],v239 offset:36864\ns_waitcnt lgkmcnt(10)\n"
"v_mfma_f32_16x16x32_bf16 a[132:135],a[212:215],a[248:251],a[132:135]\nds_read_b64_tr_b16 a[206:207],v233 offset:1024\nv_accvgpr_read_b32 v37,a37\nv_mfma_f32_16x16x32_bf16 a[160:163],a[216:219],a[248:251],a[160:163]\nv_accvgpr_read_b32 v38,a38\nv_accvgpr_read_b32 v39,a39\nv_mfma_f32_16x16x32_bf16 a[188:191],a[220:223],a[248:251],a[188:191]\nds_read_b128 a[248:251],v239 offset:38912\n"
"v_cvt_pk_bf16_f32 v32,v32,v33\nv_mfma_f32_16x16x32_bf16 a[136:139],a[212:215],a[252:255],a[136:139]\nv_cvt_pk_bf16_f32 v33,v34,v35\nv_cvt_pk_bf16_f32 v34,v36,v37\nv_mfma_f32_16x16x32_bf16 a[164:167],a[216:219],a[252:255],a[164:167]\nv_cvt_pk_bf16_f32 v35,v38,v39\ns_add_u32 s71,s71,1024\nv_mfma_f32_16x16x32_bf16 a[192:195],a[220:223],a[252:255],a[192:195]\n"
"ds_read_b128 a[252:255],v239 offset:40960\ns_waitcnt lgkmcnt(2)\nv_mfma_f32_16x16x32_bf16 a[112:115],a[196:199],a[228:231],a[112:115]\ns_waitcnt vmcnt(12)\nds_read_b64_tr_b16 a[212:213],v231 offset:4096\nv_mfma_f32_16x16x32_bf16 a[140:143],a[200:203],a[228:231],a[140:143]\nbuffer_store_dwordx4 v[32:35],v239,s[60:63],s71 offen sc1\nv_mfma_f32_16x16x32_bf16 a[168:171],a[204:207],a[228:231],a[168:171]\n"
"ds_read_b128 a[228:231],v239 offset:29696\nv_accvgpr_read_b32 v40,a64\nv_mfma_f32_16x16x32_bf16 a[116:119],a[196:199],a[232:235],a[116:119]\nds_read_b64_tr_b16 a[214:215],v231 offset:5120\nv_accvgpr_read_b32 v41,a65\nv_mfma_f32_16x16x32_bf16 a[144:147],a[200:203],a[232:235],a[144:147]\nv_accvgpr_read_b32 v42,a66\nv_accvgpr_read_b32 v43,a67\n"
"v_mfma_f32_16x16x32_bf16 a[172:175],a[204:207],a[232:235],a[172:175]\nds_read_b128 a[232:235],v239 offset:31744\nv_accvgpr_read_b32 v44,a92\nv_mfma_f32_16x16x32_bf16 a[120:123],a[196:199],a[236:239],a[120:123]\nds_read_b64_tr_b16 a[216:217],v232 offset:4096\nv_accvgpr_read_b32 v45,a93\nv_mfma_f32_16x16x32_bf16 a[148:151],a[200:203],a[236:239],a[148:151]\nv_accvgpr_read_b32 v46,a94\n"
"v_accvgpr_read_b32 v47,a95\nv_mfma_f32_16x16x32_bf16 a[176:179],a[204:207],a[236:239],a[176:179]\nds_read_b128 a[236:239],v239 offset:33792\nv_cvt_pk_bf16_f32 v40,v40,v41\nv_mfma_f32_16x16x32_bf16 a[124:127],a[196:199],a[240:243],a[124:127]\nds_read_b64_tr_b16 a[218:219],v232 offset:5120\nv_cvt_pk_bf16_f32 v41,v42,v43\nv_mfma_f32_16x16x32_bf16 a[152:155],a[200:203],a[240:243],a[152:155]\n"
"v_cvt_pk_bf16_f32 v42,v44,v45\nv_cvt_pk_bf16_f32 v43,v46,v47\nv_mfma_f32_16x16x32_bf16 a[180:183],a[204:207],a[240:243],a[180:183]\nds_read_b128 a[240:243],v239 offset:35840\ns_add_u32 s71,s71,1024\nv_mfma_f32_16x16x32_bf16 a[128:131],a[196:199],a[244:247],a[128:131]\nds_read_b64_tr_b16 a[220:221],v233 offset:4096\nv_accvgpr_read_b32 v48,a12\n"
"v_mfma_f32_16x16x32_bf16 a[156:159],a[200:203],a[244:247],a[156:159]\nbuffer_store_dwordx4 v[40:43],v239,s[60:63],s71 offen sc1\nv_mfma_f32_16x16x32_bf16 a[184:187],a[204:207],a[244:247],a[184:187]\nds_read_b128 a[244:247],v239 offset:37888\ns_waitcnt lgkmcnt(10)\nv_mfma_f32_16x16x32_bf16 a[132:135],a[196:199],a[248:251],a[132:135]\nds_read_b64_tr_b16 a[222:223],v233 offset:5120\nv_accvgpr_read_b32 v49,a13\n"
"v_mfma_f32_16x16x32_bf16 a[160:163],a[200:203],a[248:251],a[160:163]\nv_accvgpr_read_b32 v50,a14\nv_accvgpr_read_b32 v51,a15\nv_mfma_f32_16x16x32_bf16 a[188:191],a[204:207],a[248:251],a[188:191]\nds_read_b128 a[248:251],v239 offset:39936\nv_accvgpr_read_b32 v52,a40\nv_mfma_f32_16x16x32_bf16 a[136:139],a[196:199],a[252:255],a[136:139]\nv_accvgpr_read_b32 v53,a41\n"
"v_accvgpr_read_b32 v54,a42\nv_mfma_f32_16x16x32_bf16 a[164:167],a[200:203],a[252:255],a[164:167]\nv_accvgpr_read_b32 v55,a43\nv_cvt_pk_bf16_f32 v48,v48,v49\nv_mfma_f32_16x16x32_bf16 a[192:195],a[204:207],a[252:255],a[192:195]\nds_read_b128 a[252:255],v239 offset:41984\ns_waitcnt lgkmcnt(2)\nv_mfma_f32_16x16x32_bf16 a[112:115],a[212:215],a[228:231],a[112:115]\n"
"s_waitcnt vmcnt(10)\nds_read_b64_tr_b16 a[196:197],v231 offset:8192\nv_mfma_f32_16x16x32_bf16 a[140:143],a[216:219],a[228:231],a[140:143]\nv_cvt_pk_bf16_f32 v49,v50,v51\nv_cvt_pk_bf16_f32 v50,v52,v53\nv_mfma_f32_16x16x32_bf16 a[168:171],a[220:223],a[228:231],a[168:171]\nds_read_b128 a[228:231],v239 offset:43008\nv_cvt_pk_bf16_f32 v51,v54,v55\n"
"v_mfma_f32_16x16x32_bf16 a[116:119],a[212:215],a[232:235],a[116:119]\nds_read_b64_tr_b16 a[198:199],v231 offset:9216\ns_add_u32 s71,s71,1024\nv_mfma_f32_16x16x32_bf16 a[144:147],a[216:219],a[232:235],a[144:147]\nbuffer_store_dwordx4 v[48:51],v239,s[60:63],s71 offen sc1\nv_mfma_f32_16x16x32_bf16 a[172:175],a[220:223],a[232:235],a[172:175]\nds_read_b128 a[232:235],v239 offset:45056\nv_accvgpr_read_b32 v56,a68\n"
"v_mfma_f32_16x16x32_bf16 a[120:123],a[212:215],a[236:239],a[120:123]\nds_read_b64_tr_b16 a[200:201],v232 offset:8192\nv_accvgpr_read_b32 v57,a69\nv_mfma_f32_16x16x32_bf16 a[148:151],a[216:219],a[236:239],a[148:151]\nv_accvgpr_read_b32 v58,a70\nv_accvgpr_read_b32 v59,a71\nv_mfma_f32_16x16x32_bf16 a[176:179],a[220:223],a[236:239],a[176:179]\nds_read_b128 a[236:239],v239 offset:47104\n"
"v_accvgpr_read_b32 v60,a96\nv_mfma_f32_16x16x32_bf16 a[124:127],a[212:215],a[240:243],a[124:127]\nds_read_b64_tr_b16 a[202:203],v232 offset:9216\nv_accvgpr_read_b32 v61,a97\nv_mfma_f32_16x16x32_bf16 a[152:155],a[216:219],a[240:243],a[152:155]\nv_accvgpr_read_b32 v62,a98\nv_accvgpr_read_b32 v63,a99\nv_mfma_f32_16x16x32_bf16 a[180:183],a[220:223],a[240:243],a[180:183]\n"
"ds_read_b128 a[240:243],v239 offset:49152\nv_cvt_pk_bf16_f32 v56,v56,v57\nv_mfma_f32_16x16x32_bf16 a[128:131],a[212:215],a[244:247],a[128:131]\nds_read_b64_tr_b16 a[204:205],v233 offset:8192\nv_cvt_pk_bf16_f32 v57,v58,v59\nv_mfma_f32_16x16x32_bf16 a[156:159],a[216:219],a[244:247],a[156:159]\nv_cvt_pk_bf16_f32 v58,v60,v61\nv_cvt_pk_bf16_f32 v59,v62,v63\n"
"v_mfma_f32_16x16x32_bf16 a[184:187],a[220:223],a[244:247],a[184:187]\nds_read_b128 a[244:247],v239 offset:51200\ns_waitcnt lgkmcnt(10)\nv_mfma_f32_16x16x32_bf16 a[132:135],a[212:215],a[248:251],a[132:135]\nds_read_b64_tr_b16 a[206:207],v233 offset:9216\ns_add_u32 s71,s71,1024\nv_mfma_f32_16x16x32_bf16 a[160:163],a[216:219],a[248:251],a[160:163]\nbuffer_store_dwordx4 v[56:59],v239,s[60:63],s71 offen sc1\n"
"v_mfma_f32_16x16x32_bf16 a[188:191],a[220:223],a[248:251],a[188:191]\nds_read_b128 a[248:251],v239 offset:53248\nv_accvgpr_read_b32 v64,a16\nv_mfma_f32_16x16x32_bf16 a[136:139],a[212:215],a[252:255],a[136:139]\nv_accvgpr_read_b32 v65,a17\nv_accvgpr_read_b32 v66,a18\nv_mfma_f32_16x16x32_bf16 a[164:167],a[216:219],a[252:255],a[164:167]\nv_accvgpr_read_b32 v67,a19\n"
"v_accvgpr_read_b32 v68,a44\nv_mfma_f32_16x16x32_bf16 a[192:195],a[220:223],a[252:255],a[192:195]\nds_read_b128 a[252:255],v239 offset:55296\ns_waitcnt lgkmcnt(2)\nv_mfma_f32_16x16x32_bf16 a[112:115],a[196:199],a[228:231],a[112:115]\ns_waitcnt vmcnt(8)\nds_read_b64_tr_b16 a[212:213],v231 offset:12288\nv_mfma_f32_16x16x32_bf16 a[140:143],a[200:203],a[228:231],a[140:143]\n"
"v_accvgpr_read_b32 v69,a45\nv_accvgpr_read_b32 v70,a46\nv_mfma_f32_16x16x32_bf16 a[168:171],a[204:207],a[228:231],a[168:171]\nds_read_b128 a[228:231],v239 offset:44032\nv_accvgpr_read_b32 v71,a47\nv_mfma_f32_16x16x32_bf16 a[116:119],a[196:199],a[232:235],a[116:119]\nds_read_b64_tr_b16 a[214:215],v231 offset:13312\nv_cvt_pk_bf16_f32 v64,v64,v65\n"
"v_mfma_f32_16x16x32_bf16 a[144:147],a[200:203],a[232:235],a[144:147]\nv_cvt_pk_bf16_f32 v65,v66,v67\nv_cvt_pk_bf16_f32 v66,v68,v69\nv_mfma_f32_16x16x32_bf16 a[172:175],a[204:207],a[232:235],a[172:175]\nds_read_b128 a[232:235],v239 offset:46080\nv_cvt_pk_bf16_f32 v67,v70,v71\nv_mfma_f32_16x16x32_bf16 a[120:123],a[196:199],a[236:239],a[120:123]\nds_read_b64_tr_b16 a[216:217],v232 offset:12288\n"
"s_add_u32 s71,s71,1024\nv_mfma_f32_16x16x32_bf16 a[148:151],a[200:203],a[236:239],a[148:151]\nbuffer_store_dwordx4 v[64:67],v239,s[60:63],s71 offen sc1\nv_mfma_f32_16x16x32_bf16 a[176:179],a[204:207],a[236:239],a[176:179]\nds_read_b128 a[236:239],v239 offset:48128\nv_accvgpr_read_b32 v72,a72\nv_mfma_f32_16x16x32_bf16 a[124:127],a[196:199],a[240:243],a[124:127]\nds_read_b64_tr_b16 a[218:219],v232 offset:13312\n"
"v_accvgpr_read_b32 v73,a73\nv_mfma_f32_16x16x32_bf16 a[152:155],a[200:203],a[240:243],a[152:155]\nv_accvgpr_read_b32 v74,a74\nv_accvgpr_read_b32 v75,a75\nv_mfma_f32_16x16x32_bf16 a[180:183],a[204:207],a[240:243],a[180:183]\nds_read_b128 a[240:243],v239 offset:50176\nv_accvgpr_read_b32 v76,a100\nv_mfma_f32_16x16x32_bf16 a[128:131],a[196:199],a[244:247],a[128:131]\n"
"ds_read_b64_tr_b16 a[220:221],v233 offset:12288\nv_accvgpr_read_b32 v77,a101\nv_mfma_f32_16x16x32_bf16 a[156:159],a[200:203],a[244:247],a[156:159]\nv_accvgpr_read_b32 v78,a102\nv_accvgpr_read_b32 v79,a103\nv_mfma_f32_16x16x32_bf16 a[184:187],a[204:207],a[244:247],a[184:187]\nds_read_b128 a[244:247],v239 offset:52224\ns_waitcnt lgkmcnt(10)\n"
"v_mfma_f32_16x16x32_bf16 a[132:135],a[196:199],a[248:251],a[132:135]\nds_read_b64_tr_b16 a[222:223],v233 offset:13312\nv_cvt_pk_bf16_f32 v72,v72,v73\nv_mfma_f32_16x16x32_bf16 a[160:163],a[200:203],a[248:251],a[160:163]\nv_cvt_pk_bf16_f32 v73,v74,v75\nv_cvt_pk_bf16_f32 v74,v76,v77\nv_mfma_f32_16x16x32_bf16 a[188:191],a[204:207],a[248:251],a[188:191]\nds_read_b128 a[248:251],v239 offset:54272\n"
"v_cvt_pk_bf16_f32 v75,v78,v79\nv_mfma_f32_16x16x32_bf16 a[136:139],a[196:199],a[252:255],a[136:139]\ns_add_u32 s71,s71,1024\nbuffer_store_dwordx4 v[72:75],v239,s[60:63],s71 offen sc1\nv_mfma_f32_16x16x32_bf16 a[164:167],a[200:203],a[252:255],a[164:167]\nv_accvgpr_read_b32 v80,a20\nv_accvgpr_read_b32 v81,a21\nv_mfma_f32_16x16x32_bf16 a[192:195],a[204:207],a[252:255],a[192:195]\n"
"ds_read_b128 a[252:255],v239 offset:56320\ns_waitcnt lgkmcnt(2)\nv_mfma_f32_16x16x32_bf16 a[112:115],a[212:215],a[228:231],a[112:115]\nv_accvgpr_read_b32 v82,a22\nv_accvgpr_read_b32 v83,a23\nv_mfma_f32_16x16x32_bf16 a[140:143],a[216:219],a[228:231],a[140:143]\nv_accvgpr_read_b32 v84,a48\nv_accvgpr_read_b32 v85,a49\n"
"v_mfma_f32_16x16x32_bf16 a[168:171],a[220:223],a[228:231],a[168:171]\nv_accvgpr_read_b32 v86,a50\nv_accvgpr_read_b32 v87,a51\nv_mfma_f32_16x16x32_bf16 a[116:119],a[212:215],a[232:235],a[116:119]\nv_cvt_pk_bf16_f32 v80,v80,v81\nv_cvt_pk_bf16_f32 v81,v82,v83\nv_mfma_f32_16x16x32_bf16 a[144:147],a[216:219],a[232:235],a[144:147]\nv_cvt_pk_bf16_f32 v82,v84,v85\n"
"v_cvt_pk_bf16_f32 v83,v86,v87\nv_mfma_f32_16x16x32_bf16 a[172:175],a[220:223],a[232:235],a[172:175]\ns_add_u32 s71,s71,1024\nbuffer_store_dwordx4 v[80:83],v239,s[60:63],s71 offen sc1\nv_mfma_f32_16x16x32_bf16 a[120:123],a[212:215],a[236:239],a[120:123]\nv_accvgpr_read_b32 v88,a76\nv_accvgpr_read_b32 v89,a77\nv_mfma_f32_16x16x32_bf16 a[148:151],a[216:219],a[236:239],a[148:151]\n"
"v_accvgpr_read_b32 v90,a78\nv_accvgpr_read_b32 v91,a79\nv_mfma_f32_16x16x32_bf16 a[176:179],a[220:223],a[236:239],a[176:179]\nv_accvgpr_read_b32 v92,a104\nv_accvgpr_read_b32 v93,a105\nv_mfma_f32_16x16x32_bf16 a[124:127],a[212:215],a[240:243],a[124:127]\nv_accvgpr_read_b32 v94,a106\nv_accvgpr_read_b32 v95,a107\n"
"v_cvt_pk_bf16_f32 v88,v88,v89\nv_cvt_pk_bf16_f32 v89,v90,v91\nv_cvt_pk_bf16_f32 v90,v92,v93\nv_cvt_pk_bf16_f32 v91,v94,v95\ns_add_u32 s71,s71,1024\nbuffer_store_dwordx4 v[88:91],v239,s[60:63],s71 offen sc1\nv_accvgpr_read_b32 v0,a24\nv_accvgpr_read_b32 v1,a25\n"
"v_accvgpr_read_b32 v2,a26\nv_accvgpr_read_b32 v3,a27\nv_accvgpr_read_b32 v4,a52\nv_accvgpr_read_b32 v5,a53\nv_accvgpr_read_b32 v6,a54\nv_accvgpr_read_b32 v7,a55\nv_cvt_pk_bf16_f32 v0,v0,v1\nv_cvt_pk_bf16_f32 v1,v2,v3\n"
"v_cvt_pk_bf16_f32 v2,v4,v5\nv_cvt_pk_bf16_f32 v3,v6,v7\nv_mfma_f32_16x16x32_bf16 a[152:155],a[216:219],a[240:243],a[152:155]\ns_add_u32 s71,s71,1024\nbuffer_store_dwordx4 v[0:3],v239,s[60:63],s71 offen sc1\nv_accvgpr_read_b32 v8,a80\nv_accvgpr_read_b32 v9,a81\nv_accvgpr_read_b32 v10,a82\n"
"v_accvgpr_read_b32 v11,a83\nv_accvgpr_read_b32 v12,a108\nv_accvgpr_read_b32 v13,a109\nv_accvgpr_read_b32 v14,a110\nv_accvgpr_read_b32 v15,a111\nv_cvt_pk_bf16_f32 v8,v8,v9\nv_cvt_pk_bf16_f32 v9,v10,v11\nv_cvt_pk_bf16_f32 v10,v12,v13\n"
"v_cvt_pk_bf16_f32 v11,v14,v15\ns_add_u32 s71,s71,1024\nbuffer_store_dwordx4 v[8:11],v239,s[60:63],s71 offen sc1\nv_accvgpr_read_b32 v16,a112\nv_accvgpr_read_b32 v17,a113\nv_accvgpr_read_b32 v18,a114\nv_mfma_f32_16x16x32_bf16 a[180:183],a[220:223],a[240:243],a[180:183]\nv_accvgpr_read_b32 v19,a115\n"
"v_accvgpr_read_b32 v20,a140\nv_accvgpr_read_b32 v21,a141\nv_accvgpr_read_b32 v22,a142\nv_accvgpr_read_b32 v23,a143\nv_cvt_pk_bf16_f32 v16,v16,v17\nv_cvt_pk_bf16_f32 v17,v18,v19\nv_cvt_pk_bf16_f32 v18,v20,v21\nv_cvt_pk_bf16_f32 v19,v22,v23\n"
"s_add_u32 s71,s71,1024\nbuffer_store_dwordx4 v[16:19],v239,s[60:63],s71 offen sc1\nv_accvgpr_read_b32 v24,a168\nv_accvgpr_read_b32 v25,a169\nv_accvgpr_read_b32 v26,a170\nv_accvgpr_read_b32 v27,a171\nv_cvt_pk_bf16_f32 v24,v24,v25\nv_cvt_pk_bf16_f32 v25,v26,v27\n"
"s_add_u32 s71,s71,1024\nbuffer_store_dwordx2 v[24:25],v255,s[60:63],s71 offen sc1\nv_mfma_f32_16x16x32_bf16 a[128:131],a[212:215],a[244:247],a[128:131]\nv_accvgpr_read_b32 v32,a116\nv_accvgpr_read_b32 v33,a117\nv_accvgpr_read_b32 v34,a118\nv_accvgpr_read_b32 v35,a119\nv_accvgpr_read_b32 v36,a144\n"
"v_accvgpr_read_b32 v37,a145\nv_accvgpr_read_b32 v38,a146\nv_accvgpr_read_b32 v39,a147\nv_mfma_f32_16x16x32_bf16 a[156:159],a[216:219],a[244:247],a[156:159]\nv_cvt_pk_bf16_f32 v32,v32,v33\nv_cvt_pk_bf16_f32 v33,v34,v35\nv_cvt_pk_bf16_f32 v34,v36,v37\nv_cvt_pk_bf16_f32 v35,v38,v39\n"
"s_add_u32 s71,s71,512\nbuffer_store_dwordx4 v[32:35],v239,s[60:63],s71 offen sc1\nv_accvgpr_read_b32 v40,a172\nv_mfma_f32_16x16x32_bf16 a[184:187],a[220:223],a[244:247],a[184:187]\ns_waitcnt lgkmcnt(0)\nv_accvgpr_read_b32 v41,a173\nv_accvgpr_read_b32 v42,a174\nv_accvgpr_read_b32 v43,a175\n"
"v_cvt_pk_bf16_f32 v40,v40,v41\nv_cvt_pk_bf16_f32 v41,v42,v43\ns_add_u32 s71,s71,1024\nbuffer_store_dwordx2 v[40:41],v255,s[60:63],s71 offen sc1\nv_mfma_f32_16x16x32_bf16 a[132:135],a[212:215],a[248:251],a[132:135]\nv_accvgpr_read_b32 v48,a120\nv_accvgpr_read_b32 v49,a121\nv_accvgpr_read_b32 v50,a122\n"
"v_accvgpr_read_b32 v51,a123\nv_accvgpr_read_b32 v52,a148\nv_accvgpr_read_b32 v53,a149\nv_accvgpr_read_b32 v54,a150\nv_accvgpr_read_b32 v55,a151\nv_mfma_f32_16x16x32_bf16 a[160:163],a[216:219],a[248:251],a[160:163]\nv_cvt_pk_bf16_f32 v48,v48,v49\nv_cvt_pk_bf16_f32 v49,v50,v51\n"
"v_cvt_pk_bf16_f32 v50,v52,v53\nv_cvt_pk_bf16_f32 v51,v54,v55\ns_add_u32 s71,s71,512\nbuffer_store_dwordx4 v[48:51],v239,s[60:63],s71 offen sc1\nv_accvgpr_read_b32 v56,a176\nv_mfma_f32_16x16x32_bf16 a[188:191],a[220:223],a[248:251],a[188:191]\nv_accvgpr_read_b32 v57,a177\nv_accvgpr_read_b32 v58,a178\n"
"v_accvgpr_read_b32 v59,a179\nv_cvt_pk_bf16_f32 v56,v56,v57\nv_cvt_pk_bf16_f32 v57,v58,v59\ns_add_u32 s71,s71,1024\nbuffer_store_dwordx2 v[56:57],v255,s[60:63],s71 offen sc1\nv_mfma_f32_16x16x32_bf16 a[136:139],a[212:215],a[252:255],a[136:139]\nv_accvgpr_read_b32 v64,a124\nv_accvgpr_read_b32 v65,a125\n"
"v_accvgpr_read_b32 v66,a126\nv_accvgpr_read_b32 v67,a127\nv_accvgpr_read_b32 v68,a152\nv_accvgpr_read_b32 v69,a153\nv_accvgpr_read_b32 v70,a154\nv_accvgpr_read_b32 v71,a155\nv_mfma_f32_16x16x32_bf16 a[164:167],a[216:219],a[252:255],a[164:167]\nv_cvt_pk_bf16_f32 v64,v64,v65\n"
"v_cvt_pk_bf16_f32 v65,v66,v67\nv_cvt_pk_bf16_f32 v66,v68,v69\nv_cvt_pk_bf16_f32 v67,v70,v71\ns_add_u32 s71,s71,512\nbuffer_store_dwordx4 v[64:67],v239,s[60:63],s71 offen sc1\nv_accvgpr_read_b32 v72,a180\nv_mfma_f32_16x16x32_bf16 a[192:195],a[220:223],a[252:255],a[192:195]\nv_accvgpr_read_b32 v73,a181\n"
"v_accvgpr_read_b32 v74,a182\nv_accvgpr_read_b32 v75,a183\nv_cvt_pk_bf16_f32 v72,v72,v73\nv_cvt_pk_bf16_f32 v73,v74,v75\ns_add_u32 s71,s71,1024\nbuffer_store_dwordx2 v[72:73],v255,s[60:63],s71 offen sc1\nv_accvgpr_read_b32 v80,a128\nv_accvgpr_read_b32 v81,a129\n"
"v_accvgpr_read_b32 v82,a130\nv_accvgpr_read_b32 v83,a131\nv_accvgpr_read_b32 v84,a156\nv_accvgpr_read_b32 v85,a157\nv_accvgpr_read_b32 v86,a158\nv_accvgpr_read_b32 v87,a159\nv_cvt_pk_bf16_f32 v80,v80,v81\nv_cvt_pk_bf16_f32 v81,v82,v83\n"
"v_cvt_pk_bf16_f32 v82,v84,v85\nv_cvt_pk_bf16_f32 v83,v86,v87\ns_add_u32 s71,s71,512\nbuffer_store_dwordx4 v[80:83],v239,s[60:63],s71 offen sc1\nv_accvgpr_read_b32 v88,a184\nv_accvgpr_read_b32 v89,a185\nv_accvgpr_read_b32 v90,a186\nv_accvgpr_read_b32 v91,a187\n"
"v_cvt_pk_bf16_f32 v88,v88,v89\nv_cvt_pk_bf16_f32 v89,v90,v91\ns_add_u32 s71,s71,1024\nbuffer_store_dwordx2 v[88:89],v255,s[60:63],s71 offen sc1\nv_accvgpr_read_b32 v0,a132\nv_accvgpr_read_b32 v1,a133\nv_accvgpr_read_b32 v2,a134\nv_accvgpr_read_b32 v3,a135\n"
"v_accvgpr_read_b32 v4,a160\nv_accvgpr_read_b32 v5,a161\nv_accvgpr_read_b32 v6,a162\nv_accvgpr_read_b32 v7,a163\nv_cvt_pk_bf16_f32 v0,v0,v1\nv_cvt_pk_bf16_f32 v1,v2,v3\nv_cvt_pk_bf16_f32 v2,v4,v5\nv_cvt_pk_bf16_f32 v3,v6,v7\n"
"s_add_u32 s71,s71,512\nbuffer_store_dwordx4 v[0:3],v239,s[60:63],s71 offen sc1\nv_accvgpr_read_b32 v8,a188\nv_accvgpr_read_b32 v9,a189\nv_accvgpr_read_b32 v10,a190\nv_accvgpr_read_b32 v11,a191\nv_cvt_pk_bf16_f32 v8,v8,v9\nv_cvt_pk_bf16_f32 v9,v10,v11\n"
"s_add_u32 s71,s71,1024\nbuffer_store_dwordx2 v[8:9],v255,s[60:63],s71 offen sc1\nv_accvgpr_read_b32 v16,a136\nv_accvgpr_read_b32 v17,a137\nv_accvgpr_read_b32 v18,a138\nv_accvgpr_read_b32 v19,a139\nv_accvgpr_read_b32 v20,a164\nv_accvgpr_read_b32 v21,a165\n"
"v_accvgpr_read_b32 v22,a166\nv_accvgpr_read_b32 v23,a167\nv_cvt_pk_bf16_f32 v16,v16,v17\nv_cvt_pk_bf16_f32 v17,v18,v19\nv_cvt_pk_bf16_f32 v18,v20,v21\nv_cvt_pk_bf16_f32 v19,v22,v23\ns_add_u32 s71,s71,512\nbuffer_store_dwordx4 v[16:19],v239,s[60:63],s71 offen sc1\n"
"v_accvgpr_read_b32 v24,a192\nv_accvgpr_read_b32 v25,a193\nv_accvgpr_read_b32 v26,a194\nv_accvgpr_read_b32 v27,a195\nv_cvt_pk_bf16_f32 v24,v24,v25\nv_cvt_pk_bf16_f32 v25,v26,v27\ns_add_u32 s71,s71,1024\nbuffer_store_dwordx2 v[24:25],v255,s[60:63],s71 offen sc1\n"
"s_waitcnt vmcnt(0)\ns_barrier\ns_cmp_lg_u32 s70,0\ns_cbranch_scc1 3f\nbuffer_store_dword v203,v201,s[96:99],0 offen offset:1024 sc1\ns_mov_b32 s72,16777216\n1:\nbuffer_load_dword v205,v200,s[96:99],0 offen offset:1024 sc1\n"
"buffer_load_dword v206,v200,s[96:99],0 offen sc1\ns_waitcnt vmcnt(1)\nv_cmp_ne_u32 vcc,v205,v204\ns_nop 4\ns_cbranch_vccz 3f\ns_waitcnt vmcnt(0)\nv_cmp_ne_u32 vcc,v206,v203\ns_nop 4\n"
"s_cbranch_vccz 2f\nv_mov_b32 v203,v206\nbuffer_store_dword v203,v201,s[96:99],0 offen offset:1024 sc1\n2:\ns_sub_u32 s72,s72,1\ns_cmp_lg_u32 s72,0\ns_cbranch_scc1 1b\n3:\n"
"s_waitcnt vmcnt(0)\ns_barrier\n"
"s_branch 78f\n77:\n"
"s_mov_b32 s70,%6\nv_mbcnt_lo_u32_b32 v0,-1,0\nv_mbcnt_hi_u32_b32 v0,-1,v0\nv_lshlrev_b32 v239,4,v0\ns_mov_b64 s[40:41],%0\ns_mov_b64 s[44:45],%1\ns_mov_b64 s[48:49],%2\ns_mov_b64 s[52:53],%3\n"
"s_mov_b64 s[56:57],%4\ns_mov_b64 s[60:61],%5\ns_mov_b32 s42,100352\ns_mov_b32 s46,3662848\ns_mov_b32 s50,524288\ns_mov_b32 s54,524288\ns_mov_b32 s58,%7\ns_mov_b32 s62,100352\n"
"s_mov_b32 s43,0x20000\ns_mov_b32 s47,0x20000\ns_mov_b32 s51,0x20000\ns_mov_b32 s55,0x20000\ns_mov_b32 s59,0x20000\ns_mov_b32 s63,0x20000\ns_mov_b64 s[96:97],%13\ns_mov_b32 s98,2048\n"
"s_mov_b32 s99,0x20000\ns_mul_i32 s64,s70,20480\ns_add_u32 s64,s64,65536\ns_lshl_b32 s65,s70,12\ns_cmp_lt_u32 s70,3\ns_cselect_b32 s67,14336,0\ns_cselect_b32 s66,2048,0\ns_add_u32 s66,s66,s65\n"
"s_lshl_b32 s68,s70,5\ns_sub_u32 s68,512,s68\ns_mov_b32 s69,0x3db8aa3b\ns_mov_b32 s35,0x3f803f80\ns_mul_i32 s71,s70,25088\nv_lshrrev_b32 v1,3,v0\nv_and_b32 v2,7,v0\nv_lshrrev_b32 v4,1,v1\n"
"v_xor_b32 v5,v2,v4\nv_lshl_add_u32 v6,s70,5,v1\nv_mul_u32_u24 v6,896,v6\nv_lshl_add_u32 v229,v5,4,v6\nv_xor_b32 v230,64,v229\nv_and_b32 v3,4,v1\nv_add_u32 v3,v3,v1\nv_lshlrev_b32 v6,1,v4\n"
"v_xor_b32 v6,v2,v6\nv_lshlrev_b32 v6,4,v6\nv_lshl_add_u32 v6,s70,7,v6\nv_lshl_add_u32 v224,v3,13,v6\nv_lshl_add_u32 v225,v3,9,v6\nv_mul_u32_u24 v7,896,v1\nv_add_u32 v226,v7,v6\ns_lshr_b32 s74,s70,1\n"
"s_and_b32 s75,s70,1\nv_lshl_add_u32 v7,s74,5,v3\nv_lshl_add_u32 v7,s75,2,v7\nv_lshlrev_b32 v7,9,v7\nv_lshl_add_u32 v227,v5,4,v7\nv_xor_b32 v228,64,v227\nv_lshl_add_u32 v240,s74,10,v239\nv_lshl_add_u32 v240,s75,3,v240\n"
"v_and_b32 v1,15,v0\nv_lshrrev_b32 v2,4,v0\nv_lshrrev_b32 v3,1,v1\nv_xor_b32 v3,v3,v2\nv_lshlrev_b32 v3,4,v3\nv_lshl_add_u32 v237,v1,7,v3\nv_xor_b32 v238,64,v237\nv_add_u32 v235,s64,v237\n"
"v_add_u32 v236,s64,v238\nv_bfe_u32 v1,v0,2,3\nv_lshrrev_b32 v2,5,v0\nv_lshlrev_b32 v2,11,v2\nv_lshl_add_u32 v2,v1,7,v2\nv_lshrrev_b32 v1,1,v1\nv_lshl_add_u32 v2,v1,5,v2\nv_and_b32 v1,3,v0\n"
"v_lshl_add_u32 v2,v1,3,v2\nv_add_u32 v231,s64,v2\nv_xor_b32 v232,32,v231\nv_xor_b32 v233,64,v231\nv_xor_b32 v234,0x60,v231\ns_add_u32 m0,s65,0\ns_mov_b32 s73,0\nbuffer_load_dwordx4 v229,s[40:43],s73 offen offset:0 lds\n"
"s_mov_b32 s74,6144\nbuffer_load_dwordx4 v230,s[40:43],s74 offen offset:1024 lds\ns_add_u32 m0,s66,0\ns_add_u32 s75,s67,0\nbuffer_load_dwordx4 v229,s[40:43],s75 offen offset:0 lds\ns_add_u32 s72,s67,6144\nbuffer_load_dwordx4 v230,s[40:43],s72 offen offset:1024 lds\ns_add_u32 m0,s64,0\n"
"s_mov_b32 s73,0\nbuffer_load_dwordx4 v224,s[44:47],s73 offen offset:0 lds\ns_mov_b32 s74,31744\nbuffer_load_dwordx4 v224,s[44:47],s74 offen offset:1024 lds\ns_mov_b32 s75,129024\nbuffer_load_dwordx4 v224,s[44:47],s75 offen offset:2048 lds\ns_mov_b32 s72,160768\nbuffer_load_dwordx4 v224,s[44:47],s72 offen offset:3072 lds\n"
"v_min_u32 v1,55,v0\nv_lshrrev_b32 v2,3,v1\nv_and_b32 v1,7,v1\nv_lshlrev_b32 v1,7,v1\nv_lshl_add_u32 v1,v2,13,v1\ns_mul_i32 s72,%10,57344\nbuffer_load_dword v255,v1,s[44:47],s72 offen offset:0\ns_add_u32 m0,s64,4096\n"
"s_mov_b32 s73,262144\nbuffer_load_dwordx4 v224,s[44:47],s73 offen offset:0 lds\ns_mov_b32 s74,293888\nbuffer_load_dwordx4 v224,s[44:47],s74 offen offset:1024 lds\ns_mov_b32 s75,391168\nbuffer_load_dwordx4 v224,s[44:47],s75 offen offset:2048 lds\ns_mov_b32 s72,422912\nbuffer_load_dwordx4 v224,s[44:47],s72 offen offset:3072 lds\n"
"s_add_u32 m0,s65,14336\ns_mov_b32 s73,128\nbuffer_load_dwordx4 v229,s[40:43],s73 offen offset:0 lds\ns_mov_b32 s74,6272\nbuffer_load_dwordx4 v230,s[40:43],s74 offen offset:1024 lds\ns_add_u32 m0,s66,14336\ns_add_u32 s75,s67,128\nbuffer_load_dwordx4 v229,s[40:43],s75 offen offset:0 lds\n"
"s_add_u32 s72,s67,6272\nbuffer_load_dwordx4 v230,s[40:43],s72 offen offset:1024 lds\ns_add_u32 m0,s64,8192\ns_mov_b32 s73,524288\nbuffer_load_dwordx4 v224,s[44:47],s73 offen offset:0 lds\ns_mov_b32 s74,556032\nbuffer_load_dwordx4 v224,s[44:47],s74 offen offset:1024 lds\ns_mov_b32 s75,653312\n"
"buffer_load_dwordx4 v224,s[44:47],s75 offen offset:2048 lds\ns_mov_b32 s72,685056\nbuffer_load_dwordx4 v224,s[44:47],s72 offen offset:3072 lds\ns_add_u32 m0,s65,28672\ns_mov_b32 s73,256\nbuffer_load_dwordx4 v229,s[40:43],s73 offen offset:0 lds\ns_mov_b32 s74,6400\nbuffer_load_dwordx4 v230,s[40:43],s74 offen offset:1024 lds\n"
"s_add_u32 m0,s66,28672\ns_add_u32 s75,s67,256\nbuffer_load_dwordx4 v229,s[40:43],s75 offen offset:0 lds\ns_add_u32 s72,s67,6400\nbuffer_load_dwordx4 v230,s[40:43],s72 offen offset:1024 lds\ns_add_u32 m0,s64,12288\ns_mov_b32 s73,786432\nbuffer_load_dwordx4 v224,s[44:47],s73 offen offset:0 lds\n"
"s_mov_b32 s74,818176\nbuffer_load_dwordx4 v224,s[44:47],s74 offen offset:1024 lds\ns_mov_b32 s75,915456\nbuffer_load_dwordx4 v224,s[44:47],s75 offen offset:2048 lds\ns_mov_b32 s72,947200\nbuffer_load_dwordx4 v224,s[44:47],s72 offen offset:3072 lds\ns_add_u32 m0,s65,43008\ns_mov_b32 s73,384\n"
"buffer_load_dwordx4 v229,s[40:43],s73 offen offset:0 lds\ns_mov_b32 s74,6528\nbuffer_load_dwordx4 v230,s[40:43],s74 offen offset:1024 lds\ns_add_u32 m0,s66,43008\ns_add_u32 s75,s67,384\nbuffer_load_dwordx4 v229,s[40:43],s75 offen offset:0 lds\ns_add_u32 s72,s67,6528\nbuffer_load_dwordx4 v230,s[40:43],s72 offen offset:1024 lds\n"
"v_lshlrev_b32 v1,7,v0\ns_mul_i32 s72,%10,7168\nbuffer_load_dword v255,v1,s[56:59],s72 offen offset:0\ns_waitcnt vmcnt(26)\nds_read_b64_tr_b16 a[196:197],v231 offset:0\nds_read_b64_tr_b16 a[198:199],v231 offset:1024\nds_read_b64_tr_b16 a[200:201],v232 offset:0\nds_read_b64_tr_b16 a[202:203],v232 offset:1024\n"
"ds_read_b64_tr_b16 a[204:205],v233 offset:0\nds_read_b64_tr_b16 a[206:207],v233 offset:1024\nds_read_b64_tr_b16 a[208:209],v234 offset:0\nds_read_b64_tr_b16 a[210:211],v234 offset:1024\ns_barrier\nds_read_b128 a[228:231],v237 offset:0\nds_read_b128 a[232:235],v237 offset:2048\nds_read_b128 a[236:239],v237 offset:4096\n"
"ds_read_b128 a[240:243],v237 offset:6144\nds_read_b128 a[244:247],v237 offset:8192\nds_read_b128 a[248:251],v237 offset:10240\nds_read_b128 a[252:255],v237 offset:12288\ns_waitcnt lgkmcnt(6)\nv_mfma_f32_16x16x32_bf16 v[0:3],a[196:199],a[228:231],0\ns_waitcnt vmcnt(21)\nds_read_b64_tr_b16 a[212:213],v231 offset:4096\n"
"v_mfma_f32_16x16x32_bf16 v[28:31],a[200:203],a[228:231],0\ns_add_u32 m0,s64,16384\nv_lshl_add_u32 v252,s70,11,v239\nv_lshl_add_u32 v253,s70,11,v237\nv_lshl_add_u32 v254,s70,11,v238\nv_mfma_f32_16x16x32_bf16 v[56:59],a[204:207],a[228:231],0\nds_read_b64_tr_b16 a[214:215],v231 offset:5120\ns_mov_b32 s73,1048576\n"
"v_mfma_f32_16x16x32_bf16 v[84:87],a[208:211],a[228:231],0\nds_read_b128 a[228:231],v238 offset:0\ns_waitcnt lgkmcnt(8)\nv_mfma_f32_16x16x32_bf16 v[4:7],a[196:199],a[232:235],0\nbuffer_load_dwordx4 v224,s[44:47],s73 offen offset:0 lds\ns_mov_b32 s74,1080320\nv_mfma_f32_16x16x32_bf16 v[32:35],a[200:203],a[232:235],0\nds_read_b64_tr_b16 a[216:217],v232 offset:4096\n"
"v_mfma_f32_16x16x32_bf16 v[60:63],a[204:207],a[232:235],0\nv_mfma_f32_16x16x32_bf16 v[88:91],a[208:211],a[232:235],0\nds_read_b128 a[232:235],v238 offset:2048\ns_waitcnt lgkmcnt(9)\nv_mfma_f32_16x16x32_bf16 v[8:11],a[196:199],a[236:239],0\nbuffer_load_dwordx4 v224,s[44:47],s74 offen offset:1024 lds\ns_mov_b32 s75,1177600\nv_mfma_f32_16x16x32_bf16 v[36:39],a[200:203],a[236:239],0\n"
"ds_read_b64_tr_b16 a[218:219],v232 offset:5120\nv_mfma_f32_16x16x32_bf16 v[64:67],a[204:207],a[236:239],0\nv_mfma_f32_16x16x32_bf16 v[92:95],a[208:211],a[236:239],0\nds_read_b128 a[236:239],v238 offset:4096\ns_waitcnt lgkmcnt(9)\nv_mfma_f32_16x16x32_bf16 v[12:15],a[196:199],a[240:243],0\nbuffer_load_dwordx4 v224,s[44:47],s75 offen offset:2048 lds\ns_mov_b32 s72,1209344\n"
"v_mfma_f32_16x16x32_bf16 v[40:43],a[200:203],a[240:243],0\nds_read_b64_tr_b16 a[220:221],v233 offset:4096\nv_mfma_f32_16x16x32_bf16 v[68:71],a[204:207],a[240:243],0\nds_read_b64_tr_b16 a[222:223],v233 offset:5120\nv_mfma_f32_16x16x32_bf16 v[96:99],a[208:211],a[240:243],0\nds_read_b128 a[240:243],v238 offset:6144\nv_mfma_f32_16x16x32_bf16 v[16:19],a[196:199],a[244:247],0\nbuffer_load_dwordx4 v224,s[44:47],s72 offen offset:3072 lds\n"
"s_add_u32 m0,s64,0\nv_mfma_f32_16x16x32_bf16 v[44:47],a[200:203],a[244:247],0\nds_read_b64_tr_b16 a[224:225],v234 offset:4096\ns_mov_b32 s73,1310720\nv_mfma_f32_16x16x32_bf16 v[72:75],a[204:207],a[244:247],0\nv_mfma_f32_16x16x32_bf16 v[100:103],a[208:211],a[244:247],0\nds_read_b128 a[244:247],v238 offset:8192\ns_waitcnt lgkmcnt(12)\n"
"v_mfma_f32_16x16x32_bf16 v[20:23],a[196:199],a[248:251],0\nbuffer_load_dwordx4 v224,s[44:47],s73 offen offset:0 lds\ns_mov_b32 s74,1342464\nv_mfma_f32_16x16x32_bf16 v[48:51],a[200:203],a[248:251],0\nds_read_b64_tr_b16 a[226:227],v234 offset:5120\nv_mfma_f32_16x16x32_bf16 v[76:79],a[204:207],a[248:251],0\nv_mfma_f32_16x16x32_bf16 v[104:107],a[208:211],a[248:251],0\nds_read_b128 a[248:251],v238 offset:10240\n"
"v_mfma_f32_16x16x32_bf16 v[24:27],a[196:199],a[252:255],0\nbuffer_load_dwordx4 v224,s[44:47],s74 offen offset:1024 lds\ns_mov_b32 s75,1439744\nv_mfma_f32_16x16x32_bf16 v[52:55],a[200:203],a[252:255],0\nv_mfma_f32_16x16x32_bf16 v[80:83],a[204:207],a[252:255],0\nv_mfma_f32_16x16x32_bf16 v[108:111],a[208:211],a[252:255],0\nds_read_b128 a[252:255],v238 offset:12288\ns_waitcnt lgkmcnt(2)\n"
"v_mfma_f32_16x16x32_bf16 v[0:3],a[212:215],a[228:231],v[0:3]\nbuffer_load_dwordx4 v224,s[44:47],s75 offen offset:2048 lds\nv_mov_b32 v250,0xff800000\nv_mfma_f32_16x16x32_bf16 v[28:31],a[216:219],a[228:231],v[28:31]\ns_waitcnt vmcnt(20)\nds_read_b64_tr_b16 a[196:197],v231 offset:8192\nv_lshrrev_b32 v248,2,v239\nv_mfma_f32_16x16x32_bf16 v[56:59],a[220:223],a[228:231],v[56:59]\n"
"ds_read_b64_tr_b16 a[198:199],v231 offset:9216\nv_lshrrev_b32 v242,9,v239\nv_mfma_f32_16x16x32_bf16 v[84:87],a[224:227],a[228:231],v[84:87]\ns_waitcnt lgkmcnt(2)\ns_barrier\nds_read_b128 a[228:231],v237 offset:14336\nv_mfma_f32_16x16x32_bf16 v[4:7],a[212:215],a[232:235],v[4:7]\ns_mov_b32 s72,1471488\n"
"buffer_load_dwordx4 v224,s[44:47],s72 offen offset:3072 lds\nv_mfma_f32_16x16x32_bf16 v[32:35],a[216:219],a[232:235],v[32:35]\nds_read_b64_tr_b16 a[200:201],v232 offset:8192\nv_and_b32 v241,0xf0,v239\nv_lshl_or_b32 v241,v242,8,v241\nv_mfma_f32_16x16x32_bf16 v[60:63],a[220:223],a[232:235],v[60:63]\ns_add_u32 m0,s65,0\nv_bfe_u32 v242,v239,8,1\n"
"v_lshl_or_b32 v241,v242,3,v241\nv_mfma_f32_16x16x32_bf16 v[88:91],a[224:227],a[232:235],v[88:91]\nds_read_b128 a[232:235],v237 offset:16384\nv_mov_b32 v242,14336\nv_mad_u32_u24 v241,v242,s70,v241\nv_mfma_f32_16x16x32_bf16 v[8:11],a[212:215],a[236:239],v[8:11]\ns_mov_b32 s73,512\nbuffer_load_dwordx4 v229,s[40:43],s73 offen offset:0 lds\n"
"v_mfma_f32_16x16x32_bf16 v[36:39],a[216:219],a[236:239],v[36:39]\nds_read_b64_tr_b16 a[202:203],v232 offset:9216\ns_mov_b32 s74,6656\nv_mfma_f32_16x16x32_bf16 v[64:67],a[220:223],a[236:239],v[64:67]\nv_mfma_f32_16x16x32_bf16 v[92:95],a[224:227],a[236:239],v[92:95]\nds_read_b128 a[236:239],v237 offset:18432\nv_mfma_f32_16x16x32_bf16 v[12:15],a[212:215],a[240:243],v[12:15]\nbuffer_load_dwordx4 v230,s[40:43],s74 offen offset:1024 lds\n"
"s_add_u32 m0,s66,0\nv_mfma_f32_16x16x32_bf16 v[40:43],a[216:219],a[240:243],v[40:43]\nds_read_b64_tr_b16 a[204:205],v233 offset:8192\ns_add_u32 s75,s67,512\nv_mfma_f32_16x16x32_bf16 v[68:71],a[220:223],a[240:243],v[68:71]\nds_read_b64_tr_b16 a[206:207],v233 offset:9216\nv_mfma_f32_16x16x32_bf16 v[96:99],a[224:227],a[240:243],v[96:99]\nds_read_b128 a[240:243],v237 offset:20480\n"
"v_mfma_f32_16x16x32_bf16 v[16:19],a[212:215],a[244:247],v[16:19]\nbuffer_load_dwordx4 v229,s[40:43],s75 offen offset:0 lds\ns_add_u32 s72,s67,6656\nv_mfma_f32_16x16x32_bf16 v[44:47],a[216:219],a[244:247],v[44:47]\nds_read_b64_tr_b16 a[208:209],v234 offset:8192\nv_mfma_f32_16x16x32_bf16 v[72:75],a[220:223],a[244:247],v[72:75]\nv_mfma_f32_16x16x32_bf16 v[100:103],a[224:227],a[244:247],v[100:103]\nds_read_b128 a[244:247],v237 offset:22528\n"
"v_mfma_f32_16x16x32_bf16 v[20:23],a[212:215],a[248:251],v[20:23]\nbuffer_load_dwordx4 v230,s[40:43],s72 offen offset:1024 lds\ns_add_u32 m0,s64,4096\nv_mfma_f32_16x16x32_bf16 v[48:51],a[216:219],a[248:251],v[48:51]\nds_read_b64_tr_b16 a[210:211],v234 offset:9216\ns_mov_b32 s73,1572864\nv_mfma_f32_16x16x32_bf16 v[76:79],a[220:223],a[248:251],v[76:79]\nv_mfma_f32_16x16x32_bf16 v[104:107],a[224:227],a[248:251],v[104:107]\n"
"ds_read_b128 a[248:251],v237 offset:24576\nv_mfma_f32_16x16x32_bf16 v[24:27],a[212:215],a[252:255],v[24:27]\nbuffer_load_dwordx4 v224,s[44:47],s73 offen offset:0 lds\ns_mov_b32 s74,1604608\nv_mfma_f32_16x16x32_bf16 v[52:55],a[216:219],a[252:255],v[52:55]\nv_mfma_f32_16x16x32_bf16 v[80:83],a[220:223],a[252:255],v[80:83]\nv_mfma_f32_16x16x32_bf16 v[108:111],a[224:227],a[252:255],v[108:111]\nds_read_b128 a[252:255],v237 offset:26624\n"
"s_waitcnt lgkmcnt(2)\nv_mfma_f32_16x16x32_bf16 v[0:3],a[196:199],a[228:231],v[0:3]\nbuffer_load_dwordx4 v224,s[44:47],s74 offen offset:1024 lds\nv_lshrrev_b32 v244,2,v239\nv_mfma_f32_16x16x32_bf16 v[28:31],a[200:203],a[228:231],v[28:31]\ns_waitcnt vmcnt(19)\nds_read_b64_tr_b16 a[212:213],v231 offset:12288\nv_lshlrev_b32 v245,3,v239\n"
"v_mfma_f32_16x16x32_bf16 v[56:59],a[204:207],a[228:231],v[56:59]\nds_read_b64_tr_b16 a[214:215],v231 offset:13312\nv_lshl_add_u32 v244,%12,7,v244\nv_mfma_f32_16x16x32_bf16 v[84:87],a[208:211],a[228:231],v[84:87]\nds_read_b128 a[228:231],v238 offset:14336\nv_lshl_add_u32 v245,%12,2,v245\nv_lshrrev_b32 v246,7,v239\nv_mfma_f32_16x16x32_bf16 v[4:7],a[196:199],a[232:235],v[4:7]\n"
"s_mov_b32 s75,1701888\nbuffer_load_dwordx4 v224,s[44:47],s75 offen offset:2048 lds\nv_mfma_f32_16x16x32_bf16 v[32:35],a[200:203],a[232:235],v[32:35]\nds_read_b64_tr_b16 a[216:217],v232 offset:12288\nv_add_u32 v246,s70,v246\nv_lshl_add_u32 v244,v246,16,v244\nv_mfma_f32_16x16x32_bf16 v[60:63],a[204:207],a[232:235],v[60:63]\ns_mov_b32 s72,1733632\n"
"v_lshl_add_u32 v245,v246,16,v245\nv_mfma_f32_16x16x32_bf16 v[88:91],a[208:211],a[232:235],v[88:91]\nds_read_b128 a[232:235],v238 offset:16384\nbuffer_load_dword v244,v244,s[96:99],0 offen offset:1024 sc1\nv_mfma_f32_16x16x32_bf16 v[8:11],a[196:199],a[236:239],v[8:11]\nbuffer_load_dwordx4 v224,s[44:47],s72 offen offset:3072 lds\ns_add_u32 m0,s64,8192\nv_mfma_f32_16x16x32_bf16 v[36:39],a[200:203],a[236:239],v[36:39]\n"
"ds_read_b64_tr_b16 a[218:219],v232 offset:13312\ns_mov_b32 s73,1835008\nv_mfma_f32_16x16x32_bf16 v[64:67],a[204:207],a[236:239],v[64:67]\nv_mfma_f32_16x16x32_bf16 v[92:95],a[208:211],a[236:239],v[92:95]\nds_read_b128 a[236:239],v238 offset:18432\nv_mfma_f32_16x16x32_bf16 v[12:15],a[196:199],a[240:243],v[12:15]\nbuffer_load_dwordx4 v224,s[44:47],s73 offen offset:0 lds\ns_mov_b32 s74,1866752\n"
"v_mfma_f32_16x16x32_bf16 v[40:43],a[200:203],a[240:243],v[40:43]\nds_read_b64_tr_b16 a[220:221],v233 offset:12288\nv_mfma_f32_16x16x32_bf16 v[68:71],a[204:207],a[240:243],v[68:71]\nds_read_b64_tr_b16 a[222:223],v233 offset:13312\nv_mfma_f32_16x16x32_bf16 v[96:99],a[208:211],a[240:243],v[96:99]\nds_read_b128 a[240:243],v238 offset:20480\nv_mfma_f32_16x16x32_bf16 v[16:19],a[196:199],a[244:247],v[16:19]\nbuffer_load_dwordx4 v224,s[44:47],s74 offen offset:1024 lds\n"
"s_mov_b32 s75,1964032\nv_mfma_f32_16x16x32_bf16 v[44:47],a[200:203],a[244:247],v[44:47]\nds_read_b64_tr_b16 a[224:225],v234 offset:12288\nv_mfma_f32_16x16x32_bf16 v[72:75],a[204:207],a[244:247],v[72:75]\nv_mfma_f32_16x16x32_bf16 v[100:103],a[208:211],a[244:247],v[100:103]\nds_read_b128 a[244:247],v238 offset:22528\ns_waitcnt lgkmcnt(12)\nv_mfma_f32_16x16x32_bf16 v[20:23],a[196:199],a[248:251],v[20:23]\n"
"buffer_load_dwordx4 v224,s[44:47],s75 offen offset:2048 lds\ns_mov_b32 s72,1995776\nv_mfma_f32_16x16x32_bf16 v[48:51],a[200:203],a[248:251],v[48:51]\nds_read_b64_tr_b16 a[226:227],v234 offset:13312\nv_mfma_f32_16x16x32_bf16 v[76:79],a[204:207],a[248:251],v[76:79]\nv_mfma_f32_16x16x32_bf16 v[104:107],a[208:211],a[248:251],v[104:107]\nds_read_b128 a[248:251],v238 offset:24576\nv_mfma_f32_16x16x32_bf16 v[24:27],a[196:199],a[252:255],v[24:27]\n"
"buffer_load_dwordx4 v224,s[44:47],s72 offen offset:3072 lds\ns_add_u32 m0,s64,12288\nv_mfma_f32_16x16x32_bf16 v[52:55],a[200:203],a[252:255],v[52:55]\ns_mov_b32 s73,2097152\nv_mfma_f32_16x16x32_bf16 v[80:83],a[204:207],a[252:255],v[80:83]\nv_mfma_f32_16x16x32_bf16 v[108:111],a[208:211],a[252:255],v[108:111]\nds_read_b128 a[252:255],v238 offset:26624\ns_waitcnt lgkmcnt(2)\n"
"v_mfma_f32_16x16x32_bf16 v[0:3],a[212:215],a[228:231],v[0:3]\nbuffer_load_dwordx4 v224,s[44:47],s73 offen offset:0 lds\ns_mov_b32 s74,2128896\nv_mfma_f32_16x16x32_bf16 v[28:31],a[216:219],a[228:231],v[28:31]\ns_waitcnt vmcnt(18)\nds_read_b64_tr_b16 a[196:197],v231 offset:16384\nv_mfma_f32_16x16x32_bf16 v[56:59],a[220:223],a[228:231],v[56:59]\nds_read_b64_tr_b16 a[198:199],v231 offset:17408\n"
"v_mfma_f32_16x16x32_bf16 v[84:87],a[224:227],a[228:231],v[84:87]\ns_waitcnt lgkmcnt(2)\ns_barrier\nds_read_b128 a[228:231],v237 offset:28672\nv_mfma_f32_16x16x32_bf16 v[4:7],a[212:215],a[232:235],v[4:7]\nbuffer_load_dwordx4 v224,s[44:47],s74 offen offset:1024 lds\ns_add_u32 m0,s65,14336\nv_mfma_f32_16x16x32_bf16 v[32:35],a[216:219],a[232:235],v[32:35]\n"
"ds_read_b64_tr_b16 a[200:201],v232 offset:16384\ns_mov_b32 s75,640\nv_mfma_f32_16x16x32_bf16 v[60:63],a[220:223],a[232:235],v[60:63]\nv_mfma_f32_16x16x32_bf16 v[88:91],a[224:227],a[232:235],v[88:91]\nds_read_b128 a[232:235],v237 offset:30720\nv_mfma_f32_16x16x32_bf16 v[8:11],a[212:215],a[236:239],v[8:11]\nbuffer_load_dwordx4 v229,s[40:43],s75 offen offset:0 lds\ns_mov_b32 s72,6784\n"
"v_mfma_f32_16x16x32_bf16 v[36:39],a[216:219],a[236:239],v[36:39]\nds_read_b64_tr_b16 a[202:203],v232 offset:17408\nv_mfma_f32_16x16x32_bf16 v[64:67],a[220:223],a[236:239],v[64:67]\nv_mfma_f32_16x16x32_bf16 v[92:95],a[224:227],a[236:239],v[92:95]\nds_read_b128 a[236:239],v237 offset:32768\nv_mfma_f32_16x16x32_bf16 v[12:15],a[212:215],a[240:243],v[12:15]\nbuffer_load_dwordx4 v230,s[40:43],s72 offen offset:1024 lds\ns_add_u32 m0,s66,14336\n"
"v_mfma_f32_16x16x32_bf16 v[40:43],a[216:219],a[240:243],v[40:43]\nds_read_b64_tr_b16 a[204:205],v233 offset:16384\ns_add_u32 s73,s67,640\nv_mfma_f32_16x16x32_bf16 v[68:71],a[220:223],a[240:243],v[68:71]\nds_read_b64_tr_b16 a[206:207],v233 offset:17408\nv_mfma_f32_16x16x32_bf16 v[96:99],a[224:227],a[240:243],v[96:99]\nds_read_b128 a[240:243],v237 offset:34816\nv_mfma_f32_16x16x32_bf16 v[16:19],a[212:215],a[244:247],v[16:19]\n"
"buffer_load_dwordx4 v229,s[40:43],s73 offen offset:0 lds\ns_add_u32 s74,s67,6784\nv_mfma_f32_16x16x32_bf16 v[44:47],a[216:219],a[244:247],v[44:47]\nds_read_b64_tr_b16 a[208:209],v234 offset:16384\nv_mfma_f32_16x16x32_bf16 v[72:75],a[220:223],a[244:247],v[72:75]\nv_mfma_f32_16x16x32_bf16 v[100:103],a[224:227],a[244:247],v[100:103]\nds_read_b128 a[244:247],v237 offset:36864\nv_mfma_f32_16x16x32_bf16 v[20:23],a[212:215],a[248:251],v[20:23]\n"
"buffer_load_dwordx4 v230,s[40:43],s74 offen offset:1024 lds\ns_add_u32 m0,s64,12288\nv_mfma_f32_16x16x32_bf16 v[48:51],a[216:219],a[248:251],v[48:51]\nds_read_b64_tr_b16 a[210:211],v234 offset:17408\ns_mov_b32 s75,2226176\nv_mfma_f32_16x16x32_bf16 v[76:79],a[220:223],a[248:251],v[76:79]\nv_mfma_f32_16x16x32_bf16 v[104:107],a[224:227],a[248:251],v[104:107]\nds_read_b128 a[248:251],v237 offset:38912\n"
"v_mfma_f32_16x16x32_bf16 v[24:27],a[212:215],a[252:255],v[24:27]\nbuffer_load_dwordx4 v224,s[44:47],s75 offen offset:2048 lds\ns_mov_b32 s72,2257920\nv_mfma_f32_16x16x32_bf16 v[52:55],a[216:219],a[252:255],v[52:55]\nv_mfma_f32_16x16x32_bf16 v[80:83],a[220:223],a[252:255],v[80:83]\nv_mfma_f32_16x16x32_bf16 v[108:111],a[224:227],a[252:255],v[108:111]\nds_read_b128 a[252:255],v237 offset:40960\ns_waitcnt lgkmcnt(2)\n"
"v_mfma_f32_16x16x32_bf16 v[0:3],a[196:199],a[228:231],v[0:3]\nbuffer_load_dwordx4 v224,s[44:47],s72 offen offset:3072 lds\ns_add_u32 m0,s64,16384\nv_mfma_f32_16x16x32_bf16 v[28:31],a[200:203],a[228:231],v[28:31]\ns_waitcnt vmcnt(21)\nds_read_b64_tr_b16 a[212:213],v231 offset:0\nv_mfma_f32_16x16x32_bf16 v[56:59],a[204:207],a[228:231],v[56:59]\nds_read_b64_tr_b16 a[214:215],v231 offset:1024\n"
"s_mov_b32 s73,2359296\nv_mfma_f32_16x16x32_bf16 v[84:87],a[208:211],a[228:231],v[84:87]\nds_read_b128 a[228:231],v238 offset:28672\nv_mfma_f32_16x16x32_bf16 v[4:7],a[196:199],a[232:235],v[4:7]\nbuffer_load_dwordx4 v224,s[44:47],s73 offen offset:0 lds\ns_mov_b32 s74,2391040\nv_mfma_f32_16x16x32_bf16 v[32:35],a[200:203],a[232:235],v[32:35]\nds_read_b64_tr_b16 a[216:217],v232 offset:0\n"
"v_mfma_f32_16x16x32_bf16 v[60:63],a[204:207],a[232:235],v[60:63]\nv_mfma_f32_16x16x32_bf16 v[88:91],a[208:211],a[232:235],v[88:91]\nds_read_b128 a[232:235],v238 offset:30720\nv_mfma_f32_16x16x32_bf16 v[8:11],a[196:199],a[236:239],v[8:11]\nbuffer_load_dwordx4 v224,s[44:47],s74 offen offset:1024 lds\ns_mov_b32 s75,2488320\nv_mfma_f32_16x16x32_bf16 v[36:39],a[200:203],a[236:239],v[36:39]\nds_read_b64_tr_b16 a[218:219],v232 offset:1024\n"
"v_mfma_f32_16x16x32_bf16 v[64:67],a[204:207],a[236:239],v[64:67]\nv_mfma_f32_16x16x32_bf16 v[92:95],a[208:211],a[236:239],v[92:95]\nds_read_b128 a[236:239],v238 offset:32768\nv_mfma_f32_16x16x32_bf16 v[12:15],a[196:199],a[240:243],v[12:15]\nbuffer_load_dwordx4 v224,s[44:47],s75 offen offset:2048 lds\ns_mov_b32 s72,2520064\nv_mfma_f32_16x16x32_bf16 v[40:43],a[200:203],a[240:243],v[40:43]\nds_read_b64_tr_b16 a[220:221],v233 offset:0\n"
"v_mfma_f32_16x16x32_bf16 v[68:71],a[204:207],a[240:243],v[68:71]\nds_read_b64_tr_b16 a[222:223],v233 offset:1024\nv_mfma_f32_16x16x32_bf16 v[96:99],a[208:211],a[240:243],v[96:99]\nds_read_b128 a[240:243],v238 offset:34816\nv_mfma_f32_16x16x32_bf16 v[16:19],a[196:199],a[244:247],v[16:19]\nbuffer_load_dwordx4 v224,s[44:47],s72 offen offset:3072 lds\nv_mfma_f32_16x16x32_bf16 v[44:47],a[200:203],a[244:247],v[44:47]\nds_read_b64_tr_b16 a[224:225],v234 offset:0\n"
"v_mfma_f32_16x16x32_bf16 v[72:75],a[204:207],a[244:247],v[72:75]\nv_mfma_f32_16x16x32_bf16 v[100:103],a[208:211],a[244:247],v[100:103]\nds_read_b128 a[244:247],v238 offset:36864\ns_waitcnt lgkmcnt(12)\nv_mfma_f32_16x16x32_bf16 v[20:23],a[196:199],a[248:251],v[20:23]\nds_read_b64_tr_b16 a[226:227],v234 offset:1024\ns_add_u32 m0,s64,0\nv_mfma_f32_16x16x32_bf16 v[48:51],a[200:203],a[248:251],v[48:51]\n"
"s_mov_b32 s73,2621440\nv_mfma_f32_16x16x32_bf16 v[76:79],a[204:207],a[248:251],v[76:79]\nv_mfma_f32_16x16x32_bf16 v[104:107],a[208:211],a[248:251],v[104:107]\nds_read_b128 a[248:251],v238 offset:38912\nv_mfma_f32_16x16x32_bf16 v[24:27],a[196:199],a[252:255],v[24:27]\nv_mfma_f32_16x16x32_bf16 v[52:55],a[200:203],a[252:255],v[52:55]\nv_mfma_f32_16x16x32_bf16 v[80:83],a[204:207],a[252:255],v[80:83]\nv_mfma_f32_16x16x32_bf16 v[108:111],a[208:211],a[252:255],v[108:111]\n"
"ds_read_b128 a[252:255],v238 offset:40960\ns_waitcnt lgkmcnt(2)\nv_mfma_f32_16x16x32_bf16 v[0:3],a[212:215],a[228:231],v[0:3]\nbuffer_load_dwordx4 v224,s[44:47],s73 offen offset:0 lds\ns_mov_b32 s74,2653184\nv_mfma_f32_16x16x32_bf16 v[28:31],a[216:219],a[228:231],v[28:31]\ns_waitcnt vmcnt(17)\nds_read_b64_tr_b16 a[196:197],v231 offset:4096\n"
"v_mfma_f32_16x16x32_bf16 v[56:59],a[220:223],a[228:231],v[56:59]\nds_read_b64_tr_b16 a[198:199],v231 offset:5120\nv_mfma_f32_16x16x32_bf16 v[84:87],a[224:227],a[228:231],v[84:87]\ns_waitcnt lgkmcnt(2)\ns_barrier\nds_read_b128 a[228:231],v237 offset:43008\nv_mfma_f32_16x16x32_bf16 v[4:7],a[212:215],a[232:235],v[4:7]\nbuffer_load_dwordx4 v224,s[44:47],s74 offen offset:1024 lds\n"
"s_add_u32 m0,s65,28672\nv_mfma_f32_16x16x32_bf16 v[32:35],a[216:219],a[232:235],v[32:35]\nds_read_b64_tr_b16 a[200:201],v232 offset:4096\ns_mov_b32 s75,768\nv_mfma_f32_16x16x32_bf16 v[60:63],a[220:223],a[232:235],v[60:63]\nv_mfma_f32_16x16x32_bf16 v[88:91],a[224:227],a[232:235],v[88:91]\nds_read_b128 a[232:235],v237 offset:45056\nv_mfma_f32_16x16x32_bf16 v[8:11],a[212:215],a[236:239],v[8:11]\n"
"buffer_load_dwordx4 v229,s[40:43],s75 offen offset:0 lds\ns_mov_b32 s72,6912\nv_mfma_f32_16x16x32_bf16 v[36:39],a[216:219],a[236:239],v[36:39]\nds_read_b64_tr_b16 a[202:203],v232 offset:5120\nv_mfma_f32_16x16x32_bf16 v[64:67],a[220:223],a[236:239],v[64:67]\nv_mfma_f32_16x16x32_bf16 v[92:95],a[224:227],a[236:239],v[92:95]\nds_read_b128 a[236:239],v237 offset:47104\nv_mfma_f32_16x16x32_bf16 v[12:15],a[212:215],a[240:243],v[12:15]\n"
"buffer_load_dwordx4 v230,s[40:43],s72 offen offset:1024 lds\ns_add_u32 m0,s66,28672\nv_mfma_f32_16x16x32_bf16 v[40:43],a[216:219],a[240:243],v[40:43]\nds_read_b64_tr_b16 a[204:205],v233 offset:4096\ns_add_u32 s73,s67,768\nv_mfma_f32_16x16x32_bf16 v[68:71],a[220:223],a[240:243],v[68:71]\nds_read_b64_tr_b16 a[206:207],v233 offset:5120\nv_mfma_f32_16x16x32_bf16 v[96:99],a[224:227],a[240:243],v[96:99]\n"
"ds_read_b128 a[240:243],v237 offset:49152\nv_mfma_f32_16x16x32_bf16 v[16:19],a[212:215],a[244:247],v[16:19]\nbuffer_load_dwordx4 v229,s[40:43],s73 offen offset:0 lds\ns_add_u32 s74,s67,6912\nv_mfma_f32_16x16x32_bf16 v[44:47],a[216:219],a[244:247],v[44:47]\nds_read_b64_tr_b16 a[208:209],v234 offset:4096\nv_mfma_f32_16x16x32_bf16 v[72:75],a[220:223],a[244:247],v[72:75]\nv_mfma_f32_16x16x32_bf16 v[100:103],a[224:227],a[244:247],v[100:103]\n"
"ds_read_b128 a[244:247],v237 offset:51200\nv_mfma_f32_16x16x32_bf16 v[20:23],a[212:215],a[248:251],v[20:23]\nbuffer_load_dwordx4 v230,s[40:43],s74 offen offset:1024 lds\ns_add_u32 m0,s64,0\nv_mfma_f32_16x16x32_bf16 v[48:51],a[216:219],a[248:251],v[48:51]\nds_read_b64_tr_b16 a[210:211],v234 offset:5120\ns_mov_b32 s75,2750464\nv_mfma_f32_16x16x32_bf16 v[76:79],a[220:223],a[248:251],v[76:79]\n"
"v_mfma_f32_16x16x32_bf16 v[104:107],a[224:227],a[248:251],v[104:107]\nds_read_b128 a[248:251],v237 offset:53248\nv_mfma_f32_16x16x32_bf16 v[24:27],a[212:215],a[252:255],v[24:27]\nbuffer_load_dwordx4 v224,s[44:47],s75 offen offset:2048 lds\ns_mov_b32 s72,2782208\nv_mfma_f32_16x16x32_bf16 v[52:55],a[216:219],a[252:255],v[52:55]\nv_mfma_f32_16x16x32_bf16 v[80:83],a[220:223],a[252:255],v[80:83]\nv_mfma_f32_16x16x32_bf16 v[108:111],a[224:227],a[252:255],v[108:111]\n"
"ds_read_b128 a[252:255],v237 offset:55296\ns_waitcnt lgkmcnt(2)\nv_mfma_f32_16x16x32_bf16 v[0:3],a[196:199],a[228:231],v[0:3]\nbuffer_load_dwordx4 v224,s[44:47],s72 offen offset:3072 lds\ns_add_u32 m0,s64,4096\nv_mfma_f32_16x16x32_bf16 v[28:31],a[200:203],a[228:231],v[28:31]\ns_waitcnt vmcnt(20)\nds_read_b64_tr_b16 a[212:213],v231 offset:8192\n"
"v_mfma_f32_16x16x32_bf16 v[56:59],a[204:207],a[228:231],v[56:59]\nds_read_b64_tr_b16 a[214:215],v231 offset:9216\ns_mov_b32 s73,2883584\nv_mfma_f32_16x16x32_bf16 v[84:87],a[208:211],a[228:231],v[84:87]\nds_read_b128 a[228:231],v238 offset:43008\nv_mfma_f32_16x16x32_bf16 v[4:7],a[196:199],a[232:235],v[4:7]\nbuffer_load_dwordx4 v224,s[44:47],s73 offen offset:0 lds\ns_mov_b32 s74,2915328\n"
"v_mfma_f32_16x16x32_bf16 v[32:35],a[200:203],a[232:235],v[32:35]\nds_read_b64_tr_b16 a[216:217],v232 offset:8192\nv_mfma_f32_16x16x32_bf16 v[60:63],a[204:207],a[232:235],v[60:63]\nv_mfma_f32_16x16x32_bf16 v[88:91],a[208:211],a[232:235],v[88:91]\nds_read_b128 a[232:235],v238 offset:45056\nv_mfma_f32_16x16x32_bf16 v[8:11],a[196:199],a[236:239],v[8:11]\nbuffer_load_dwordx4 v224,s[44:47],s74 offen offset:1024 lds\ns_mov_b32 s75,3012608\n"
"v_mfma_f32_16x16x32_bf16 v[36:39],a[200:203],a[236:239],v[36:39]\nds_read_b64_tr_b16 a[218:219],v232 offset:9216\nv_mfma_f32_16x16x32_bf16 v[64:67],a[204:207],a[236:239],v[64:67]\nv_mfma_f32_16x16x32_bf16 v[92:95],a[208:211],a[236:239],v[92:95]\nds_read_b128 a[236:239],v238 offset:47104\nv_mfma_f32_16x16x32_bf16 v[12:15],a[196:199],a[240:243],v[12:15]\nbuffer_load_dwordx4 v224,s[44:47],s75 offen offset:2048 lds\ns_mov_b32 s72,3044352\n"
"v_mfma_f32_16x16x32_bf16 v[40:43],a[200:203],a[240:243],v[40:43]\nds_read_b64_tr_b16 a[220:221],v233 offset:8192\nv_mfma_f32_16x16x32_bf16 v[68:71],a[204:207],a[240:243],v[68:71]\nds_read_b64_tr_b16 a[222:223],v233 offset:9216\nv_mfma_f32_16x16x32_bf16 v[96:99],a[208:211],a[240:243],v[96:99]\nds_read_b128 a[240:243],v238 offset:49152\nv_mfma_f32_16x16x32_bf16 v[16:19],a[196:199],a[244:247],v[16:19]\nbuffer_load_dwordx4 v224,s[44:47],s72 offen offset:3072 lds\n"
"v_mfma_f32_16x16x32_bf16 v[44:47],a[200:203],a[244:247],v[44:47]\nds_read_b64_tr_b16 a[224:225],v234 offset:8192\nv_mfma_f32_16x16x32_bf16 v[72:75],a[204:207],a[244:247],v[72:75]\nv_mfma_f32_16x16x32_bf16 v[100:103],a[208:211],a[244:247],v[100:103]\nds_read_b128 a[244:247],v238 offset:51200\ns_waitcnt lgkmcnt(12)\nv_mfma_f32_16x16x32_bf16 v[20:23],a[196:199],a[248:251],v[20:23]\nds_read_b64_tr_b16 a[226:227],v234 offset:9216\n"
"s_add_u32 m0,s64,8192\nv_mfma_f32_16x16x32_bf16 v[48:51],a[200:203],a[248:251],v[48:51]\ns_mov_b32 s73,3145728\nv_mfma_f32_16x16x32_bf16 v[76:79],a[204:207],a[248:251],v[76:79]\nv_mfma_f32_16x16x32_bf16 v[104:107],a[208:211],a[248:251],v[104:107]\nds_read_b128 a[248:251],v238 offset:53248\nv_mfma_f32_16x16x32_bf16 v[24:27],a[196:199],a[252:255],v[24:27]\nv_mfma_f32_16x16x32_bf16 v[52:55],a[200:203],a[252:255],v[52:55]\n"
"v_mfma_f32_16x16x32_bf16 v[80:83],a[204:207],a[252:255],v[80:83]\nv_mfma_f32_16x16x32_bf16 v[108:111],a[208:211],a[252:255],v[108:111]\nds_read_b128 a[252:255],v238 offset:55296\ns_waitcnt lgkmcnt(2)\nv_mfma_f32_16x16x32_bf16 v[0:3],a[212:215],a[228:231],v[0:3]\nbuffer_load_dwordx4 v224,s[44:47],s73 offen offset:0 lds\ns_mov_b32 s74,3177472\nv_mfma_f32_16x16x32_bf16 v[28:31],a[216:219],a[228:231],v[28:31]\n"
"s_waitcnt vmcnt(17)\nds_read_b64_tr_b16 a[196:197],v231 offset:12288\nv_mfma_f32_16x16x32_bf16 v[56:59],a[220:223],a[228:231],v[56:59]\nds_read_b64_tr_b16 a[198:199],v231 offset:13312\nv_mfma_f32_16x16x32_bf16 v[84:87],a[224:227],a[228:231],v[84:87]\ns_waitcnt lgkmcnt(2)\ns_barrier\nds_read_b128 a[228:231],v237 offset:0\n"
"v_mfma_f32_16x16x32_bf16 v[4:7],a[212:215],a[232:235],v[4:7]\nbuffer_load_dwordx4 v224,s[44:47],s74 offen offset:1024 lds\ns_add_u32 m0,s65,147456\nv_mfma_f32_16x16x32_bf16 v[32:35],a[216:219],a[232:235],v[32:35]\nds_read_b64_tr_b16 a[200:201],v232 offset:12288\ns_mov_b32 s75,0\nv_mfma_f32_16x16x32_bf16 v[60:63],a[220:223],a[232:235],v[60:63]\nv_mfma_f32_16x16x32_bf16 v[88:91],a[224:227],a[232:235],v[88:91]\n"
"ds_read_b128 a[232:235],v237 offset:2048\nv_mfma_f32_16x16x32_bf16 v[8:11],a[212:215],a[236:239],v[8:11]\nbuffer_load_dwordx4 v229,s[40:43],s75 offen offset:0 lds\ns_mov_b32 s72,6144\nv_mfma_f32_16x16x32_bf16 v[36:39],a[216:219],a[236:239],v[36:39]\nds_read_b64_tr_b16 a[202:203],v232 offset:13312\nv_mfma_f32_16x16x32_bf16 v[64:67],a[220:223],a[236:239],v[64:67]\nv_mfma_f32_16x16x32_bf16 v[92:95],a[224:227],a[236:239],v[92:95]\n"
"ds_read_b128 a[236:239],v237 offset:4096\nv_mfma_f32_16x16x32_bf16 v[12:15],a[212:215],a[240:243],v[12:15]\nbuffer_load_dwordx4 v230,s[40:43],s72 offen offset:1024 lds\ns_add_u32 m0,s66,147456\nv_mfma_f32_16x16x32_bf16 v[40:43],a[216:219],a[240:243],v[40:43]\nds_read_b64_tr_b16 a[204:205],v233 offset:12288\ns_add_u32 s73,s67,0\nv_mfma_f32_16x16x32_bf16 v[68:71],a[220:223],a[240:243],v[68:71]\n"
"ds_read_b64_tr_b16 a[206:207],v233 offset:13312\nv_mfma_f32_16x16x32_bf16 v[96:99],a[224:227],a[240:243],v[96:99]\nds_read_b128 a[240:243],v237 offset:6144\nv_mfma_f32_16x16x32_bf16 v[16:19],a[212:215],a[244:247],v[16:19]\nbuffer_load_dwordx4 v229,s[40:43],s73 offen offset:0 lds\ns_add_u32 s74,s67,6144\nv_mfma_f32_16x16x32_bf16 v[44:47],a[216:219],a[244:247],v[44:47]\nds_read_b64_tr_b16 a[208:209],v234 offset:12288\n"
"v_mfma_f32_16x16x32_bf16 v[72:75],a[220:223],a[244:247],v[72:75]\nv_mfma_f32_16x16x32_bf16 v[100:103],a[224:227],a[244:247],v[100:103]\nds_read_b128 a[244:247],v237 offset:8192\nv_mfma_f32_16x16x32_bf16 v[20:23],a[212:215],a[248:251],v[20:23]\nbuffer_load_dwordx4 v230,s[40:43],s74 offen offset:1024 lds\ns_add_u32 m0,s64,8192\nv_mfma_f32_16x16x32_bf16 v[48:51],a[216:219],a[248:251],v[48:51]\nds_read_b64_tr_b16 a[210:211],v234 offset:13312\n"
"s_mov_b32 s75,3274752\nv_mfma_f32_16x16x32_bf16 v[76:79],a[220:223],a[248:251],v[76:79]\nv_mfma_f32_16x16x32_bf16 v[104:107],a[224:227],a[248:251],v[104:107]\nds_read_b128 a[248:251],v237 offset:10240\nv_mfma_f32_16x16x32_bf16 v[24:27],a[212:215],a[252:255],v[24:27]\nbuffer_load_dwordx4 v224,s[44:47],s75 offen offset:2048 lds\ns_mov_b32 s72,3306496\nv_mfma_f32_16x16x32_bf16 v[52:55],a[216:219],a[252:255],v[52:55]\n"
"v_mfma_f32_16x16x32_bf16 v[80:83],a[220:223],a[252:255],v[80:83]\nv_mfma_f32_16x16x32_bf16 v[108:111],a[224:227],a[252:255],v[108:111]\nds_read_b128 a[252:255],v237 offset:12288\ns_waitcnt lgkmcnt(2)\nv_mfma_f32_16x16x32_bf16 v[0:3],a[196:199],a[228:231],v[0:3]\nbuffer_load_dwordx4 v224,s[44:47],s72 offen offset:3072 lds\ns_add_u32 m0,s64,12288\nv_mfma_f32_16x16x32_bf16 v[28:31],a[200:203],a[228:231],v[28:31]\n"
"s_waitcnt vmcnt(20)\nds_read_b64_tr_b16 a[212:213],v231 offset:16384\nv_mfma_f32_16x16x32_bf16 v[56:59],a[204:207],a[228:231],v[56:59]\nds_read_b64_tr_b16 a[214:215],v231 offset:17408\ns_mov_b32 s73,3407872\nv_mfma_f32_16x16x32_bf16 v[84:87],a[208:211],a[228:231],v[84:87]\nds_read_b128 a[228:231],v238 offset:0\nv_mfma_f32_16x16x32_bf16 v[4:7],a[196:199],a[232:235],v[4:7]\n"
"buffer_load_dwordx4 v224,s[44:47],s73 offen offset:0 lds\ns_mov_b32 s74,3439616\nv_mfma_f32_16x16x32_bf16 v[32:35],a[200:203],a[232:235],v[32:35]\nds_read_b64_tr_b16 a[216:217],v232 offset:16384\nv_mfma_f32_16x16x32_bf16 v[60:63],a[204:207],a[232:235],v[60:63]\nv_mfma_f32_16x16x32_bf16 v[88:91],a[208:211],a[232:235],v[88:91]\nds_read_b128 a[232:235],v238 offset:2048\nv_mfma_f32_16x16x32_bf16 v[8:11],a[196:199],a[236:239],v[8:11]\n"
"buffer_load_dwordx4 v224,s[44:47],s74 offen offset:1024 lds\ns_mov_b32 s75,3536896\nv_mfma_f32_16x16x32_bf16 v[36:39],a[200:203],a[236:239],v[36:39]\nds_read_b64_tr_b16 a[218:219],v232 offset:17408\nv_mfma_f32_16x16x32_bf16 v[64:67],a[204:207],a[236:239],v[64:67]\nv_mfma_f32_16x16x32_bf16 v[92:95],a[208:211],a[236:239],v[92:95]\nds_read_b128 a[236:239],v238 offset:4096\nv_mfma_f32_16x16x32_bf16 v[12:15],a[196:199],a[240:243],v[12:15]\n"
"buffer_load_dwordx4 v224,s[44:47],s75 offen offset:2048 lds\ns_mov_b32 s72,3568640\nv_mfma_f32_16x16x32_bf16 v[40:43],a[200:203],a[240:243],v[40:43]\nds_read_b64_tr_b16 a[220:221],v233 offset:16384\nv_mfma_f32_16x16x32_bf16 v[68:71],a[204:207],a[240:243],v[68:71]\nds_read_b64_tr_b16 a[222:223],v233 offset:17408\nv_mfma_f32_16x16x32_bf16 v[96:99],a[208:211],a[240:243],v[96:99]\nds_read_b128 a[240:243],v238 offset:6144\n"
"v_mfma_f32_16x16x32_bf16 v[16:19],a[196:199],a[244:247],v[16:19]\nbuffer_load_dwordx4 v224,s[44:47],s72 offen offset:3072 lds\nv_mfma_f32_16x16x32_bf16 v[44:47],a[200:203],a[244:247],v[44:47]\nds_read_b64_tr_b16 a[224:225],v234 offset:16384\nv_mfma_f32_16x16x32_bf16 v[72:75],a[204:207],a[244:247],v[72:75]\nv_mfma_f32_16x16x32_bf16 v[100:103],a[208:211],a[244:247],v[100:103]\nds_read_b128 a[244:247],v238 offset:8192\ns_waitcnt lgkmcnt(12)\n"
"v_mfma_f32_16x16x32_bf16 v[20:23],a[196:199],a[248:251],v[20:23]\nds_read_b64_tr_b16 a[226:227],v234 offset:17408\ns_add_u32 m0,s64,16384\nv_mfma_f32_16x16x32_bf16 v[48:51],a[200:203],a[248:251],v[48:51]\ns_mov_b32 s73,0\nv_mfma_f32_16x16x32_bf16 v[76:79],a[204:207],a[248:251],v[76:79]\nv_mfma_f32_16x16x32_bf16 v[104:107],a[208:211],a[248:251],v[104:107]\nds_read_b128 a[248:251],v238 offset:10240\n"
"v_mfma_f32_16x16x32_bf16 v[24:27],a[196:199],a[252:255],v[24:27]\nv_mfma_f32_16x16x32_bf16 v[52:55],a[200:203],a[252:255],v[52:55]\nv_mfma_f32_16x16x32_bf16 v[80:83],a[204:207],a[252:255],v[80:83]\nv_mfma_f32_16x16x32_bf16 v[108:111],a[208:211],a[252:255],v[108:111]\nds_read_b128 a[252:255],v238 offset:12288\ns_waitcnt lgkmcnt(2)\nv_mfma_f32_16x16x32_bf16 v[0:3],a[212:215],a[228:231],v[0:3]\nbuffer_load_dwordx4 v227,s[48:51],s73 offen offset:0 nt lds\n"
"v_add_u32 v244,1,v244\nv_mfma_f32_16x16x32_bf16 v[28:31],a[216:219],a[228:231],v[28:31]\ns_waitcnt vmcnt(17)\nds_read_b64_tr_b16 a[196:197],v231 offset:0\nv_mfma_f32_16x16x32_bf16 v[56:59],a[220:223],a[228:231],v[56:59]\nds_read_b64_tr_b16 a[198:199],v231 offset:1024\ns_mov_b32 s74,7168\nv_mfma_f32_16x16x32_bf16 v[84:87],a[224:227],a[228:231],v[84:87]\n"
"s_waitcnt lgkmcnt(2)\ns_barrier\nds_read_b128 a[228:231],v237 offset:14336\nv_mfma_f32_16x16x32_bf16 v[4:7],a[212:215],a[232:235],v[4:7]\nbuffer_load_dwordx4 v228,s[48:51],s74 offen offset:1024 nt lds\ns_mov_b32 s75,30720\nv_mfma_f32_16x16x32_bf16 v[32:35],a[216:219],a[232:235],v[32:35]\nds_read_b64_tr_b16 a[200:201],v232 offset:0\n"
"v_mfma_f32_16x16x32_bf16 v[60:63],a[220:223],a[232:235],v[60:63]\nbuffer_store_dword v244,v245,s[96:99],0 offen sc1\nv_mfma_f32_16x16x32_bf16 v[88:91],a[224:227],a[232:235],v[88:91]\nds_read_b128 a[232:235],v237 offset:16384\nv_mfma_f32_16x16x32_bf16 v[8:11],a[212:215],a[236:239],v[8:11]\nbuffer_load_dwordx4 v227,s[48:51],s75 offen offset:2048 nt lds\ns_mov_b32 s72,37888\nv_mfma_f32_16x16x32_bf16 v[36:39],a[216:219],a[236:239],v[36:39]\n"
"ds_read_b64_tr_b16 a[202:203],v232 offset:1024\nv_mfma_f32_16x16x32_bf16 v[64:67],a[220:223],a[236:239],v[64:67]\nv_mfma_f32_16x16x32_bf16 v[92:95],a[224:227],a[236:239],v[92:95]\nds_read_b128 a[236:239],v237 offset:18432\nv_mfma_f32_16x16x32_bf16 v[12:15],a[212:215],a[240:243],v[12:15]\nbuffer_load_dwordx4 v228,s[48:51],s72 offen offset:3072 nt lds\nv_mfma_f32_16x16x32_bf16 v[40:43],a[216:219],a[240:243],v[40:43]\nds_read_b64_tr_b16 a[204:205],v233 offset:0\n"
"v_mfma_f32_16x16x32_bf16 v[68:71],a[220:223],a[240:243],v[68:71]\nds_read_b64_tr_b16 a[206:207],v233 offset:1024\nv_mfma_f32_16x16x32_bf16 v[96:99],a[224:227],a[240:243],v[96:99]\nds_read_b128 a[240:243],v237 offset:20480\nv_mfma_f32_16x16x32_bf16 v[16:19],a[212:215],a[244:247],v[16:19]\nds_read_b64_tr_b16 a[208:209],v234 offset:0\nv_mfma_f32_16x16x32_bf16 v[44:47],a[216:219],a[244:247],v[44:47]\nv_mfma_f32_16x16x32_bf16 v[72:75],a[220:223],a[244:247],v[72:75]\n"
"v_mfma_f32_16x16x32_bf16 v[100:103],a[224:227],a[244:247],v[100:103]\nds_read_b128 a[244:247],v237 offset:22528\nv_mfma_f32_16x16x32_bf16 v[20:23],a[212:215],a[248:251],v[20:23]\nds_read_b64_tr_b16 a[210:211],v234 offset:1024\ns_add_u32 m0,s64,0\nv_mfma_f32_16x16x32_bf16 v[48:51],a[216:219],a[248:251],v[48:51]\ns_mov_b32 s73,65536\nv_mfma_f32_16x16x32_bf16 v[76:79],a[220:223],a[248:251],v[76:79]\n"
"v_mfma_f32_16x16x32_bf16 v[104:107],a[224:227],a[248:251],v[104:107]\nds_read_b128 a[248:251],v237 offset:24576\nv_mfma_f32_16x16x32_bf16 v[24:27],a[212:215],a[252:255],v[24:27]\nv_mfma_f32_16x16x32_bf16 v[52:55],a[216:219],a[252:255],v[52:55]\nv_mfma_f32_16x16x32_bf16 v[80:83],a[220:223],a[252:255],v[80:83]\nv_mfma_f32_16x16x32_bf16 v[108:111],a[224:227],a[252:255],v[108:111]\nds_read_b128 a[252:255],v237 offset:26624\ns_waitcnt lgkmcnt(2)\n"
"v_mfma_f32_16x16x32_bf16 v[0:3],a[196:199],a[228:231],v[0:3]\nbuffer_load_dwordx4 v227,s[48:51],s73 offen offset:0 nt lds\ns_mov_b32 s74,72704\nv_mfma_f32_16x16x32_bf16 v[28:31],a[200:203],a[228:231],v[28:31]\ns_waitcnt vmcnt(18)\nds_read_b64_tr_b16 a[212:213],v231 offset:4096\nv_mfma_f32_16x16x32_bf16 v[56:59],a[204:207],a[228:231],v[56:59]\nds_read_b64_tr_b16 a[214:215],v231 offset:5120\n"
"v_mfma_f32_16x16x32_bf16 v[84:87],a[208:211],a[228:231],v[84:87]\nds_read_b128 a[228:231],v238 offset:14336\nv_mfma_f32_16x16x32_bf16 v[4:7],a[196:199],a[232:235],v[4:7]\nbuffer_load_dwordx4 v228,s[48:51],s74 offen offset:1024 nt lds\ns_mov_b32 s75,96256\nv_mfma_f32_16x16x32_bf16 v[32:35],a[200:203],a[232:235],v[32:35]\nds_read_b64_tr_b16 a[216:217],v232 offset:4096\nv_mfma_f32_16x16x32_bf16 v[60:63],a[204:207],a[232:235],v[60:63]\n"
"v_mfma_f32_16x16x32_bf16 v[88:91],a[208:211],a[232:235],v[88:91]\nds_read_b128 a[232:235],v238 offset:16384\nv_mfma_f32_16x16x32_bf16 v[8:11],a[196:199],a[236:239],v[8:11]\nbuffer_load_dwordx4 v227,s[48:51],s75 offen offset:2048 nt lds\ns_mov_b32 s72,103424\nv_mfma_f32_16x16x32_bf16 v[36:39],a[200:203],a[236:239],v[36:39]\nds_read_b64_tr_b16 a[218:219],v232 offset:5120\nv_mfma_f32_16x16x32_bf16 v[64:67],a[204:207],a[236:239],v[64:67]\n"
"v_mfma_f32_16x16x32_bf16 v[92:95],a[208:211],a[236:239],v[92:95]\nds_read_b128 a[236:239],v238 offset:18432\nv_mfma_f32_16x16x32_bf16 v[12:15],a[196:199],a[240:243],v[12:15]\nbuffer_load_dwordx4 v228,s[48:51],s72 offen offset:3072 nt lds\nv_mfma_f32_16x16x32_bf16 v[40:43],a[200:203],a[240:243],v[40:43]\nds_read_b64_tr_b16 a[220:221],v233 offset:4096\nv_mfma_f32_16x16x32_bf16 v[68:71],a[204:207],a[240:243],v[68:71]\nds_read_b64_tr_b16 a[222:223],v233 offset:5120\n"
"v_mfma_f32_16x16x32_bf16 v[96:99],a[208:211],a[240:243],v[96:99]\nds_read_b128 a[240:243],v238 offset:20480\nv_mfma_f32_16x16x32_bf16 v[16:19],a[196:199],a[244:247],v[16:19]\nds_read_b64_tr_b16 a[224:225],v234 offset:4096\nv_mfma_f32_16x16x32_bf16 v[44:47],a[200:203],a[244:247],v[44:47]\nv_mfma_f32_16x16x32_bf16 v[72:75],a[204:207],a[244:247],v[72:75]\nv_mfma_f32_16x16x32_bf16 v[100:103],a[208:211],a[244:247],v[100:103]\nds_read_b128 a[244:247],v238 offset:22528\n"
"s_waitcnt lgkmcnt(12)\nv_mfma_f32_16x16x32_bf16 v[20:23],a[196:199],a[248:251],v[20:23]\nds_read_b64_tr_b16 a[226:227],v234 offset:5120\ns_add_u32 m0,s64,4096\nv_mfma_f32_16x16x32_bf16 v[48:51],a[200:203],a[248:251],v[48:51]\ns_mov_b32 s73,512\nv_mfma_f32_16x16x32_bf16 v[76:79],a[204:207],a[248:251],v[76:79]\nv_mfma_f32_16x16x32_bf16 v[104:107],a[208:211],a[248:251],v[104:107]\n"
"ds_read_b128 a[248:251],v238 offset:24576\nv_mfma_f32_16x16x32_bf16 v[24:27],a[196:199],a[252:255],v[24:27]\nv_mfma_f32_16x16x32_bf16 v[52:55],a[200:203],a[252:255],v[52:55]\nv_mfma_f32_16x16x32_bf16 v[80:83],a[204:207],a[252:255],v[80:83]\nv_mfma_f32_16x16x32_bf16 v[108:111],a[208:211],a[252:255],v[108:111]\nds_read_b128 a[252:255],v238 offset:26624\ns_waitcnt lgkmcnt(2)\nv_mfma_f32_16x16x32_bf16 v[0:3],a[212:215],a[228:231],v[0:3]\n"
"buffer_load_dwordx4 v224,s[44:47],s73 offen offset:0 lds\ns_mov_b32 s74,32256\nv_mfma_f32_16x16x32_bf16 v[28:31],a[216:219],a[228:231],v[28:31]\ns_waitcnt vmcnt(14)\nds_read_b64_tr_b16 a[196:197],v231 offset:8192\nv_mfma_f32_16x16x32_bf16 v[56:59],a[220:223],a[228:231],v[56:59]\nds_read_b64_tr_b16 a[198:199],v231 offset:9216\nv_mfma_f32_16x16x32_bf16 v[84:87],a[224:227],a[228:231],v[84:87]\n"
"s_waitcnt lgkmcnt(2)\ns_barrier\nds_read_b128 a[228:231],v237 offset:28672\nv_mfma_f32_16x16x32_bf16 v[4:7],a[212:215],a[232:235],v[4:7]\nbuffer_load_dwordx4 v224,s[44:47],s74 offen offset:1024 lds\ns_mov_b32 s75,129536\nv_mfma_f32_16x16x32_bf16 v[32:35],a[216:219],a[232:235],v[32:35]\nds_read_b64_tr_b16 a[200:201],v232 offset:8192\n"
"v_mfma_f32_16x16x32_bf16 v[60:63],a[220:223],a[232:235],v[60:63]\nv_mfma_f32_16x16x32_bf16 v[88:91],a[224:227],a[232:235],v[88:91]\nds_read_b128 a[232:235],v237 offset:30720\nv_mfma_f32_16x16x32_bf16 v[8:11],a[212:215],a[236:239],v[8:11]\nbuffer_load_dwordx4 v224,s[44:47],s75 offen offset:2048 lds\ns_mov_b32 s72,161280\nv_mfma_f32_16x16x32_bf16 v[36:39],a[216:219],a[236:239],v[36:39]\nds_read_b64_tr_b16 a[202:203],v232 offset:9216\n"
"v_mfma_f32_16x16x32_bf16 v[64:67],a[220:223],a[236:239],v[64:67]\nv_mfma_f32_16x16x32_bf16 v[92:95],a[224:227],a[236:239],v[92:95]\nds_read_b128 a[236:239],v237 offset:32768\nv_mfma_f32_16x16x32_bf16 v[12:15],a[212:215],a[240:243],v[12:15]\nbuffer_load_dwordx4 v224,s[44:47],s72 offen offset:3072 lds\nv_mfma_f32_16x16x32_bf16 v[40:43],a[216:219],a[240:243],v[40:43]\nds_read_b64_tr_b16 a[204:205],v233 offset:8192\nv_mfma_f32_16x16x32_bf16 v[68:71],a[220:223],a[240:243],v[68:71]\n"
"ds_read_b64_tr_b16 a[206:207],v233 offset:9216\nv_mfma_f32_16x16x32_bf16 v[96:99],a[224:227],a[240:243],v[96:99]\nds_read_b128 a[240:243],v237 offset:34816\nv_mfma_f32_16x16x32_bf16 v[16:19],a[212:215],a[244:247],v[16:19]\nds_read_b64_tr_b16 a[208:209],v234 offset:8192\nv_mfma_f32_16x16x32_bf16 v[44:47],a[216:219],a[244:247],v[44:47]\nv_mfma_f32_16x16x32_bf16 v[72:75],a[220:223],a[244:247],v[72:75]\nv_mfma_f32_16x16x32_bf16 v[100:103],a[224:227],a[244:247],v[100:103]\n"
"ds_read_b128 a[244:247],v237 offset:36864\nv_mfma_f32_16x16x32_bf16 v[20:23],a[212:215],a[248:251],v[20:23]\nds_read_b64_tr_b16 a[210:211],v234 offset:9216\ns_add_u32 m0,s64,8192\nv_mfma_f32_16x16x32_bf16 v[48:51],a[216:219],a[248:251],v[48:51]\ns_mov_b32 s73,262656\nv_mfma_f32_16x16x32_bf16 v[76:79],a[220:223],a[248:251],v[76:79]\nv_mfma_f32_16x16x32_bf16 v[104:107],a[224:227],a[248:251],v[104:107]\n"
"ds_read_b128 a[248:251],v237 offset:38912\nv_mfma_f32_16x16x32_bf16 v[24:27],a[212:215],a[252:255],v[24:27]\nv_mfma_f32_16x16x32_bf16 v[52:55],a[216:219],a[252:255],v[52:55]\nv_mfma_f32_16x16x32_bf16 v[80:83],a[220:223],a[252:255],v[80:83]\nv_mfma_f32_16x16x32_bf16 v[108:111],a[224:227],a[252:255],v[108:111]\nds_read_b128 a[252:255],v237 offset:40960\ns_waitcnt lgkmcnt(2)\nv_mfma_f32_16x16x32_bf16 v[0:3],a[196:199],a[228:231],v[0:3]\n"
"buffer_load_dwordx4 v224,s[44:47],s73 offen offset:0 lds\ns_mov_b32 s74,294400\nv_mfma_f32_16x16x32_bf16 v[28:31],a[200:203],a[228:231],v[28:31]\ns_waitcnt vmcnt(14)\nds_read_b64_tr_b16 a[212:213],v231 offset:12288\nv_mfma_f32_16x16x32_bf16 v[56:59],a[204:207],a[228:231],v[56:59]\nds_read_b64_tr_b16 a[214:215],v231 offset:13312\nv_mfma_f32_16x16x32_bf16 v[84:87],a[208:211],a[228:231],v[84:87]\n"
"ds_read_b128 a[228:231],v238 offset:28672\nv_mfma_f32_16x16x32_bf16 v[4:7],a[196:199],a[232:235],v[4:7]\nbuffer_load_dwordx4 v224,s[44:47],s74 offen offset:1024 lds\ns_mov_b32 s75,391680\nv_mfma_f32_16x16x32_bf16 v[32:35],a[200:203],a[232:235],v[32:35]\nds_read_b64_tr_b16 a[216:217],v232 offset:12288\nv_mfma_f32_16x16x32_bf16 v[60:63],a[204:207],a[232:235],v[60:63]\nv_mfma_f32_16x16x32_bf16 v[88:91],a[208:211],a[232:235],v[88:91]\n"
"ds_read_b128 a[232:235],v238 offset:30720\nv_mfma_f32_16x16x32_bf16 v[8:11],a[196:199],a[236:239],v[8:11]\nbuffer_load_dwordx4 v224,s[44:47],s75 offen offset:2048 lds\ns_mov_b32 s72,423424\nv_mfma_f32_16x16x32_bf16 v[36:39],a[200:203],a[236:239],v[36:39]\nds_read_b64_tr_b16 a[218:219],v232 offset:13312\nv_mfma_f32_16x16x32_bf16 v[64:67],a[204:207],a[236:239],v[64:67]\nv_mfma_f32_16x16x32_bf16 v[92:95],a[208:211],a[236:239],v[92:95]\n"
"ds_read_b128 a[236:239],v238 offset:32768\nv_mfma_f32_16x16x32_bf16 v[12:15],a[196:199],a[240:243],v[12:15]\nbuffer_load_dwordx4 v224,s[44:47],s72 offen offset:3072 lds\nv_mfma_f32_16x16x32_bf16 v[40:43],a[200:203],a[240:243],v[40:43]\nds_read_b64_tr_b16 a[220:221],v233 offset:12288\nv_mfma_f32_16x16x32_bf16 v[68:71],a[204:207],a[240:243],v[68:71]\nds_read_b64_tr_b16 a[222:223],v233 offset:13312\nv_mfma_f32_16x16x32_bf16 v[96:99],a[208:211],a[240:243],v[96:99]\n"
"ds_read_b128 a[240:243],v238 offset:34816\nv_mfma_f32_16x16x32_bf16 v[16:19],a[196:199],a[244:247],v[16:19]\nds_read_b64_tr_b16 a[224:225],v234 offset:12288\nv_mfma_f32_16x16x32_bf16 v[44:47],a[200:203],a[244:247],v[44:47]\nv_mfma_f32_16x16x32_bf16 v[72:75],a[204:207],a[244:247],v[72:75]\nv_mfma_f32_16x16x32_bf16 v[100:103],a[208:211],a[244:247],v[100:103]\nds_read_b128 a[244:247],v238 offset:36864\ns_waitcnt lgkmcnt(12)\n"
"v_mfma_f32_16x16x32_bf16 v[20:23],a[196:199],a[248:251],v[20:23]\nds_read_b64_tr_b16 a[226:227],v234 offset:13312\ns_add_u32 m0,s64,12288\nv_mfma_f32_16x16x32_bf16 v[48:51],a[200:203],a[248:251],v[48:51]\ns_mov_b32 s73,128\nv_mfma_f32_16x16x32_bf16 v[76:79],a[204:207],a[248:251],v[76:79]\nv_mfma_f32_16x16x32_bf16 v[104:107],a[208:211],a[248:251],v[104:107]\nds_read_b128 a[248:251],v238 offset:38912\n"
"v_mfma_f32_16x16x32_bf16 v[24:27],a[196:199],a[252:255],v[24:27]\nv_mfma_f32_16x16x32_bf16 v[52:55],a[200:203],a[252:255],v[52:55]\nv_mfma_f32_16x16x32_bf16 v[80:83],a[204:207],a[252:255],v[80:83]\nv_mfma_f32_16x16x32_bf16 v[108:111],a[208:211],a[252:255],v[108:111]\nds_read_b128 a[252:255],v238 offset:40960\ns_waitcnt lgkmcnt(2)\nv_mfma_f32_16x16x32_bf16 v[0:3],a[212:215],a[228:231],v[0:3]\nbuffer_load_dwordx4 v227,s[48:51],s73 offen offset:0 nt lds\n"
"s_mov_b32 s74,7296\nv_mfma_f32_16x16x32_bf16 v[28:31],a[216:219],a[228:231],v[28:31]\ns_waitcnt vmcnt(13)\nds_read_b128 a[196:199],v235 offset:16384\nv_mfma_f32_16x16x32_bf16 v[56:59],a[220:223],a[228:231],v[56:59]\nv_mfma_f32_16x16x32_bf16 v[84:87],a[224:227],a[228:231],v[84:87]\ns_waitcnt lgkmcnt(0)\ns_barrier\n"
"v_mfma_f32_16x16x32_bf16 v[4:7],a[212:215],a[232:235],v[4:7]\nbuffer_load_dwordx4 v228,s[48:51],s74 offen offset:1024 nt lds\ns_mov_b32 s75,30848\nv_mfma_f32_16x16x32_bf16 v[32:35],a[216:219],a[232:235],v[32:35]\nds_read_b128 a[200:203],v235 offset:18432\nv_mfma_f32_16x16x32_bf16 v[60:63],a[220:223],a[232:235],v[60:63]\nds_write_b32 v248,v250 offset:57344\nv_mfma_f32_16x16x32_bf16 v[88:91],a[224:227],a[232:235],v[88:91]\n"
"v_mfma_f32_16x16x32_bf16 v[8:11],a[212:215],a[236:239],v[8:11]\nbuffer_load_dwordx4 v227,s[48:51],s75 offen offset:2048 nt lds\nv_cvt_pk_bf16_f32 v0,v0,v1\nv_mfma_f32_16x16x32_bf16 v[36:39],a[216:219],a[236:239],v[36:39]\ns_mov_b32 s72,38016\nds_write_b32 v248,v250 offset:57600\nv_mfma_f32_16x16x32_bf16 v[64:67],a[220:223],a[236:239],v[64:67]\nv_cvt_pk_bf16_f32 v1,v2,v3\n"
"v_cvt_pk_bf16_f32 v28,v28,v29\nv_mfma_f32_16x16x32_bf16 v[92:95],a[224:227],a[236:239],v[92:95]\nds_write_b64 v241,v[0:1] offset:0\nv_mfma_f32_16x16x32_bf16 v[12:15],a[212:215],a[240:243],v[12:15]\nbuffer_load_dwordx4 v228,s[48:51],s72 offen offset:3072 nt lds\nv_cvt_pk_bf16_f32 v29,v30,v31\nv_mfma_f32_16x16x32_bf16 v[40:43],a[216:219],a[240:243],v[40:43]\ns_waitcnt vmcnt(12)\n"
"ds_read_b128 a[204:207],v235 offset:0\nv_cvt_pk_bf16_f32 v56,v56,v57\nv_mfma_f32_16x16x32_bf16 v[68:71],a[220:223],a[240:243],v[68:71]\nds_write_b64 v241,v[28:29] offset:512\nv_mfma_f32_16x16x32_bf16 v[96:99],a[224:227],a[240:243],v[96:99]\nv_cvt_pk_bf16_f32 v57,v58,v59\nv_cvt_pk_bf16_f32 v84,v84,v85\nv_cvt_pk_bf16_f32 v85,v86,v87\n"
"v_mfma_f32_16x16x32_bf16 v[16:19],a[212:215],a[244:247],v[16:19]\nds_read_b128 a[208:211],v235 offset:2048\nv_cvt_pk_bf16_f32 v4,v4,v5\nv_mfma_f32_16x16x32_bf16 v[44:47],a[216:219],a[244:247],v[44:47]\nds_write_b64 v241,v[56:57] offset:1024\nv_mfma_f32_16x16x32_bf16 v[72:75],a[220:223],a[244:247],v[72:75]\nv_cvt_pk_bf16_f32 v5,v6,v7\nv_cvt_pk_bf16_f32 v32,v32,v33\n"
"v_mfma_f32_16x16x32_bf16 v[100:103],a[224:227],a[244:247],v[100:103]\nds_write_b64 v241,v[84:85] offset:1536\nv_mfma_f32_16x16x32_bf16 v[20:23],a[212:215],a[248:251],v[20:23]\nv_cvt_pk_bf16_f32 v33,v34,v35\nv_cvt_pk_bf16_f32 v60,v60,v61\nv_mfma_f32_16x16x32_bf16 v[48:51],a[216:219],a[248:251],v[48:51]\nds_write_b64 v241,v[4:5] offset:2048\nv_mfma_f32_16x16x32_bf16 v[76:79],a[220:223],a[248:251],v[76:79]\n"
"v_cvt_pk_bf16_f32 v61,v62,v63\nv_cvt_pk_bf16_f32 v88,v88,v89\nv_mfma_f32_16x16x32_bf16 v[104:107],a[224:227],a[248:251],v[104:107]\nds_write_b64 v241,v[32:33] offset:2560\nv_mfma_f32_16x16x32_bf16 v[24:27],a[212:215],a[252:255],v[24:27]\nv_cvt_pk_bf16_f32 v89,v90,v91\nv_cvt_pk_bf16_f32 v8,v8,v9\nv_mfma_f32_16x16x32_bf16 v[52:55],a[216:219],a[252:255],v[52:55]\n"
"ds_write_b64 v241,v[60:61] offset:3072\nv_mfma_f32_16x16x32_bf16 v[80:83],a[220:223],a[252:255],v[80:83]\nv_cvt_pk_bf16_f32 v9,v10,v11\nv_cvt_pk_bf16_f32 v36,v36,v37\nv_mfma_f32_16x16x32_bf16 v[108:111],a[224:227],a[252:255],v[108:111]\nds_write_b64 v241,v[88:89] offset:3584\nds_write_b64 v241,v[8:9] offset:4096\nv_cvt_pk_bf16_f32 v37,v38,v39\n"
"ds_write_b64 v241,v[36:37] offset:4608\nv_cvt_pk_bf16_f32 v64,v64,v65\nv_cvt_pk_bf16_f32 v65,v66,v67\nds_write_b64 v241,v[64:65] offset:5120\nv_cvt_pk_bf16_f32 v92,v92,v93\nv_cvt_pk_bf16_f32 v93,v94,v95\nds_write_b64 v241,v[92:93] offset:5632\nv_cvt_pk_bf16_f32 v12,v12,v13\n"
"v_cvt_pk_bf16_f32 v13,v14,v15\nds_write_b64 v241,v[12:13] offset:6144\nv_cvt_pk_bf16_f32 v40,v40,v41\nv_cvt_pk_bf16_f32 v41,v42,v43\nds_write_b64 v241,v[40:41] offset:6656\nv_cvt_pk_bf16_f32 v68,v68,v69\nv_cvt_pk_bf16_f32 v69,v70,v71\nds_write_b64 v241,v[68:69] offset:7168\n"
"v_cvt_pk_bf16_f32 v96,v96,v97\nv_cvt_pk_bf16_f32 v97,v98,v99\nds_write_b64 v241,v[96:97] offset:7680\nv_cvt_pk_bf16_f32 v16,v16,v17\nv_cvt_pk_bf16_f32 v17,v18,v19\nds_write_b64 v241,v[16:17] offset:8192\nv_cvt_pk_bf16_f32 v44,v44,v45\nv_cvt_pk_bf16_f32 v45,v46,v47\n"
"ds_write_b64 v241,v[44:45] offset:8704\nv_cvt_pk_bf16_f32 v72,v72,v73\nv_cvt_pk_bf16_f32 v73,v74,v75\nds_write_b64 v241,v[72:73] offset:9216\nv_cvt_pk_bf16_f32 v100,v100,v101\nv_cvt_pk_bf16_f32 v101,v102,v103\nds_write_b64 v241,v[100:101] offset:9728\nv_cvt_pk_bf16_f32 v20,v20,v21\n"
"v_cvt_pk_bf16_f32 v21,v22,v23\nds_write_b64 v241,v[20:21] offset:10240\nv_cvt_pk_bf16_f32 v48,v48,v49\nv_cvt_pk_bf16_f32 v49,v50,v51\nds_write_b64 v241,v[48:49] offset:10752\nv_cvt_pk_bf16_f32 v76,v76,v77\nv_cvt_pk_bf16_f32 v77,v78,v79\nds_write_b64 v241,v[76:77] offset:11264\n"
"v_cvt_pk_bf16_f32 v104,v104,v105\nv_cvt_pk_bf16_f32 v105,v106,v107\nds_write_b64 v241,v[104:105] offset:11776\nv_cvt_pk_bf16_f32 v24,v24,v25\nv_cvt_pk_bf16_f32 v25,v26,v27\nds_write_b64 v241,v[24:25] offset:12288\nv_cvt_pk_bf16_f32 v52,v52,v53\nv_cvt_pk_bf16_f32 v53,v54,v55\n"
"ds_write_b64 v241,v[52:53] offset:12800\nv_cvt_pk_bf16_f32 v80,v80,v81\nv_cvt_pk_bf16_f32 v81,v82,v83\nds_write_b64 v241,v[80:81] offset:13312\nv_cvt_pk_bf16_f32 v108,v108,v109\nv_cvt_pk_bf16_f32 v109,v110,v111\nds_write_b64 v241,v[108:109] offset:13824\ns_waitcnt lgkmcnt(0)\n"
"s_barrier\nds_read_b128 a[228:231],v239 offset:0\nds_read_b128 a[232:235],v239 offset:2048\nds_read_b128 a[236:239],v239 offset:4096\nds_read_b128 a[240:243],v239 offset:6144\nds_read_b128 a[244:247],v239 offset:8192\nds_read_b128 a[248:251],v239 offset:10240\nds_read_b128 a[252:255],v239 offset:12288\n"
"s_waitcnt lgkmcnt(6)\nv_mfma_f32_16x16x32_bf16 v[0:3],a[196:199],a[228:231],0\nds_read_b128 a[212:215],v236 offset:16384\nv_add_u32 v237,147456,v237\nv_mfma_f32_16x16x32_bf16 v[28:31],a[200:203],a[228:231],0\nv_add_u32 v238,147456,v238\nv_mfma_f32_16x16x32_bf16 v[56:59],a[204:207],a[228:231],0\nv_mfma_f32_16x16x32_bf16 v[84:87],a[208:211],a[228:231],0\n"
"ds_read_b128 a[228:231],v239 offset:1024\ns_waitcnt lgkmcnt(7)\nv_mfma_f32_16x16x32_bf16 v[4:7],a[196:199],a[232:235],0\nds_read_b128 a[216:219],v236 offset:18432\ns_add_u32 m0,s64,16384\nv_mfma_f32_16x16x32_bf16 v[32:35],a[200:203],a[232:235],0\ns_mov_b32 s73,65664\nv_mfma_f32_16x16x32_bf16 v[60:63],a[204:207],a[232:235],0\n"
"v_mfma_f32_16x16x32_bf16 v[88:91],a[208:211],a[232:235],0\nds_read_b128 a[232:235],v239 offset:3072\ns_waitcnt lgkmcnt(8)\nv_mfma_f32_16x16x32_bf16 v[8:11],a[196:199],a[236:239],0\nv_mfma_f32_16x16x32_bf16 v[36:39],a[200:203],a[236:239],0\nv_mfma_f32_16x16x32_bf16 v[64:67],a[204:207],a[236:239],0\nv_mfma_f32_16x16x32_bf16 v[92:95],a[208:211],a[236:239],0\nds_read_b128 a[236:239],v239 offset:5120\n"
"s_waitcnt lgkmcnt(7)\nv_mfma_f32_16x16x32_bf16 v[12:15],a[196:199],a[240:243],0\nds_read_b128 a[220:223],v236 offset:0\nv_mfma_f32_16x16x32_bf16 v[40:43],a[200:203],a[240:243],0\nv_mfma_f32_16x16x32_bf16 v[68:71],a[204:207],a[240:243],0\nv_mfma_f32_16x16x32_bf16 v[96:99],a[208:211],a[240:243],0\nds_read_b128 a[240:243],v239 offset:7168\nv_mfma_f32_16x16x32_bf16 v[16:19],a[196:199],a[244:247],0\n"
"ds_read_b128 a[224:227],v236 offset:2048\nv_mfma_f32_16x16x32_bf16 v[44:47],a[200:203],a[244:247],0\nv_mfma_f32_16x16x32_bf16 v[72:75],a[204:207],a[244:247],0\nv_mfma_f32_16x16x32_bf16 v[100:103],a[208:211],a[244:247],0\nds_read_b128 a[244:247],v239 offset:9216\ns_waitcnt lgkmcnt(9)\nv_mfma_f32_16x16x32_bf16 v[20:23],a[196:199],a[248:251],0\nv_mfma_f32_16x16x32_bf16 v[48:51],a[200:203],a[248:251],0\n"
"v_mfma_f32_16x16x32_bf16 v[76:79],a[204:207],a[248:251],0\nv_mfma_f32_16x16x32_bf16 v[104:107],a[208:211],a[248:251],0\nds_read_b128 a[248:251],v239 offset:11264\nv_mfma_f32_16x16x32_bf16 v[24:27],a[196:199],a[252:255],0\nv_mfma_f32_16x16x32_bf16 v[52:55],a[200:203],a[252:255],0\nv_mfma_f32_16x16x32_bf16 v[80:83],a[204:207],a[252:255],0\nv_mfma_f32_16x16x32_bf16 v[108:111],a[208:211],a[252:255],0\nds_read_b128 a[252:255],v239 offset:13312\n"
"s_waitcnt lgkmcnt(3)\nv_mfma_f32_16x16x32_bf16 v[0:3],a[212:215],a[228:231],v[0:3]\nbuffer_load_dwordx4 v227,s[48:51],s73 offen offset:0 nt lds\ns_mov_b32 s74,72832\nv_mfma_f32_16x16x32_bf16 v[28:31],a[216:219],a[228:231],v[28:31]\ns_waitcnt vmcnt(9)\nds_read_b64_tr_b16 a[196:197],v231 offset:4096\nv_mfma_f32_16x16x32_bf16 v[56:59],a[220:223],a[228:231],v[56:59]\n"
"ds_read_b64_tr_b16 a[198:199],v231 offset:5120\nv_mfma_f32_16x16x32_bf16 v[84:87],a[224:227],a[228:231],v[84:87]\ns_waitcnt lgkmcnt(2)\ns_barrier\nds_read_b128 a[228:231],v237 offset:0\nv_mfma_f32_16x16x32_bf16 v[4:7],a[212:215],a[232:235],v[4:7]\nbuffer_load_dwordx4 v228,s[48:51],s74 offen offset:1024 nt lds\ns_mov_b32 s75,96384\n"
"v_mfma_f32_16x16x32_bf16 v[32:35],a[216:219],a[232:235],v[32:35]\nds_read_b64_tr_b16 a[200:201],v232 offset:4096\nv_mfma_f32_16x16x32_bf16 v[60:63],a[220:223],a[232:235],v[60:63]\nv_mfma_f32_16x16x32_bf16 v[88:91],a[224:227],a[232:235],v[88:91]\nds_read_b128 a[232:235],v237 offset:2048\nv_mfma_f32_16x16x32_bf16 v[8:11],a[212:215],a[236:239],v[8:11]\nbuffer_load_dwordx4 v227,s[48:51],s75 offen offset:2048 nt lds\ns_mov_b32 s72,103552\n"
"v_mfma_f32_16x16x32_bf16 v[36:39],a[216:219],a[236:239],v[36:39]\nds_read_b64_tr_b16 a[202:203],v232 offset:5120\nv_mfma_f32_16x16x32_bf16 v[64:67],a[220:223],a[236:239],v[64:67]\nv_mfma_f32_16x16x32_bf16 v[92:95],a[224:227],a[236:239],v[92:95]\nds_read_b128 a[236:239],v237 offset:4096\nv_mfma_f32_16x16x32_bf16 v[12:15],a[212:215],a[240:243],v[12:15]\nbuffer_load_dwordx4 v228,s[48:51],s72 offen offset:3072 nt lds\ns_add_u32 m0,s64,0\n"
"v_mfma_f32_16x16x32_bf16 v[40:43],a[216:219],a[240:243],v[40:43]\nds_read_b64_tr_b16 a[204:205],v233 offset:4096\ns_mov_b32 s73,524800\nv_mfma_f32_16x16x32_bf16 v[68:71],a[220:223],a[240:243],v[68:71]\nds_read_b64_tr_b16 a[206:207],v233 offset:5120\nv_mfma_f32_16x16x32_bf16 v[96:99],a[224:227],a[240:243],v[96:99]\nds_read_b128 a[240:243],v237 offset:6144\nv_mfma_f32_16x16x32_bf16 v[16:19],a[212:215],a[244:247],v[16:19]\n"
"buffer_load_dwordx4 v224,s[44:47],s73 offen offset:0 lds\ns_mov_b32 s74,556544\nv_mfma_f32_16x16x32_bf16 v[44:47],a[216:219],a[244:247],v[44:47]\nds_read_b64_tr_b16 a[208:209],v234 offset:4096\nv_mfma_f32_16x16x32_bf16 v[72:75],a[220:223],a[244:247],v[72:75]\nv_mfma_f32_16x16x32_bf16 v[100:103],a[224:227],a[244:247],v[100:103]\nds_read_b128 a[244:247],v237 offset:8192\nv_mfma_f32_16x16x32_bf16 v[20:23],a[212:215],a[248:251],v[20:23]\n"
"buffer_load_dwordx4 v224,s[44:47],s74 offen offset:1024 lds\ns_mov_b32 s75,653824\nv_mfma_f32_16x16x32_bf16 v[48:51],a[216:219],a[248:251],v[48:51]\nds_read_b64_tr_b16 a[210:211],v234 offset:5120\nv_mfma_f32_16x16x32_bf16 v[76:79],a[220:223],a[248:251],v[76:79]\nv_mfma_f32_16x16x32_bf16 v[104:107],a[224:227],a[248:251],v[104:107]\nds_read_b128 a[248:251],v237 offset:10240\nv_mfma_f32_16x16x32_bf16 v[24:27],a[212:215],a[252:255],v[24:27]\n"
"buffer_load_dwordx4 v224,s[44:47],s75 offen offset:2048 lds\ns_mov_b32 s72,685568\nv_mfma_f32_16x16x32_bf16 v[52:55],a[216:219],a[252:255],v[52:55]\nv_mfma_f32_16x16x32_bf16 v[80:83],a[220:223],a[252:255],v[80:83]\nv_mfma_f32_16x16x32_bf16 v[108:111],a[224:227],a[252:255],v[108:111]\nds_read_b128 a[252:255],v237 offset:12288\ns_waitcnt lgkmcnt(2)\nv_mfma_f32_16x16x32_bf16 a[0:3],a[196:199],a[228:231],0\n"
"buffer_load_dwordx4 v224,s[44:47],s72 offen offset:3072 lds\ns_add_u32 m0,s64,4096\nv_mfma_f32_16x16x32_bf16 a[28:31],a[200:203],a[228:231],0\ns_waitcnt vmcnt(12)\nds_read_b64_tr_b16 a[212:213],v231 offset:8192\nv_mfma_f32_16x16x32_bf16 a[56:59],a[204:207],a[228:231],0\nds_read_b64_tr_b16 a[214:215],v231 offset:9216\ns_mov_b32 s73,786944\n"
"v_mfma_f32_16x16x32_bf16 a[84:87],a[208:211],a[228:231],0\nds_read_b128 a[228:231],v238 offset:0\nv_mfma_f32_16x16x32_bf16 a[4:7],a[196:199],a[232:235],0\nbuffer_load_dwordx4 v224,s[44:47],s73 offen offset:0 lds\ns_mov_b32 s74,818688\nv_mfma_f32_16x16x32_bf16 a[32:35],a[200:203],a[232:235],0\nds_read_b64_tr_b16 a[216:217],v232 offset:8192\nv_mfma_f32_16x16x32_bf16 a[60:63],a[204:207],a[232:235],0\n"
"v_mfma_f32_16x16x32_bf16 a[88:91],a[208:211],a[232:235],0\nds_read_b128 a[232:235],v238 offset:2048\nv_mfma_f32_16x16x32_bf16 a[8:11],a[196:199],a[236:239],0\nbuffer_load_dwordx4 v224,s[44:47],s74 offen offset:1024 lds\ns_mov_b32 s75,915968\nv_mfma_f32_16x16x32_bf16 a[36:39],a[200:203],a[236:239],0\nds_read_b64_tr_b16 a[218:219],v232 offset:9216\nv_mfma_f32_16x16x32_bf16 a[64:67],a[204:207],a[236:239],0\n"
"v_mfma_f32_16x16x32_bf16 a[92:95],a[208:211],a[236:239],0\nds_read_b128 a[236:239],v238 offset:4096\nv_mfma_f32_16x16x32_bf16 a[12:15],a[196:199],a[240:243],0\nbuffer_load_dwordx4 v224,s[44:47],s75 offen offset:2048 lds\ns_mov_b32 s72,947712\nv_mfma_f32_16x16x32_bf16 a[40:43],a[200:203],a[240:243],0\nds_read_b64_tr_b16 a[220:221],v233 offset:8192\nv_mfma_f32_16x16x32_bf16 a[68:71],a[204:207],a[240:243],0\n"
"ds_read_b64_tr_b16 a[222:223],v233 offset:9216\nv_mfma_f32_16x16x32_bf16 a[96:99],a[208:211],a[240:243],0\nds_read_b128 a[240:243],v238 offset:6144\nv_mfma_f32_16x16x32_bf16 a[16:19],a[196:199],a[244:247],0\nbuffer_load_dwordx4 v224,s[44:47],s72 offen offset:3072 lds\nv_mfma_f32_16x16x32_bf16 a[44:47],a[200:203],a[244:247],0\nds_read_b64_tr_b16 a[224:225],v234 offset:8192\nv_mfma_f32_16x16x32_bf16 a[72:75],a[204:207],a[244:247],0\n"
"v_mfma_f32_16x16x32_bf16 a[100:103],a[208:211],a[244:247],0\nds_read_b128 a[244:247],v238 offset:8192\ns_waitcnt lgkmcnt(12)\nv_mfma_f32_16x16x32_bf16 a[20:23],a[196:199],a[248:251],0\nds_read_b64_tr_b16 a[226:227],v234 offset:9216\ns_add_u32 m0,s64,8192\nv_mfma_f32_16x16x32_bf16 a[48:51],a[200:203],a[248:251],0\ns_mov_b32 s73,256\n"
"v_mfma_f32_16x16x32_bf16 a[76:79],a[204:207],a[248:251],0\nv_mfma_f32_16x16x32_bf16 a[104:107],a[208:211],a[248:251],0\nds_read_b128 a[248:251],v238 offset:10240\nv_mfma_f32_16x16x32_bf16 a[24:27],a[196:199],a[252:255],0\nv_mfma_f32_16x16x32_bf16 a[52:55],a[200:203],a[252:255],0\nv_mfma_f32_16x16x32_bf16 a[80:83],a[204:207],a[252:255],0\nv_mfma_f32_16x16x32_bf16 a[108:111],a[208:211],a[252:255],0\nds_read_b128 a[252:255],v238 offset:12288\n"
"s_waitcnt lgkmcnt(2)\nv_mfma_f32_16x16x32_bf16 a[0:3],a[212:215],a[228:231],a[0:3]\nbuffer_load_dwordx4 v227,s[48:51],s73 offen offset:0 nt lds\ns_mov_b32 s74,7424\nv_mfma_f32_16x16x32_bf16 a[28:31],a[216:219],a[228:231],a[28:31]\ns_waitcnt vmcnt(13)\nds_read_b128 a[196:199],v235 offset:12288\nv_mfma_f32_16x16x32_bf16 a[56:59],a[220:223],a[228:231],a[56:59]\n"
"v_mfma_f32_16x16x32_bf16 a[84:87],a[224:227],a[228:231],a[84:87]\ns_waitcnt lgkmcnt(1)\ns_barrier\nds_read_b128 a[228:231],v239 offset:14336\nv_mfma_f32_16x16x32_bf16 a[4:7],a[212:215],a[232:235],a[4:7]\nbuffer_load_dwordx4 v228,s[48:51],s74 offen offset:1024 nt lds\ns_add_u32 m0,s65,147456\nv_mfma_f32_16x16x32_bf16 a[32:35],a[216:219],a[232:235],a[32:35]\n"
"ds_read_b128 a[200:203],v235 offset:14336\ns_mov_b32 s75,128\nv_mfma_f32_16x16x32_bf16 a[60:63],a[220:223],a[232:235],a[60:63]\nv_mfma_f32_16x16x32_bf16 a[88:91],a[224:227],a[232:235],a[88:91]\nds_read_b128 a[232:235],v239 offset:16384\nv_mfma_f32_16x16x32_bf16 a[8:11],a[212:215],a[236:239],a[8:11]\nbuffer_load_dwordx4 v229,s[40:43],s75 offen offset:0 lds\ns_mov_b32 s72,6272\n"
"v_mfma_f32_16x16x32_bf16 a[36:39],a[216:219],a[236:239],a[36:39]\nv_mfma_f32_16x16x32_bf16 a[64:67],a[220:223],a[236:239],a[64:67]\nv_mfma_f32_16x16x32_bf16 a[92:95],a[224:227],a[236:239],a[92:95]\nds_read_b128 a[236:239],v239 offset:18432\nv_mfma_f32_16x16x32_bf16 a[12:15],a[212:215],a[240:243],a[12:15]\nbuffer_load_dwordx4 v230,s[40:43],s72 offen offset:1024 lds\ns_add_u32 m0,s66,147456\nv_mfma_f32_16x16x32_bf16 a[40:43],a[216:219],a[240:243],a[40:43]\n"
"s_waitcnt vmcnt(12)\nds_read_b128 a[204:207],v235 offset:16384\nv_mfma_f32_16x16x32_bf16 a[68:71],a[220:223],a[240:243],a[68:71]\ns_add_u32 s73,s67,128\nv_mfma_f32_16x16x32_bf16 a[96:99],a[224:227],a[240:243],a[96:99]\nds_read_b128 a[240:243],v239 offset:20480\nv_mfma_f32_16x16x32_bf16 a[16:19],a[212:215],a[244:247],a[16:19]\nbuffer_load_dwordx4 v229,s[40:43],s73 offen offset:0 lds\n"
"s_add_u32 s74,s67,6272\nv_mfma_f32_16x16x32_bf16 a[44:47],a[216:219],a[244:247],a[44:47]\nds_read_b128 a[208:211],v235 offset:18432\nv_mfma_f32_16x16x32_bf16 a[72:75],a[220:223],a[244:247],a[72:75]\nv_mfma_f32_16x16x32_bf16 a[100:103],a[224:227],a[244:247],a[100:103]\nds_read_b128 a[244:247],v239 offset:22528\nv_mfma_f32_16x16x32_bf16 a[20:23],a[212:215],a[248:251],a[20:23]\nbuffer_load_dwordx4 v230,s[40:43],s74 offen offset:1024 lds\n"
"s_add_u32 m0,s64,8192\nv_mfma_f32_16x16x32_bf16 a[48:51],a[216:219],a[248:251],a[48:51]\ns_mov_b32 s75,30976\nv_mfma_f32_16x16x32_bf16 a[76:79],a[220:223],a[248:251],a[76:79]\nv_mfma_f32_16x16x32_bf16 a[104:107],a[224:227],a[248:251],a[104:107]\nds_read_b128 a[248:251],v239 offset:24576\nv_mfma_f32_16x16x32_bf16 a[24:27],a[212:215],a[252:255],a[24:27]\nbuffer_load_dwordx4 v227,s[48:51],s75 offen offset:2048 nt lds\n"
"s_mov_b32 s72,38144\nv_mfma_f32_16x16x32_bf16 a[52:55],a[216:219],a[252:255],a[52:55]\nv_mfma_f32_16x16x32_bf16 a[80:83],a[220:223],a[252:255],a[80:83]\nv_mfma_f32_16x16x32_bf16 a[108:111],a[224:227],a[252:255],a[108:111]\nds_read_b128 a[252:255],v239 offset:26624\ns_waitcnt lgkmcnt(3)\nv_mfma_f32_16x16x32_bf16 v[0:3],a[196:199],a[228:231],v[0:3]\nbuffer_load_dwordx4 v228,s[48:51],s72 offen offset:3072 nt lds\n"
"v_mfma_f32_16x16x32_bf16 v[28:31],a[200:203],a[228:231],v[28:31]\nds_read_b128 a[212:215],v236 offset:12288\nv_mfma_f32_16x16x32_bf16 v[56:59],a[204:207],a[228:231],v[56:59]\nv_mfma_f32_16x16x32_bf16 v[84:87],a[208:211],a[228:231],v[84:87]\nds_read_b128 a[228:231],v239 offset:15360\nv_mfma_f32_16x16x32_bf16 v[4:7],a[196:199],a[232:235],v[4:7]\nds_read_b128 a[216:219],v236 offset:14336\ns_add_u32 m0,s64,12288\n"
"v_mfma_f32_16x16x32_bf16 v[32:35],a[200:203],a[232:235],v[32:35]\ns_mov_b32 s73,65792\nv_mfma_f32_16x16x32_bf16 v[60:63],a[204:207],a[232:235],v[60:63]\nv_mfma_f32_16x16x32_bf16 v[88:91],a[208:211],a[232:235],v[88:91]\nds_read_b128 a[232:235],v239 offset:17408\nv_mfma_f32_16x16x32_bf16 v[8:11],a[196:199],a[236:239],v[8:11]\nv_mfma_f32_16x16x32_bf16 v[36:39],a[200:203],a[236:239],v[36:39]\nv_mfma_f32_16x16x32_bf16 v[64:67],a[204:207],a[236:239],v[64:67]\n"
"v_mfma_f32_16x16x32_bf16 v[92:95],a[208:211],a[236:239],v[92:95]\nds_read_b128 a[236:239],v239 offset:19456\ns_waitcnt lgkmcnt(7)\nv_mfma_f32_16x16x32_bf16 v[12:15],a[196:199],a[240:243],v[12:15]\nds_read_b128 a[220:223],v236 offset:16384\nv_mfma_f32_16x16x32_bf16 v[40:43],a[200:203],a[240:243],v[40:43]\nv_mfma_f32_16x16x32_bf16 v[68:71],a[204:207],a[240:243],v[68:71]\nv_mfma_f32_16x16x32_bf16 v[96:99],a[208:211],a[240:243],v[96:99]\n"
"ds_read_b128 a[240:243],v239 offset:21504\nv_mfma_f32_16x16x32_bf16 v[16:19],a[196:199],a[244:247],v[16:19]\nds_read_b128 a[224:227],v236 offset:18432\nv_mfma_f32_16x16x32_bf16 v[44:47],a[200:203],a[244:247],v[44:47]\nv_mfma_f32_16x16x32_bf16 v[72:75],a[204:207],a[244:247],v[72:75]\nv_mfma_f32_16x16x32_bf16 v[100:103],a[208:211],a[244:247],v[100:103]\nds_read_b128 a[244:247],v239 offset:23552\ns_waitcnt lgkmcnt(9)\n"
"v_mfma_f32_16x16x32_bf16 v[20:23],a[196:199],a[248:251],v[20:23]\nv_mfma_f32_16x16x32_bf16 v[48:51],a[200:203],a[248:251],v[48:51]\nv_mfma_f32_16x16x32_bf16 v[76:79],a[204:207],a[248:251],v[76:79]\nv_mfma_f32_16x16x32_bf16 v[104:107],a[208:211],a[248:251],v[104:107]\nds_read_b128 a[248:251],v239 offset:25600\nv_mfma_f32_16x16x32_bf16 v[24:27],a[196:199],a[252:255],v[24:27]\nv_mfma_f32_16x16x32_bf16 v[52:55],a[200:203],a[252:255],v[52:55]\nv_mfma_f32_16x16x32_bf16 v[80:83],a[204:207],a[252:255],v[80:83]\n"
"v_mfma_f32_16x16x32_bf16 v[108:111],a[208:211],a[252:255],v[108:111]\nds_read_b128 a[252:255],v239 offset:27648\ns_waitcnt lgkmcnt(3)\nv_mfma_f32_16x16x32_bf16 v[0:3],a[212:215],a[228:231],v[0:3]\nbuffer_load_dwordx4 v227,s[48:51],s73 offen offset:0 nt lds\ns_mov_b32 s74,72960\nv_mfma_f32_16x16x32_bf16 v[28:31],a[216:219],a[228:231],v[28:31]\ns_waitcnt vmcnt(13)\n"
"ds_read_b64_tr_b16 a[196:197],v231 offset:0\nv_mfma_f32_16x16x32_bf16 v[56:59],a[220:223],a[228:231],v[56:59]\nds_read_b64_tr_b16 a[198:199],v231 offset:1024\nv_mfma_f32_16x16x32_bf16 v[84:87],a[224:227],a[228:231],v[84:87]\ns_waitcnt vmcnt(3) lgkmcnt(2)\ns_barrier\nds_read_b128 a[228:231],v237 offset:0\nv_mfma_f32_16x16x32_bf16 v[4:7],a[212:215],a[232:235],v[4:7]\n"
"buffer_load_dwordx4 v228,s[48:51],s74 offen offset:1024 nt lds\ns_mov_b32 s75,96512\nv_mfma_f32_16x16x32_bf16 v[32:35],a[216:219],a[232:235],v[32:35]\nds_read_b64_tr_b16 a[200:201],v232 offset:0\nv_mfma_f32_16x16x32_bf16 v[60:63],a[220:223],a[232:235],v[60:63]\nv_mfma_f32_16x16x32_bf16 v[88:91],a[224:227],a[232:235],v[88:91]\nds_read_b128 a[232:235],v237 offset:2048\nv_mfma_f32_16x16x32_bf16 v[8:11],a[212:215],a[236:239],v[8:11]\n"
"buffer_load_dwordx4 v227,s[48:51],s75 offen offset:2048 nt lds\ns_mov_b32 s72,103680\nv_mfma_f32_16x16x32_bf16 v[36:39],a[216:219],a[236:239],v[36:39]\nds_read_b64_tr_b16 a[202:203],v232 offset:1024\nv_mfma_f32_16x16x32_bf16 v[64:67],a[220:223],a[236:239],v[64:67]\nv_mfma_f32_16x16x32_bf16 v[92:95],a[224:227],a[236:239],v[92:95]\nds_read_b128 a[236:239],v237 offset:4096\nv_mfma_f32_16x16x32_bf16 v[12:15],a[212:215],a[240:243],v[12:15]\n"
"buffer_load_dwordx4 v228,s[48:51],s72 offen offset:3072 nt lds\ns_add_u32 m0,s64,16384\nv_mfma_f32_16x16x32_bf16 v[40:43],a[216:219],a[240:243],v[40:43]\nds_read_b64_tr_b16 a[204:205],v233 offset:0\ns_mov_b32 s73,1049088\nv_mfma_f32_16x16x32_bf16 v[68:71],a[220:223],a[240:243],v[68:71]\nds_read_b64_tr_b16 a[206:207],v233 offset:1024\nv_mfma_f32_16x16x32_bf16 v[96:99],a[224:227],a[240:243],v[96:99]\n"
"ds_read_b128 a[240:243],v237 offset:6144\nv_mfma_f32_16x16x32_bf16 v[16:19],a[212:215],a[244:247],v[16:19]\nbuffer_load_dwordx4 v224,s[44:47],s73 offen offset:0 lds\ns_mov_b32 s74,1080832\nv_mfma_f32_16x16x32_bf16 v[44:47],a[216:219],a[244:247],v[44:47]\nds_read_b64_tr_b16 a[208:209],v234 offset:0\nv_mfma_f32_16x16x32_bf16 v[72:75],a[220:223],a[244:247],v[72:75]\nv_mfma_f32_16x16x32_bf16 v[100:103],a[224:227],a[244:247],v[100:103]\n"
"ds_read_b128 a[244:247],v237 offset:8192\nv_mfma_f32_16x16x32_bf16 v[20:23],a[212:215],a[248:251],v[20:23]\nbuffer_load_dwordx4 v224,s[44:47],s74 offen offset:1024 lds\ns_mov_b32 s75,1178112\nv_mfma_f32_16x16x32_bf16 v[48:51],a[216:219],a[248:251],v[48:51]\nds_read_b64_tr_b16 a[210:211],v234 offset:1024\nv_mfma_f32_16x16x32_bf16 v[76:79],a[220:223],a[248:251],v[76:79]\nv_mfma_f32_16x16x32_bf16 v[104:107],a[224:227],a[248:251],v[104:107]\n"
"ds_read_b128 a[248:251],v237 offset:10240\nv_mfma_f32_16x16x32_bf16 v[24:27],a[212:215],a[252:255],v[24:27]\nbuffer_load_dwordx4 v224,s[44:47],s75 offen offset:2048 lds\ns_mov_b32 s72,1209856\nv_mfma_f32_16x16x32_bf16 v[52:55],a[216:219],a[252:255],v[52:55]\nv_mfma_f32_16x16x32_bf16 v[80:83],a[220:223],a[252:255],v[80:83]\nv_mfma_f32_16x16x32_bf16 v[108:111],a[224:227],a[252:255],v[108:111]\nds_read_b128 a[252:255],v237 offset:12288\n"
"s_waitcnt lgkmcnt(2)\nv_mfma_f32_16x16x32_bf16 a[0:3],a[196:199],a[228:231],a[0:3]\nbuffer_load_dwordx4 v224,s[44:47],s72 offen offset:3072 lds\ns_add_u32 m0,s64,0\nv_mfma_f32_16x16x32_bf16 a[28:31],a[200:203],a[228:231],a[28:31]\nds_read_b64_tr_b16 a[212:213],v231 offset:4096\ns_mov_b32 s73,1311232\nv_mfma_f32_16x16x32_bf16 a[56:59],a[204:207],a[228:231],a[56:59]\n"
"ds_read_b64_tr_b16 a[214:215],v231 offset:5120\nv_mfma_f32_16x16x32_bf16 a[84:87],a[208:211],a[228:231],a[84:87]\nds_read_b128 a[228:231],v238 offset:0\nv_mfma_f32_16x16x32_bf16 a[4:7],a[196:199],a[232:235],a[4:7]\nbuffer_load_dwordx4 v224,s[44:47],s73 offen offset:0 lds\ns_mov_b32 s74,1342976\nv_mfma_f32_16x16x32_bf16 a[32:35],a[200:203],a[232:235],a[32:35]\nds_read_b64_tr_b16 a[216:217],v232 offset:4096\n"
"v_mfma_f32_16x16x32_bf16 a[60:63],a[204:207],a[232:235],a[60:63]\nv_mfma_f32_16x16x32_bf16 a[88:91],a[208:211],a[232:235],a[88:91]\nds_read_b128 a[232:235],v238 offset:2048\nv_mfma_f32_16x16x32_bf16 a[8:11],a[196:199],a[236:239],a[8:11]\nbuffer_load_dwordx4 v224,s[44:47],s74 offen offset:1024 lds\ns_mov_b32 s75,1440256\nv_mfma_f32_16x16x32_bf16 a[36:39],a[200:203],a[236:239],a[36:39]\nds_read_b64_tr_b16 a[218:219],v232 offset:5120\n"
"v_mfma_f32_16x16x32_bf16 a[64:67],a[204:207],a[236:239],a[64:67]\nv_mfma_f32_16x16x32_bf16 a[92:95],a[208:211],a[236:239],a[92:95]\nds_read_b128 a[236:239],v238 offset:4096\nv_mfma_f32_16x16x32_bf16 a[12:15],a[196:199],a[240:243],a[12:15]\nbuffer_load_dwordx4 v224,s[44:47],s75 offen offset:2048 lds\ns_mov_b32 s72,1472000\nv_mfma_f32_16x16x32_bf16 a[40:43],a[200:203],a[240:243],a[40:43]\nds_read_b64_tr_b16 a[220:221],v233 offset:4096\n"
"v_mfma_f32_16x16x32_bf16 a[68:71],a[204:207],a[240:243],a[68:71]\nds_read_b64_tr_b16 a[222:223],v233 offset:5120\nv_mfma_f32_16x16x32_bf16 a[96:99],a[208:211],a[240:243],a[96:99]\nds_read_b128 a[240:243],v238 offset:6144\nv_mfma_f32_16x16x32_bf16 a[16:19],a[196:199],a[244:247],a[16:19]\nbuffer_load_dwordx4 v224,s[44:47],s72 offen offset:3072 lds\nv_mfma_f32_16x16x32_bf16 a[44:47],a[200:203],a[244:247],a[44:47]\nds_read_b64_tr_b16 a[224:225],v234 offset:4096\n"
"v_mfma_f32_16x16x32_bf16 a[72:75],a[204:207],a[244:247],a[72:75]\nv_mfma_f32_16x16x32_bf16 a[100:103],a[208:211],a[244:247],a[100:103]\nds_read_b128 a[244:247],v238 offset:8192\ns_waitcnt lgkmcnt(12)\nv_mfma_f32_16x16x32_bf16 a[20:23],a[196:199],a[248:251],a[20:23]\nds_read_b64_tr_b16 a[226:227],v234 offset:5120\ns_add_u32 m0,s64,4096\nv_mfma_f32_16x16x32_bf16 a[48:51],a[200:203],a[248:251],a[48:51]\n"
"s_mov_b32 s73,384\nv_mfma_f32_16x16x32_bf16 a[76:79],a[204:207],a[248:251],a[76:79]\nv_mfma_f32_16x16x32_bf16 a[104:107],a[208:211],a[248:251],a[104:107]\nds_read_b128 a[248:251],v238 offset:10240\nv_mfma_f32_16x16x32_bf16 a[24:27],a[196:199],a[252:255],a[24:27]\nv_mfma_f32_16x16x32_bf16 a[52:55],a[200:203],a[252:255],a[52:55]\nv_mfma_f32_16x16x32_bf16 a[80:83],a[204:207],a[252:255],a[80:83]\nv_mfma_f32_16x16x32_bf16 a[108:111],a[208:211],a[252:255],a[108:111]\n"
"ds_read_b128 a[252:255],v238 offset:12288\ns_waitcnt lgkmcnt(2)\nv_mfma_f32_16x16x32_bf16 a[0:3],a[212:215],a[228:231],a[0:3]\nbuffer_load_dwordx4 v227,s[48:51],s73 offen offset:0 nt lds\ns_mov_b32 s74,7552\nv_mfma_f32_16x16x32_bf16 a[28:31],a[216:219],a[228:231],a[28:31]\ns_waitcnt vmcnt(13)\nds_read_b128 a[196:199],v235 offset:8192\n"
"v_mfma_f32_16x16x32_bf16 a[56:59],a[220:223],a[228:231],a[56:59]\nv_mfma_f32_16x16x32_bf16 a[84:87],a[224:227],a[228:231],a[84:87]\ns_waitcnt lgkmcnt(1)\ns_barrier\nds_read_b128 a[228:231],v239 offset:28672\nv_mfma_f32_16x16x32_bf16 a[4:7],a[212:215],a[232:235],a[4:7]\nbuffer_load_dwordx4 v228,s[48:51],s74 offen offset:1024 nt lds\ns_add_u32 m0,s65,147456\n"
"v_mfma_f32_16x16x32_bf16 a[32:35],a[216:219],a[232:235],a[32:35]\nds_read_b128 a[200:203],v235 offset:10240\ns_mov_b32 s75,256\nv_mfma_f32_16x16x32_bf16 a[60:63],a[220:223],a[232:235],a[60:63]\nv_mfma_f32_16x16x32_bf16 a[88:91],a[224:227],a[232:235],a[88:91]\nds_read_b128 a[232:235],v239 offset:30720\nv_mfma_f32_16x16x32_bf16 a[8:11],a[212:215],a[236:239],a[8:11]\nbuffer_load_dwordx4 v229,s[40:43],s75 offen offset:0 lds\n"
"s_mov_b32 s72,6400\nv_mfma_f32_16x16x32_bf16 a[36:39],a[216:219],a[236:239],a[36:39]\nv_mfma_f32_16x16x32_bf16 a[64:67],a[220:223],a[236:239],a[64:67]\nv_mfma_f32_16x16x32_bf16 a[92:95],a[224:227],a[236:239],a[92:95]\nds_read_b128 a[236:239],v239 offset:32768\nv_mfma_f32_16x16x32_bf16 a[12:15],a[212:215],a[240:243],a[12:15]\nbuffer_load_dwordx4 v230,s[40:43],s72 offen offset:1024 lds\ns_add_u32 m0,s66,147456\n"
"v_mfma_f32_16x16x32_bf16 a[40:43],a[216:219],a[240:243],a[40:43]\ns_waitcnt vmcnt(12)\nds_read_b128 a[204:207],v235 offset:12288\nv_mfma_f32_16x16x32_bf16 a[68:71],a[220:223],a[240:243],a[68:71]\ns_add_u32 s73,s67,256\nv_mfma_f32_16x16x32_bf16 a[96:99],a[224:227],a[240:243],a[96:99]\nds_read_b128 a[240:243],v239 offset:34816\nv_mfma_f32_16x16x32_bf16 a[16:19],a[212:215],a[244:247],a[16:19]\n"
"buffer_load_dwordx4 v229,s[40:43],s73 offen offset:0 lds\ns_add_u32 s74,s67,6400\nv_mfma_f32_16x16x32_bf16 a[44:47],a[216:219],a[244:247],a[44:47]\nds_read_b128 a[208:211],v235 offset:14336\nv_mfma_f32_16x16x32_bf16 a[72:75],a[220:223],a[244:247],a[72:75]\nv_mfma_f32_16x16x32_bf16 a[100:103],a[224:227],a[244:247],a[100:103]\nds_read_b128 a[244:247],v239 offset:36864\nv_mfma_f32_16x16x32_bf16 a[20:23],a[212:215],a[248:251],a[20:23]\n"
"buffer_load_dwordx4 v230,s[40:43],s74 offen offset:1024 lds\ns_add_u32 m0,s64,4096\nv_mfma_f32_16x16x32_bf16 a[48:51],a[216:219],a[248:251],a[48:51]\ns_mov_b32 s75,31104\nv_mfma_f32_16x16x32_bf16 a[76:79],a[220:223],a[248:251],a[76:79]\nv_mfma_f32_16x16x32_bf16 a[104:107],a[224:227],a[248:251],a[104:107]\nds_read_b128 a[248:251],v239 offset:38912\nv_mfma_f32_16x16x32_bf16 a[24:27],a[212:215],a[252:255],a[24:27]\n"
"buffer_load_dwordx4 v227,s[48:51],s75 offen offset:2048 nt lds\ns_mov_b32 s72,38272\nv_mfma_f32_16x16x32_bf16 a[52:55],a[216:219],a[252:255],a[52:55]\nv_mfma_f32_16x16x32_bf16 a[80:83],a[220:223],a[252:255],a[80:83]\nv_mfma_f32_16x16x32_bf16 a[108:111],a[224:227],a[252:255],a[108:111]\nds_read_b128 a[252:255],v239 offset:40960\ns_waitcnt lgkmcnt(3)\nv_mfma_f32_16x16x32_bf16 v[0:3],a[196:199],a[228:231],v[0:3]\n"
"buffer_load_dwordx4 v228,s[48:51],s72 offen offset:3072 nt lds\nv_mfma_f32_16x16x32_bf16 v[28:31],a[200:203],a[228:231],v[28:31]\nds_read_b128 a[212:215],v236 offset:8192\nv_mfma_f32_16x16x32_bf16 v[56:59],a[204:207],a[228:231],v[56:59]\nv_mfma_f32_16x16x32_bf16 v[84:87],a[208:211],a[228:231],v[84:87]\nds_read_b128 a[228:231],v239 offset:29696\nv_mfma_f32_16x16x32_bf16 v[4:7],a[196:199],a[232:235],v[4:7]\nds_read_b128 a[216:219],v236 offset:10240\n"
"s_add_u32 m0,s64,8192\nv_mfma_f32_16x16x32_bf16 v[32:35],a[200:203],a[232:235],v[32:35]\ns_mov_b32 s73,65920\nv_mfma_f32_16x16x32_bf16 v[60:63],a[204:207],a[232:235],v[60:63]\nv_mfma_f32_16x16x32_bf16 v[88:91],a[208:211],a[232:235],v[88:91]\nds_read_b128 a[232:235],v239 offset:31744\nv_mfma_f32_16x16x32_bf16 v[8:11],a[196:199],a[236:239],v[8:11]\nv_mfma_f32_16x16x32_bf16 v[36:39],a[200:203],a[236:239],v[36:39]\n"
"v_mfma_f32_16x16x32_bf16 v[64:67],a[204:207],a[236:239],v[64:67]\nv_mfma_f32_16x16x32_bf16 v[92:95],a[208:211],a[236:239],v[92:95]\nds_read_b128 a[236:239],v239 offset:33792\ns_waitcnt lgkmcnt(7)\nv_mfma_f32_16x16x32_bf16 v[12:15],a[196:199],a[240:243],v[12:15]\nds_read_b128 a[220:223],v236 offset:12288\nv_mfma_f32_16x16x32_bf16 v[40:43],a[200:203],a[240:243],v[40:43]\nv_mfma_f32_16x16x32_bf16 v[68:71],a[204:207],a[240:243],v[68:71]\n"
"v_mfma_f32_16x16x32_bf16 v[96:99],a[208:211],a[240:243],v[96:99]\nds_read_b128 a[240:243],v239 offset:35840\nv_mfma_f32_16x16x32_bf16 v[16:19],a[196:199],a[244:247],v[16:19]\nds_read_b128 a[224:227],v236 offset:14336\nv_mfma_f32_16x16x32_bf16 v[44:47],a[200:203],a[244:247],v[44:47]\nv_mfma_f32_16x16x32_bf16 v[72:75],a[204:207],a[244:247],v[72:75]\nv_mfma_f32_16x16x32_bf16 v[100:103],a[208:211],a[244:247],v[100:103]\nds_read_b128 a[244:247],v239 offset:37888\n"
"s_waitcnt lgkmcnt(9)\nv_mfma_f32_16x16x32_bf16 v[20:23],a[196:199],a[248:251],v[20:23]\nv_mfma_f32_16x16x32_bf16 v[48:51],a[200:203],a[248:251],v[48:51]\nv_mfma_f32_16x16x32_bf16 v[76:79],a[204:207],a[248:251],v[76:79]\nv_mfma_f32_16x16x32_bf16 v[104:107],a[208:211],a[248:251],v[104:107]\nds_read_b128 a[248:251],v239 offset:39936\nv_mfma_f32_16x16x32_bf16 v[24:27],a[196:199],a[252:255],v[24:27]\nv_mfma_f32_16x16x32_bf16 v[52:55],a[200:203],a[252:255],v[52:55]\n"
"v_mfma_f32_16x16x32_bf16 v[80:83],a[204:207],a[252:255],v[80:83]\nv_mfma_f32_16x16x32_bf16 v[108:111],a[208:211],a[252:255],v[108:111]\nds_read_b128 a[252:255],v239 offset:41984\ns_waitcnt lgkmcnt(3)\nv_mfma_f32_16x16x32_bf16 v[0:3],a[212:215],a[228:231],v[0:3]\nbuffer_load_dwordx4 v227,s[48:51],s73 offen offset:0 nt lds\ns_mov_b32 s74,73088\nv_mfma_f32_16x16x32_bf16 v[28:31],a[216:219],a[228:231],v[28:31]\n"
"s_waitcnt vmcnt(13)\nds_read_b64_tr_b16 a[196:197],v231 offset:16384\nv_mfma_f32_16x16x32_bf16 v[56:59],a[220:223],a[228:231],v[56:59]\nds_read_b64_tr_b16 a[198:199],v231 offset:17408\nv_mfma_f32_16x16x32_bf16 v[84:87],a[224:227],a[228:231],v[84:87]\ns_waitcnt vmcnt(3) lgkmcnt(2)\ns_barrier\nds_read_b128 a[228:231],v237 offset:0\n"
"v_mfma_f32_16x16x32_bf16 v[4:7],a[212:215],a[232:235],v[4:7]\nbuffer_load_dwordx4 v228,s[48:51],s74 offen offset:1024 nt lds\ns_mov_b32 s75,96640\nv_mfma_f32_16x16x32_bf16 v[32:35],a[216:219],a[232:235],v[32:35]\nds_read_b64_tr_b16 a[200:201],v232 offset:16384\nv_mfma_f32_16x16x32_bf16 v[60:63],a[220:223],a[232:235],v[60:63]\nv_mfma_f32_16x16x32_bf16 v[88:91],a[224:227],a[232:235],v[88:91]\nds_read_b128 a[232:235],v237 offset:2048\n"
"v_mfma_f32_16x16x32_bf16 v[8:11],a[212:215],a[236:239],v[8:11]\nbuffer_load_dwordx4 v227,s[48:51],s75 offen offset:2048 nt lds\ns_mov_b32 s72,103808\nv_mfma_f32_16x16x32_bf16 v[36:39],a[216:219],a[236:239],v[36:39]\nds_read_b64_tr_b16 a[202:203],v232 offset:17408\nv_mfma_f32_16x16x32_bf16 v[64:67],a[220:223],a[236:239],v[64:67]\nv_mfma_f32_16x16x32_bf16 v[92:95],a[224:227],a[236:239],v[92:95]\nds_read_b128 a[236:239],v237 offset:4096\n"
"v_mfma_f32_16x16x32_bf16 v[12:15],a[212:215],a[240:243],v[12:15]\nbuffer_load_dwordx4 v228,s[48:51],s72 offen offset:3072 nt lds\ns_add_u32 m0,s64,12288\nv_mfma_f32_16x16x32_bf16 v[40:43],a[216:219],a[240:243],v[40:43]\nds_read_b64_tr_b16 a[204:205],v233 offset:16384\ns_mov_b32 s73,131072\nv_mfma_f32_16x16x32_bf16 v[68:71],a[220:223],a[240:243],v[68:71]\nds_read_b64_tr_b16 a[206:207],v233 offset:17408\n"
"v_mfma_f32_16x16x32_bf16 v[96:99],a[224:227],a[240:243],v[96:99]\nds_read_b128 a[240:243],v237 offset:6144\nv_mfma_f32_16x16x32_bf16 v[16:19],a[212:215],a[244:247],v[16:19]\nbuffer_load_dwordx4 v227,s[48:51],s73 offen offset:0 nt lds\ns_mov_b32 s74,138240\nv_mfma_f32_16x16x32_bf16 v[44:47],a[216:219],a[244:247],v[44:47]\nds_read_b64_tr_b16 a[208:209],v234 offset:16384\nv_mfma_f32_16x16x32_bf16 v[72:75],a[220:223],a[244:247],v[72:75]\n"
"v_mfma_f32_16x16x32_bf16 v[100:103],a[224:227],a[244:247],v[100:103]\nds_read_b128 a[244:247],v237 offset:8192\nv_mfma_f32_16x16x32_bf16 v[20:23],a[212:215],a[248:251],v[20:23]\nbuffer_load_dwordx4 v228,s[48:51],s74 offen offset:1024 nt lds\ns_mov_b32 s75,161792\nv_mfma_f32_16x16x32_bf16 v[48:51],a[216:219],a[248:251],v[48:51]\nds_read_b64_tr_b16 a[210:211],v234 offset:17408\nv_mfma_f32_16x16x32_bf16 v[76:79],a[220:223],a[248:251],v[76:79]\n"
"v_mfma_f32_16x16x32_bf16 v[104:107],a[224:227],a[248:251],v[104:107]\nds_read_b128 a[248:251],v237 offset:10240\nv_mfma_f32_16x16x32_bf16 v[24:27],a[212:215],a[252:255],v[24:27]\nbuffer_load_dwordx4 v227,s[48:51],s75 offen offset:2048 nt lds\ns_mov_b32 s72,168960\nv_mfma_f32_16x16x32_bf16 v[52:55],a[216:219],a[252:255],v[52:55]\nv_mfma_f32_16x16x32_bf16 v[80:83],a[220:223],a[252:255],v[80:83]\nv_mfma_f32_16x16x32_bf16 v[108:111],a[224:227],a[252:255],v[108:111]\n"
"ds_read_b128 a[252:255],v237 offset:12288\ns_waitcnt lgkmcnt(2)\nv_mfma_f32_16x16x32_bf16 a[0:3],a[196:199],a[228:231],a[0:3]\nbuffer_load_dwordx4 v228,s[48:51],s72 offen offset:3072 nt lds\ns_add_u32 m0,s64,16384\nv_mfma_f32_16x16x32_bf16 a[28:31],a[200:203],a[228:231],a[28:31]\nds_read_b64_tr_b16 a[212:213],v231 offset:0\ns_mov_b32 s73,196608\n"
"v_mfma_f32_16x16x32_bf16 a[56:59],a[204:207],a[228:231],a[56:59]\nds_read_b64_tr_b16 a[214:215],v231 offset:1024\nv_mfma_f32_16x16x32_bf16 a[84:87],a[208:211],a[228:231],a[84:87]\nds_read_b128 a[228:231],v238 offset:0\nv_mfma_f32_16x16x32_bf16 a[4:7],a[196:199],a[232:235],a[4:7]\nbuffer_load_dwordx4 v227,s[48:51],s73 offen offset:0 nt lds\ns_mov_b32 s74,203776\nv_mfma_f32_16x16x32_bf16 a[32:35],a[200:203],a[232:235],a[32:35]\n"
"ds_read_b64_tr_b16 a[216:217],v232 offset:0\nv_mfma_f32_16x16x32_bf16 a[60:63],a[204:207],a[232:235],a[60:63]\nv_mfma_f32_16x16x32_bf16 a[88:91],a[208:211],a[232:235],a[88:91]\nds_read_b128 a[232:235],v238 offset:2048\nv_mfma_f32_16x16x32_bf16 a[8:11],a[196:199],a[236:239],a[8:11]\nbuffer_load_dwordx4 v228,s[48:51],s74 offen offset:1024 nt lds\ns_mov_b32 s75,227328\nv_mfma_f32_16x16x32_bf16 a[36:39],a[200:203],a[236:239],a[36:39]\n"
"ds_read_b64_tr_b16 a[218:219],v232 offset:1024\nv_mfma_f32_16x16x32_bf16 a[64:67],a[204:207],a[236:239],a[64:67]\nv_mfma_f32_16x16x32_bf16 a[92:95],a[208:211],a[236:239],a[92:95]\nds_read_b128 a[236:239],v238 offset:4096\nv_mfma_f32_16x16x32_bf16 a[12:15],a[196:199],a[240:243],a[12:15]\nbuffer_load_dwordx4 v227,s[48:51],s75 offen offset:2048 nt lds\ns_mov_b32 s72,234496\nv_mfma_f32_16x16x32_bf16 a[40:43],a[200:203],a[240:243],a[40:43]\n"
"ds_read_b64_tr_b16 a[220:221],v233 offset:0\nv_mfma_f32_16x16x32_bf16 a[68:71],a[204:207],a[240:243],a[68:71]\nds_read_b64_tr_b16 a[222:223],v233 offset:1024\nv_mfma_f32_16x16x32_bf16 a[96:99],a[208:211],a[240:243],a[96:99]\nds_read_b128 a[240:243],v238 offset:6144\nv_mfma_f32_16x16x32_bf16 a[16:19],a[196:199],a[244:247],a[16:19]\nbuffer_load_dwordx4 v228,s[48:51],s72 offen offset:3072 nt lds\nv_mfma_f32_16x16x32_bf16 a[44:47],a[200:203],a[244:247],a[44:47]\n"
"ds_read_b64_tr_b16 a[224:225],v234 offset:0\nv_mfma_f32_16x16x32_bf16 a[72:75],a[204:207],a[244:247],a[72:75]\nv_mfma_f32_16x16x32_bf16 a[100:103],a[208:211],a[244:247],a[100:103]\nds_read_b128 a[244:247],v238 offset:8192\ns_waitcnt lgkmcnt(12)\nv_mfma_f32_16x16x32_bf16 a[20:23],a[196:199],a[248:251],a[20:23]\nds_read_b64_tr_b16 a[226:227],v234 offset:1024\ns_add_u32 m0,s64,0\n"
"v_mfma_f32_16x16x32_bf16 a[48:51],a[200:203],a[248:251],a[48:51]\ns_mov_b32 s73,1573376\nv_mfma_f32_16x16x32_bf16 a[76:79],a[204:207],a[248:251],a[76:79]\nv_mfma_f32_16x16x32_bf16 a[104:107],a[208:211],a[248:251],a[104:107]\nds_read_b128 a[248:251],v238 offset:10240\nv_mfma_f32_16x16x32_bf16 a[24:27],a[196:199],a[252:255],a[24:27]\nv_mfma_f32_16x16x32_bf16 a[52:55],a[200:203],a[252:255],a[52:55]\nv_mfma_f32_16x16x32_bf16 a[80:83],a[204:207],a[252:255],a[80:83]\n"
"v_mfma_f32_16x16x32_bf16 a[108:111],a[208:211],a[252:255],a[108:111]\nds_read_b128 a[252:255],v238 offset:12288\ns_waitcnt lgkmcnt(2)\nv_mfma_f32_16x16x32_bf16 a[0:3],a[212:215],a[228:231],a[0:3]\nbuffer_load_dwordx4 v224,s[44:47],s73 offen offset:0 lds\ns_mov_b32 s74,1605120\nv_mfma_f32_16x16x32_bf16 a[28:31],a[216:219],a[228:231],a[28:31]\ns_waitcnt vmcnt(13)\n"
"ds_read_b128 a[196:199],v235 offset:4096\nv_mfma_f32_16x16x32_bf16 a[56:59],a[220:223],a[228:231],a[56:59]\nv_mfma_f32_16x16x32_bf16 a[84:87],a[224:227],a[228:231],a[84:87]\ns_waitcnt lgkmcnt(1)\ns_barrier\nds_read_b128 a[228:231],v239 offset:43008\nv_mfma_f32_16x16x32_bf16 a[4:7],a[212:215],a[232:235],a[4:7]\nbuffer_load_dwordx4 v224,s[44:47],s74 offen offset:1024 lds\n"
"s_add_u32 m0,s65,147456\nv_mfma_f32_16x16x32_bf16 a[32:35],a[216:219],a[232:235],a[32:35]\nds_read_b128 a[200:203],v235 offset:6144\ns_mov_b32 s75,384\nv_mfma_f32_16x16x32_bf16 a[60:63],a[220:223],a[232:235],a[60:63]\nv_mfma_f32_16x16x32_bf16 a[88:91],a[224:227],a[232:235],a[88:91]\nds_read_b128 a[232:235],v239 offset:45056\nv_mfma_f32_16x16x32_bf16 a[8:11],a[212:215],a[236:239],a[8:11]\n"
"buffer_load_dwordx4 v229,s[40:43],s75 offen offset:0 lds\ns_mov_b32 s72,6528\nv_mfma_f32_16x16x32_bf16 a[36:39],a[216:219],a[236:239],a[36:39]\nv_mfma_f32_16x16x32_bf16 a[64:67],a[220:223],a[236:239],a[64:67]\nv_mfma_f32_16x16x32_bf16 a[92:95],a[224:227],a[236:239],a[92:95]\nds_read_b128 a[236:239],v239 offset:47104\nv_mfma_f32_16x16x32_bf16 a[12:15],a[212:215],a[240:243],a[12:15]\nbuffer_load_dwordx4 v230,s[40:43],s72 offen offset:1024 lds\n"
"s_add_u32 m0,s66,147456\nv_mfma_f32_16x16x32_bf16 a[40:43],a[216:219],a[240:243],a[40:43]\ns_waitcnt vmcnt(12)\nds_read_b128 a[204:207],v235 offset:8192\nv_mfma_f32_16x16x32_bf16 a[68:71],a[220:223],a[240:243],a[68:71]\ns_add_u32 s73,s67,384\nv_mfma_f32_16x16x32_bf16 a[96:99],a[224:227],a[240:243],a[96:99]\nds_read_b128 a[240:243],v239 offset:49152\n"
"v_mfma_f32_16x16x32_bf16 a[16:19],a[212:215],a[244:247],a[16:19]\nbuffer_load_dwordx4 v229,s[40:43],s73 offen offset:0 lds\ns_add_u32 s74,s67,6528\nv_mfma_f32_16x16x32_bf16 a[44:47],a[216:219],a[244:247],a[44:47]\nds_read_b128 a[208:211],v235 offset:10240\nv_mfma_f32_16x16x32_bf16 a[72:75],a[220:223],a[244:247],a[72:75]\nv_mfma_f32_16x16x32_bf16 a[100:103],a[224:227],a[244:247],a[100:103]\nds_read_b128 a[244:247],v239 offset:51200\n"
"v_mfma_f32_16x16x32_bf16 a[20:23],a[212:215],a[248:251],a[20:23]\nbuffer_load_dwordx4 v230,s[40:43],s74 offen offset:1024 lds\ns_add_u32 m0,s64,0\nv_mfma_f32_16x16x32_bf16 a[48:51],a[216:219],a[248:251],a[48:51]\ns_mov_b32 s75,1702400\nv_mfma_f32_16x16x32_bf16 a[76:79],a[220:223],a[248:251],a[76:79]\nv_mfma_f32_16x16x32_bf16 a[104:107],a[224:227],a[248:251],a[104:107]\nds_read_b128 a[248:251],v239 offset:53248\n"
"v_mfma_f32_16x16x32_bf16 a[24:27],a[212:215],a[252:255],a[24:27]\nbuffer_load_dwordx4 v224,s[44:47],s75 offen offset:2048 lds\ns_mov_b32 s72,1734144\nv_mfma_f32_16x16x32_bf16 a[52:55],a[216:219],a[252:255],a[52:55]\nv_mfma_f32_16x16x32_bf16 a[80:83],a[220:223],a[252:255],a[80:83]\nv_mfma_f32_16x16x32_bf16 a[108:111],a[224:227],a[252:255],a[108:111]\nds_read_b128 a[252:255],v239 offset:55296\ns_waitcnt lgkmcnt(3)\n"
"v_mfma_f32_16x16x32_bf16 v[0:3],a[196:199],a[228:231],v[0:3]\nbuffer_load_dwordx4 v224,s[44:47],s72 offen offset:3072 lds\nv_mfma_f32_16x16x32_bf16 v[28:31],a[200:203],a[228:231],v[28:31]\nds_read_b128 a[212:215],v236 offset:4096\nv_mfma_f32_16x16x32_bf16 v[56:59],a[204:207],a[228:231],v[56:59]\nv_mfma_f32_16x16x32_bf16 v[84:87],a[208:211],a[228:231],v[84:87]\nds_read_b128 a[228:231],v239 offset:44032\nv_mfma_f32_16x16x32_bf16 v[4:7],a[196:199],a[232:235],v[4:7]\n"
"ds_read_b128 a[216:219],v236 offset:6144\ns_add_u32 m0,s64,4096\nv_mfma_f32_16x16x32_bf16 v[32:35],a[200:203],a[232:235],v[32:35]\ns_mov_b32 s73,1835520\nv_mfma_f32_16x16x32_bf16 v[60:63],a[204:207],a[232:235],v[60:63]\nv_mfma_f32_16x16x32_bf16 v[88:91],a[208:211],a[232:235],v[88:91]\nds_read_b128 a[232:235],v239 offset:46080\nv_mfma_f32_16x16x32_bf16 v[8:11],a[196:199],a[236:239],v[8:11]\n"
"v_mfma_f32_16x16x32_bf16 v[36:39],a[200:203],a[236:239],v[36:39]\nv_mfma_f32_16x16x32_bf16 v[64:67],a[204:207],a[236:239],v[64:67]\nv_mfma_f32_16x16x32_bf16 v[92:95],a[208:211],a[236:239],v[92:95]\nds_read_b128 a[236:239],v239 offset:48128\ns_waitcnt lgkmcnt(7)\nv_mfma_f32_16x16x32_bf16 v[12:15],a[196:199],a[240:243],v[12:15]\nds_read_b128 a[220:223],v236 offset:8192\nv_mfma_f32_16x16x32_bf16 v[40:43],a[200:203],a[240:243],v[40:43]\n"
"v_mfma_f32_16x16x32_bf16 v[68:71],a[204:207],a[240:243],v[68:71]\nv_mfma_f32_16x16x32_bf16 v[96:99],a[208:211],a[240:243],v[96:99]\nds_read_b128 a[240:243],v239 offset:50176\nv_mfma_f32_16x16x32_bf16 v[16:19],a[196:199],a[244:247],v[16:19]\nds_read_b128 a[224:227],v236 offset:10240\nv_mfma_f32_16x16x32_bf16 v[44:47],a[200:203],a[244:247],v[44:47]\nv_mfma_f32_16x16x32_bf16 v[72:75],a[204:207],a[244:247],v[72:75]\nv_mfma_f32_16x16x32_bf16 v[100:103],a[208:211],a[244:247],v[100:103]\n"
"ds_read_b128 a[244:247],v239 offset:52224\ns_waitcnt lgkmcnt(9)\nv_mfma_f32_16x16x32_bf16 v[20:23],a[196:199],a[248:251],v[20:23]\nv_mfma_f32_16x16x32_bf16 v[48:51],a[200:203],a[248:251],v[48:51]\nv_mfma_f32_16x16x32_bf16 v[76:79],a[204:207],a[248:251],v[76:79]\nv_mfma_f32_16x16x32_bf16 v[104:107],a[208:211],a[248:251],v[104:107]\nds_read_b128 a[248:251],v239 offset:54272\nv_mfma_f32_16x16x32_bf16 v[24:27],a[196:199],a[252:255],v[24:27]\n"
"v_mfma_f32_16x16x32_bf16 v[52:55],a[200:203],a[252:255],v[52:55]\nv_mfma_f32_16x16x32_bf16 v[80:83],a[204:207],a[252:255],v[80:83]\nv_mfma_f32_16x16x32_bf16 v[108:111],a[208:211],a[252:255],v[108:111]\nds_read_b128 a[252:255],v239 offset:56320\ns_waitcnt lgkmcnt(3)\nv_mfma_f32_16x16x32_bf16 v[0:3],a[212:215],a[228:231],v[0:3]\nbuffer_load_dwordx4 v224,s[44:47],s73 offen offset:0 lds\nv_bfe_u32 v255,v239,2,6\n"
"v_mfma_f32_16x16x32_bf16 v[28:31],a[216:219],a[228:231],v[28:31]\ns_waitcnt vmcnt(13)\nds_read_b128 a[196:199],v235 offset:12288\nv_mfma_f32_16x16x32_bf16 v[56:59],a[220:223],a[228:231],v[56:59]\ns_mov_b32 s74,1867264\nv_mfma_f32_16x16x32_bf16 v[84:87],a[224:227],a[228:231],v[84:87]\nds_read_b128 a[228:231],v239 offset:0\nv_mfma_f32_16x16x32_bf16 v[4:7],a[212:215],a[232:235],v[4:7]\n"
"buffer_load_dwordx4 v224,s[44:47],s74 offen offset:1024 lds\ns_mov_b32 s75,1964544\nv_mfma_f32_16x16x32_bf16 v[32:35],a[216:219],a[232:235],v[32:35]\nds_read_b128 a[200:203],v235 offset:14336\nv_mfma_f32_16x16x32_bf16 v[60:63],a[220:223],a[232:235],v[60:63]\nv_mfma_f32_16x16x32_bf16 v[88:91],a[224:227],a[232:235],v[88:91]\nds_read_b128 a[232:235],v239 offset:2048\nv_mfma_f32_16x16x32_bf16 v[8:11],a[212:215],a[236:239],v[8:11]\n"
"buffer_load_dwordx4 v224,s[44:47],s75 offen offset:2048 lds\nv_max3_f32 v241,v0,v1,v0\nv_mfma_f32_16x16x32_bf16 v[36:39],a[216:219],a[236:239],v[36:39]\ns_mov_b32 s72,1996288\nv_max3_f32 v241,v2,v3,v241\nv_max3_f32 v241,v28,v29,v241\nv_max3_f32 v241,v30,v31,v241\nv_mfma_f32_16x16x32_bf16 v[64:67],a[220:223],a[236:239],v[64:67]\n"
"v_max3_f32 v241,v56,v57,v241\nv_max3_f32 v241,v58,v59,v241\nv_mfma_f32_16x16x32_bf16 v[92:95],a[224:227],a[236:239],v[92:95]\nds_read_b128 a[236:239],v239 offset:4096\ns_waitcnt lgkmcnt(7)\nv_max3_f32 v241,v84,v85,v241\nv_mfma_f32_16x16x32_bf16 v[12:15],a[212:215],a[240:243],v[12:15]\nbuffer_load_dwordx4 v224,s[44:47],s72 offen offset:3072 lds\n"
"v_max3_f32 v241,v86,v87,v241\nv_mfma_f32_16x16x32_bf16 v[40:43],a[216:219],a[240:243],v[40:43]\ns_waitcnt vmcnt(12)\nds_read_b128 a[204:207],v235 offset:16384\nv_mul_f32 v241,s69,v241\nv_mfma_f32_16x16x32_bf16 v[68:71],a[220:223],a[240:243],v[68:71]\ns_add_u32 m0,s64,8192\nds_max_f32 v255,v241 offset:57344\n"
"v_mfma_f32_16x16x32_bf16 v[96:99],a[224:227],a[240:243],v[96:99]\nds_read_b128 a[240:243],v239 offset:6144\nv_max3_f32 v242,v4,v5,v4\nv_max3_f32 v242,v6,v7,v242\nv_mfma_f32_16x16x32_bf16 v[16:19],a[212:215],a[244:247],v[16:19]\ns_mov_b32 s73,131200\nbuffer_load_dwordx4 v227,s[48:51],s73 offen offset:0 nt lds\nv_mfma_f32_16x16x32_bf16 v[44:47],a[216:219],a[244:247],v[44:47]\n"
"ds_read_b128 a[208:211],v235 offset:18432\nv_max3_f32 v242,v32,v33,v242\nv_max3_f32 v242,v34,v35,v242\nv_mfma_f32_16x16x32_bf16 v[72:75],a[220:223],a[244:247],v[72:75]\ns_mov_b32 s74,138368\nv_max3_f32 v242,v60,v61,v242\nv_max3_f32 v242,v62,v63,v242\nv_mfma_f32_16x16x32_bf16 v[100:103],a[224:227],a[244:247],v[100:103]\n"
"ds_read_b128 a[244:247],v239 offset:8192\ns_waitcnt lgkmcnt(10)\nv_max3_f32 v242,v88,v89,v242\nv_mfma_f32_16x16x32_bf16 v[20:23],a[212:215],a[248:251],v[20:23]\nbuffer_load_dwordx4 v228,s[48:51],s74 offen offset:1024 nt lds\nv_max3_f32 v242,v90,v91,v242\nv_mfma_f32_16x16x32_bf16 v[48:51],a[216:219],a[248:251],v[48:51]\ns_mov_b32 s75,161920\n"
"v_mul_f32 v242,s69,v242\nv_max3_f32 v243,v8,v9,v8\nv_max3_f32 v243,v10,v11,v243\nv_mfma_f32_16x16x32_bf16 v[76:79],a[220:223],a[248:251],v[76:79]\nds_max_f32 v255,v242 offset:57408\nv_mfma_f32_16x16x32_bf16 v[104:107],a[224:227],a[248:251],v[104:107]\nds_read_b128 a[248:251],v239 offset:10240\nv_max3_f32 v243,v36,v37,v243\n"
"v_max3_f32 v243,v38,v39,v243\nv_mfma_f32_16x16x32_bf16 v[24:27],a[212:215],a[252:255],v[24:27]\nbuffer_load_dwordx4 v227,s[48:51],s75 offen offset:2048 nt lds\nv_max3_f32 v243,v64,v65,v243\nv_mfma_f32_16x16x32_bf16 v[52:55],a[216:219],a[252:255],v[52:55]\ns_mov_b32 s72,169088\nv_max3_f32 v243,v66,v67,v243\nv_max3_f32 v243,v92,v93,v243\n"
"v_max3_f32 v243,v94,v95,v243\nv_mfma_f32_16x16x32_bf16 v[80:83],a[220:223],a[252:255],v[80:83]\nv_mul_f32 v243,s69,v243\nv_max3_f32 v244,v12,v13,v12\nv_mfma_f32_16x16x32_bf16 v[108:111],a[224:227],a[252:255],v[108:111]\nds_read_b128 a[252:255],v239 offset:12288\ns_waitcnt lgkmcnt(4)\nv_max3_f32 v244,v14,v15,v244\n"
"v_mfma_f32_16x16x32_bf16 v[112:115],a[196:199],a[228:231],0\nbuffer_load_dwordx4 v228,s[48:51],s72 offen offset:3072 nt lds\nv_max3_f32 v244,v40,v41,v244\nv_mfma_f32_16x16x32_bf16 v[140:143],a[200:203],a[228:231],0\nds_read_b128 a[212:215],v236 offset:12288\nv_max3_f32 v244,v42,v43,v244\nv_max3_f32 v244,v68,v69,v244\nv_mfma_f32_16x16x32_bf16 v[168:171],a[204:207],a[228:231],0\n"
"ds_max_f32 v255,v243 offset:57472\nv_mfma_f32_16x16x32_bf16 v[196:199],a[208:211],a[228:231],0\nds_read_b128 a[228:231],v239 offset:1024\nv_max3_f32 v244,v70,v71,v244\nv_max3_f32 v244,v96,v97,v244\nv_mfma_f32_16x16x32_bf16 v[116:119],a[196:199],a[232:235],0\nds_read_b128 a[216:219],v236 offset:14336\nv_max3_f32 v244,v98,v99,v244\n"
"v_mfma_f32_16x16x32_bf16 v[144:147],a[200:203],a[232:235],0\ns_add_u32 m0,s64,12288\nv_mul_f32 v244,s69,v244\nv_max3_f32 v245,v16,v17,v16\nv_max3_f32 v245,v18,v19,v245\nv_mfma_f32_16x16x32_bf16 v[172:175],a[204:207],a[232:235],0\ns_mov_b32 s73,196736\nds_max_f32 v255,v244 offset:57536\n"
"v_mfma_f32_16x16x32_bf16 v[200:203],a[208:211],a[232:235],0\nds_read_b128 a[232:235],v239 offset:3072\nv_max3_f32 v245,v44,v45,v245\nv_max3_f32 v245,v46,v47,v245\nv_mfma_f32_16x16x32_bf16 v[120:123],a[196:199],a[236:239],0\nv_max3_f32 v245,v72,v73,v245\nv_max3_f32 v245,v74,v75,v245\nv_mfma_f32_16x16x32_bf16 v[148:151],a[200:203],a[236:239],0\n"
"v_max3_f32 v245,v100,v101,v245\nv_max3_f32 v245,v102,v103,v245\nv_mul_f32 v245,s69,v245\nv_mfma_f32_16x16x32_bf16 v[176:179],a[204:207],a[236:239],0\nds_max_f32 v255,v245 offset:57600\nv_mfma_f32_16x16x32_bf16 v[204:207],a[208:211],a[236:239],0\nds_read_b128 a[236:239],v239 offset:5120\ns_waitcnt lgkmcnt(11)\n"
"v_max3_f32 v246,v20,v21,v20\nv_mfma_f32_16x16x32_bf16 v[124:127],a[196:199],a[240:243],0\nds_read_b128 a[220:223],v236 offset:16384\nv_max3_f32 v246,v22,v23,v246\nv_mfma_f32_16x16x32_bf16 v[152:155],a[200:203],a[240:243],0\nv_max3_f32 v246,v48,v49,v246\nv_max3_f32 v246,v50,v51,v246\nv_max3_f32 v246,v76,v77,v246\n"
"v_mfma_f32_16x16x32_bf16 v[180:183],a[204:207],a[240:243],0\nv_max3_f32 v246,v78,v79,v246\nv_max3_f32 v246,v104,v105,v246\nv_mfma_f32_16x16x32_bf16 v[208:211],a[208:211],a[240:243],0\nds_read_b128 a[240:243],v239 offset:7168\nv_max3_f32 v246,v106,v107,v246\nv_mul_f32 v246,s69,v246\nv_mfma_f32_16x16x32_bf16 v[128:131],a[196:199],a[244:247],0\n"
"ds_read_b128 a[224:227],v236 offset:18432\nv_max3_f32 v247,v24,v25,v24\nv_mfma_f32_16x16x32_bf16 v[156:159],a[200:203],a[244:247],0\nds_max_f32 v255,v246 offset:57664\nv_mfma_f32_16x16x32_bf16 v[184:187],a[204:207],a[244:247],0\nv_max3_f32 v247,v26,v27,v247\nv_max3_f32 v247,v52,v53,v247\nv_mfma_f32_16x16x32_bf16 v[212:215],a[208:211],a[244:247],0\n"
"ds_read_b128 a[244:247],v239 offset:9216\ns_waitcnt lgkmcnt(13)\nv_max3_f32 v247,v54,v55,v247\nv_mfma_f32_16x16x32_bf16 v[132:135],a[196:199],a[248:251],0\nv_max3_f32 v247,v80,v81,v247\nv_max3_f32 v247,v82,v83,v247\nv_mfma_f32_16x16x32_bf16 v[160:163],a[200:203],a[248:251],0\nv_max3_f32 v247,v108,v109,v247\n"
"v_max3_f32 v247,v110,v111,v247\nv_mul_f32 v247,s69,v247\nv_mfma_f32_16x16x32_bf16 v[188:191],a[204:207],a[248:251],0\nds_max_f32 v255,v247 offset:57728\nv_mfma_f32_16x16x32_bf16 v[216:219],a[208:211],a[248:251],0\nds_read_b128 a[248:251],v239 offset:11264\nv_mfma_f32_16x16x32_bf16 v[136:139],a[196:199],a[252:255],0\nv_mfma_f32_16x16x32_bf16 v[164:167],a[200:203],a[252:255],0\n"
"v_mfma_f32_16x16x32_bf16 v[192:195],a[204:207],a[252:255],0\nv_mfma_f32_16x16x32_bf16 v[220:223],a[208:211],a[252:255],0\nds_read_b128 a[252:255],v239 offset:13312\ns_waitcnt lgkmcnt(5)\ns_waitcnt lgkmcnt(1)\nv_mfma_f32_16x16x32_bf16 v[112:115],a[212:215],a[228:231],v[112:115]\nbuffer_load_dwordx4 v227,s[48:51],s73 offen offset:0 nt lds\ns_barrier\n"
"v_mfma_f32_16x16x32_bf16 v[140:143],a[216:219],a[228:231],v[140:143]\ns_waitcnt vmcnt(9)\nds_read_b64_tr_b16 a[196:197],v231 offset:0\nv_mfma_f32_16x16x32_bf16 v[168:171],a[220:223],a[228:231],v[168:171]\nds_read_b64_tr_b16 a[198:199],v231 offset:1024\ns_mov_b32 s74,203904\nv_mfma_f32_16x16x32_bf16 v[196:199],a[224:227],a[228:231],v[196:199]\ns_waitcnt lgkmcnt(2)\n"
"s_barrier\nds_read_b128 a[228:231],v237 offset:0\nv_mfma_f32_16x16x32_bf16 v[116:119],a[212:215],a[232:235],v[116:119]\nbuffer_load_dwordx4 v228,s[48:51],s74 offen offset:1024 nt lds\ns_mov_b32 s75,227456\nv_mfma_f32_16x16x32_bf16 v[144:147],a[216:219],a[232:235],v[144:147]\nds_read_b64_tr_b16 a[200:201],v232 offset:0\nds_read_b32 v241,v255 offset:57344\n"
"v_mfma_f32_16x16x32_bf16 v[172:175],a[220:223],a[232:235],v[172:175]\nds_read_b32 v242,v255 offset:57408\nv_mfma_f32_16x16x32_bf16 v[200:203],a[224:227],a[232:235],v[200:203]\nds_read_b128 a[232:235],v237 offset:2048\nds_read_b32 v243,v255 offset:57472\nv_mfma_f32_16x16x32_bf16 v[120:123],a[212:215],a[236:239],v[120:123]\nbuffer_load_dwordx4 v227,s[48:51],s75 offen offset:2048 nt lds\ns_mov_b32 s72,234624\n"
"v_mfma_f32_16x16x32_bf16 v[148:151],a[216:219],a[236:239],v[148:151]\nds_read_b64_tr_b16 a[202:203],v232 offset:1024\nds_read_b32 v244,v255 offset:57536\nv_mfma_f32_16x16x32_bf16 v[176:179],a[220:223],a[236:239],v[176:179]\nds_read_b32 v245,v255 offset:57600\nv_mfma_f32_16x16x32_bf16 v[204:207],a[224:227],a[236:239],v[204:207]\nds_read_b128 a[236:239],v237 offset:4096\nds_read_b32 v246,v255 offset:57664\n"
"v_mfma_f32_16x16x32_bf16 v[124:127],a[212:215],a[240:243],v[124:127]\nbuffer_load_dwordx4 v228,s[48:51],s72 offen offset:3072 nt lds\ns_add_u32 m0,s64,16384\nv_mfma_f32_16x16x32_bf16 v[152:155],a[216:219],a[240:243],v[152:155]\nds_read_b64_tr_b16 a[204:205],v233 offset:0\nds_read_b32 v247,v255 offset:57728\nv_mfma_f32_16x16x32_bf16 v[180:183],a[220:223],a[240:243],v[180:183]\nds_read_b64_tr_b16 a[206:207],v233 offset:1024\n"
"s_mov_b32 s73,2097664\nv_mfma_f32_16x16x32_bf16 v[208:211],a[224:227],a[240:243],v[208:211]\nds_read_b128 a[240:243],v237 offset:6144\nv_mfma_f32_16x16x32_bf16 v[128:131],a[212:215],a[244:247],v[128:131]\nbuffer_load_dwordx4 v224,s[44:47],s73 offen offset:0 lds\ns_mov_b32 s74,2129408\nv_mfma_f32_16x16x32_bf16 v[156:159],a[216:219],a[244:247],v[156:159]\nds_read_b64_tr_b16 a[208:209],v234 offset:0\n"
"s_waitcnt lgkmcnt(3)\nv_fma_f32 v0,v0,s69,-v241\nv_mfma_f32_16x16x32_bf16 v[184:187],a[220:223],a[244:247],v[184:187]\nv_fma_f32 v1,v1,s69,-v241\nv_fma_f32 v2,v2,s69,-v241\nv_mfma_f32_16x16x32_bf16 v[212:215],a[224:227],a[244:247],v[212:215]\nds_read_b128 a[244:247],v237 offset:8192\nv_fma_f32 v3,v3,s69,-v241\n"
"v_fma_f32 v4,v4,s69,-v242\nv_mfma_f32_16x16x32_bf16 v[132:135],a[212:215],a[248:251],v[132:135]\nbuffer_load_dwordx4 v224,s[44:47],s74 offen offset:1024 lds\nv_fma_f32 v5,v5,s69,-v242\nv_mfma_f32_16x16x32_bf16 v[160:163],a[216:219],a[248:251],v[160:163]\nds_read_b64_tr_b16 a[210:211],v234 offset:1024\nv_exp_f32 v0,v0\nv_mfma_f32_16x16x32_bf16 v[188:191],a[220:223],a[248:251],v[188:191]\n"
"s_mov_b32 s75,2226688\nv_exp_f32 v1,v1\nv_mfma_f32_16x16x32_bf16 v[216:219],a[224:227],a[248:251],v[216:219]\nds_read_b128 a[248:251],v237 offset:10240\nv_exp_f32 v2,v2\nv_mfma_f32_16x16x32_bf16 v[136:139],a[212:215],a[252:255],v[136:139]\nbuffer_load_dwordx4 v224,s[44:47],s75 offen offset:2048 lds\nv_cvt_pk_bf16_f32 v0,v0,v1\n"
"v_mfma_f32_16x16x32_bf16 v[164:167],a[216:219],a[252:255],v[164:167]\ns_mov_b32 s72,2258432\nv_exp_f32 v3,v3\nv_fma_f32 v6,v6,s69,-v242\nv_mfma_f32_16x16x32_bf16 v[192:195],a[220:223],a[252:255],v[192:195]\nv_cvt_pk_bf16_f32 v1,v2,v3\nv_fma_f32 v7,v7,s69,-v242\nv_mfma_f32_16x16x32_bf16 v[220:223],a[224:227],a[252:255],v[220:223]\n"
"ds_read_b128 a[252:255],v237 offset:12288\ns_waitcnt lgkmcnt(2)\nv_fma_f32 v8,v8,s69,-v243\nv_mfma_f32_16x16x32_bf16 a[0:3],a[196:199],a[228:231],a[0:3]\nbuffer_load_dwordx4 v224,s[44:47],s72 offen offset:3072 lds\nv_fma_f32 v9,v9,s69,-v243\nv_mfma_f32_16x16x32_bf16 a[28:31],a[200:203],a[228:231],a[28:31]\ns_waitcnt vmcnt(12)\n"
"ds_read_b64_tr_b16 a[212:213],v231 offset:4096\nv_fma_f32 v10,v10,s69,-v243\nv_mfma_f32_16x16x32_bf16 a[56:59],a[204:207],a[228:231],a[56:59]\nds_read_b64_tr_b16 a[214:215],v231 offset:5120\nv_fma_f32 v11,v11,s69,-v243\nv_mfma_f32_16x16x32_bf16 a[84:87],a[208:211],a[228:231],a[84:87]\nds_read_b128 a[228:231],v238 offset:0\nv_exp_f32 v4,v4\n"
"v_mfma_f32_16x16x32_bf16 a[4:7],a[196:199],a[232:235],a[4:7]\ns_add_u32 m0,s64,0\nv_exp_f32 v5,v5\nv_mfma_f32_16x16x32_bf16 a[32:35],a[200:203],a[232:235],a[32:35]\ns_mov_b32 s73,2359808\nbuffer_load_dwordx4 v224,s[44:47],s73 offen offset:0 lds\nv_cvt_pk_bf16_f32 v4,v4,v5\nv_mfma_f32_16x16x32_bf16 a[60:63],a[204:207],a[232:235],a[60:63]\n"
"ds_read_b64_tr_b16 a[216:217],v232 offset:4096\nv_fma_f32 v12,v12,s69,-v244\nv_mfma_f32_16x16x32_bf16 a[88:91],a[208:211],a[232:235],a[88:91]\nds_read_b128 a[232:235],v238 offset:2048\nv_exp_f32 v6,v6\nv_mfma_f32_16x16x32_bf16 a[8:11],a[196:199],a[236:239],a[8:11]\ns_mov_b32 s74,2391552\nbuffer_load_dwordx4 v224,s[44:47],s74 offen offset:1024 lds\n"
"v_mfma_f32_16x16x32_bf16 a[36:39],a[200:203],a[236:239],a[36:39]\nds_read_b64_tr_b16 a[218:219],v232 offset:5120\nv_exp_f32 v7,v7\nv_mfma_f32_16x16x32_bf16 a[64:67],a[204:207],a[236:239],a[64:67]\ns_mov_b32 s75,2488832\nv_cvt_pk_bf16_f32 v5,v6,v7\nv_fma_f32 v13,v13,s69,-v244\nv_mfma_f32_16x16x32_bf16 a[92:95],a[208:211],a[236:239],a[92:95]\n"
"ds_read_b128 a[236:239],v238 offset:4096\nv_exp_f32 v8,v8\nv_mfma_f32_16x16x32_bf16 a[12:15],a[196:199],a[240:243],a[12:15]\nbuffer_load_dwordx4 v224,s[44:47],s75 offen offset:2048 lds\nv_fma_f32 v14,v14,s69,-v244\nv_mfma_f32_16x16x32_bf16 a[40:43],a[200:203],a[240:243],a[40:43]\nds_read_b64_tr_b16 a[220:221],v233 offset:4096\nv_exp_f32 v9,v9\n"
"v_mfma_f32_16x16x32_bf16 a[68:71],a[204:207],a[240:243],a[68:71]\nds_read_b64_tr_b16 a[222:223],v233 offset:5120\nv_cvt_pk_bf16_f32 v8,v8,v9\nv_mfma_f32_16x16x32_bf16 a[96:99],a[208:211],a[240:243],a[96:99]\nds_read_b128 a[240:243],v238 offset:6144\nv_exp_f32 v10,v10\nv_mfma_f32_16x16x32_bf16 a[16:19],a[196:199],a[244:247],a[16:19]\ns_mov_b32 s72,2520576\n"
"buffer_load_dwordx4 v224,s[44:47],s72 offen offset:3072 lds\nv_mfma_f32_16x16x32_bf16 a[44:47],a[200:203],a[244:247],a[44:47]\nds_read_b64_tr_b16 a[224:225],v234 offset:4096\nv_exp_f32 v11,v11\nv_mfma_f32_16x16x32_bf16 a[72:75],a[204:207],a[244:247],a[72:75]\nv_cvt_pk_bf16_f32 v9,v10,v11\nv_fma_f32 v15,v15,s69,-v244\nv_mfma_f32_16x16x32_bf16 a[100:103],a[208:211],a[244:247],a[100:103]\n"
"ds_read_b128 a[244:247],v238 offset:8192\ns_waitcnt lgkmcnt(12)\nv_fma_f32 v16,v16,s69,-v245\nv_mfma_f32_16x16x32_bf16 a[20:23],a[196:199],a[248:251],a[20:23]\nds_read_b64_tr_b16 a[226:227],v234 offset:5120\nv_fma_f32 v17,v17,s69,-v245\nv_mfma_f32_16x16x32_bf16 a[48:51],a[200:203],a[248:251],a[48:51]\ns_add_u32 m0,s64,4096\n"
"v_exp_f32 v12,v12\nv_fma_f32 v18,v18,s69,-v245\nv_mfma_f32_16x16x32_bf16 a[76:79],a[204:207],a[248:251],a[76:79]\ns_mov_b32 s73,131328\nv_exp_f32 v13,v13\nv_mfma_f32_16x16x32_bf16 a[104:107],a[208:211],a[248:251],a[104:107]\nds_read_b128 a[248:251],v238 offset:10240\nv_exp_f32 v14,v14\n"
"v_mfma_f32_16x16x32_bf16 a[24:27],a[196:199],a[252:255],a[24:27]\nv_exp_f32 v15,v15\nv_mfma_f32_16x16x32_bf16 a[52:55],a[200:203],a[252:255],a[52:55]\nv_cvt_pk_bf16_f32 v12,v12,v13\nv_cvt_pk_bf16_f32 v13,v14,v15\nv_fma_f32 v19,v19,s69,-v245\nv_mfma_f32_16x16x32_bf16 a[80:83],a[204:207],a[252:255],a[80:83]\nv_exp_f32 v16,v16\n"
"v_mfma_f32_16x16x32_bf16 a[108:111],a[208:211],a[252:255],a[108:111]\nds_read_b128 a[252:255],v238 offset:12288\ns_waitcnt lgkmcnt(2)\nv_fma_f32 v20,v20,s69,-v246\nv_mfma_f32_16x16x32_bf16 a[0:3],a[212:215],a[228:231],a[0:3]\nbuffer_load_dwordx4 v227,s[48:51],s73 offen offset:0 nt lds\nv_fma_f32 v21,v21,s69,-v246\nv_mfma_f32_16x16x32_bf16 a[28:31],a[216:219],a[228:231],a[28:31]\n"
"s_waitcnt vmcnt(13)\nds_read_b128 a[196:199],v235 offset:8192\nv_fma_f32 v22,v22,s69,-v246\nv_mfma_f32_16x16x32_bf16 a[56:59],a[220:223],a[228:231],a[56:59]\ns_mov_b32 s74,138496\nv_exp_f32 v17,v17\nv_mfma_f32_16x16x32_bf16 a[84:87],a[224:227],a[228:231],a[84:87]\ns_waitcnt lgkmcnt(1)\n"
"s_barrier\nds_read_b128 a[228:231],v239 offset:14336\nv_mfma_f32_16x16x32_bf16 a[4:7],a[212:215],a[232:235],a[4:7]\nbuffer_load_dwordx4 v228,s[48:51],s74 offen offset:1024 nt lds\nv_cvt_pk_bf16_f32 v16,v16,v17\nv_mfma_f32_16x16x32_bf16 a[32:35],a[216:219],a[232:235],a[32:35]\nds_read_b128 a[200:203],v235 offset:10240\nv_exp_f32 v18,v18\n"
"v_mfma_f32_16x16x32_bf16 a[60:63],a[220:223],a[232:235],a[60:63]\ns_add_u32 m0,s65,147456\nv_exp_f32 v19,v19\nv_mfma_f32_16x16x32_bf16 a[88:91],a[224:227],a[232:235],a[88:91]\nds_read_b128 a[232:235],v239 offset:16384\nv_cvt_pk_bf16_f32 v17,v18,v19\nv_fma_f32 v23,v23,s69,-v246\nv_mfma_f32_16x16x32_bf16 a[8:11],a[212:215],a[236:239],a[8:11]\n"
"s_mov_b32 s75,512\nbuffer_load_dwordx4 v229,s[40:43],s75 offen offset:0 lds\nv_mfma_f32_16x16x32_bf16 a[36:39],a[216:219],a[236:239],a[36:39]\ns_mov_b32 s72,6656\nv_exp_f32 v20,v20\nv_fma_f32 v24,v24,s69,-v247\nv_mfma_f32_16x16x32_bf16 a[64:67],a[220:223],a[236:239],a[64:67]\nv_exp_f32 v21,v21\n"
"v_mfma_f32_16x16x32_bf16 a[92:95],a[224:227],a[236:239],a[92:95]\nds_read_b128 a[236:239],v239 offset:18432\nv_exp_f32 v22,v22\nv_mfma_f32_16x16x32_bf16 a[12:15],a[212:215],a[240:243],a[12:15]\nbuffer_load_dwordx4 v230,s[40:43],s72 offen offset:1024 lds\nv_cvt_pk_bf16_f32 v20,v20,v21\nv_mfma_f32_16x16x32_bf16 a[40:43],a[216:219],a[240:243],a[40:43]\ns_waitcnt vmcnt(12)\n"
"ds_read_b128 a[204:207],v235 offset:12288\nv_fma_f32 v25,v25,s69,-v247\nv_mfma_f32_16x16x32_bf16 a[68:71],a[220:223],a[240:243],a[68:71]\ns_add_u32 m0,s66,147456\nv_exp_f32 v23,v23\nv_mfma_f32_16x16x32_bf16 a[96:99],a[224:227],a[240:243],a[96:99]\nds_read_b128 a[240:243],v239 offset:20480\nv_cvt_pk_bf16_f32 v21,v22,v23\n"
"v_fma_f32 v26,v26,s69,-v247\nv_mfma_f32_16x16x32_bf16 a[16:19],a[212:215],a[244:247],a[16:19]\ns_add_u32 s73,s67,512\nbuffer_load_dwordx4 v229,s[40:43],s73 offen offset:0 lds\nv_mfma_f32_16x16x32_bf16 a[44:47],a[216:219],a[244:247],a[44:47]\nds_read_b128 a[208:211],v235 offset:14336\nv_fma_f32 v27,v27,s69,-v247\nv_fma_f32 v28,v28,s69,-v241\n"
"v_mfma_f32_16x16x32_bf16 a[72:75],a[220:223],a[244:247],a[72:75]\ns_add_u32 s74,s67,6656\nv_exp_f32 v24,v24\nv_mfma_f32_16x16x32_bf16 a[100:103],a[224:227],a[244:247],a[100:103]\nds_read_b128 a[244:247],v239 offset:22528\nv_exp_f32 v25,v25\nv_mfma_f32_16x16x32_bf16 a[20:23],a[212:215],a[248:251],a[20:23]\nbuffer_load_dwordx4 v230,s[40:43],s74 offen offset:1024 lds\n"
"v_cvt_pk_bf16_f32 v24,v24,v25\nv_mfma_f32_16x16x32_bf16 a[48:51],a[216:219],a[248:251],a[48:51]\ns_add_u32 m0,s64,4096\nv_exp_f32 v26,v26\nv_fma_f32 v29,v29,s69,-v241\nv_mfma_f32_16x16x32_bf16 a[76:79],a[220:223],a[248:251],a[76:79]\ns_mov_b32 s75,162048\nv_exp_f32 v27,v27\n"
"v_mfma_f32_16x16x32_bf16 a[104:107],a[224:227],a[248:251],a[104:107]\nds_read_b128 a[248:251],v239 offset:24576\nv_cvt_pk_bf16_f32 v25,v26,v27\nv_fma_f32 v30,v30,s69,-v241\nv_mfma_f32_16x16x32_bf16 a[24:27],a[212:215],a[252:255],a[24:27]\nbuffer_load_dwordx4 v227,s[48:51],s75 offen offset:2048 nt lds\nv_fma_f32 v31,v31,s69,-v241\nv_mfma_f32_16x16x32_bf16 a[52:55],a[216:219],a[252:255],a[52:55]\n"
"s_mov_b32 s72,169216\nv_exp_f32 v28,v28\nv_fma_f32 v32,v32,s69,-v242\nv_mfma_f32_16x16x32_bf16 a[80:83],a[220:223],a[252:255],a[80:83]\nv_exp_f32 v29,v29\nv_mfma_f32_16x16x32_bf16 a[108:111],a[224:227],a[252:255],a[108:111]\nds_read_b128 a[252:255],v239 offset:26624\ns_waitcnt lgkmcnt(3)\n"
"v_cvt_pk_bf16_f32 v28,v28,v29\nv_mfma_f32_16x16x32_bf16 v[112:115],a[196:199],a[228:231],v[112:115]\nbuffer_load_dwordx4 v228,s[48:51],s72 offen offset:3072 nt lds\nv_fma_f32 v33,v33,s69,-v242\nv_mfma_f32_16x16x32_bf16 v[140:143],a[200:203],a[228:231],v[140:143]\nds_read_b128 a[212:215],v236 offset:8192\nv_exp_f32 v30,v30\nv_mfma_f32_16x16x32_bf16 v[168:171],a[204:207],a[228:231],v[168:171]\n"
"v_exp_f32 v31,v31\nv_mfma_f32_16x16x32_bf16 v[196:199],a[208:211],a[228:231],v[196:199]\nds_read_b128 a[228:231],v239 offset:15360\nv_cvt_pk_bf16_f32 v29,v30,v31\nv_fma_f32 v34,v34,s69,-v242\nv_mfma_f32_16x16x32_bf16 v[116:119],a[196:199],a[232:235],v[116:119]\nds_read_b128 a[216:219],v236 offset:10240\nv_fma_f32 v35,v35,s69,-v242\n"
"v_mfma_f32_16x16x32_bf16 v[144:147],a[200:203],a[232:235],v[144:147]\ns_add_u32 m0,s64,8192\nv_exp_f32 v32,v32\nv_fma_f32 v36,v36,s69,-v243\nv_mfma_f32_16x16x32_bf16 v[172:175],a[204:207],a[232:235],v[172:175]\ns_mov_b32 s73,196864\nv_exp_f32 v33,v33\nv_mfma_f32_16x16x32_bf16 v[200:203],a[208:211],a[232:235],v[200:203]\n"
"ds_read_b128 a[232:235],v239 offset:17408\nv_exp_f32 v34,v34\nv_mfma_f32_16x16x32_bf16 v[120:123],a[196:199],a[236:239],v[120:123]\nv_exp_f32 v35,v35\nv_mfma_f32_16x16x32_bf16 v[148:151],a[200:203],a[236:239],v[148:151]\nv_cvt_pk_bf16_f32 v32,v32,v33\nv_cvt_pk_bf16_f32 v33,v34,v35\nv_fma_f32 v37,v37,s69,-v243\n"
"v_mfma_f32_16x16x32_bf16 v[176:179],a[204:207],a[236:239],v[176:179]\nv_fma_f32 v38,v38,s69,-v243\nv_fma_f32 v39,v39,s69,-v243\nv_mfma_f32_16x16x32_bf16 v[204:207],a[208:211],a[236:239],v[204:207]\nds_read_b128 a[236:239],v239 offset:19456\ns_waitcnt lgkmcnt(7)\nv_fma_f32 v40,v40,s69,-v244\nv_mfma_f32_16x16x32_bf16 v[124:127],a[196:199],a[240:243],v[124:127]\n"
"ds_read_b128 a[220:223],v236 offset:12288\nv_fma_f32 v41,v41,s69,-v244\nv_mfma_f32_16x16x32_bf16 v[152:155],a[200:203],a[240:243],v[152:155]\nv_exp_f32 v36,v36\nv_fma_f32 v42,v42,s69,-v244\nv_mfma_f32_16x16x32_bf16 v[180:183],a[204:207],a[240:243],v[180:183]\nv_exp_f32 v37,v37\nv_mfma_f32_16x16x32_bf16 v[208:211],a[208:211],a[240:243],v[208:211]\n"
"ds_read_b128 a[240:243],v239 offset:21504\nv_exp_f32 v38,v38\nv_mfma_f32_16x16x32_bf16 v[128:131],a[196:199],a[244:247],v[128:131]\nds_read_b128 a[224:227],v236 offset:14336\nv_cvt_pk_bf16_f32 v36,v36,v37\nv_mfma_f32_16x16x32_bf16 v[156:159],a[200:203],a[244:247],v[156:159]\nv_exp_f32 v39,v39\nv_fma_f32 v43,v43,s69,-v244\n"
"v_mfma_f32_16x16x32_bf16 v[184:187],a[204:207],a[244:247],v[184:187]\nv_cvt_pk_bf16_f32 v37,v38,v39\nv_fma_f32 v44,v44,s69,-v245\nv_mfma_f32_16x16x32_bf16 v[212:215],a[208:211],a[244:247],v[212:215]\nds_read_b128 a[244:247],v239 offset:23552\ns_waitcnt lgkmcnt(9)\nv_fma_f32 v45,v45,s69,-v245\nv_mfma_f32_16x16x32_bf16 v[132:135],a[196:199],a[248:251],v[132:135]\n"
"v_exp_f32 v40,v40\nv_mfma_f32_16x16x32_bf16 v[160:163],a[200:203],a[248:251],v[160:163]\nv_exp_f32 v41,v41\nv_fma_f32 v46,v46,s69,-v245\nv_mfma_f32_16x16x32_bf16 v[188:191],a[204:207],a[248:251],v[188:191]\nv_exp_f32 v42,v42\nv_mfma_f32_16x16x32_bf16 v[216:219],a[208:211],a[248:251],v[216:219]\nds_read_b128 a[248:251],v239 offset:25600\n"
"v_exp_f32 v43,v43\nv_mfma_f32_16x16x32_bf16 v[136:139],a[196:199],a[252:255],v[136:139]\nv_cvt_pk_bf16_f32 v40,v40,v41\nv_cvt_pk_bf16_f32 v41,v42,v43\nv_mfma_f32_16x16x32_bf16 v[164:167],a[200:203],a[252:255],v[164:167]\nv_fma_f32 v47,v47,s69,-v245\nv_exp_f32 v44,v44\nv_mfma_f32_16x16x32_bf16 v[192:195],a[204:207],a[252:255],v[192:195]\n"
"v_exp_f32 v45,v45\nv_mfma_f32_16x16x32_bf16 v[220:223],a[208:211],a[252:255],v[220:223]\nds_read_b128 a[252:255],v239 offset:27648\ns_waitcnt lgkmcnt(3)\nv_cvt_pk_bf16_f32 v44,v44,v45\nv_mfma_f32_16x16x32_bf16 v[112:115],a[212:215],a[228:231],v[112:115]\nbuffer_load_dwordx4 v227,s[48:51],s73 offen offset:0 nt lds\nv_fma_f32 v48,v48,s69,-v246\n"
"v_mfma_f32_16x16x32_bf16 v[140:143],a[216:219],a[228:231],v[140:143]\ns_waitcnt vmcnt(13)\nds_read_b64_tr_b16 a[196:197],v231 offset:16384\nv_fma_f32 v49,v49,s69,-v246\nv_mfma_f32_16x16x32_bf16 v[168:171],a[220:223],a[228:231],v[168:171]\nds_read_b64_tr_b16 a[198:199],v231 offset:17408\nv_fma_f32 v50,v50,s69,-v246\nv_mfma_f32_16x16x32_bf16 v[196:199],a[224:227],a[228:231],v[196:199]\n"
"s_waitcnt vmcnt(3) lgkmcnt(2)\ns_barrier\nds_read_b128 a[228:231],v237 offset:0\nv_mfma_f32_16x16x32_bf16 v[116:119],a[212:215],a[232:235],v[116:119]\ns_mov_b32 s74,204032\nbuffer_load_dwordx4 v228,s[48:51],s74 offen offset:1024 nt lds\nv_mfma_f32_16x16x32_bf16 v[144:147],a[216:219],a[232:235],v[144:147]\nds_read_b64_tr_b16 a[200:201],v232 offset:16384\n"
"v_exp_f32 v46,v46\nv_mfma_f32_16x16x32_bf16 v[172:175],a[220:223],a[232:235],v[172:175]\ns_mov_b32 s75,227584\nv_exp_f32 v47,v47\nv_mfma_f32_16x16x32_bf16 v[200:203],a[224:227],a[232:235],v[200:203]\nds_read_b128 a[232:235],v237 offset:2048\nv_cvt_pk_bf16_f32 v45,v46,v47\nv_fma_f32 v51,v51,s69,-v246\n"
"v_mfma_f32_16x16x32_bf16 v[120:123],a[212:215],a[236:239],v[120:123]\nbuffer_load_dwordx4 v227,s[48:51],s75 offen offset:2048 nt lds\nv_fma_f32 v52,v52,s69,-v247\nv_mfma_f32_16x16x32_bf16 v[148:151],a[216:219],a[236:239],v[148:151]\nds_read_b64_tr_b16 a[202:203],v232 offset:17408\nv_exp_f32 v48,v48\nv_mfma_f32_16x16x32_bf16 v[176:179],a[220:223],a[236:239],v[176:179]\ns_mov_b32 s72,234752\n"
"v_exp_f32 v49,v49\nv_mfma_f32_16x16x32_bf16 v[204:207],a[224:227],a[236:239],v[204:207]\nds_read_b128 a[236:239],v237 offset:4096\nv_exp_f32 v50,v50\nv_mfma_f32_16x16x32_bf16 v[124:127],a[212:215],a[240:243],v[124:127]\nbuffer_load_dwordx4 v228,s[48:51],s72 offen offset:3072 nt lds\nv_cvt_pk_bf16_f32 v48,v48,v49\nv_mfma_f32_16x16x32_bf16 v[152:155],a[216:219],a[240:243],v[152:155]\n"
"ds_read_b64_tr_b16 a[204:205],v233 offset:16384\nv_exp_f32 v51,v51\nv_mfma_f32_16x16x32_bf16 v[180:183],a[220:223],a[240:243],v[180:183]\nds_read_b64_tr_b16 a[206:207],v233 offset:17408\nv_cvt_pk_bf16_f32 v49,v50,v51\nv_mfma_f32_16x16x32_bf16 v[208:211],a[224:227],a[240:243],v[208:211]\nds_read_b128 a[240:243],v237 offset:6144\nv_fma_f32 v53,v53,s69,-v247\n"
"v_fma_f32 v54,v54,s69,-v247\nv_mfma_f32_16x16x32_bf16 v[128:131],a[212:215],a[244:247],v[128:131]\ns_add_u32 m0,s64,12288\nv_fma_f32 v55,v55,s69,-v247\nv_fma_f32 v56,v56,s69,-v241\nv_mfma_f32_16x16x32_bf16 v[156:159],a[216:219],a[244:247],v[156:159]\ns_mov_b32 s73,2621952\nbuffer_load_dwordx4 v224,s[44:47],s73 offen offset:0 lds\n"
"v_fma_f32 v57,v57,s69,-v241\nv_mfma_f32_16x16x32_bf16 v[184:187],a[220:223],a[244:247],v[184:187]\nds_read_b64_tr_b16 a[208:209],v234 offset:16384\nv_fma_f32 v58,v58,s69,-v241\nv_mfma_f32_16x16x32_bf16 v[212:215],a[224:227],a[244:247],v[212:215]\nds_read_b128 a[244:247],v237 offset:8192\nv_exp_f32 v52,v52\nv_mfma_f32_16x16x32_bf16 v[132:135],a[212:215],a[248:251],v[132:135]\n"
"s_mov_b32 s74,2653696\nbuffer_load_dwordx4 v224,s[44:47],s74 offen offset:1024 lds\nv_mfma_f32_16x16x32_bf16 v[160:163],a[216:219],a[248:251],v[160:163]\nds_read_b64_tr_b16 a[210:211],v234 offset:17408\nv_exp_f32 v53,v53\nv_mfma_f32_16x16x32_bf16 v[188:191],a[220:223],a[248:251],v[188:191]\ns_mov_b32 s75,2750976\nv_exp_f32 v54,v54\n"
"v_mfma_f32_16x16x32_bf16 v[216:219],a[224:227],a[248:251],v[216:219]\nds_read_b128 a[248:251],v237 offset:10240\nv_exp_f32 v55,v55\nv_mfma_f32_16x16x32_bf16 v[136:139],a[212:215],a[252:255],v[136:139]\nbuffer_load_dwordx4 v224,s[44:47],s75 offen offset:2048 lds\nv_cvt_pk_bf16_f32 v52,v52,v53\nv_mfma_f32_16x16x32_bf16 v[164:167],a[216:219],a[252:255],v[164:167]\ns_mov_b32 s72,2782720\n"
"v_cvt_pk_bf16_f32 v53,v54,v55\nv_fma_f32 v59,v59,s69,-v241\nv_fma_f32 v60,v60,s69,-v242\nv_mfma_f32_16x16x32_bf16 v[192:195],a[220:223],a[252:255],v[192:195]\nv_exp_f32 v56,v56\nv_mfma_f32_16x16x32_bf16 v[220:223],a[224:227],a[252:255],v[220:223]\nds_read_b128 a[252:255],v237 offset:12288\ns_waitcnt lgkmcnt(2)\n"
"v_fma_f32 v61,v61,s69,-v242\nv_mfma_f32_16x16x32_bf16 a[0:3],a[196:199],a[228:231],a[0:3]\nbuffer_load_dwordx4 v224,s[44:47],s72 offen offset:3072 lds\nv_fma_f32 v62,v62,s69,-v242\nv_mfma_f32_16x16x32_bf16 a[28:31],a[200:203],a[228:231],a[28:31]\nds_read_b64_tr_b16 a[212:213],v231 offset:0\nv_exp_f32 v57,v57\nv_mfma_f32_16x16x32_bf16 a[56:59],a[204:207],a[228:231],a[56:59]\n"
"ds_read_b64_tr_b16 a[214:215],v231 offset:1024\nv_cvt_pk_bf16_f32 v56,v56,v57\nv_mfma_f32_16x16x32_bf16 a[84:87],a[208:211],a[228:231],a[84:87]\nds_read_b128 a[228:231],v238 offset:0\nv_exp_f32 v58,v58\nv_mfma_f32_16x16x32_bf16 a[4:7],a[196:199],a[232:235],a[4:7]\ns_add_u32 m0,s64,16384\nv_exp_f32 v59,v59\n"
"v_mfma_f32_16x16x32_bf16 a[32:35],a[200:203],a[232:235],a[32:35]\ns_mov_b32 s73,2884096\nbuffer_load_dwordx4 v224,s[44:47],s73 offen offset:0 lds\nv_cvt_pk_bf16_f32 v57,v58,v59\nv_mfma_f32_16x16x32_bf16 a[60:63],a[204:207],a[232:235],a[60:63]\nds_read_b64_tr_b16 a[216:217],v232 offset:0\nv_fma_f32 v63,v63,s69,-v242\nv_mfma_f32_16x16x32_bf16 a[88:91],a[208:211],a[232:235],a[88:91]\n"
"ds_read_b128 a[232:235],v238 offset:2048\nv_exp_f32 v60,v60\nv_mfma_f32_16x16x32_bf16 a[8:11],a[196:199],a[236:239],a[8:11]\ns_mov_b32 s74,2915840\nbuffer_load_dwordx4 v224,s[44:47],s74 offen offset:1024 lds\nv_mfma_f32_16x16x32_bf16 a[36:39],a[200:203],a[236:239],a[36:39]\nds_read_b64_tr_b16 a[218:219],v232 offset:1024\nv_exp_f32 v61,v61\n"
"v_mfma_f32_16x16x32_bf16 a[64:67],a[204:207],a[236:239],a[64:67]\ns_mov_b32 s75,3013120\nv_exp_f32 v62,v62\nv_mfma_f32_16x16x32_bf16 a[92:95],a[208:211],a[236:239],a[92:95]\nds_read_b128 a[236:239],v238 offset:4096\nv_exp_f32 v63,v63\nv_mfma_f32_16x16x32_bf16 a[12:15],a[196:199],a[240:243],a[12:15]\nbuffer_load_dwordx4 v224,s[44:47],s75 offen offset:2048 lds\n"
"v_cvt_pk_bf16_f32 v60,v60,v61\nv_mfma_f32_16x16x32_bf16 a[40:43],a[200:203],a[240:243],a[40:43]\nds_read_b64_tr_b16 a[220:221],v233 offset:0\nv_cvt_pk_bf16_f32 v61,v62,v63\nv_fma_f32 v64,v64,s69,-v243\nv_mfma_f32_16x16x32_bf16 a[68:71],a[204:207],a[240:243],a[68:71]\nds_read_b64_tr_b16 a[222:223],v233 offset:1024\nv_fma_f32 v65,v65,s69,-v243\n"
"v_mfma_f32_16x16x32_bf16 a[96:99],a[208:211],a[240:243],a[96:99]\nds_read_b128 a[240:243],v238 offset:6144\nv_fma_f32 v66,v66,s69,-v243\nv_fma_f32 v67,v67,s69,-v243\nv_mfma_f32_16x16x32_bf16 a[16:19],a[196:199],a[244:247],a[16:19]\ns_mov_b32 s72,3044864\nbuffer_load_dwordx4 v224,s[44:47],s72 offen offset:3072 lds\nv_mfma_f32_16x16x32_bf16 a[44:47],a[200:203],a[244:247],a[44:47]\n"
"ds_read_b64_tr_b16 a[224:225],v234 offset:0\nv_exp_f32 v64,v64\nv_mfma_f32_16x16x32_bf16 a[72:75],a[204:207],a[244:247],a[72:75]\nv_exp_f32 v65,v65\nv_mfma_f32_16x16x32_bf16 a[100:103],a[208:211],a[244:247],a[100:103]\nds_read_b128 a[244:247],v238 offset:8192\ns_waitcnt lgkmcnt(12)\nv_cvt_pk_bf16_f32 v64,v64,v65\n"
"v_mfma_f32_16x16x32_bf16 a[20:23],a[196:199],a[248:251],a[20:23]\nds_read_b64_tr_b16 a[226:227],v234 offset:1024\nv_fma_f32 v68,v68,s69,-v244\nv_mfma_f32_16x16x32_bf16 a[48:51],a[200:203],a[248:251],a[48:51]\ns_add_u32 m0,s64,0\nv_exp_f32 v66,v66\nv_fma_f32 v69,v69,s69,-v244\nv_mfma_f32_16x16x32_bf16 a[76:79],a[204:207],a[248:251],a[76:79]\n"
"s_mov_b32 s73,131456\nv_exp_f32 v67,v67\nv_mfma_f32_16x16x32_bf16 a[104:107],a[208:211],a[248:251],a[104:107]\nds_read_b128 a[248:251],v238 offset:10240\nv_cvt_pk_bf16_f32 v65,v66,v67\nv_fma_f32 v70,v70,s69,-v244\nv_mfma_f32_16x16x32_bf16 a[24:27],a[196:199],a[252:255],a[24:27]\nv_fma_f32 v71,v71,s69,-v244\n"
"v_fma_f32 v72,v72,s69,-v245\nv_mfma_f32_16x16x32_bf16 a[52:55],a[200:203],a[252:255],a[52:55]\nv_exp_f32 v68,v68\nv_fma_f32 v73,v73,s69,-v245\nv_mfma_f32_16x16x32_bf16 a[80:83],a[204:207],a[252:255],a[80:83]\nv_exp_f32 v69,v69\nv_mfma_f32_16x16x32_bf16 a[108:111],a[208:211],a[252:255],a[108:111]\nds_read_b128 a[252:255],v238 offset:12288\n"
"s_waitcnt lgkmcnt(2)\nv_cvt_pk_bf16_f32 v68,v68,v69\nv_mfma_f32_16x16x32_bf16 a[0:3],a[212:215],a[228:231],a[0:3]\nbuffer_load_dwordx4 v227,s[48:51],s73 offen offset:0 nt lds\nv_fma_f32 v74,v74,s69,-v245\nv_mfma_f32_16x16x32_bf16 a[28:31],a[216:219],a[228:231],a[28:31]\ns_waitcnt vmcnt(13)\nds_read_b128 a[196:199],v235 offset:4096\n"
"v_fma_f32 v75,v75,s69,-v245\nv_mfma_f32_16x16x32_bf16 a[56:59],a[220:223],a[228:231],a[56:59]\ns_mov_b32 s74,138624\nv_exp_f32 v70,v70\nv_mfma_f32_16x16x32_bf16 a[84:87],a[224:227],a[228:231],a[84:87]\ns_waitcnt lgkmcnt(1)\ns_barrier\nds_read_b128 a[228:231],v239 offset:28672\n"
"v_mfma_f32_16x16x32_bf16 a[4:7],a[212:215],a[232:235],a[4:7]\nbuffer_load_dwordx4 v228,s[48:51],s74 offen offset:1024 nt lds\nv_fma_f32 v76,v76,s69,-v246\nv_mfma_f32_16x16x32_bf16 a[32:35],a[216:219],a[232:235],a[32:35]\nds_read_b128 a[200:203],v235 offset:6144\nv_exp_f32 v71,v71\nv_mfma_f32_16x16x32_bf16 a[60:63],a[220:223],a[232:235],a[60:63]\ns_add_u32 m0,s65,147456\n"
"v_cvt_pk_bf16_f32 v69,v70,v71\nv_fma_f32 v77,v77,s69,-v246\nv_mfma_f32_16x16x32_bf16 a[88:91],a[224:227],a[232:235],a[88:91]\nds_read_b128 a[232:235],v239 offset:30720\nv_exp_f32 v72,v72\nv_mfma_f32_16x16x32_bf16 a[8:11],a[212:215],a[236:239],a[8:11]\ns_mov_b32 s75,640\nbuffer_load_dwordx4 v229,s[40:43],s75 offen offset:0 lds\n"
"v_mfma_f32_16x16x32_bf16 a[36:39],a[216:219],a[236:239],a[36:39]\ns_mov_b32 s72,6784\nv_exp_f32 v73,v73\nv_fma_f32 v78,v78,s69,-v246\nv_mfma_f32_16x16x32_bf16 a[64:67],a[220:223],a[236:239],a[64:67]\nv_exp_f32 v74,v74\nv_mfma_f32_16x16x32_bf16 a[92:95],a[224:227],a[236:239],a[92:95]\nds_read_b128 a[236:239],v239 offset:32768\n"
"v_exp_f32 v75,v75\nv_mfma_f32_16x16x32_bf16 a[12:15],a[212:215],a[240:243],a[12:15]\nbuffer_load_dwordx4 v230,s[40:43],s72 offen offset:1024 lds\nv_cvt_pk_bf16_f32 v72,v72,v73\nv_mfma_f32_16x16x32_bf16 a[40:43],a[216:219],a[240:243],a[40:43]\ns_waitcnt vmcnt(12)\nds_read_b128 a[204:207],v235 offset:8192\nv_cvt_pk_bf16_f32 v73,v74,v75\n"
"v_mfma_f32_16x16x32_bf16 a[68:71],a[220:223],a[240:243],a[68:71]\ns_add_u32 m0,s66,147456\nv_fma_f32 v79,v79,s69,-v246\nv_fma_f32 v80,v80,s69,-v247\nv_mfma_f32_16x16x32_bf16 a[96:99],a[224:227],a[240:243],a[96:99]\nds_read_b128 a[240:243],v239 offset:34816\nv_exp_f32 v76,v76\nv_mfma_f32_16x16x32_bf16 a[16:19],a[212:215],a[244:247],a[16:19]\n"
"s_add_u32 s73,s67,640\nbuffer_load_dwordx4 v229,s[40:43],s73 offen offset:0 lds\nv_mfma_f32_16x16x32_bf16 a[44:47],a[216:219],a[244:247],a[44:47]\nds_read_b128 a[208:211],v235 offset:10240\nv_exp_f32 v77,v77\nv_mfma_f32_16x16x32_bf16 a[72:75],a[220:223],a[244:247],a[72:75]\ns_add_u32 s74,s67,6784\nv_exp_f32 v78,v78\n"
"v_mfma_f32_16x16x32_bf16 a[100:103],a[224:227],a[244:247],a[100:103]\nds_read_b128 a[244:247],v239 offset:36864\nv_exp_f32 v79,v79\nv_mfma_f32_16x16x32_bf16 a[20:23],a[212:215],a[248:251],a[20:23]\nbuffer_load_dwordx4 v230,s[40:43],s74 offen offset:1024 lds\nv_cvt_pk_bf16_f32 v76,v76,v77\nv_mfma_f32_16x16x32_bf16 a[48:51],a[216:219],a[248:251],a[48:51]\ns_add_u32 m0,s64,0\n"
"v_cvt_pk_bf16_f32 v77,v78,v79\nv_fma_f32 v81,v81,s69,-v247\nv_fma_f32 v82,v82,s69,-v247\nv_mfma_f32_16x16x32_bf16 a[76:79],a[220:223],a[248:251],a[76:79]\ns_mov_b32 s75,162176\nv_fma_f32 v83,v83,s69,-v247\nv_fma_f32 v84,v84,s69,-v241\nv_mfma_f32_16x16x32_bf16 a[104:107],a[224:227],a[248:251],a[104:107]\n"
"ds_read_b128 a[248:251],v239 offset:38912\nv_exp_f32 v80,v80\nv_mfma_f32_16x16x32_bf16 a[24:27],a[212:215],a[252:255],a[24:27]\nbuffer_load_dwordx4 v227,s[48:51],s75 offen offset:2048 nt lds\nv_fma_f32 v85,v85,s69,-v241\nv_mfma_f32_16x16x32_bf16 a[52:55],a[216:219],a[252:255],a[52:55]\ns_mov_b32 s72,169344\nv_exp_f32 v81,v81\n"
"v_fma_f32 v86,v86,s69,-v241\nv_mfma_f32_16x16x32_bf16 a[80:83],a[220:223],a[252:255],a[80:83]\nv_exp_f32 v82,v82\nv_mfma_f32_16x16x32_bf16 a[108:111],a[224:227],a[252:255],a[108:111]\nds_read_b128 a[252:255],v239 offset:40960\ns_waitcnt lgkmcnt(3)\nv_cvt_pk_bf16_f32 v80,v80,v81\nv_mfma_f32_16x16x32_bf16 v[112:115],a[196:199],a[228:231],v[112:115]\n"
"buffer_load_dwordx4 v228,s[48:51],s72 offen offset:3072 nt lds\nv_fma_f32 v87,v87,s69,-v241\nv_mfma_f32_16x16x32_bf16 v[140:143],a[200:203],a[228:231],v[140:143]\nds_read_b128 a[212:215],v236 offset:4096\nv_exp_f32 v83,v83\nv_mfma_f32_16x16x32_bf16 v[168:171],a[204:207],a[228:231],v[168:171]\nv_cvt_pk_bf16_f32 v81,v82,v83\nv_fma_f32 v88,v88,s69,-v242\n"
"v_mfma_f32_16x16x32_bf16 v[196:199],a[208:211],a[228:231],v[196:199]\nds_read_b128 a[228:231],v239 offset:29696\nv_exp_f32 v84,v84\nv_mfma_f32_16x16x32_bf16 v[116:119],a[196:199],a[232:235],v[116:119]\nds_read_b128 a[216:219],v236 offset:6144\nv_fma_f32 v89,v89,s69,-v242\nv_mfma_f32_16x16x32_bf16 v[144:147],a[200:203],a[232:235],v[144:147]\ns_add_u32 m0,s64,4096\n"
"v_exp_f32 v85,v85\nv_fma_f32 v90,v90,s69,-v242\nv_mfma_f32_16x16x32_bf16 v[172:175],a[204:207],a[232:235],v[172:175]\ns_mov_b32 s73,196992\nv_exp_f32 v86,v86\nv_mfma_f32_16x16x32_bf16 v[200:203],a[208:211],a[232:235],v[200:203]\nds_read_b128 a[232:235],v239 offset:31744\nv_exp_f32 v87,v87\n"
"v_mfma_f32_16x16x32_bf16 v[120:123],a[196:199],a[236:239],v[120:123]\nv_cvt_pk_bf16_f32 v84,v84,v85\nv_cvt_pk_bf16_f32 v85,v86,v87\nv_mfma_f32_16x16x32_bf16 v[148:151],a[200:203],a[236:239],v[148:151]\nv_fma_f32 v91,v91,s69,-v242\nv_exp_f32 v88,v88\nv_mfma_f32_16x16x32_bf16 v[176:179],a[204:207],a[236:239],v[176:179]\nv_exp_f32 v89,v89\n"
"v_mfma_f32_16x16x32_bf16 v[204:207],a[208:211],a[236:239],v[204:207]\nds_read_b128 a[236:239],v239 offset:33792\ns_waitcnt lgkmcnt(7)\nv_cvt_pk_bf16_f32 v88,v88,v89\nv_mfma_f32_16x16x32_bf16 v[124:127],a[196:199],a[240:243],v[124:127]\nds_read_b128 a[220:223],v236 offset:8192\nv_fma_f32 v92,v92,s69,-v243\nv_mfma_f32_16x16x32_bf16 v[152:155],a[200:203],a[240:243],v[152:155]\n"
"v_exp_f32 v90,v90\nv_fma_f32 v93,v93,s69,-v243\nv_mfma_f32_16x16x32_bf16 v[180:183],a[204:207],a[240:243],v[180:183]\nv_exp_f32 v91,v91\nv_mfma_f32_16x16x32_bf16 v[208:211],a[208:211],a[240:243],v[208:211]\nds_read_b128 a[240:243],v239 offset:35840\nv_cvt_pk_bf16_f32 v89,v90,v91\nv_fma_f32 v94,v94,s69,-v243\n"
"v_mfma_f32_16x16x32_bf16 v[128:131],a[196:199],a[244:247],v[128:131]\nds_read_b128 a[224:227],v236 offset:10240\nv_fma_f32 v95,v95,s69,-v243\nv_mfma_f32_16x16x32_bf16 v[156:159],a[200:203],a[244:247],v[156:159]\nv_exp_f32 v92,v92\nv_fma_f32 v96,v96,s69,-v244\nv_mfma_f32_16x16x32_bf16 v[184:187],a[204:207],a[244:247],v[184:187]\nv_exp_f32 v93,v93\n"
"v_mfma_f32_16x16x32_bf16 v[212:215],a[208:211],a[244:247],v[212:215]\nds_read_b128 a[244:247],v239 offset:37888\ns_waitcnt lgkmcnt(9)\nv_cvt_pk_bf16_f32 v92,v92,v93\nv_mfma_f32_16x16x32_bf16 v[132:135],a[196:199],a[248:251],v[132:135]\nv_exp_f32 v94,v94\nv_mfma_f32_16x16x32_bf16 v[160:163],a[200:203],a[248:251],v[160:163]\nv_exp_f32 v95,v95\n"
"v_fma_f32 v97,v97,s69,-v244\nv_mfma_f32_16x16x32_bf16 v[188:191],a[204:207],a[248:251],v[188:191]\nv_cvt_pk_bf16_f32 v93,v94,v95\nv_fma_f32 v98,v98,s69,-v244\nv_mfma_f32_16x16x32_bf16 v[216:219],a[208:211],a[248:251],v[216:219]\nds_read_b128 a[248:251],v239 offset:39936\nv_fma_f32 v99,v99,s69,-v244\nv_fma_f32 v100,v100,s69,-v245\n"
"v_mfma_f32_16x16x32_bf16 v[136:139],a[196:199],a[252:255],v[136:139]\nv_exp_f32 v96,v96\nv_mfma_f32_16x16x32_bf16 v[164:167],a[200:203],a[252:255],v[164:167]\nv_exp_f32 v97,v97\nv_fma_f32 v101,v101,s69,-v245\nv_mfma_f32_16x16x32_bf16 v[192:195],a[204:207],a[252:255],v[192:195]\nv_exp_f32 v98,v98\nv_mfma_f32_16x16x32_bf16 v[220:223],a[208:211],a[252:255],v[220:223]\n"
"ds_read_b128 a[252:255],v239 offset:41984\ns_waitcnt lgkmcnt(3)\nv_cvt_pk_bf16_f32 v96,v96,v97\nv_mfma_f32_16x16x32_bf16 v[112:115],a[212:215],a[228:231],v[112:115]\nbuffer_load_dwordx4 v227,s[48:51],s73 offen offset:0 nt lds\nv_fma_f32 v102,v102,s69,-v245\nv_mfma_f32_16x16x32_bf16 v[140:143],a[216:219],a[228:231],v[140:143]\ns_waitcnt vmcnt(13)\n"
"ds_read_b64_tr_b16 a[196:197],v231 offset:12288\nv_fma_f32 v103,v103,s69,-v245\nv_mfma_f32_16x16x32_bf16 v[168:171],a[220:223],a[228:231],v[168:171]\nds_read_b64_tr_b16 a[198:199],v231 offset:13312\nv_fma_f32 v104,v104,s69,-v246\nv_mfma_f32_16x16x32_bf16 v[196:199],a[224:227],a[228:231],v[196:199]\ns_waitcnt vmcnt(3) lgkmcnt(2)\ns_barrier\n"
"ds_read_b128 a[228:231],v237 offset:0\nv_mfma_f32_16x16x32_bf16 v[116:119],a[212:215],a[232:235],v[116:119]\ns_mov_b32 s74,204160\nbuffer_load_dwordx4 v228,s[48:51],s74 offen offset:1024 nt lds\nv_mfma_f32_16x16x32_bf16 v[144:147],a[216:219],a[232:235],v[144:147]\nds_read_b64_tr_b16 a[200:201],v232 offset:12288\nv_exp_f32 v99,v99\nv_mfma_f32_16x16x32_bf16 v[172:175],a[220:223],a[232:235],v[172:175]\n"
"s_mov_b32 s75,227712\nv_cvt_pk_bf16_f32 v97,v98,v99\nv_fma_f32 v105,v105,s69,-v246\nv_mfma_f32_16x16x32_bf16 v[200:203],a[224:227],a[232:235],v[200:203]\nds_read_b128 a[232:235],v237 offset:2048\nv_exp_f32 v100,v100\nv_mfma_f32_16x16x32_bf16 v[120:123],a[212:215],a[236:239],v[120:123]\nbuffer_load_dwordx4 v227,s[48:51],s75 offen offset:2048 nt lds\n"
"v_fma_f32 v106,v106,s69,-v246\nv_mfma_f32_16x16x32_bf16 v[148:151],a[216:219],a[236:239],v[148:151]\nds_read_b64_tr_b16 a[202:203],v232 offset:13312\nv_exp_f32 v101,v101\nv_mfma_f32_16x16x32_bf16 v[176:179],a[220:223],a[236:239],v[176:179]\ns_mov_b32 s72,234880\nv_exp_f32 v102,v102\nv_mfma_f32_16x16x32_bf16 v[204:207],a[224:227],a[236:239],v[204:207]\n"
"ds_read_b128 a[236:239],v237 offset:4096\nv_exp_f32 v103,v103\nv_mfma_f32_16x16x32_bf16 v[124:127],a[212:215],a[240:243],v[124:127]\nbuffer_load_dwordx4 v228,s[48:51],s72 offen offset:3072 nt lds\nv_cvt_pk_bf16_f32 v100,v100,v101\nv_mfma_f32_16x16x32_bf16 v[152:155],a[216:219],a[240:243],v[152:155]\nds_read_b64_tr_b16 a[204:205],v233 offset:12288\nv_cvt_pk_bf16_f32 v101,v102,v103\n"
"v_fma_f32 v107,v107,s69,-v246\nv_mfma_f32_16x16x32_bf16 v[180:183],a[220:223],a[240:243],v[180:183]\nds_read_b64_tr_b16 a[206:207],v233 offset:13312\nv_fma_f32 v108,v108,s69,-v247\nv_mfma_f32_16x16x32_bf16 v[208:211],a[224:227],a[240:243],v[208:211]\nds_read_b128 a[240:243],v237 offset:6144\nv_exp_f32 v104,v104\nv_mfma_f32_16x16x32_bf16 v[128:131],a[212:215],a[244:247],v[128:131]\n"
"s_add_u32 m0,s64,8192\nv_exp_f32 v105,v105\nv_mfma_f32_16x16x32_bf16 v[156:159],a[216:219],a[244:247],v[156:159]\ns_mov_b32 s73,0\nbuffer_load_dwordx4 v225,s[52:55],s73 offen offset:0 nt lds\nv_cvt_pk_bf16_f32 v104,v104,v105\nv_mfma_f32_16x16x32_bf16 v[184:187],a[220:223],a[244:247],v[184:187]\nds_read_b64_tr_b16 a[208:209],v234 offset:12288\n"
"v_fma_f32 v109,v109,s69,-v247\nv_mfma_f32_16x16x32_bf16 v[212:215],a[224:227],a[244:247],v[212:215]\nds_read_b128 a[244:247],v237 offset:8192\nv_exp_f32 v106,v106\nv_mfma_f32_16x16x32_bf16 v[132:135],a[212:215],a[248:251],v[132:135]\ns_mov_b32 s74,1024\nbuffer_load_dwordx4 v225,s[52:55],s74 offen offset:1024 nt lds\nv_mfma_f32_16x16x32_bf16 v[160:163],a[216:219],a[248:251],v[160:163]\n"
"ds_read_b64_tr_b16 a[210:211],v234 offset:13312\nv_exp_f32 v107,v107\nv_mfma_f32_16x16x32_bf16 v[188:191],a[220:223],a[248:251],v[188:191]\ns_mov_b32 s75,6144\nv_cvt_pk_bf16_f32 v105,v106,v107\nv_fma_f32 v110,v110,s69,-v247\nv_mfma_f32_16x16x32_bf16 v[216:219],a[224:227],a[248:251],v[216:219]\nds_read_b128 a[248:251],v237 offset:10240\n"
"v_fma_f32 v111,v111,s69,-v247\nv_mfma_f32_16x16x32_bf16 v[136:139],a[212:215],a[252:255],v[136:139]\nbuffer_load_dwordx4 v225,s[52:55],s75 offen offset:2048 nt lds\ns_mov_b32 s72,7168\nv_mfma_f32_16x16x32_bf16 v[164:167],a[216:219],a[252:255],v[164:167]\nv_exp_f32 v108,v108\nv_mfma_f32_16x16x32_bf16 v[192:195],a[220:223],a[252:255],v[192:195]\nv_exp_f32 v109,v109\n"
"v_mfma_f32_16x16x32_bf16 v[220:223],a[224:227],a[252:255],v[220:223]\nds_read_b128 a[252:255],v237 offset:12288\ns_waitcnt lgkmcnt(2)\nv_cvt_pk_bf16_f32 v108,v108,v109\nv_mfma_f32_16x16x32_bf16 a[0:3],a[196:199],a[228:231],a[0:3]\nbuffer_load_dwordx4 v225,s[52:55],s72 offen offset:3072 nt lds\ns_add_u32 m0,s64,12288\nv_mfma_f32_16x16x32_bf16 a[28:31],a[200:203],a[228:231],a[28:31]\n"
"ds_read_b64_tr_b16 a[212:213],v231 offset:16384\nv_exp_f32 v110,v110\nv_mfma_f32_16x16x32_bf16 a[56:59],a[204:207],a[228:231],a[56:59]\nds_read_b64_tr_b16 a[214:215],v231 offset:17408\ns_mov_b32 s73,16384\nv_mfma_f32_16x16x32_bf16 a[84:87],a[208:211],a[228:231],a[84:87]\nds_read_b128 a[228:231],v238 offset:0\nv_exp_f32 v111,v111\n"
"v_mfma_f32_16x16x32_bf16 a[4:7],a[196:199],a[232:235],a[4:7]\nbuffer_load_dwordx4 v225,s[52:55],s73 offen offset:0 nt lds\nv_cvt_pk_bf16_f32 v109,v110,v111\nv_mfma_f32_16x16x32_bf16 a[32:35],a[200:203],a[232:235],a[32:35]\nds_read_b64_tr_b16 a[216:217],v232 offset:16384\ns_mov_b32 s74,17408\nv_mfma_f32_16x16x32_bf16 a[60:63],a[204:207],a[232:235],a[60:63]\nv_mfma_f32_16x16x32_bf16 a[88:91],a[208:211],a[232:235],a[88:91]\n"
"ds_read_b128 a[232:235],v238 offset:2048\nv_mfma_f32_16x16x32_bf16 a[8:11],a[196:199],a[236:239],a[8:11]\nbuffer_load_dwordx4 v225,s[52:55],s74 offen offset:1024 nt lds\ns_mov_b32 s75,22528\nv_mfma_f32_16x16x32_bf16 a[36:39],a[200:203],a[236:239],a[36:39]\nds_read_b64_tr_b16 a[218:219],v232 offset:17408\nv_mfma_f32_16x16x32_bf16 a[64:67],a[204:207],a[236:239],a[64:67]\nv_mfma_f32_16x16x32_bf16 a[92:95],a[208:211],a[236:239],a[92:95]\n"
"ds_read_b128 a[236:239],v238 offset:4096\nv_mfma_f32_16x16x32_bf16 a[12:15],a[196:199],a[240:243],a[12:15]\nbuffer_load_dwordx4 v225,s[52:55],s75 offen offset:2048 nt lds\ns_mov_b32 s72,23552\nv_mfma_f32_16x16x32_bf16 a[40:43],a[200:203],a[240:243],a[40:43]\nds_read_b64_tr_b16 a[220:221],v233 offset:16384\nv_mfma_f32_16x16x32_bf16 a[68:71],a[204:207],a[240:243],a[68:71]\nds_read_b64_tr_b16 a[222:223],v233 offset:17408\n"
"v_mfma_f32_16x16x32_bf16 a[96:99],a[208:211],a[240:243],a[96:99]\nds_read_b128 a[240:243],v238 offset:6144\nv_mfma_f32_16x16x32_bf16 a[16:19],a[196:199],a[244:247],a[16:19]\nbuffer_load_dwordx4 v225,s[52:55],s72 offen offset:3072 nt lds\nv_mfma_f32_16x16x32_bf16 a[44:47],a[200:203],a[244:247],a[44:47]\nds_read_b64_tr_b16 a[224:225],v234 offset:16384\nv_mfma_f32_16x16x32_bf16 a[72:75],a[204:207],a[244:247],a[72:75]\nv_mfma_f32_16x16x32_bf16 a[100:103],a[208:211],a[244:247],a[100:103]\n"
"ds_read_b128 a[244:247],v238 offset:8192\ns_waitcnt lgkmcnt(12)\nv_mfma_f32_16x16x32_bf16 a[20:23],a[196:199],a[248:251],a[20:23]\nds_read_b64_tr_b16 a[226:227],v234 offset:17408\ns_add_u32 m0,s64,16384\nv_mfma_f32_16x16x32_bf16 a[48:51],a[200:203],a[248:251],a[48:51]\ns_mov_b32 s73,3146240\nv_mfma_f32_16x16x32_bf16 a[76:79],a[204:207],a[248:251],a[76:79]\n"
"v_mfma_f32_16x16x32_bf16 a[104:107],a[208:211],a[248:251],a[104:107]\nds_read_b128 a[248:251],v238 offset:10240\nv_mfma_f32_16x16x32_bf16 a[24:27],a[196:199],a[252:255],a[24:27]\nv_mfma_f32_16x16x32_bf16 a[52:55],a[200:203],a[252:255],a[52:55]\nv_mfma_f32_16x16x32_bf16 a[80:83],a[204:207],a[252:255],a[80:83]\nv_mfma_f32_16x16x32_bf16 a[108:111],a[208:211],a[252:255],a[108:111]\nds_read_b128 a[252:255],v238 offset:12288\ns_waitcnt lgkmcnt(2)\n"
"v_mfma_f32_16x16x32_bf16 a[0:3],a[212:215],a[228:231],a[0:3]\nbuffer_load_dwordx4 v224,s[44:47],s73 offen offset:0 lds\ns_mov_b32 s74,3177984\nv_mfma_f32_16x16x32_bf16 a[28:31],a[216:219],a[228:231],a[28:31]\ns_waitcnt vmcnt(13)\nds_read_b128 a[196:199],v235 offset:0\nv_mfma_f32_16x16x32_bf16 a[56:59],a[220:223],a[228:231],a[56:59]\nv_mfma_f32_16x16x32_bf16 a[84:87],a[224:227],a[228:231],a[84:87]\n"
"s_waitcnt lgkmcnt(1)\ns_barrier\nds_read_b128 a[228:231],v239 offset:43008\nv_mfma_f32_16x16x32_bf16 a[4:7],a[212:215],a[232:235],a[4:7]\nbuffer_load_dwordx4 v224,s[44:47],s74 offen offset:1024 lds\ns_add_u32 m0,s65,147456\nv_mfma_f32_16x16x32_bf16 a[32:35],a[216:219],a[232:235],a[32:35]\nds_read_b128 a[200:203],v235 offset:2048\n"
"s_mov_b32 s75,768\nv_mfma_f32_16x16x32_bf16 a[60:63],a[220:223],a[232:235],a[60:63]\nv_mfma_f32_16x16x32_bf16 a[88:91],a[224:227],a[232:235],a[88:91]\nds_read_b128 a[232:235],v239 offset:45056\nv_mfma_f32_16x16x32_bf16 a[8:11],a[212:215],a[236:239],a[8:11]\nbuffer_load_dwordx4 v229,s[40:43],s75 offen offset:0 lds\ns_mov_b32 s72,6912\nv_mfma_f32_16x16x32_bf16 a[36:39],a[216:219],a[236:239],a[36:39]\n"
"v_mfma_f32_16x16x32_bf16 a[64:67],a[220:223],a[236:239],a[64:67]\nv_mfma_f32_16x16x32_bf16 a[92:95],a[224:227],a[236:239],a[92:95]\nds_read_b128 a[236:239],v239 offset:47104\nv_mfma_f32_16x16x32_bf16 a[12:15],a[212:215],a[240:243],a[12:15]\nbuffer_load_dwordx4 v230,s[40:43],s72 offen offset:1024 lds\ns_add_u32 m0,s66,147456\nv_mfma_f32_16x16x32_bf16 a[40:43],a[216:219],a[240:243],a[40:43]\ns_waitcnt vmcnt(12)\n"
"ds_read_b128 a[204:207],v235 offset:4096\nv_mfma_f32_16x16x32_bf16 a[68:71],a[220:223],a[240:243],a[68:71]\ns_add_u32 s73,s67,768\nv_mfma_f32_16x16x32_bf16 a[96:99],a[224:227],a[240:243],a[96:99]\nds_read_b128 a[240:243],v239 offset:49152\nv_mfma_f32_16x16x32_bf16 a[16:19],a[212:215],a[244:247],a[16:19]\nbuffer_load_dwordx4 v229,s[40:43],s73 offen offset:0 lds\ns_add_u32 s74,s67,6912\n"
"v_mfma_f32_16x16x32_bf16 a[44:47],a[216:219],a[244:247],a[44:47]\nds_read_b128 a[208:211],v235 offset:6144\nv_mfma_f32_16x16x32_bf16 a[72:75],a[220:223],a[244:247],a[72:75]\nv_mfma_f32_16x16x32_bf16 a[100:103],a[224:227],a[244:247],a[100:103]\nds_read_b128 a[244:247],v239 offset:51200\nv_mfma_f32_16x16x32_bf16 a[20:23],a[212:215],a[248:251],a[20:23]\nbuffer_load_dwordx4 v230,s[40:43],s74 offen offset:1024 lds\ns_add_u32 m0,s64,16384\n"
"v_mfma_f32_16x16x32_bf16 a[48:51],a[216:219],a[248:251],a[48:51]\ns_mov_b32 s75,3275264\nv_mfma_f32_16x16x32_bf16 a[76:79],a[220:223],a[248:251],a[76:79]\nv_mfma_f32_16x16x32_bf16 a[104:107],a[224:227],a[248:251],a[104:107]\nds_read_b128 a[248:251],v239 offset:53248\nv_mfma_f32_16x16x32_bf16 a[24:27],a[212:215],a[252:255],a[24:27]\nbuffer_load_dwordx4 v224,s[44:47],s75 offen offset:2048 lds\ns_mov_b32 s72,3307008\n"
"v_mfma_f32_16x16x32_bf16 a[52:55],a[216:219],a[252:255],a[52:55]\nv_mfma_f32_16x16x32_bf16 a[80:83],a[220:223],a[252:255],a[80:83]\nv_mfma_f32_16x16x32_bf16 a[108:111],a[224:227],a[252:255],a[108:111]\nds_read_b128 a[252:255],v239 offset:55296\ns_waitcnt lgkmcnt(3)\nv_mfma_f32_16x16x32_bf16 v[112:115],a[196:199],a[228:231],v[112:115]\nbuffer_load_dwordx4 v224,s[44:47],s72 offen offset:3072 lds\nv_mfma_f32_16x16x32_bf16 v[140:143],a[200:203],a[228:231],v[140:143]\n"
"ds_read_b128 a[212:215],v236 offset:0\nv_mfma_f32_16x16x32_bf16 v[168:171],a[204:207],a[228:231],v[168:171]\nv_mfma_f32_16x16x32_bf16 v[196:199],a[208:211],a[228:231],v[196:199]\nds_read_b128 a[228:231],v239 offset:44032\nv_mfma_f32_16x16x32_bf16 v[116:119],a[196:199],a[232:235],v[116:119]\nds_read_b128 a[216:219],v236 offset:2048\ns_add_u32 m0,s64,0\nv_mfma_f32_16x16x32_bf16 v[144:147],a[200:203],a[232:235],v[144:147]\n"
"s_mov_b32 s73,3408384\nv_mfma_f32_16x16x32_bf16 v[172:175],a[204:207],a[232:235],v[172:175]\nv_mfma_f32_16x16x32_bf16 v[200:203],a[208:211],a[232:235],v[200:203]\nds_read_b128 a[232:235],v239 offset:46080\nv_mfma_f32_16x16x32_bf16 v[120:123],a[196:199],a[236:239],v[120:123]\nv_mfma_f32_16x16x32_bf16 v[148:151],a[200:203],a[236:239],v[148:151]\nv_mfma_f32_16x16x32_bf16 v[176:179],a[204:207],a[236:239],v[176:179]\nv_mfma_f32_16x16x32_bf16 v[204:207],a[208:211],a[236:239],v[204:207]\n"
"ds_read_b128 a[236:239],v239 offset:48128\ns_waitcnt lgkmcnt(7)\nv_mfma_f32_16x16x32_bf16 v[124:127],a[196:199],a[240:243],v[124:127]\nds_read_b128 a[220:223],v236 offset:4096\nv_mfma_f32_16x16x32_bf16 v[152:155],a[200:203],a[240:243],v[152:155]\nv_mfma_f32_16x16x32_bf16 v[180:183],a[204:207],a[240:243],v[180:183]\nv_mfma_f32_16x16x32_bf16 v[208:211],a[208:211],a[240:243],v[208:211]\nds_read_b128 a[240:243],v239 offset:50176\n"
"v_mfma_f32_16x16x32_bf16 v[128:131],a[196:199],a[244:247],v[128:131]\nds_read_b128 a[224:227],v236 offset:6144\nv_mfma_f32_16x16x32_bf16 v[156:159],a[200:203],a[244:247],v[156:159]\nv_mfma_f32_16x16x32_bf16 v[184:187],a[204:207],a[244:247],v[184:187]\nv_mfma_f32_16x16x32_bf16 v[212:215],a[208:211],a[244:247],v[212:215]\nds_read_b128 a[244:247],v239 offset:52224\ns_waitcnt lgkmcnt(9)\nv_mfma_f32_16x16x32_bf16 v[132:135],a[196:199],a[248:251],v[132:135]\n"
"v_mfma_f32_16x16x32_bf16 v[160:163],a[200:203],a[248:251],v[160:163]\nv_mfma_f32_16x16x32_bf16 v[188:191],a[204:207],a[248:251],v[188:191]\nv_mfma_f32_16x16x32_bf16 v[216:219],a[208:211],a[248:251],v[216:219]\nds_read_b128 a[248:251],v239 offset:54272\nv_mfma_f32_16x16x32_bf16 v[136:139],a[196:199],a[252:255],v[136:139]\nv_mfma_f32_16x16x32_bf16 v[164:167],a[200:203],a[252:255],v[164:167]\nv_mfma_f32_16x16x32_bf16 v[192:195],a[204:207],a[252:255],v[192:195]\nv_mfma_f32_16x16x32_bf16 v[220:223],a[208:211],a[252:255],v[220:223]\n"
"ds_read_b128 a[252:255],v239 offset:56320\ns_waitcnt lgkmcnt(3)\nv_mfma_f32_16x16x32_bf16 v[112:115],a[212:215],a[228:231],v[112:115]\nbuffer_load_dwordx4 v224,s[44:47],s73 offen offset:0 lds\ns_mov_b32 s74,3440128\nv_mfma_f32_16x16x32_bf16 v[140:143],a[216:219],a[228:231],v[140:143]\ns_waitcnt vmcnt(13)\nds_read_b64_tr_b16 a[196:197],v231 offset:8192\n"
"v_mfma_f32_16x16x32_bf16 v[168:171],a[220:223],a[228:231],v[168:171]\nds_read_b64_tr_b16 a[198:199],v231 offset:9216\nv_mfma_f32_16x16x32_bf16 v[196:199],a[224:227],a[228:231],v[196:199]\ns_waitcnt lgkmcnt(2)\ns_barrier\nv_mfma_f32_16x16x32_bf16 v[116:119],a[212:215],a[232:235],v[116:119]\nbuffer_load_dwordx4 v224,s[44:47],s74 offen offset:1024 lds\nds_write_b64 v240,v[0:1] offset:0\n"
"v_mfma_f32_16x16x32_bf16 v[144:147],a[216:219],a[232:235],v[144:147]\nds_read_b64_tr_b16 a[200:201],v232 offset:8192\ns_mov_b32 s75,3537408\nv_mfma_f32_16x16x32_bf16 v[172:175],a[220:223],a[232:235],v[172:175]\nv_mfma_f32_16x16x32_bf16 v[200:203],a[224:227],a[232:235],v[200:203]\nv_mfma_f32_16x16x32_bf16 v[120:123],a[212:215],a[236:239],v[120:123]\nbuffer_load_dwordx4 v224,s[44:47],s75 offen offset:2048 lds\nds_write_b64 v240,v[4:5] offset:2048\n"
"v_mfma_f32_16x16x32_bf16 v[148:151],a[216:219],a[236:239],v[148:151]\nds_read_b64_tr_b16 a[202:203],v232 offset:9216\ns_mov_b32 s72,3569152\nv_mfma_f32_16x16x32_bf16 v[176:179],a[220:223],a[236:239],v[176:179]\nv_mfma_f32_16x16x32_bf16 v[204:207],a[224:227],a[236:239],v[204:207]\nv_mfma_f32_16x16x32_bf16 v[124:127],a[212:215],a[240:243],v[124:127]\nbuffer_load_dwordx4 v224,s[44:47],s72 offen offset:3072 lds\nds_write_b64 v240,v[8:9] offset:4096\n"
"v_mfma_f32_16x16x32_bf16 v[152:155],a[216:219],a[240:243],v[152:155]\nds_read_b64_tr_b16 a[204:205],v233 offset:8192\ns_add_u32 m0,s64,4096\nv_mfma_f32_16x16x32_bf16 v[180:183],a[220:223],a[240:243],v[180:183]\nds_read_b64_tr_b16 a[206:207],v233 offset:9216\ns_mov_b32 s73,32768\nv_mfma_f32_16x16x32_bf16 v[208:211],a[224:227],a[240:243],v[208:211]\nv_mfma_f32_16x16x32_bf16 v[128:131],a[212:215],a[244:247],v[128:131]\n"
"buffer_load_dwordx4 v225,s[52:55],s73 offen offset:0 nt lds\nds_write_b64 v240,v[12:13] offset:6144\nv_mfma_f32_16x16x32_bf16 v[156:159],a[216:219],a[244:247],v[156:159]\nds_read_b64_tr_b16 a[208:209],v234 offset:8192\ns_mov_b32 s74,33792\nv_mfma_f32_16x16x32_bf16 v[184:187],a[220:223],a[244:247],v[184:187]\nv_mfma_f32_16x16x32_bf16 v[212:215],a[224:227],a[244:247],v[212:215]\nv_mfma_f32_16x16x32_bf16 v[132:135],a[212:215],a[248:251],v[132:135]\n"
"buffer_load_dwordx4 v225,s[52:55],s74 offen offset:1024 nt lds\nds_write_b64 v240,v[16:17] offset:8192\nv_mfma_f32_16x16x32_bf16 v[160:163],a[216:219],a[248:251],v[160:163]\nds_read_b64_tr_b16 a[210:211],v234 offset:9216\ns_mov_b32 s75,38912\nv_mfma_f32_16x16x32_bf16 v[188:191],a[220:223],a[248:251],v[188:191]\nv_mfma_f32_16x16x32_bf16 v[216:219],a[224:227],a[248:251],v[216:219]\nv_mfma_f32_16x16x32_bf16 v[136:139],a[212:215],a[252:255],v[136:139]\n"
"buffer_load_dwordx4 v225,s[52:55],s75 offen offset:2048 nt lds\nds_write_b64 v240,v[20:21] offset:10240\nv_mfma_f32_16x16x32_bf16 v[164:167],a[216:219],a[252:255],v[164:167]\ns_mov_b32 s72,39936\nv_mfma_f32_16x16x32_bf16 v[192:195],a[220:223],a[252:255],v[192:195]\nv_mfma_f32_16x16x32_bf16 v[220:223],a[224:227],a[252:255],v[220:223]\nds_write_b64 v240,v[24:25] offset:12288\nv_max3_f32 v248,v112,v113,v112\n"
"v_max3_f32 v248,v114,v115,v248\nv_max3_f32 v248,v140,v141,v248\nv_max3_f32 v248,v142,v143,v248\nv_max3_f32 v248,v168,v169,v248\nv_max3_f32 v248,v170,v171,v248\nds_write_b64 v240,v[28:29] offset:14336\nv_max3_f32 v248,v196,v197,v248\nv_max3_f32 v248,v198,v199,v248\n"
"v_mul_f32 v248,s69,v248\nds_max_f32 v255,v248 offset:57344\nv_max3_f32 v249,v116,v117,v116\nv_max3_f32 v249,v118,v119,v249\nbuffer_load_dwordx4 v225,s[52:55],s72 offen offset:3072 nt lds\nds_write_b64 v240,v[32:33] offset:16384\nv_max3_f32 v249,v144,v145,v249\nv_max3_f32 v249,v146,v147,v249\n"
"v_max3_f32 v249,v172,v173,v249\nv_max3_f32 v249,v174,v175,v249\nv_max3_f32 v249,v200,v201,v249\nv_max3_f32 v249,v202,v203,v249\nds_write_b64 v240,v[36:37] offset:18432\nv_mul_f32 v249,s69,v249\nds_max_f32 v255,v249 offset:57408\nv_max3_f32 v250,v120,v121,v120\n"
"v_max3_f32 v250,v122,v123,v250\nv_max3_f32 v250,v148,v149,v250\nv_max3_f32 v250,v150,v151,v250\nds_write_b64 v240,v[40:41] offset:20480\nv_max3_f32 v250,v176,v177,v250\nv_max3_f32 v250,v178,v179,v250\nv_max3_f32 v250,v204,v205,v250\nv_max3_f32 v250,v206,v207,v250\n"
"v_mul_f32 v250,s69,v250\nds_max_f32 v255,v250 offset:57472\nds_write_b64 v240,v[44:45] offset:22528\nv_max3_f32 v251,v124,v125,v124\nv_max3_f32 v251,v126,v127,v251\nv_max3_f32 v251,v152,v153,v251\nv_max3_f32 v251,v154,v155,v251\nv_max3_f32 v251,v180,v181,v251\n"
"v_max3_f32 v251,v182,v183,v251\nds_write_b64 v240,v[48:49] offset:24576\nv_max3_f32 v251,v208,v209,v251\nv_max3_f32 v251,v210,v211,v251\nv_mul_f32 v251,s69,v251\nds_max_f32 v255,v251 offset:57536\nv_max3_f32 v252,v128,v129,v128\nv_max3_f32 v252,v130,v131,v252\n"
"ds_write_b64 v240,v[52:53] offset:26624\nv_max3_f32 v252,v156,v157,v252\nv_max3_f32 v252,v158,v159,v252\nv_max3_f32 v252,v184,v185,v252\nv_max3_f32 v252,v186,v187,v252\nv_max3_f32 v252,v212,v213,v252\nv_max3_f32 v252,v214,v215,v252\nds_write_b64 v240,v[56:57] offset:28672\n"
"v_mul_f32 v252,s69,v252\nds_max_f32 v255,v252 offset:57600\nv_max3_f32 v253,v132,v133,v132\nv_max3_f32 v253,v134,v135,v253\nv_max3_f32 v253,v160,v161,v253\nv_max3_f32 v253,v162,v163,v253\nds_write_b64 v240,v[60:61] offset:30720\nv_max3_f32 v253,v188,v189,v253\n"
"v_max3_f32 v253,v190,v191,v253\nv_max3_f32 v253,v216,v217,v253\nv_max3_f32 v253,v218,v219,v253\nv_mul_f32 v253,s69,v253\nds_max_f32 v255,v253 offset:57664\nds_write_b64 v240,v[64:65] offset:32768\nv_max3_f32 v254,v136,v137,v136\nv_max3_f32 v254,v138,v139,v254\n"
"v_max3_f32 v254,v164,v165,v254\nv_max3_f32 v254,v166,v167,v254\nv_max3_f32 v254,v192,v193,v254\nv_max3_f32 v254,v194,v195,v254\nds_write_b64 v240,v[68:69] offset:34816\nv_max3_f32 v254,v220,v221,v254\nv_max3_f32 v254,v222,v223,v254\nv_mul_f32 v254,s69,v254\n"
"ds_max_f32 v255,v254 offset:57728\nv_dot2_f32_bf16 v2,v0,s35,0\nv_dot2_f32_bf16 v2,v1,s35,v2\nds_write_b64 v240,v[72:73] offset:36864\nv_dot2_f32_bf16 v6,v4,s35,0\nv_dot2_f32_bf16 v6,v5,s35,v6\nv_dot2_f32_bf16 v10,v8,s35,0\nv_dot2_f32_bf16 v10,v9,s35,v10\n"
"v_dot2_f32_bf16 v14,v12,s35,0\nv_dot2_f32_bf16 v14,v13,s35,v14\nds_write_b64 v240,v[76:77] offset:38912\nv_dot2_f32_bf16 v18,v16,s35,0\nv_dot2_f32_bf16 v18,v17,s35,v18\nv_dot2_f32_bf16 v22,v20,s35,0\nv_dot2_f32_bf16 v22,v21,s35,v22\nv_dot2_f32_bf16 v26,v24,s35,0\n"
"v_dot2_f32_bf16 v26,v25,s35,v26\nds_write_b64 v240,v[80:81] offset:40960\nv_dot2_f32_bf16 v2,v28,s35,v2\nv_dot2_f32_bf16 v2,v29,s35,v2\nv_dot2_f32_bf16 v6,v32,s35,v6\nv_dot2_f32_bf16 v6,v33,s35,v6\nv_dot2_f32_bf16 v10,v36,s35,v10\nv_dot2_f32_bf16 v10,v37,s35,v10\n"
"ds_write_b64 v240,v[84:85] offset:43008\nv_dot2_f32_bf16 v14,v40,s35,v14\nv_dot2_f32_bf16 v14,v41,s35,v14\nv_dot2_f32_bf16 v18,v44,s35,v18\nv_dot2_f32_bf16 v18,v45,s35,v18\nv_dot2_f32_bf16 v22,v48,s35,v22\nv_dot2_f32_bf16 v22,v49,s35,v22\nds_write_b64 v240,v[88:89] offset:45056\n"
"v_dot2_f32_bf16 v26,v52,s35,v26\nv_dot2_f32_bf16 v26,v53,s35,v26\nv_dot2_f32_bf16 v2,v56,s35,v2\nv_dot2_f32_bf16 v2,v57,s35,v2\nv_dot2_f32_bf16 v6,v60,s35,v6\nv_dot2_f32_bf16 v6,v61,s35,v6\nds_write_b64 v240,v[92:93] offset:47104\nv_dot2_f32_bf16 v10,v64,s35,v10\n"
"v_dot2_f32_bf16 v10,v65,s35,v10\nv_dot2_f32_bf16 v14,v68,s35,v14\nv_dot2_f32_bf16 v14,v69,s35,v14\nv_dot2_f32_bf16 v18,v72,s35,v18\nv_dot2_f32_bf16 v18,v73,s35,v18\nds_write_b64 v240,v[96:97] offset:49152\nv_dot2_f32_bf16 v22,v76,s35,v22\nv_dot2_f32_bf16 v22,v77,s35,v22\n"
"v_dot2_f32_bf16 v26,v80,s35,v26\nv_dot2_f32_bf16 v26,v81,s35,v26\nv_dot2_f32_bf16 v2,v84,s35,v2\nv_dot2_f32_bf16 v2,v85,s35,v2\nds_write_b64 v240,v[100:101] offset:51200\nv_dot2_f32_bf16 v6,v88,s35,v6\nv_dot2_f32_bf16 v6,v89,s35,v6\nv_dot2_f32_bf16 v10,v92,s35,v10\n"
"v_dot2_f32_bf16 v10,v93,s35,v10\nv_dot2_f32_bf16 v14,v96,s35,v14\nv_dot2_f32_bf16 v14,v97,s35,v14\nds_write_b64 v240,v[104:105] offset:53248\nv_dot2_f32_bf16 v18,v100,s35,v18\nv_dot2_f32_bf16 v18,v101,s35,v18\nv_dot2_f32_bf16 v22,v104,s35,v22\nv_dot2_f32_bf16 v22,v105,s35,v22\n"
"v_dot2_f32_bf16 v26,v108,s35,v26\nv_dot2_f32_bf16 v26,v109,s35,v26\nds_write_b64 v240,v[108:109] offset:55296\nv_mov_b32 v3,0\nv_permlane32_swap_b32 v2,v6\nv_add_f32 v2,v2,v6\nv_permlane32_swap_b32 v10,v14\nv_add_f32 v10,v10,v14\n"
"v_permlane32_swap_b32 v18,v22\nv_add_f32 v18,v18,v22\nv_permlane32_swap_b32 v26,v3\nv_add_f32 v26,v26,v3\nv_bfe_u32 v6,v239,8,1\nv_lshrrev_b32 v14,9,v239\nv_permlane16_swap_b32 v2,v10\nv_add_f32 v2,v2,v10\n"
"v_permlane16_swap_b32 v18,v26\nv_add_f32 v18,v18,v26\nv_lshl_or_b32 v6,v6,1,v14\nv_and_b32 v14,0xf0,v239\nv_lshl_or_b32 v6,v6,8,v14\nv_lshl_add_u32 v6,s70,2,v6\nds_write_b32 v6,v2 offset:59392\nds_write_b32 v6,v18 offset:60416\n"
"s_waitcnt lgkmcnt(0)\ns_barrier\nds_read_b128 a[228:231],v239 offset:0\nds_read_b128 a[232:235],v239 offset:2048\nds_read_b128 a[236:239],v239 offset:4096\nds_read_b128 a[240:243],v239 offset:6144\nds_read_b128 a[244:247],v239 offset:8192\nds_read_b128 a[248:251],v239 offset:10240\n"
"ds_read_b128 a[252:255],v239 offset:12288\ns_waitcnt lgkmcnt(6)\nv_mfma_f32_16x16x32_bf16 v[0:3],a[196:199],a[228:231],0\ns_waitcnt vmcnt(16)\nds_read_b64_tr_b16 a[212:213],v231 offset:12288\nv_mfma_f32_16x16x32_bf16 v[28:31],a[200:203],a[228:231],0\ns_add_u32 m0,s64,8192\nds_read_b32 v248,v255 offset:57344\n"
"v_mfma_f32_16x16x32_bf16 v[56:59],a[204:207],a[228:231],0\nds_read_b64_tr_b16 a[214:215],v231 offset:13312\ns_mov_b32 s73,49152\nv_mfma_f32_16x16x32_bf16 v[84:87],a[208:211],a[228:231],0\nds_read_b128 a[228:231],v239 offset:1024\ns_waitcnt lgkmcnt(9)\nv_mfma_f32_16x16x32_bf16 v[4:7],a[196:199],a[232:235],0\nbuffer_load_dwordx4 v225,s[52:55],s73 offen offset:0 nt lds\n"
"s_mov_b32 s74,50176\nv_mfma_f32_16x16x32_bf16 v[32:35],a[200:203],a[232:235],0\nds_read_b64_tr_b16 a[216:217],v232 offset:12288\nds_read_b32 v249,v255 offset:57408\nv_mfma_f32_16x16x32_bf16 v[60:63],a[204:207],a[232:235],0\nds_read_b32 v250,v255 offset:57472\nv_mfma_f32_16x16x32_bf16 v[88:91],a[208:211],a[232:235],0\nds_read_b128 a[232:235],v239 offset:3072\n"
"s_waitcnt lgkmcnt(12)\nv_mfma_f32_16x16x32_bf16 v[8:11],a[196:199],a[236:239],0\nbuffer_load_dwordx4 v225,s[52:55],s74 offen offset:1024 nt lds\ns_mov_b32 s75,55296\nv_mfma_f32_16x16x32_bf16 v[36:39],a[200:203],a[236:239],0\nds_read_b64_tr_b16 a[218:219],v232 offset:13312\nds_read_b32 v251,v255 offset:57536\nv_mfma_f32_16x16x32_bf16 v[64:67],a[204:207],a[236:239],0\n"
"ds_read_b32 v252,v255 offset:57600\nv_mfma_f32_16x16x32_bf16 v[92:95],a[208:211],a[236:239],0\nds_read_b128 a[236:239],v239 offset:5120\ns_waitcnt lgkmcnt(14)\nv_mfma_f32_16x16x32_bf16 v[12:15],a[196:199],a[240:243],0\nbuffer_load_dwordx4 v225,s[52:55],s75 offen offset:2048 nt lds\ns_mov_b32 s72,56320\nv_mfma_f32_16x16x32_bf16 v[40:43],a[200:203],a[240:243],0\n"
"ds_read_b64_tr_b16 a[220:221],v233 offset:12288\nds_read_b32 v253,v255 offset:57664\nv_mfma_f32_16x16x32_bf16 v[68:71],a[204:207],a[240:243],0\nds_read_b64_tr_b16 a[222:223],v233 offset:13312\nv_mfma_f32_16x16x32_bf16 v[96:99],a[208:211],a[240:243],0\nds_read_b128 a[240:243],v239 offset:7168\nds_read_b32 v254,v255 offset:57728\nv_mfma_f32_16x16x32_bf16 v[16:19],a[196:199],a[244:247],0\n"
"buffer_load_dwordx4 v225,s[52:55],s72 offen offset:3072 nt lds\nv_mfma_f32_16x16x32_bf16 v[44:47],a[200:203],a[244:247],0\nds_read_b64_tr_b16 a[224:225],v234 offset:12288\nv_mfma_f32_16x16x32_bf16 v[72:75],a[204:207],a[244:247],0\nv_mfma_f32_16x16x32_bf16 v[100:103],a[208:211],a[244:247],0\nds_read_b128 a[244:247],v239 offset:9216\ns_waitcnt lgkmcnt(15)\ns_waitcnt lgkmcnt(2)\n"
"v_mfma_f32_16x16x32_bf16 v[20:23],a[196:199],a[248:251],0\nds_read_b64_tr_b16 a[226:227],v234 offset:13312\nv_sub_f32 v241,v241,v248\nv_mfma_f32_16x16x32_bf16 v[48:51],a[200:203],a[248:251],0\ns_add_u32 m0,s64,12288\nv_sub_f32 v242,v242,v249\nv_sub_f32 v243,v243,v250\nv_sub_f32 v244,v244,v251\n"
"v_mfma_f32_16x16x32_bf16 v[76:79],a[204:207],a[248:251],0\ns_mov_b32 s73,65536\nv_sub_f32 v245,v245,v252\nv_sub_f32 v246,v246,v253\nv_mfma_f32_16x16x32_bf16 v[104:107],a[208:211],a[248:251],0\nds_read_b128 a[248:251],v239 offset:11264\nv_sub_f32 v247,v247,v254\nv_fma_f32 v112,v112,s69,-v248\n"
"v_mfma_f32_16x16x32_bf16 v[24:27],a[196:199],a[252:255],0\nv_exp_f32 v241,v241\nv_mfma_f32_16x16x32_bf16 v[52:55],a[200:203],a[252:255],0\nv_exp_f32 v242,v242\nv_fma_f32 v113,v113,s69,-v248\nv_mfma_f32_16x16x32_bf16 v[80:83],a[204:207],a[252:255],0\nv_exp_f32 v243,v243\nv_mfma_f32_16x16x32_bf16 v[108:111],a[208:211],a[252:255],0\n"
"ds_read_b128 a[252:255],v239 offset:13312\ns_waitcnt lgkmcnt(2)\nv_fma_f32 v114,v114,s69,-v248\nv_mfma_f32_16x16x32_bf16 v[0:3],a[212:215],a[228:231],v[0:3]\nbuffer_load_dwordx4 v225,s[52:55],s73 offen offset:0 nt lds\nv_fma_f32 v115,v115,s69,-v248\nv_mfma_f32_16x16x32_bf16 v[28:31],a[216:219],a[228:231],v[28:31]\ns_waitcnt vmcnt(13)\n"
"ds_read_b64_tr_b16 a[196:197],v231 offset:16384\nv_fma_f32 v116,v116,s69,-v249\nv_mfma_f32_16x16x32_bf16 v[56:59],a[220:223],a[228:231],v[56:59]\nds_read_b64_tr_b16 a[198:199],v231 offset:17408\nv_fma_f32 v117,v117,s69,-v249\nv_mfma_f32_16x16x32_bf16 v[84:87],a[224:227],a[228:231],v[84:87]\ns_waitcnt lgkmcnt(2)\ns_barrier\n"
"ds_read_b128 a[228:231],v237 offset:0\nv_mfma_f32_16x16x32_bf16 v[4:7],a[212:215],a[232:235],v[4:7]\ns_mov_b32 s74,66560\nbuffer_load_dwordx4 v225,s[52:55],s74 offen offset:1024 nt lds\nv_mfma_f32_16x16x32_bf16 v[32:35],a[216:219],a[232:235],v[32:35]\nds_read_b64_tr_b16 a[200:201],v232 offset:16384\nv_exp_f32 v244,v244\nv_mfma_f32_16x16x32_bf16 v[60:63],a[220:223],a[232:235],v[60:63]\n"
"s_mov_b32 s75,71680\nv_exp_f32 v245,v245\nv_mfma_f32_16x16x32_bf16 v[88:91],a[224:227],a[232:235],v[88:91]\nds_read_b128 a[232:235],v237 offset:2048\nv_exp_f32 v246,v246\nv_mfma_f32_16x16x32_bf16 v[8:11],a[212:215],a[236:239],v[8:11]\nbuffer_load_dwordx4 v225,s[52:55],s75 offen offset:2048 nt lds\nv_fma_f32 v118,v118,s69,-v249\n"
"v_mfma_f32_16x16x32_bf16 v[36:39],a[216:219],a[236:239],v[36:39]\nds_read_b64_tr_b16 a[202:203],v232 offset:17408\nv_exp_f32 v247,v247\nv_mfma_f32_16x16x32_bf16 v[64:67],a[220:223],a[236:239],v[64:67]\ns_mov_b32 s72,72704\nv_exp_f32 v112,v112\nv_mfma_f32_16x16x32_bf16 v[92:95],a[224:227],a[236:239],v[92:95]\nds_read_b128 a[236:239],v237 offset:4096\n"
"v_exp_f32 v113,v113\nv_mfma_f32_16x16x32_bf16 v[12:15],a[212:215],a[240:243],v[12:15]\nbuffer_load_dwordx4 v225,s[52:55],s72 offen offset:3072 nt lds\nv_add_f32 v255,v112,v113\nv_mfma_f32_16x16x32_bf16 v[40:43],a[216:219],a[240:243],v[40:43]\nds_read_b64_tr_b16 a[204:205],v233 offset:16384\nv_exp_f32 v114,v114\nv_mfma_f32_16x16x32_bf16 v[68:71],a[220:223],a[240:243],v[68:71]\n"
"ds_read_b64_tr_b16 a[206:207],v233 offset:17408\nv_cvt_pk_bf16_f32 v112,v112,v113\nv_mfma_f32_16x16x32_bf16 v[96:99],a[224:227],a[240:243],v[96:99]\nds_read_b128 a[240:243],v237 offset:6144\nv_exp_f32 v115,v115\nv_mfma_f32_16x16x32_bf16 v[16:19],a[212:215],a[244:247],v[16:19]\nds_read_b64_tr_b16 a[208:209],v234 offset:16384\nv_cvt_pk_bf16_f32 v113,v114,v115\n"
"v_mfma_f32_16x16x32_bf16 v[44:47],a[216:219],a[244:247],v[44:47]\nv_add_f32 v114,v114,v115\nv_add_f32 v114,v114,v255\nv_fma_f32 v119,v119,s69,-v249\nv_mfma_f32_16x16x32_bf16 v[72:75],a[220:223],a[244:247],v[72:75]\nv_exp_f32 v116,v116\nv_mfma_f32_16x16x32_bf16 v[100:103],a[224:227],a[244:247],v[100:103]\nds_read_b128 a[244:247],v237 offset:8192\n"
"v_exp_f32 v117,v117\nv_mfma_f32_16x16x32_bf16 v[20:23],a[212:215],a[248:251],v[20:23]\nds_read_b64_tr_b16 a[210:211],v234 offset:17408\nv_add_f32 v255,v116,v117\nv_mfma_f32_16x16x32_bf16 v[48:51],a[216:219],a[248:251],v[48:51]\ns_add_u32 m0,s64,16384\nv_exp_f32 v118,v118\nv_cvt_pk_bf16_f32 v116,v116,v117\n"
"v_mfma_f32_16x16x32_bf16 v[76:79],a[220:223],a[248:251],v[76:79]\ns_mov_b32 s73,81920\nv_exp_f32 v119,v119\nv_mfma_f32_16x16x32_bf16 v[104:107],a[224:227],a[248:251],v[104:107]\nds_read_b128 a[248:251],v237 offset:10240\nv_cvt_pk_bf16_f32 v117,v118,v119\nv_add_f32 v118,v118,v119\nv_mfma_f32_16x16x32_bf16 v[24:27],a[212:215],a[252:255],v[24:27]\n"
"v_add_f32 v118,v118,v255\nv_fma_f32 v120,v120,s69,-v250\nv_mfma_f32_16x16x32_bf16 v[52:55],a[216:219],a[252:255],v[52:55]\nv_fma_f32 v121,v121,s69,-v250\nv_fma_f32 v122,v122,s69,-v250\nv_fma_f32 v123,v123,s69,-v250\nv_mfma_f32_16x16x32_bf16 v[80:83],a[220:223],a[252:255],v[80:83]\nv_exp_f32 v120,v120\n"
"v_mfma_f32_16x16x32_bf16 v[108:111],a[224:227],a[252:255],v[108:111]\nds_read_b128 a[252:255],v237 offset:12288\ns_waitcnt lgkmcnt(2)\nv_fma_f32 v124,v124,s69,-v251\nv_mfma_f32_16x16x32_bf16 a[0:3],a[196:199],a[228:231],a[0:3]\nbuffer_load_dwordx4 v225,s[52:55],s73 offen offset:0 nt lds\nv_fma_f32 v125,v125,s69,-v251\nv_mfma_f32_16x16x32_bf16 a[28:31],a[200:203],a[228:231],a[28:31]\n"
"s_waitcnt vmcnt(13)\nds_read_b64_tr_b16 a[212:213],v231 offset:0\nv_fma_f32 v126,v126,s69,-v251\nv_mfma_f32_16x16x32_bf16 a[56:59],a[204:207],a[228:231],a[56:59]\nds_read_b64_tr_b16 a[214:215],v231 offset:1024\nv_fma_f32 v127,v127,s69,-v251\nv_mfma_f32_16x16x32_bf16 a[84:87],a[208:211],a[228:231],a[84:87]\nds_read_b128 a[228:231],v238 offset:0\n"
"v_exp_f32 v121,v121\nv_mfma_f32_16x16x32_bf16 a[4:7],a[196:199],a[232:235],a[4:7]\ns_mov_b32 s74,82944\nbuffer_load_dwordx4 v225,s[52:55],s74 offen offset:1024 nt lds\nv_mfma_f32_16x16x32_bf16 a[32:35],a[200:203],a[232:235],a[32:35]\nds_read_b64_tr_b16 a[216:217],v232 offset:0\nv_exp_f32 v122,v122\nv_mfma_f32_16x16x32_bf16 a[60:63],a[204:207],a[232:235],a[60:63]\n"
"s_mov_b32 s75,88064\nv_exp_f32 v123,v123\nv_mfma_f32_16x16x32_bf16 a[88:91],a[208:211],a[232:235],a[88:91]\nds_read_b128 a[232:235],v238 offset:2048\nv_add_f32 v255,v120,v121\nv_cvt_pk_bf16_f32 v120,v120,v121\nv_mfma_f32_16x16x32_bf16 a[8:11],a[196:199],a[236:239],a[8:11]\nbuffer_load_dwordx4 v225,s[52:55],s75 offen offset:2048 nt lds\n"
"v_cvt_pk_bf16_f32 v121,v122,v123\nv_mfma_f32_16x16x32_bf16 a[36:39],a[200:203],a[236:239],a[36:39]\nds_read_b64_tr_b16 a[218:219],v232 offset:1024\nv_add_f32 v122,v122,v123\nv_add_f32 v122,v122,v255\nv_mfma_f32_16x16x32_bf16 a[64:67],a[204:207],a[236:239],a[64:67]\ns_mov_b32 s72,89088\nv_exp_f32 v124,v124\n"
"v_mfma_f32_16x16x32_bf16 a[92:95],a[208:211],a[236:239],a[92:95]\nds_read_b128 a[236:239],v238 offset:4096\nv_exp_f32 v125,v125\nv_mfma_f32_16x16x32_bf16 a[12:15],a[196:199],a[240:243],a[12:15]\nbuffer_load_dwordx4 v225,s[52:55],s72 offen offset:3072 nt lds\nv_add_f32 v255,v124,v125\nv_mfma_f32_16x16x32_bf16 a[40:43],a[200:203],a[240:243],a[40:43]\nds_read_b64_tr_b16 a[220:221],v233 offset:0\n"
"v_exp_f32 v126,v126\nv_mfma_f32_16x16x32_bf16 a[68:71],a[204:207],a[240:243],a[68:71]\nds_read_b64_tr_b16 a[222:223],v233 offset:1024\nv_cvt_pk_bf16_f32 v124,v124,v125\nv_mfma_f32_16x16x32_bf16 a[96:99],a[208:211],a[240:243],a[96:99]\nds_read_b128 a[240:243],v238 offset:6144\nv_exp_f32 v127,v127\nv_mfma_f32_16x16x32_bf16 a[16:19],a[196:199],a[244:247],a[16:19]\n"
"ds_read_b64_tr_b16 a[224:225],v234 offset:0\nv_cvt_pk_bf16_f32 v125,v126,v127\nv_mfma_f32_16x16x32_bf16 a[44:47],a[200:203],a[244:247],a[44:47]\nv_add_f32 v126,v126,v127\nv_add_f32 v126,v126,v255\nv_fma_f32 v128,v128,s69,-v252\nv_mfma_f32_16x16x32_bf16 a[72:75],a[204:207],a[244:247],a[72:75]\nv_fma_f32 v129,v129,s69,-v252\n"
"v_fma_f32 v130,v130,s69,-v252\nv_mfma_f32_16x16x32_bf16 a[100:103],a[208:211],a[244:247],a[100:103]\nds_read_b128 a[244:247],v238 offset:8192\ns_waitcnt lgkmcnt(12)\nv_fma_f32 v131,v131,s69,-v252\nv_mfma_f32_16x16x32_bf16 a[20:23],a[196:199],a[248:251],a[20:23]\nds_read_b64_tr_b16 a[226:227],v234 offset:1024\nv_fma_f32 v132,v132,s69,-v253\n"
"v_mfma_f32_16x16x32_bf16 a[48:51],a[200:203],a[248:251],a[48:51]\ns_add_u32 m0,s64,0\nv_exp_f32 v128,v128\nv_fma_f32 v133,v133,s69,-v253\nv_mfma_f32_16x16x32_bf16 a[76:79],a[204:207],a[248:251],a[76:79]\ns_mov_b32 s73,98304\nv_exp_f32 v129,v129\nv_mfma_f32_16x16x32_bf16 a[104:107],a[208:211],a[248:251],a[104:107]\n"
"ds_read_b128 a[248:251],v238 offset:10240\nv_exp_f32 v130,v130\nv_mfma_f32_16x16x32_bf16 a[24:27],a[196:199],a[252:255],a[24:27]\nv_exp_f32 v131,v131\nv_mfma_f32_16x16x32_bf16 a[52:55],a[200:203],a[252:255],a[52:55]\nv_add_f32 v255,v128,v129\nv_cvt_pk_bf16_f32 v128,v128,v129\nv_cvt_pk_bf16_f32 v129,v130,v131\n"
"v_mfma_f32_16x16x32_bf16 a[80:83],a[204:207],a[252:255],a[80:83]\nv_add_f32 v130,v130,v131\nv_add_f32 v130,v130,v255\nv_mfma_f32_16x16x32_bf16 a[108:111],a[208:211],a[252:255],a[108:111]\nds_read_b128 a[252:255],v238 offset:12288\ns_waitcnt lgkmcnt(2)\nv_fma_f32 v134,v134,s69,-v253\nv_mfma_f32_16x16x32_bf16 a[0:3],a[212:215],a[228:231],a[0:3]\n"
"buffer_load_dwordx4 v225,s[52:55],s73 offen offset:0 nt lds\nv_fma_f32 v135,v135,s69,-v253\nv_mfma_f32_16x16x32_bf16 a[28:31],a[216:219],a[228:231],a[28:31]\ns_waitcnt vmcnt(13)\nds_read_b64_tr_b16 a[196:197],v231 offset:4096\nv_fma_f32 v136,v136,s69,-v254\nv_mfma_f32_16x16x32_bf16 a[56:59],a[220:223],a[228:231],a[56:59]\nds_read_b64_tr_b16 a[198:199],v231 offset:5120\n"
"v_fma_f32 v137,v137,s69,-v254\nv_mfma_f32_16x16x32_bf16 a[84:87],a[224:227],a[228:231],a[84:87]\ns_waitcnt lgkmcnt(2)\ns_barrier\nds_read_b128 a[228:231],v239 offset:14336\nv_mfma_f32_16x16x32_bf16 a[4:7],a[212:215],a[232:235],a[4:7]\ns_mov_b32 s74,99328\nbuffer_load_dwordx4 v225,s[52:55],s74 offen offset:1024 nt lds\n"
"v_mfma_f32_16x16x32_bf16 a[32:35],a[216:219],a[232:235],a[32:35]\nds_read_b64_tr_b16 a[200:201],v232 offset:4096\nv_exp_f32 v132,v132\nv_mfma_f32_16x16x32_bf16 a[60:63],a[220:223],a[232:235],a[60:63]\ns_mov_b32 s75,104448\nv_exp_f32 v133,v133\nv_mfma_f32_16x16x32_bf16 a[88:91],a[224:227],a[232:235],a[88:91]\nds_read_b128 a[232:235],v239 offset:16384\n"
"v_exp_f32 v134,v134\nv_mfma_f32_16x16x32_bf16 a[8:11],a[212:215],a[236:239],a[8:11]\nbuffer_load_dwordx4 v225,s[52:55],s75 offen offset:2048 nt lds\nv_add_f32 v255,v132,v133\nv_mfma_f32_16x16x32_bf16 a[36:39],a[216:219],a[236:239],a[36:39]\nds_read_b64_tr_b16 a[202:203],v232 offset:5120\nv_exp_f32 v135,v135\nv_mfma_f32_16x16x32_bf16 a[64:67],a[220:223],a[236:239],a[64:67]\n"
"s_mov_b32 s72,105472\nv_cvt_pk_bf16_f32 v132,v132,v133\nv_cvt_pk_bf16_f32 v133,v134,v135\nv_mfma_f32_16x16x32_bf16 a[92:95],a[224:227],a[236:239],a[92:95]\nds_read_b128 a[236:239],v239 offset:18432\nv_add_f32 v134,v134,v135\nv_add_f32 v134,v134,v255\nv_mfma_f32_16x16x32_bf16 a[12:15],a[212:215],a[240:243],a[12:15]\n"
"buffer_load_dwordx4 v225,s[52:55],s72 offen offset:3072 nt lds\nv_fma_f32 v138,v138,s69,-v254\nv_mfma_f32_16x16x32_bf16 a[40:43],a[216:219],a[240:243],a[40:43]\nds_read_b64_tr_b16 a[204:205],v233 offset:4096\nv_fma_f32 v139,v139,s69,-v254\nv_fma_f32 v140,v140,s69,-v248\nv_mfma_f32_16x16x32_bf16 a[68:71],a[220:223],a[240:243],a[68:71]\nds_read_b64_tr_b16 a[206:207],v233 offset:5120\n"
"v_fma_f32 v141,v141,s69,-v248\nv_mfma_f32_16x16x32_bf16 a[96:99],a[224:227],a[240:243],a[96:99]\nds_read_b128 a[240:243],v239 offset:20480\nv_exp_f32 v136,v136\nv_mfma_f32_16x16x32_bf16 a[16:19],a[212:215],a[244:247],a[16:19]\nds_read_b64_tr_b16 a[208:209],v234 offset:4096\nv_fma_f32 v142,v142,s69,-v248\nv_mfma_f32_16x16x32_bf16 a[44:47],a[216:219],a[244:247],a[44:47]\n"
"v_exp_f32 v137,v137\nv_fma_f32 v143,v143,s69,-v248\nv_mfma_f32_16x16x32_bf16 a[72:75],a[220:223],a[244:247],a[72:75]\nv_exp_f32 v138,v138\nv_mfma_f32_16x16x32_bf16 a[100:103],a[224:227],a[244:247],a[100:103]\nds_read_b128 a[244:247],v239 offset:22528\nv_exp_f32 v139,v139\nv_mfma_f32_16x16x32_bf16 a[20:23],a[212:215],a[248:251],a[20:23]\n"
"ds_read_b64_tr_b16 a[210:211],v234 offset:5120\nv_add_f32 v255,v136,v137\nv_mfma_f32_16x16x32_bf16 a[48:51],a[216:219],a[248:251],a[48:51]\ns_add_u32 m0,s64,4096\nv_cvt_pk_bf16_f32 v136,v136,v137\nv_cvt_pk_bf16_f32 v137,v138,v139\nv_add_f32 v138,v138,v139\nv_mfma_f32_16x16x32_bf16 a[76:79],a[220:223],a[248:251],a[76:79]\n"
"s_mov_b32 s73,114688\nv_add_f32 v138,v138,v255\nv_fma_f32 v144,v144,s69,-v249\nv_mfma_f32_16x16x32_bf16 a[104:107],a[224:227],a[248:251],a[104:107]\nds_read_b128 a[248:251],v239 offset:24576\nv_exp_f32 v140,v140\nv_mfma_f32_16x16x32_bf16 a[24:27],a[212:215],a[252:255],a[24:27]\nds_write_b64 v240,v[112:113] offset:0\n"
"v_mfma_f32_16x16x32_bf16 a[52:55],a[216:219],a[252:255],a[52:55]\nv_fma_f32 v145,v145,s69,-v249\nv_fma_f32 v146,v146,s69,-v249\nv_fma_f32 v147,v147,s69,-v249\nv_mfma_f32_16x16x32_bf16 a[80:83],a[220:223],a[252:255],a[80:83]\nds_write_b64 v240,v[116:117] offset:2048\nv_mfma_f32_16x16x32_bf16 a[108:111],a[224:227],a[252:255],a[108:111]\nds_read_b128 a[252:255],v239 offset:26624\n"
"s_waitcnt lgkmcnt(4)\nv_fma_f32 v148,v148,s69,-v250\nv_mfma_f32_16x16x32_bf16 v[0:3],a[196:199],a[228:231],v[0:3]\nbuffer_load_dwordx4 v225,s[52:55],s73 offen offset:0 nt lds\nv_fma_f32 v149,v149,s69,-v250\nv_mfma_f32_16x16x32_bf16 v[28:31],a[200:203],a[228:231],v[28:31]\ns_waitcnt vmcnt(13)\nds_read_b64_tr_b16 a[212:213],v231 offset:8192\n"
"v_fma_f32 v150,v150,s69,-v250\nv_mfma_f32_16x16x32_bf16 v[56:59],a[204:207],a[228:231],v[56:59]\nds_read_b64_tr_b16 a[214:215],v231 offset:9216\nv_fma_f32 v151,v151,s69,-v250\nv_mfma_f32_16x16x32_bf16 v[84:87],a[208:211],a[228:231],v[84:87]\nds_read_b128 a[228:231],v239 offset:15360\nv_exp_f32 v144,v144\nv_mfma_f32_16x16x32_bf16 v[4:7],a[196:199],a[232:235],v[4:7]\n"
"s_mov_b32 s74,115712\nbuffer_load_dwordx4 v225,s[52:55],s74 offen offset:1024 nt lds\nv_mfma_f32_16x16x32_bf16 v[32:35],a[200:203],a[232:235],v[32:35]\nds_read_b64_tr_b16 a[216:217],v232 offset:8192\nv_exp_f32 v148,v148\nv_mfma_f32_16x16x32_bf16 v[60:63],a[204:207],a[232:235],v[60:63]\ns_mov_b32 s75,120832\nds_write_b64 v240,v[120:121] offset:4096\n"
"v_mfma_f32_16x16x32_bf16 v[88:91],a[208:211],a[232:235],v[88:91]\nds_read_b128 a[232:235],v239 offset:17408\nv_fma_f32 v152,v152,s69,-v251\nv_fma_f32 v153,v153,s69,-v251\nv_mfma_f32_16x16x32_bf16 v[8:11],a[196:199],a[236:239],v[8:11]\nbuffer_load_dwordx4 v225,s[52:55],s75 offen offset:2048 nt lds\nv_fma_f32 v154,v154,s69,-v251\nv_mfma_f32_16x16x32_bf16 v[36:39],a[200:203],a[236:239],v[36:39]\n"
"ds_read_b64_tr_b16 a[218:219],v232 offset:9216\nv_fma_f32 v155,v155,s69,-v251\nv_fma_f32 v156,v156,s69,-v252\nv_mfma_f32_16x16x32_bf16 v[64:67],a[204:207],a[236:239],v[64:67]\ns_mov_b32 s72,121856\nds_write_b64 v240,v[124:125] offset:6144\nv_mfma_f32_16x16x32_bf16 v[92:95],a[208:211],a[236:239],v[92:95]\nds_read_b128 a[236:239],v239 offset:19456\n"
"v_exp_f32 v152,v152\nv_mfma_f32_16x16x32_bf16 v[12:15],a[196:199],a[240:243],v[12:15]\nbuffer_load_dwordx4 v225,s[52:55],s72 offen offset:3072 nt lds\nv_fma_f32 v157,v157,s69,-v252\nv_mfma_f32_16x16x32_bf16 v[40:43],a[200:203],a[240:243],v[40:43]\nds_read_b64_tr_b16 a[220:221],v233 offset:8192\nv_fma_f32 v158,v158,s69,-v252\nv_fma_f32 v159,v159,s69,-v252\n"
"v_mfma_f32_16x16x32_bf16 v[68:71],a[204:207],a[240:243],v[68:71]\nds_read_b64_tr_b16 a[222:223],v233 offset:9216\nv_fma_f32 v160,v160,s69,-v253\nv_mfma_f32_16x16x32_bf16 v[96:99],a[208:211],a[240:243],v[96:99]\nds_read_b128 a[240:243],v239 offset:21504\nv_exp_f32 v156,v156\nv_mfma_f32_16x16x32_bf16 v[16:19],a[196:199],a[244:247],v[16:19]\nds_read_b64_tr_b16 a[224:225],v234 offset:8192\n"
"v_fma_f32 v161,v161,s69,-v253\nv_mfma_f32_16x16x32_bf16 v[44:47],a[200:203],a[244:247],v[44:47]\nds_write_b64 v240,v[128:129] offset:8192\nv_mfma_f32_16x16x32_bf16 v[72:75],a[204:207],a[244:247],v[72:75]\nv_fma_f32 v162,v162,s69,-v253\nv_fma_f32 v163,v163,s69,-v253\nv_mfma_f32_16x16x32_bf16 v[100:103],a[208:211],a[244:247],v[100:103]\nds_read_b128 a[244:247],v239 offset:23552\n"
"s_waitcnt lgkmcnt(15)\nv_fma_f32 v164,v164,s69,-v254\nv_mfma_f32_16x16x32_bf16 v[20:23],a[196:199],a[248:251],v[20:23]\nds_read_b64_tr_b16 a[226:227],v234 offset:9216\nv_fma_f32 v165,v165,s69,-v254\nv_mfma_f32_16x16x32_bf16 v[48:51],a[200:203],a[248:251],v[48:51]\ns_add_u32 m0,s64,8192\nds_write_b64 v240,v[132:133] offset:10240\n"
"v_mfma_f32_16x16x32_bf16 v[76:79],a[204:207],a[248:251],v[76:79]\ns_mov_b32 s73,131072\nv_exp_f32 v160,v160\nv_mfma_f32_16x16x32_bf16 v[104:107],a[208:211],a[248:251],v[104:107]\nds_read_b128 a[248:251],v239 offset:25600\nv_fma_f32 v166,v166,s69,-v254\nv_fma_f32 v167,v167,s69,-v254\nv_mfma_f32_16x16x32_bf16 v[24:27],a[196:199],a[252:255],v[24:27]\n"
"ds_write_b64 v240,v[136:137] offset:12288\nv_mfma_f32_16x16x32_bf16 v[52:55],a[200:203],a[252:255],v[52:55]\nv_exp_f32 v164,v164\nv_add_f32 v114,v140,v114\nv_mfma_f32_16x16x32_bf16 v[80:83],a[204:207],a[252:255],v[80:83]\nv_exp_f32 v141,v141\nv_mfma_f32_16x16x32_bf16 v[108:111],a[208:211],a[252:255],v[108:111]\nds_read_b128 a[252:255],v239 offset:27648\n"
"s_waitcnt lgkmcnt(4)\nv_add_f32 v114,v141,v114\nv_mfma_f32_16x16x32_bf16 v[0:3],a[212:215],a[228:231],v[0:3]\nbuffer_load_dwordx4 v225,s[52:55],s73 offen offset:0 nt lds\nv_cvt_pk_bf16_f32 v140,v140,v141\nv_mfma_f32_16x16x32_bf16 v[28:31],a[216:219],a[228:231],v[28:31]\ns_waitcnt vmcnt(13)\nds_read_b64_tr_b16 a[196:197],v231 offset:12288\n"
"v_add_f32 v118,v144,v118\nv_mfma_f32_16x16x32_bf16 v[56:59],a[220:223],a[228:231],v[56:59]\nds_read_b64_tr_b16 a[198:199],v231 offset:13312\nv_add_f32 v122,v148,v122\nv_mfma_f32_16x16x32_bf16 v[84:87],a[224:227],a[228:231],v[84:87]\ns_waitcnt lgkmcnt(2)\ns_barrier\nds_read_b128 a[228:231],v239 offset:28672\n"
"v_mfma_f32_16x16x32_bf16 v[4:7],a[212:215],a[232:235],v[4:7]\ns_mov_b32 s74,132096\nbuffer_load_dwordx4 v225,s[52:55],s74 offen offset:1024 nt lds\nv_mfma_f32_16x16x32_bf16 v[32:35],a[216:219],a[232:235],v[32:35]\nds_read_b64_tr_b16 a[200:201],v232 offset:12288\nv_exp_f32 v142,v142\nv_mfma_f32_16x16x32_bf16 v[60:63],a[220:223],a[232:235],v[60:63]\ns_mov_b32 s75,137216\n"
"v_exp_f32 v143,v143\nv_mfma_f32_16x16x32_bf16 v[88:91],a[224:227],a[232:235],v[88:91]\nds_read_b128 a[232:235],v239 offset:30720\nv_add_f32 v114,v142,v114\nv_cvt_pk_bf16_f32 v141,v142,v143\nv_mfma_f32_16x16x32_bf16 v[8:11],a[212:215],a[236:239],v[8:11]\nbuffer_load_dwordx4 v225,s[52:55],s75 offen offset:2048 nt lds\nv_add_f32 v114,v143,v114\n"
"v_mfma_f32_16x16x32_bf16 v[36:39],a[216:219],a[236:239],v[36:39]\nds_read_b64_tr_b16 a[202:203],v232 offset:13312\nv_exp_f32 v145,v145\nv_mfma_f32_16x16x32_bf16 v[64:67],a[220:223],a[236:239],v[64:67]\ns_mov_b32 s72,138240\nv_exp_f32 v146,v146\nv_mfma_f32_16x16x32_bf16 v[92:95],a[224:227],a[236:239],v[92:95]\nds_read_b128 a[236:239],v239 offset:32768\n"
"v_exp_f32 v147,v147\nv_mfma_f32_16x16x32_bf16 v[12:15],a[212:215],a[240:243],v[12:15]\nbuffer_load_dwordx4 v225,s[52:55],s72 offen offset:3072 nt lds\nv_add_f32 v118,v145,v118\nv_mfma_f32_16x16x32_bf16 v[40:43],a[216:219],a[240:243],v[40:43]\nds_read_b64_tr_b16 a[204:205],v233 offset:12288\nv_cvt_pk_bf16_f32 v144,v144,v145\nv_add_f32 v118,v146,v118\n"
"v_mfma_f32_16x16x32_bf16 v[68:71],a[220:223],a[240:243],v[68:71]\nds_read_b64_tr_b16 a[206:207],v233 offset:13312\nv_cvt_pk_bf16_f32 v145,v146,v147\nv_mfma_f32_16x16x32_bf16 v[96:99],a[224:227],a[240:243],v[96:99]\nds_read_b128 a[240:243],v239 offset:34816\nv_add_f32 v118,v147,v118\nv_add_f32 v126,v152,v126\nv_mfma_f32_16x16x32_bf16 v[16:19],a[212:215],a[244:247],v[16:19]\n"
"ds_read_b64_tr_b16 a[208:209],v234 offset:12288\nv_add_f32 v130,v156,v130\nv_mfma_f32_16x16x32_bf16 v[44:47],a[216:219],a[244:247],v[44:47]\nv_exp_f32 v149,v149\nv_mfma_f32_16x16x32_bf16 v[72:75],a[220:223],a[244:247],v[72:75]\nv_exp_f32 v150,v150\nv_mfma_f32_16x16x32_bf16 v[100:103],a[224:227],a[244:247],v[100:103]\nds_read_b128 a[244:247],v239 offset:36864\n"
"v_exp_f32 v151,v151\nv_mfma_f32_16x16x32_bf16 v[20:23],a[212:215],a[248:251],v[20:23]\nds_read_b64_tr_b16 a[210:211],v234 offset:13312\nv_add_f32 v122,v149,v122\nv_mfma_f32_16x16x32_bf16 v[48:51],a[216:219],a[248:251],v[48:51]\ns_add_u32 m0,s64,12288\nv_cvt_pk_bf16_f32 v148,v148,v149\nv_add_f32 v122,v150,v122\n"
"v_cvt_pk_bf16_f32 v149,v150,v151\nv_mfma_f32_16x16x32_bf16 v[76:79],a[220:223],a[248:251],v[76:79]\ns_mov_b32 s73,147456\nv_add_f32 v122,v151,v122\nv_add_f32 v134,v160,v134\nv_mfma_f32_16x16x32_bf16 v[104:107],a[224:227],a[248:251],v[104:107]\nds_read_b128 a[248:251],v239 offset:38912\nv_exp_f32 v153,v153\n"
"v_mfma_f32_16x16x32_bf16 v[24:27],a[212:215],a[252:255],v[24:27]\nv_exp_f32 v154,v154\nv_mfma_f32_16x16x32_bf16 v[52:55],a[216:219],a[252:255],v[52:55]\nv_exp_f32 v155,v155\nv_add_f32 v126,v153,v126\nv_mfma_f32_16x16x32_bf16 v[80:83],a[220:223],a[252:255],v[80:83]\nv_cvt_pk_bf16_f32 v152,v152,v153\nv_add_f32 v126,v154,v126\n"
"v_mfma_f32_16x16x32_bf16 v[108:111],a[224:227],a[252:255],v[108:111]\nds_read_b128 a[252:255],v239 offset:40960\ns_waitcnt lgkmcnt(2)\nv_cvt_pk_bf16_f32 v153,v154,v155\nv_mfma_f32_16x16x32_bf16 v[0:3],a[196:199],a[228:231],v[0:3]\nbuffer_load_dwordx4 v225,s[52:55],s73 offen offset:0 nt lds\nv_add_f32 v126,v155,v126\nv_mfma_f32_16x16x32_bf16 v[28:31],a[200:203],a[228:231],v[28:31]\n"
"s_waitcnt vmcnt(13)\nds_read_b64_tr_b16 a[212:213],v231 offset:16384\nv_add_f32 v138,v164,v138\nv_mfma_f32_16x16x32_bf16 v[56:59],a[204:207],a[228:231],v[56:59]\nds_read_b64_tr_b16 a[214:215],v231 offset:17408\ns_mov_b32 s74,148480\nv_mfma_f32_16x16x32_bf16 v[84:87],a[208:211],a[228:231],v[84:87]\nds_read_b128 a[228:231],v239 offset:29696\n"
"v_exp_f32 v157,v157\nv_mfma_f32_16x16x32_bf16 v[4:7],a[196:199],a[232:235],v[4:7]\nbuffer_load_dwordx4 v225,s[52:55],s74 offen offset:1024 nt lds\nv_add_f32 v130,v157,v130\nv_mfma_f32_16x16x32_bf16 v[32:35],a[200:203],a[232:235],v[32:35]\nds_read_b64_tr_b16 a[216:217],v232 offset:16384\nv_exp_f32 v158,v158\nv_mfma_f32_16x16x32_bf16 v[60:63],a[204:207],a[232:235],v[60:63]\n"
"s_mov_b32 s75,153600\nv_exp_f32 v159,v159\nv_mfma_f32_16x16x32_bf16 v[88:91],a[208:211],a[232:235],v[88:91]\nds_read_b128 a[232:235],v239 offset:31744\nv_cvt_pk_bf16_f32 v156,v156,v157\nv_add_f32 v130,v158,v130\nv_mfma_f32_16x16x32_bf16 v[8:11],a[196:199],a[236:239],v[8:11]\nbuffer_load_dwordx4 v225,s[52:55],s75 offen offset:2048 nt lds\n"
"v_cvt_pk_bf16_f32 v157,v158,v159\nv_mfma_f32_16x16x32_bf16 v[36:39],a[200:203],a[236:239],v[36:39]\nds_read_b64_tr_b16 a[218:219],v232 offset:17408\nv_add_f32 v130,v159,v130\nv_fma_f32 v168,v168,s69,-v248\nv_mfma_f32_16x16x32_bf16 v[64:67],a[204:207],a[236:239],v[64:67]\ns_mov_b32 s72,154624\nv_exp_f32 v161,v161\n"
"v_mfma_f32_16x16x32_bf16 v[92:95],a[208:211],a[236:239],v[92:95]\nds_read_b128 a[236:239],v239 offset:33792\nv_exp_f32 v162,v162\nv_mfma_f32_16x16x32_bf16 v[12:15],a[196:199],a[240:243],v[12:15]\nbuffer_load_dwordx4 v225,s[52:55],s72 offen offset:3072 nt lds\nv_add_f32 v134,v161,v134\nv_mfma_f32_16x16x32_bf16 v[40:43],a[200:203],a[240:243],v[40:43]\nds_read_b64_tr_b16 a[220:221],v233 offset:16384\n"
"v_exp_f32 v163,v163\nv_mfma_f32_16x16x32_bf16 v[68:71],a[204:207],a[240:243],v[68:71]\nds_read_b64_tr_b16 a[222:223],v233 offset:17408\nv_cvt_pk_bf16_f32 v160,v160,v161\nv_mfma_f32_16x16x32_bf16 v[96:99],a[208:211],a[240:243],v[96:99]\nds_read_b128 a[240:243],v239 offset:35840\nv_add_f32 v134,v162,v134\nv_cvt_pk_bf16_f32 v161,v162,v163\n"
"v_mfma_f32_16x16x32_bf16 v[16:19],a[196:199],a[244:247],v[16:19]\nds_read_b64_tr_b16 a[224:225],v234 offset:16384\nv_add_f32 v134,v163,v134\nv_mfma_f32_16x16x32_bf16 v[44:47],a[200:203],a[244:247],v[44:47]\nv_exp_f32 v165,v165\nv_fma_f32 v169,v169,s69,-v248\nv_mfma_f32_16x16x32_bf16 v[72:75],a[204:207],a[244:247],v[72:75]\nv_exp_f32 v166,v166\n"
"v_mfma_f32_16x16x32_bf16 v[100:103],a[208:211],a[244:247],v[100:103]\nds_read_b128 a[244:247],v239 offset:37888\ns_waitcnt lgkmcnt(12)\nv_add_f32 v138,v165,v138\nv_mfma_f32_16x16x32_bf16 v[20:23],a[196:199],a[248:251],v[20:23]\nds_read_b64_tr_b16 a[226:227],v234 offset:17408\nv_cvt_pk_bf16_f32 v164,v164,v165\nv_mfma_f32_16x16x32_bf16 v[48:51],a[200:203],a[248:251],v[48:51]\n"
"s_add_u32 m0,s64,16384\nv_exp_f32 v167,v167\nv_add_f32 v138,v166,v138\nv_mfma_f32_16x16x32_bf16 v[76:79],a[204:207],a[248:251],v[76:79]\ns_mov_b32 s73,163840\nv_cvt_pk_bf16_f32 v165,v166,v167\nv_add_f32 v138,v167,v138\nv_mfma_f32_16x16x32_bf16 v[104:107],a[208:211],a[248:251],v[104:107]\n"
"ds_read_b128 a[248:251],v239 offset:39936\nv_fma_f32 v170,v170,s69,-v248\nv_fma_f32 v171,v171,s69,-v248\nv_mfma_f32_16x16x32_bf16 v[24:27],a[196:199],a[252:255],v[24:27]\nds_write_b64 v240,v[140:141] offset:14336\nv_mfma_f32_16x16x32_bf16 v[52:55],a[200:203],a[252:255],v[52:55]\nv_exp_f32 v168,v168\nv_fma_f32 v172,v172,s69,-v249\n"
"v_mfma_f32_16x16x32_bf16 v[80:83],a[204:207],a[252:255],v[80:83]\nds_write_b64 v240,v[144:145] offset:16384\nv_mfma_f32_16x16x32_bf16 v[108:111],a[208:211],a[252:255],v[108:111]\nds_read_b128 a[252:255],v239 offset:41984\ns_waitcnt lgkmcnt(4)\nv_fma_f32 v173,v173,s69,-v249\nv_mfma_f32_16x16x32_bf16 v[0:3],a[212:215],a[228:231],v[0:3]\nbuffer_load_dwordx4 v225,s[52:55],s73 offen offset:0 nt lds\n"
"v_fma_f32 v174,v174,s69,-v249\nv_mfma_f32_16x16x32_bf16 v[28:31],a[216:219],a[228:231],v[28:31]\ns_waitcnt vmcnt(13)\nds_read_b64_tr_b16 a[196:197],v231 offset:0\nv_fma_f32 v175,v175,s69,-v249\nv_mfma_f32_16x16x32_bf16 v[56:59],a[220:223],a[228:231],v[56:59]\nds_read_b64_tr_b16 a[198:199],v231 offset:1024\nv_fma_f32 v176,v176,s69,-v250\n"
"v_mfma_f32_16x16x32_bf16 v[84:87],a[224:227],a[228:231],v[84:87]\ns_waitcnt lgkmcnt(2)\ns_barrier\nds_read_b128 a[228:231],v239 offset:43008\nv_mfma_f32_16x16x32_bf16 v[4:7],a[212:215],a[232:235],v[4:7]\ns_mov_b32 s74,164864\nbuffer_load_dwordx4 v225,s[52:55],s74 offen offset:1024 nt lds\nv_mfma_f32_16x16x32_bf16 v[32:35],a[216:219],a[232:235],v[32:35]\n"
"ds_read_b64_tr_b16 a[200:201],v232 offset:0\nv_exp_f32 v172,v172\nv_mfma_f32_16x16x32_bf16 v[60:63],a[220:223],a[232:235],v[60:63]\ns_mov_b32 s75,169984\nds_write_b64 v240,v[148:149] offset:18432\nv_mfma_f32_16x16x32_bf16 v[88:91],a[224:227],a[232:235],v[88:91]\nds_read_b128 a[232:235],v239 offset:45056\nv_fma_f32 v177,v177,s69,-v250\n"
"v_fma_f32 v178,v178,s69,-v250\nv_mfma_f32_16x16x32_bf16 v[8:11],a[212:215],a[236:239],v[8:11]\nbuffer_load_dwordx4 v225,s[52:55],s75 offen offset:2048 nt lds\nv_fma_f32 v179,v179,s69,-v250\nv_mfma_f32_16x16x32_bf16 v[36:39],a[216:219],a[236:239],v[36:39]\nds_read_b64_tr_b16 a[202:203],v232 offset:1024\nv_exp_f32 v176,v176\nv_mfma_f32_16x16x32_bf16 v[64:67],a[220:223],a[236:239],v[64:67]\n"
"s_mov_b32 s72,171008\nds_write_b64 v240,v[152:153] offset:20480\nv_mfma_f32_16x16x32_bf16 v[92:95],a[224:227],a[236:239],v[92:95]\nds_read_b128 a[236:239],v239 offset:47104\nv_fma_f32 v180,v180,s69,-v251\nv_fma_f32 v181,v181,s69,-v251\nv_mfma_f32_16x16x32_bf16 v[12:15],a[212:215],a[240:243],v[12:15]\nbuffer_load_dwordx4 v225,s[52:55],s72 offen offset:3072 nt lds\n"
"v_fma_f32 v182,v182,s69,-v251\nv_mfma_f32_16x16x32_bf16 v[40:43],a[216:219],a[240:243],v[40:43]\nds_read_b64_tr_b16 a[204:205],v233 offset:0\nv_fma_f32 v183,v183,s69,-v251\nv_fma_f32 v184,v184,s69,-v252\nv_mfma_f32_16x16x32_bf16 v[68:71],a[220:223],a[240:243],v[68:71]\nds_read_b64_tr_b16 a[206:207],v233 offset:1024\nv_fma_f32 v185,v185,s69,-v252\n"
"v_mfma_f32_16x16x32_bf16 v[96:99],a[224:227],a[240:243],v[96:99]\nds_read_b128 a[240:243],v239 offset:49152\nv_exp_f32 v180,v180\nv_mfma_f32_16x16x32_bf16 v[16:19],a[212:215],a[244:247],v[16:19]\nds_read_b64_tr_b16 a[208:209],v234 offset:0\nv_fma_f32 v186,v186,s69,-v252\nv_mfma_f32_16x16x32_bf16 v[44:47],a[216:219],a[244:247],v[44:47]\nds_write_b64 v240,v[156:157] offset:22528\n"
"v_mfma_f32_16x16x32_bf16 v[72:75],a[220:223],a[244:247],v[72:75]\nv_fma_f32 v187,v187,s69,-v252\nv_fma_f32 v188,v188,s69,-v253\nv_mfma_f32_16x16x32_bf16 v[100:103],a[224:227],a[244:247],v[100:103]\nds_read_b128 a[244:247],v239 offset:51200\nv_exp_f32 v184,v184\nv_mfma_f32_16x16x32_bf16 v[20:23],a[212:215],a[248:251],v[20:23]\nds_read_b64_tr_b16 a[210:211],v234 offset:1024\n"
"v_fma_f32 v189,v189,s69,-v253\nv_mfma_f32_16x16x32_bf16 v[48:51],a[216:219],a[248:251],v[48:51]\ns_add_u32 m0,s64,0\nds_write_b64 v240,v[160:161] offset:24576\nv_mfma_f32_16x16x32_bf16 v[76:79],a[220:223],a[248:251],v[76:79]\ns_mov_b32 s73,180224\nv_fma_f32 v190,v190,s69,-v253\nv_fma_f32 v191,v191,s69,-v253\n"
"v_mfma_f32_16x16x32_bf16 v[104:107],a[224:227],a[248:251],v[104:107]\nds_read_b128 a[248:251],v239 offset:53248\nv_exp_f32 v188,v188\nv_mfma_f32_16x16x32_bf16 v[24:27],a[212:215],a[252:255],v[24:27]\nds_write_b64 v240,v[164:165] offset:26624\nv_mfma_f32_16x16x32_bf16 v[52:55],a[216:219],a[252:255],v[52:55]\nv_fma_f32 v192,v192,s69,-v254\nv_fma_f32 v193,v193,s69,-v254\n"
"v_fma_f32 v194,v194,s69,-v254\nv_mfma_f32_16x16x32_bf16 v[80:83],a[220:223],a[252:255],v[80:83]\nv_fma_f32 v195,v195,s69,-v254\nv_mfma_f32_16x16x32_bf16 v[108:111],a[224:227],a[252:255],v[108:111]\nds_read_b128 a[252:255],v239 offset:55296\ns_waitcnt lgkmcnt(4)\nv_mfma_f32_16x16x32_bf16 v[0:3],a[196:199],a[228:231],v[0:3]\nbuffer_load_dwordx4 v225,s[52:55],s73 offen offset:0 nt lds\n"
"s_mov_b32 s74,181248\nv_mfma_f32_16x16x32_bf16 v[28:31],a[200:203],a[228:231],v[28:31]\ns_waitcnt vmcnt(13)\nds_read_b64_tr_b16 a[212:213],v231 offset:4096\nv_mfma_f32_16x16x32_bf16 v[56:59],a[204:207],a[228:231],v[56:59]\nds_read_b64_tr_b16 a[214:215],v231 offset:5120\nv_mfma_f32_16x16x32_bf16 v[84:87],a[208:211],a[228:231],v[84:87]\nds_read_b128 a[228:231],v239 offset:44032\n"
"v_exp_f32 v192,v192\nv_mfma_f32_16x16x32_bf16 v[4:7],a[196:199],a[232:235],v[4:7]\nbuffer_load_dwordx4 v225,s[52:55],s74 offen offset:1024 nt lds\nv_add_f32 v114,v168,v114\nv_mfma_f32_16x16x32_bf16 v[32:35],a[200:203],a[232:235],v[32:35]\nds_read_b64_tr_b16 a[216:217],v232 offset:4096\nv_exp_f32 v169,v169\nv_mfma_f32_16x16x32_bf16 v[60:63],a[204:207],a[232:235],v[60:63]\n"
"s_mov_b32 s75,186368\nv_exp_f32 v170,v170\nv_mfma_f32_16x16x32_bf16 v[88:91],a[208:211],a[232:235],v[88:91]\nds_read_b128 a[232:235],v239 offset:46080\nv_exp_f32 v171,v171\nv_mfma_f32_16x16x32_bf16 v[8:11],a[196:199],a[236:239],v[8:11]\nbuffer_load_dwordx4 v225,s[52:55],s75 offen offset:2048 nt lds\nv_add_f32 v114,v169,v114\n"
"v_mfma_f32_16x16x32_bf16 v[36:39],a[200:203],a[236:239],v[36:39]\nds_read_b64_tr_b16 a[218:219],v232 offset:5120\nv_cvt_pk_bf16_f32 v168,v168,v169\nv_add_f32 v114,v170,v114\nv_mfma_f32_16x16x32_bf16 v[64:67],a[204:207],a[236:239],v[64:67]\ns_mov_b32 s72,187392\nv_cvt_pk_bf16_f32 v169,v170,v171\nv_add_f32 v114,v171,v114\n"
"v_mfma_f32_16x16x32_bf16 v[92:95],a[208:211],a[236:239],v[92:95]\nds_read_b128 a[236:239],v239 offset:48128\nv_exp_f32 v173,v173\nv_mfma_f32_16x16x32_bf16 v[12:15],a[196:199],a[240:243],v[12:15]\nbuffer_load_dwordx4 v225,s[52:55],s72 offen offset:3072 nt lds\nv_add_f32 v118,v172,v118\nv_mfma_f32_16x16x32_bf16 v[40:43],a[200:203],a[240:243],v[40:43]\nds_read_b64_tr_b16 a[220:221],v233 offset:4096\n"
"v_exp_f32 v174,v174\nv_mfma_f32_16x16x32_bf16 v[68:71],a[204:207],a[240:243],v[68:71]\nds_read_b64_tr_b16 a[222:223],v233 offset:5120\nv_add_f32 v118,v173,v118\nv_mfma_f32_16x16x32_bf16 v[96:99],a[208:211],a[240:243],v[96:99]\nds_read_b128 a[240:243],v239 offset:50176\nv_exp_f32 v175,v175\nv_mfma_f32_16x16x32_bf16 v[16:19],a[196:199],a[244:247],v[16:19]\n"
"ds_read_b64_tr_b16 a[224:225],v234 offset:4096\nv_cvt_pk_bf16_f32 v172,v172,v173\nv_mfma_f32_16x16x32_bf16 v[44:47],a[200:203],a[244:247],v[44:47]\nv_add_f32 v118,v174,v118\nv_cvt_pk_bf16_f32 v173,v174,v175\nv_add_f32 v118,v175,v118\nv_mfma_f32_16x16x32_bf16 v[72:75],a[204:207],a[244:247],v[72:75]\nv_exp_f32 v177,v177\n"
"v_mfma_f32_16x16x32_bf16 v[100:103],a[208:211],a[244:247],v[100:103]\nds_read_b128 a[244:247],v239 offset:52224\ns_waitcnt lgkmcnt(12)\nv_add_f32 v122,v176,v122\nv_mfma_f32_16x16x32_bf16 v[20:23],a[196:199],a[248:251],v[20:23]\nds_read_b64_tr_b16 a[226:227],v234 offset:5120\nv_add_f32 v122,v177,v122\nv_mfma_f32_16x16x32_bf16 v[48:51],a[200:203],a[248:251],v[48:51]\n"
"s_add_u32 m0,s64,4096\nv_exp_f32 v178,v178\nv_cvt_pk_bf16_f32 v176,v176,v177\nv_mfma_f32_16x16x32_bf16 v[76:79],a[204:207],a[248:251],v[76:79]\ns_mov_b32 s73,196608\nv_exp_f32 v179,v179\nv_mfma_f32_16x16x32_bf16 v[104:107],a[208:211],a[248:251],v[104:107]\nds_read_b128 a[248:251],v239 offset:54272\n"
"v_add_f32 v122,v178,v122\nv_cvt_pk_bf16_f32 v177,v178,v179\nv_mfma_f32_16x16x32_bf16 v[24:27],a[196:199],a[252:255],v[24:27]\nv_add_f32 v122,v179,v122\nv_add_f32 v126,v180,v126\nv_mfma_f32_16x16x32_bf16 v[52:55],a[200:203],a[252:255],v[52:55]\nv_exp_f32 v181,v181\nv_add_f32 v130,v184,v130\n"
"v_mfma_f32_16x16x32_bf16 v[80:83],a[204:207],a[252:255],v[80:83]\nv_exp_f32 v182,v182\nv_mfma_f32_16x16x32_bf16 v[108:111],a[208:211],a[252:255],v[108:111]\nds_read_b128 a[252:255],v239 offset:56320\ns_waitcnt lgkmcnt(2)\nv_add_f32 v126,v181,v126\nv_mfma_f32_16x16x32_bf16 v[0:3],a[212:215],a[228:231],v[0:3]\nbuffer_load_dwordx4 v225,s[52:55],s73 offen offset:0 nt lds\n"
"v_cvt_pk_bf16_f32 v180,v180,v181\nv_mfma_f32_16x16x32_bf16 v[28:31],a[216:219],a[228:231],v[28:31]\ns_waitcnt vmcnt(13)\nds_read_b64_tr_b16 a[196:197],v231 offset:8192\nv_add_f32 v126,v182,v126\nv_mfma_f32_16x16x32_bf16 v[56:59],a[220:223],a[228:231],v[56:59]\nds_read_b64_tr_b16 a[198:199],v231 offset:9216\nv_add_f32 v134,v188,v134\n"
"v_mfma_f32_16x16x32_bf16 v[84:87],a[224:227],a[228:231],v[84:87]\ns_waitcnt lgkmcnt(2)\ns_barrier\nds_read_b128 a[228:231],v239 offset:0\nv_mfma_f32_16x16x32_bf16 v[4:7],a[212:215],a[232:235],v[4:7]\ns_mov_b32 s74,197632\nbuffer_load_dwordx4 v225,s[52:55],s74 offen offset:1024 nt lds\nv_mfma_f32_16x16x32_bf16 v[32:35],a[216:219],a[232:235],v[32:35]\n"
"ds_read_b64_tr_b16 a[200:201],v232 offset:8192\nv_exp_f32 v183,v183\nv_mfma_f32_16x16x32_bf16 v[60:63],a[220:223],a[232:235],v[60:63]\ns_mov_b32 s75,202752\nv_cvt_pk_bf16_f32 v181,v182,v183\nv_add_f32 v126,v183,v126\nv_mfma_f32_16x16x32_bf16 v[88:91],a[224:227],a[232:235],v[88:91]\nds_read_b128 a[232:235],v239 offset:2048\n"
"v_exp_f32 v185,v185\nv_mfma_f32_16x16x32_bf16 v[8:11],a[212:215],a[236:239],v[8:11]\nbuffer_load_dwordx4 v225,s[52:55],s75 offen offset:2048 nt lds\nv_add_f32 v130,v185,v130\nv_mfma_f32_16x16x32_bf16 v[36:39],a[216:219],a[236:239],v[36:39]\nds_read_b64_tr_b16 a[202:203],v232 offset:9216\nv_exp_f32 v186,v186\nv_mfma_f32_16x16x32_bf16 v[64:67],a[220:223],a[236:239],v[64:67]\n"
"s_mov_b32 s72,203776\nv_exp_f32 v187,v187\nv_mfma_f32_16x16x32_bf16 v[92:95],a[224:227],a[236:239],v[92:95]\nds_read_b128 a[236:239],v239 offset:4096\nv_cvt_pk_bf16_f32 v184,v184,v185\nv_add_f32 v130,v186,v130\nv_mfma_f32_16x16x32_bf16 v[12:15],a[212:215],a[240:243],v[12:15]\nbuffer_load_dwordx4 v225,s[52:55],s72 offen offset:3072 nt lds\n"
"v_mul_f32 v0,v241,v0\nv_mul_f32 v1,v241,v1\nv_mul_f32 v2,v241,v2\nv_mul_f32 v3,v241,v3\nv_mfma_f32_16x16x32_bf16 v[40:43],a[216:219],a[240:243],v[40:43]\nds_read_b64_tr_b16 a[204:205],v233 offset:8192\nv_mul_f32 v28,v241,v28\nv_mul_f32 v29,v241,v29\n"
"v_mul_f32 v30,v241,v30\nv_mul_f32 v31,v241,v31\nv_mfma_f32_16x16x32_bf16 v[68:71],a[220:223],a[240:243],v[68:71]\nds_read_b64_tr_b16 a[206:207],v233 offset:9216\nv_mul_f32 v56,v241,v56\nv_mul_f32 v57,v241,v57\nv_mul_f32 v58,v241,v58\nv_mul_f32 v59,v241,v59\n"
"v_mfma_f32_16x16x32_bf16 v[96:99],a[224:227],a[240:243],v[96:99]\nds_read_b128 a[240:243],v239 offset:6144\nv_mul_f32 v84,v241,v84\nv_mul_f32 v85,v241,v85\nv_mul_f32 v86,v241,v86\nv_mul_f32 v87,v241,v87\nv_mfma_f32_16x16x32_bf16 v[16:19],a[212:215],a[244:247],v[16:19]\nds_read_b64_tr_b16 a[208:209],v234 offset:8192\n"
"v_mul_f32 v4,v242,v4\nv_mul_f32 v5,v242,v5\nv_mul_f32 v6,v242,v6\nv_mul_f32 v7,v242,v7\nv_mfma_f32_16x16x32_bf16 v[44:47],a[216:219],a[244:247],v[44:47]\nv_mul_f32 v32,v242,v32\nv_mul_f32 v33,v242,v33\nv_mul_f32 v34,v242,v34\n"
"v_mul_f32 v35,v242,v35\nv_mfma_f32_16x16x32_bf16 v[72:75],a[220:223],a[244:247],v[72:75]\nv_mul_f32 v60,v242,v60\nv_mul_f32 v61,v242,v61\nv_mul_f32 v62,v242,v62\nv_mul_f32 v63,v242,v63\nv_mfma_f32_16x16x32_bf16 v[100:103],a[224:227],a[244:247],v[100:103]\nds_read_b128 a[244:247],v239 offset:8192\n"
"v_mul_f32 v88,v242,v88\nv_mul_f32 v89,v242,v89\nv_mul_f32 v90,v242,v90\nv_mul_f32 v91,v242,v91\nv_mfma_f32_16x16x32_bf16 v[20:23],a[212:215],a[248:251],v[20:23]\nds_read_b64_tr_b16 a[210:211],v234 offset:9216\nv_mul_f32 v8,v243,v8\nv_mul_f32 v9,v243,v9\n"
"v_mul_f32 v10,v243,v10\nv_mul_f32 v11,v243,v11\nv_mfma_f32_16x16x32_bf16 v[48:51],a[216:219],a[248:251],v[48:51]\ns_add_u32 m0,s64,8192\nv_mul_f32 v36,v243,v36\nv_mul_f32 v37,v243,v37\nv_mul_f32 v38,v243,v38\nv_mul_f32 v39,v243,v39\n"
"v_mfma_f32_16x16x32_bf16 v[76:79],a[220:223],a[248:251],v[76:79]\ns_mov_b32 s73,212992\nv_mul_f32 v64,v243,v64\nv_mul_f32 v65,v243,v65\nv_mul_f32 v66,v243,v66\nv_mul_f32 v67,v243,v67\nv_mfma_f32_16x16x32_bf16 v[104:107],a[224:227],a[248:251],v[104:107]\nds_read_b128 a[248:251],v239 offset:10240\n"
"v_mul_f32 v92,v243,v92\nv_mul_f32 v93,v243,v93\nv_mul_f32 v94,v243,v94\nv_mul_f32 v95,v243,v95\nv_mfma_f32_16x16x32_bf16 v[24:27],a[212:215],a[252:255],v[24:27]\nv_mul_f32 v12,v244,v12\nv_mul_f32 v13,v244,v13\nv_mul_f32 v14,v244,v14\n"
"v_mul_f32 v15,v244,v15\nv_mfma_f32_16x16x32_bf16 v[52:55],a[216:219],a[252:255],v[52:55]\nv_mul_f32 v40,v244,v40\nv_mul_f32 v41,v244,v41\nv_mul_f32 v42,v244,v42\nv_mul_f32 v43,v244,v43\nv_mfma_f32_16x16x32_bf16 v[80:83],a[220:223],a[252:255],v[80:83]\nv_mul_f32 v68,v244,v68\n"
"v_mul_f32 v69,v244,v69\nv_mul_f32 v70,v244,v70\nv_mul_f32 v71,v244,v71\nv_mfma_f32_16x16x32_bf16 v[108:111],a[224:227],a[252:255],v[108:111]\nds_read_b128 a[252:255],v239 offset:12288\ns_waitcnt lgkmcnt(2)\nv_mul_f32 v96,v244,v96\nv_mul_f32 v97,v244,v97\n"
"v_mul_f32 v98,v244,v98\nv_mul_f32 v99,v244,v99\nv_mfma_f32_16x16x32_bf16 v[0:3],a[196:199],a[228:231],v[0:3]\nbuffer_load_dwordx4 v225,s[52:55],s73 offen offset:0 nt lds\nv_mul_f32 v16,v245,v16\nv_mul_f32 v17,v245,v17\nv_mul_f32 v18,v245,v18\nv_mul_f32 v19,v245,v19\n"
"v_mfma_f32_16x16x32_bf16 v[28:31],a[200:203],a[228:231],v[28:31]\ns_waitcnt vmcnt(13)\nds_read_b64_tr_b16 a[212:213],v231 offset:12288\nv_mul_f32 v44,v245,v44\nv_mul_f32 v45,v245,v45\nv_mul_f32 v46,v245,v46\nv_mul_f32 v47,v245,v47\nv_mfma_f32_16x16x32_bf16 v[56:59],a[204:207],a[228:231],v[56:59]\n"
"ds_read_b64_tr_b16 a[214:215],v231 offset:13312\nv_mul_f32 v72,v245,v72\nv_mul_f32 v73,v245,v73\nv_mul_f32 v74,v245,v74\nv_mul_f32 v75,v245,v75\nv_mfma_f32_16x16x32_bf16 v[84:87],a[208:211],a[228:231],v[84:87]\nds_read_b128 a[228:231],v239 offset:1024\nv_mul_f32 v100,v245,v100\n"
"v_mul_f32 v101,v245,v101\nv_mul_f32 v102,v245,v102\nv_mul_f32 v103,v245,v103\nv_mfma_f32_16x16x32_bf16 v[4:7],a[196:199],a[232:235],v[4:7]\ns_mov_b32 s74,214016\nbuffer_load_dwordx4 v225,s[52:55],s74 offen offset:1024 nt lds\nv_mul_f32 v20,v246,v20\nv_mul_f32 v21,v246,v21\n"
"v_mul_f32 v22,v246,v22\nv_mul_f32 v23,v246,v23\nv_mfma_f32_16x16x32_bf16 v[32:35],a[200:203],a[232:235],v[32:35]\nds_read_b64_tr_b16 a[216:217],v232 offset:12288\nv_mul_f32 v48,v246,v48\nv_mul_f32 v49,v246,v49\nv_mul_f32 v50,v246,v50\nv_mul_f32 v51,v246,v51\n"
"v_mfma_f32_16x16x32_bf16 v[60:63],a[204:207],a[232:235],v[60:63]\ns_mov_b32 s75,219136\nv_mul_f32 v76,v246,v76\nv_mul_f32 v77,v246,v77\nv_mul_f32 v78,v246,v78\nv_mul_f32 v79,v246,v79\nv_mfma_f32_16x16x32_bf16 v[88:91],a[208:211],a[232:235],v[88:91]\nds_read_b128 a[232:235],v239 offset:3072\n"
"v_mul_f32 v104,v246,v104\nv_mul_f32 v105,v246,v105\nv_mul_f32 v106,v246,v106\nv_mul_f32 v107,v246,v107\nv_mfma_f32_16x16x32_bf16 v[8:11],a[196:199],a[236:239],v[8:11]\nbuffer_load_dwordx4 v225,s[52:55],s75 offen offset:2048 nt lds\nv_mul_f32 v24,v247,v24\nv_mul_f32 v25,v247,v25\n"
"v_mul_f32 v26,v247,v26\nv_mul_f32 v27,v247,v27\nv_mfma_f32_16x16x32_bf16 v[36:39],a[200:203],a[236:239],v[36:39]\nds_read_b64_tr_b16 a[218:219],v232 offset:13312\nv_mul_f32 v52,v247,v52\nv_mul_f32 v53,v247,v53\nv_mul_f32 v54,v247,v54\nv_mul_f32 v55,v247,v55\n"
"v_mfma_f32_16x16x32_bf16 v[64:67],a[204:207],a[236:239],v[64:67]\ns_mov_b32 s72,220160\nv_mul_f32 v80,v247,v80\nv_mul_f32 v81,v247,v81\nv_mul_f32 v82,v247,v82\nv_mul_f32 v83,v247,v83\nv_mfma_f32_16x16x32_bf16 v[92:95],a[208:211],a[236:239],v[92:95]\nds_read_b128 a[236:239],v239 offset:5120\n"
"v_mul_f32 v108,v247,v108\nv_mul_f32 v109,v247,v109\nv_mul_f32 v110,v247,v110\nv_mul_f32 v111,v247,v111\nv_mfma_f32_16x16x32_bf16 v[12:15],a[196:199],a[240:243],v[12:15]\nbuffer_load_dwordx4 v225,s[52:55],s72 offen offset:3072 nt lds\nv_cvt_pk_bf16_f32 v185,v186,v187\nv_mfma_f32_16x16x32_bf16 v[40:43],a[200:203],a[240:243],v[40:43]\n"
"ds_read_b64_tr_b16 a[220:221],v233 offset:12288\nv_add_f32 v130,v187,v130\nv_add_f32 v138,v192,v138\nv_mfma_f32_16x16x32_bf16 v[68:71],a[204:207],a[240:243],v[68:71]\nds_read_b64_tr_b16 a[222:223],v233 offset:13312\nv_fma_f32 v196,v196,s69,-v248\nv_mfma_f32_16x16x32_bf16 v[96:99],a[208:211],a[240:243],v[96:99]\nds_read_b128 a[240:243],v239 offset:7168\n"
"v_exp_f32 v189,v189\nv_mfma_f32_16x16x32_bf16 v[16:19],a[196:199],a[244:247],v[16:19]\nds_read_b64_tr_b16 a[224:225],v234 offset:12288\nv_add_f32 v134,v189,v134\nv_mfma_f32_16x16x32_bf16 v[44:47],a[200:203],a[244:247],v[44:47]\nv_exp_f32 v190,v190\nv_cvt_pk_bf16_f32 v188,v188,v189\nv_mfma_f32_16x16x32_bf16 v[72:75],a[204:207],a[244:247],v[72:75]\n"
"v_exp_f32 v191,v191\nv_mfma_f32_16x16x32_bf16 v[100:103],a[208:211],a[244:247],v[100:103]\nds_read_b128 a[244:247],v239 offset:9216\ns_waitcnt lgkmcnt(12)\nv_add_f32 v134,v190,v134\nv_mfma_f32_16x16x32_bf16 v[20:23],a[196:199],a[248:251],v[20:23]\nds_read_b64_tr_b16 a[226:227],v234 offset:13312\nv_cvt_pk_bf16_f32 v189,v190,v191\n"
"v_mfma_f32_16x16x32_bf16 v[48:51],a[200:203],a[248:251],v[48:51]\ns_add_u32 m0,s64,12288\nv_add_f32 v134,v191,v134\nv_exp_f32 v193,v193\nv_mfma_f32_16x16x32_bf16 v[76:79],a[204:207],a[248:251],v[76:79]\ns_mov_b32 s73,229376\nv_exp_f32 v194,v194\nv_mfma_f32_16x16x32_bf16 v[104:107],a[208:211],a[248:251],v[104:107]\n"
"ds_read_b128 a[248:251],v239 offset:11264\nv_exp_f32 v195,v195\nv_mfma_f32_16x16x32_bf16 v[24:27],a[196:199],a[252:255],v[24:27]\nv_add_f32 v138,v193,v138\nv_cvt_pk_bf16_f32 v192,v192,v193\nv_mfma_f32_16x16x32_bf16 v[52:55],a[200:203],a[252:255],v[52:55]\nv_add_f32 v138,v194,v138\nv_cvt_pk_bf16_f32 v193,v194,v195\n"
"v_add_f32 v138,v195,v138\nv_mfma_f32_16x16x32_bf16 v[80:83],a[204:207],a[252:255],v[80:83]\nds_write_b64 v240,v[168:169] offset:28672\nv_mfma_f32_16x16x32_bf16 v[108:111],a[208:211],a[252:255],v[108:111]\nds_read_b128 a[252:255],v239 offset:13312\ns_waitcnt lgkmcnt(3)\nv_fma_f32 v197,v197,s69,-v248\nv_mfma_f32_16x16x32_bf16 v[0:3],a[212:215],a[228:231],v[0:3]\n"
"buffer_load_dwordx4 v225,s[52:55],s73 offen offset:0 nt lds\nv_fma_f32 v198,v198,s69,-v248\nv_mfma_f32_16x16x32_bf16 v[28:31],a[216:219],a[228:231],v[28:31]\ns_waitcnt vmcnt(13)\nds_read_b64_tr_b16 a[196:197],v231 offset:16384\nv_fma_f32 v199,v199,s69,-v248\nv_mfma_f32_16x16x32_bf16 v[56:59],a[220:223],a[228:231],v[56:59]\nds_read_b64_tr_b16 a[198:199],v231 offset:17408\n"
"v_fma_f32 v200,v200,s69,-v249\nv_mfma_f32_16x16x32_bf16 v[84:87],a[224:227],a[228:231],v[84:87]\ns_waitcnt lgkmcnt(2)\ns_barrier\nds_read_b128 a[228:231],v239 offset:14336\nv_mfma_f32_16x16x32_bf16 v[4:7],a[212:215],a[232:235],v[4:7]\ns_mov_b32 s74,230400\nbuffer_load_dwordx4 v225,s[52:55],s74 offen offset:1024 nt lds\n"
"v_mfma_f32_16x16x32_bf16 v[32:35],a[216:219],a[232:235],v[32:35]\nds_read_b64_tr_b16 a[200:201],v232 offset:16384\nv_exp_f32 v196,v196\nv_mfma_f32_16x16x32_bf16 v[60:63],a[220:223],a[232:235],v[60:63]\ns_mov_b32 s75,235520\nds_write_b64 v240,v[172:173] offset:30720\nv_mfma_f32_16x16x32_bf16 v[88:91],a[224:227],a[232:235],v[88:91]\nds_read_b128 a[232:235],v239 offset:16384\n"
"v_fma_f32 v201,v201,s69,-v249\nv_fma_f32 v202,v202,s69,-v249\nv_mfma_f32_16x16x32_bf16 v[8:11],a[212:215],a[236:239],v[8:11]\nbuffer_load_dwordx4 v225,s[52:55],s75 offen offset:2048 nt lds\nv_fma_f32 v203,v203,s69,-v249\nv_mfma_f32_16x16x32_bf16 v[36:39],a[216:219],a[236:239],v[36:39]\nds_read_b64_tr_b16 a[202:203],v232 offset:17408\nv_exp_f32 v200,v200\n"
"v_mfma_f32_16x16x32_bf16 v[64:67],a[220:223],a[236:239],v[64:67]\ns_mov_b32 s72,236544\nds_write_b64 v240,v[176:177] offset:32768\nv_mfma_f32_16x16x32_bf16 v[92:95],a[224:227],a[236:239],v[92:95]\nds_read_b128 a[236:239],v239 offset:18432\nv_fma_f32 v204,v204,s69,-v250\nv_fma_f32 v205,v205,s69,-v250\nv_mfma_f32_16x16x32_bf16 v[12:15],a[212:215],a[240:243],v[12:15]\n"
"buffer_load_dwordx4 v225,s[52:55],s72 offen offset:3072 nt lds\nv_fma_f32 v206,v206,s69,-v250\nv_mfma_f32_16x16x32_bf16 v[40:43],a[216:219],a[240:243],v[40:43]\nds_read_b64_tr_b16 a[204:205],v233 offset:16384\nv_fma_f32 v207,v207,s69,-v250\nv_fma_f32 v208,v208,s69,-v251\nv_mfma_f32_16x16x32_bf16 v[68:71],a[220:223],a[240:243],v[68:71]\nds_read_b64_tr_b16 a[206:207],v233 offset:17408\n"
"v_fma_f32 v209,v209,s69,-v251\nv_mfma_f32_16x16x32_bf16 v[96:99],a[224:227],a[240:243],v[96:99]\nds_read_b128 a[240:243],v239 offset:20480\nv_exp_f32 v204,v204\nv_mfma_f32_16x16x32_bf16 v[16:19],a[212:215],a[244:247],v[16:19]\nds_read_b64_tr_b16 a[208:209],v234 offset:16384\nv_fma_f32 v210,v210,s69,-v251\nv_mfma_f32_16x16x32_bf16 v[44:47],a[216:219],a[244:247],v[44:47]\n"
"ds_write_b64 v240,v[180:181] offset:34816\nv_mfma_f32_16x16x32_bf16 v[72:75],a[220:223],a[244:247],v[72:75]\nv_fma_f32 v211,v211,s69,-v251\nv_fma_f32 v212,v212,s69,-v252\nv_mfma_f32_16x16x32_bf16 v[100:103],a[224:227],a[244:247],v[100:103]\nds_read_b128 a[244:247],v239 offset:22528\nv_exp_f32 v208,v208\nv_mfma_f32_16x16x32_bf16 v[20:23],a[212:215],a[248:251],v[20:23]\n"
"ds_read_b64_tr_b16 a[210:211],v234 offset:17408\nv_fma_f32 v213,v213,s69,-v252\nv_mfma_f32_16x16x32_bf16 v[48:51],a[216:219],a[248:251],v[48:51]\ns_add_u32 m0,s64,16384\nds_write_b64 v240,v[184:185] offset:36864\nv_mfma_f32_16x16x32_bf16 v[76:79],a[220:223],a[248:251],v[76:79]\ns_mov_b32 s73,245760\nv_fma_f32 v214,v214,s69,-v252\n"
"v_fma_f32 v215,v215,s69,-v252\nv_mfma_f32_16x16x32_bf16 v[104:107],a[224:227],a[248:251],v[104:107]\nds_read_b128 a[248:251],v239 offset:24576\nv_exp_f32 v212,v212\nv_mfma_f32_16x16x32_bf16 v[24:27],a[212:215],a[252:255],v[24:27]\nds_write_b64 v240,v[188:189] offset:38912\nv_mfma_f32_16x16x32_bf16 v[52:55],a[216:219],a[252:255],v[52:55]\nv_fma_f32 v216,v216,s69,-v253\n"
"v_fma_f32 v217,v217,s69,-v253\nv_fma_f32 v218,v218,s69,-v253\nv_mfma_f32_16x16x32_bf16 v[80:83],a[220:223],a[252:255],v[80:83]\nv_fma_f32 v219,v219,s69,-v253\nv_fma_f32 v220,v220,s69,-v254\nv_mfma_f32_16x16x32_bf16 v[108:111],a[224:227],a[252:255],v[108:111]\nds_read_b128 a[252:255],v239 offset:26624\ns_waitcnt lgkmcnt(4)\n"
"v_fma_f32 v221,v221,s69,-v254\nv_mfma_f32_16x16x32_bf16 v[0:3],a[196:199],a[228:231],v[0:3]\nbuffer_load_dwordx4 v225,s[52:55],s73 offen offset:0 nt lds\nv_fma_f32 v222,v222,s69,-v254\nv_mfma_f32_16x16x32_bf16 v[28:31],a[200:203],a[228:231],v[28:31]\ns_waitcnt vmcnt(13)\nds_read_b64_tr_b16 a[212:213],v231 offset:0\nv_fma_f32 v223,v223,s69,-v254\n"
"v_mfma_f32_16x16x32_bf16 v[56:59],a[204:207],a[228:231],v[56:59]\nds_read_b64_tr_b16 a[214:215],v231 offset:1024\ns_mov_b32 s74,246784\nv_mfma_f32_16x16x32_bf16 v[84:87],a[208:211],a[228:231],v[84:87]\nds_read_b128 a[228:231],v239 offset:15360\nv_exp_f32 v216,v216\nv_mfma_f32_16x16x32_bf16 v[4:7],a[196:199],a[232:235],v[4:7]\nbuffer_load_dwordx4 v225,s[52:55],s74 offen offset:1024 nt lds\n"
"s_mov_b32 s75,251904\nv_mfma_f32_16x16x32_bf16 v[32:35],a[200:203],a[232:235],v[32:35]\nds_read_b64_tr_b16 a[216:217],v232 offset:0\nv_exp_f32 v220,v220\nv_mfma_f32_16x16x32_bf16 v[60:63],a[204:207],a[232:235],v[60:63]\nds_write_b64 v240,v[192:193] offset:40960\nv_mfma_f32_16x16x32_bf16 v[88:91],a[208:211],a[232:235],v[88:91]\nds_read_b128 a[232:235],v239 offset:17408\n"
"v_exp_f32 v197,v197\nv_mfma_f32_16x16x32_bf16 v[8:11],a[196:199],a[236:239],v[8:11]\nbuffer_load_dwordx4 v225,s[52:55],s75 offen offset:2048 nt lds\nv_add_f32 v114,v196,v114\nv_mfma_f32_16x16x32_bf16 v[36:39],a[200:203],a[236:239],v[36:39]\nds_read_b64_tr_b16 a[218:219],v232 offset:1024\nv_exp_f32 v198,v198\nv_mfma_f32_16x16x32_bf16 v[64:67],a[204:207],a[236:239],v[64:67]\n"
"s_mov_b32 s72,252928\nv_exp_f32 v199,v199\nv_mfma_f32_16x16x32_bf16 v[92:95],a[208:211],a[236:239],v[92:95]\nds_read_b128 a[236:239],v239 offset:19456\nv_add_f32 v114,v197,v114\nv_cvt_pk_bf16_f32 v196,v196,v197\nv_mfma_f32_16x16x32_bf16 v[12:15],a[196:199],a[240:243],v[12:15]\nbuffer_load_dwordx4 v225,s[52:55],s72 offen offset:3072 nt lds\n"
"v_add_f32 v114,v198,v114\nv_mfma_f32_16x16x32_bf16 v[40:43],a[200:203],a[240:243],v[40:43]\nds_read_b64_tr_b16 a[220:221],v233 offset:0\nv_cvt_pk_bf16_f32 v197,v198,v199\nv_add_f32 v114,v199,v114\nv_mfma_f32_16x16x32_bf16 v[68:71],a[204:207],a[240:243],v[68:71]\nds_read_b64_tr_b16 a[222:223],v233 offset:1024\nv_add_f32 v118,v200,v118\n"
"v_mfma_f32_16x16x32_bf16 v[96:99],a[208:211],a[240:243],v[96:99]\nds_read_b128 a[240:243],v239 offset:21504\nv_exp_f32 v201,v201\nv_mfma_f32_16x16x32_bf16 v[16:19],a[196:199],a[244:247],v[16:19]\nds_read_b64_tr_b16 a[224:225],v234 offset:0\nv_add_f32 v118,v201,v118\nv_mfma_f32_16x16x32_bf16 v[44:47],a[200:203],a[244:247],v[44:47]\nv_exp_f32 v202,v202\n"
"v_cvt_pk_bf16_f32 v200,v200,v201\nv_mfma_f32_16x16x32_bf16 v[72:75],a[204:207],a[244:247],v[72:75]\nv_exp_f32 v203,v203\nv_mfma_f32_16x16x32_bf16 v[100:103],a[208:211],a[244:247],v[100:103]\nds_read_b128 a[244:247],v239 offset:23552\ns_waitcnt lgkmcnt(13)\nv_add_f32 v118,v202,v118\nv_mfma_f32_16x16x32_bf16 v[20:23],a[196:199],a[248:251],v[20:23]\n"
"ds_read_b64_tr_b16 a[226:227],v234 offset:1024\nv_cvt_pk_bf16_f32 v201,v202,v203\nv_mfma_f32_16x16x32_bf16 v[48:51],a[200:203],a[248:251],v[48:51]\ns_add_u32 m0,s64,0\nv_add_f32 v118,v203,v118\nv_exp_f32 v205,v205\nv_mfma_f32_16x16x32_bf16 v[76:79],a[204:207],a[248:251],v[76:79]\ns_mov_b32 s73,0\n"
"v_exp_f32 v206,v206\nv_mfma_f32_16x16x32_bf16 v[104:107],a[208:211],a[248:251],v[104:107]\nds_read_b128 a[248:251],v239 offset:25600\nv_exp_f32 v207,v207\nv_mfma_f32_16x16x32_bf16 v[24:27],a[196:199],a[252:255],v[24:27]\nv_add_f32 v122,v204,v122\nv_add_f32 v122,v205,v122\nv_mfma_f32_16x16x32_bf16 v[52:55],a[200:203],a[252:255],v[52:55]\n"
"v_cvt_pk_bf16_f32 v204,v204,v205\nv_add_f32 v122,v206,v122\nv_cvt_pk_bf16_f32 v205,v206,v207\nv_mfma_f32_16x16x32_bf16 v[80:83],a[204:207],a[252:255],v[80:83]\nv_add_f32 v122,v207,v122\nv_add_f32 v126,v208,v126\nv_mfma_f32_16x16x32_bf16 v[108:111],a[208:211],a[252:255],v[108:111]\nds_read_b128 a[252:255],v239 offset:27648\n"
"s_waitcnt lgkmcnt(2)\nv_add_f32 v130,v212,v130\nv_mfma_f32_16x16x32_bf16 v[0:3],a[212:215],a[228:231],v[0:3]\nbuffer_load_dwordx4 v226,s[56:59],s73 offen offset:0 lds\nv_add_f32 v134,v216,v134\nv_mfma_f32_16x16x32_bf16 v[28:31],a[216:219],a[228:231],v[28:31]\ns_waitcnt vmcnt(13)\nds_read_b64_tr_b16 a[196:197],v231 offset:4096\n"
"v_mfma_f32_16x16x32_bf16 v[56:59],a[220:223],a[228:231],v[56:59]\nds_read_b64_tr_b16 a[198:199],v231 offset:5120\ns_mov_b32 s74,13312\nv_mfma_f32_16x16x32_bf16 v[84:87],a[224:227],a[228:231],v[84:87]\ns_waitcnt lgkmcnt(2)\ns_barrier\nds_read_b128 a[228:231],v239 offset:28672\nv_mfma_f32_16x16x32_bf16 v[4:7],a[212:215],a[232:235],v[4:7]\n"
"buffer_load_dwordx4 v226,s[56:59],s74 offen offset:1024 lds\ns_mov_b32 s75,5120\nv_mfma_f32_16x16x32_bf16 v[32:35],a[216:219],a[232:235],v[32:35]\nds_read_b64_tr_b16 a[200:201],v232 offset:4096\nv_exp_f32 v209,v209\nv_mfma_f32_16x16x32_bf16 v[60:63],a[220:223],a[232:235],v[60:63]\nv_exp_f32 v210,v210\nv_mfma_f32_16x16x32_bf16 v[88:91],a[224:227],a[232:235],v[88:91]\n"
"ds_read_b128 a[232:235],v239 offset:30720\nv_exp_f32 v211,v211\nv_mfma_f32_16x16x32_bf16 v[8:11],a[212:215],a[236:239],v[8:11]\nbuffer_load_dwordx4 v226,s[56:59],s75 offen offset:2048 lds\nv_add_f32 v126,v209,v126\nv_mfma_f32_16x16x32_bf16 v[36:39],a[216:219],a[236:239],v[36:39]\nds_read_b64_tr_b16 a[202:203],v232 offset:5120\nv_cvt_pk_bf16_f32 v208,v208,v209\n"
"v_add_f32 v126,v210,v126\nv_mfma_f32_16x16x32_bf16 v[64:67],a[220:223],a[236:239],v[64:67]\ns_mov_b32 s72,18432\nv_cvt_pk_bf16_f32 v209,v210,v211\nv_add_f32 v126,v211,v126\nv_mfma_f32_16x16x32_bf16 v[92:95],a[224:227],a[236:239],v[92:95]\nds_read_b128 a[236:239],v239 offset:32768\nv_exp_f32 v213,v213\n"
"v_mfma_f32_16x16x32_bf16 v[12:15],a[212:215],a[240:243],v[12:15]\nbuffer_load_dwordx4 v226,s[56:59],s72 offen offset:3072 lds\nv_add_f32 v130,v213,v130\nv_mfma_f32_16x16x32_bf16 v[40:43],a[216:219],a[240:243],v[40:43]\nds_read_b64_tr_b16 a[204:205],v233 offset:4096\nv_exp_f32 v214,v214\nv_mfma_f32_16x16x32_bf16 v[68:71],a[220:223],a[240:243],v[68:71]\nds_read_b64_tr_b16 a[206:207],v233 offset:5120\n"
"v_cvt_pk_bf16_f32 v212,v212,v213\nv_mfma_f32_16x16x32_bf16 v[96:99],a[224:227],a[240:243],v[96:99]\nds_read_b128 a[240:243],v239 offset:34816\nv_exp_f32 v215,v215\nv_mfma_f32_16x16x32_bf16 v[16:19],a[212:215],a[244:247],v[16:19]\nds_read_b64_tr_b16 a[208:209],v234 offset:4096\nv_add_f32 v130,v214,v130\nv_mfma_f32_16x16x32_bf16 v[44:47],a[216:219],a[244:247],v[44:47]\n"
"v_cvt_pk_bf16_f32 v213,v214,v215\nv_add_f32 v130,v215,v130\nv_add_f32 v138,v220,v138\nv_mfma_f32_16x16x32_bf16 v[72:75],a[220:223],a[244:247],v[72:75]\nv_exp_f32 v217,v217\nv_mfma_f32_16x16x32_bf16 v[100:103],a[224:227],a[244:247],v[100:103]\nds_read_b128 a[244:247],v239 offset:36864\nv_exp_f32 v218,v218\n"
"v_mfma_f32_16x16x32_bf16 v[20:23],a[212:215],a[248:251],v[20:23]\nds_read_b64_tr_b16 a[210:211],v234 offset:5120\nv_add_f32 v134,v217,v134\nv_mfma_f32_16x16x32_bf16 v[48:51],a[216:219],a[248:251],v[48:51]\ns_add_u32 m0,s64,4096\nv_exp_f32 v219,v219\nv_cvt_pk_bf16_f32 v216,v216,v217\nv_mfma_f32_16x16x32_bf16 v[76:79],a[220:223],a[248:251],v[76:79]\n"
"s_mov_b32 s73,28672\nv_add_f32 v134,v218,v134\nv_cvt_pk_bf16_f32 v217,v218,v219\nv_mfma_f32_16x16x32_bf16 v[104:107],a[224:227],a[248:251],v[104:107]\nds_read_b128 a[248:251],v239 offset:38912\nv_add_f32 v134,v219,v134\nv_mov_b32 v115,0\nv_mfma_f32_16x16x32_bf16 v[24:27],a[212:215],a[252:255],v[24:27]\n"
"v_exp_f32 v221,v221\nv_mfma_f32_16x16x32_bf16 v[52:55],a[216:219],a[252:255],v[52:55]\nv_exp_f32 v222,v222\nv_add_f32 v138,v221,v138\nv_mfma_f32_16x16x32_bf16 v[80:83],a[220:223],a[252:255],v[80:83]\nv_exp_f32 v223,v223\nv_mfma_f32_16x16x32_bf16 v[108:111],a[224:227],a[252:255],v[108:111]\nds_read_b128 a[252:255],v239 offset:40960\n"
"s_waitcnt lgkmcnt(2)\nv_cvt_pk_bf16_f32 v220,v220,v221\nv_mfma_f32_16x16x32_bf16 v[0:3],a[196:199],a[228:231],v[0:3]\nbuffer_load_dwordx4 v226,s[56:59],s73 offen offset:0 lds\nv_add_f32 v138,v222,v138\nv_mfma_f32_16x16x32_bf16 v[28:31],a[200:203],a[228:231],v[28:31]\ns_waitcnt vmcnt(13)\nds_read_b64_tr_b16 a[212:213],v231 offset:8192\n"
"v_cvt_pk_bf16_f32 v221,v222,v223\nv_mfma_f32_16x16x32_bf16 v[56:59],a[204:207],a[228:231],v[56:59]\nds_read_b64_tr_b16 a[214:215],v231 offset:9216\nv_add_f32 v138,v223,v138\nv_mfma_f32_16x16x32_bf16 v[84:87],a[208:211],a[228:231],v[84:87]\nds_read_b128 a[228:231],v239 offset:29696\nv_permlane32_swap_b32 v114,v118\nv_add_f32 v114,v114,v118\n"
"v_mfma_f32_16x16x32_bf16 v[4:7],a[196:199],a[232:235],v[4:7]\ns_mov_b32 s74,41984\nbuffer_load_dwordx4 v226,s[56:59],s74 offen offset:1024 lds\nv_mfma_f32_16x16x32_bf16 v[32:35],a[200:203],a[232:235],v[32:35]\nds_read_b64_tr_b16 a[216:217],v232 offset:8192\nv_permlane32_swap_b32 v122,v126\nv_add_f32 v122,v122,v126\nv_mfma_f32_16x16x32_bf16 v[60:63],a[204:207],a[232:235],v[60:63]\n"
"s_mov_b32 s75,33792\nds_write_b64 v240,v[196:197] offset:43008\nv_mfma_f32_16x16x32_bf16 v[88:91],a[208:211],a[232:235],v[88:91]\nds_read_b128 a[232:235],v239 offset:31744\nv_permlane32_swap_b32 v130,v134\nv_add_f32 v130,v130,v134\nv_mfma_f32_16x16x32_bf16 v[8:11],a[196:199],a[236:239],v[8:11]\nbuffer_load_dwordx4 v226,s[56:59],s75 offen offset:2048 lds\n"
"v_permlane32_swap_b32 v138,v115\nv_mfma_f32_16x16x32_bf16 v[36:39],a[200:203],a[236:239],v[36:39]\nds_read_b64_tr_b16 a[218:219],v232 offset:9216\nv_add_f32 v138,v138,v115\nv_bfe_u32 v118,v239,8,1\nv_mfma_f32_16x16x32_bf16 v[64:67],a[204:207],a[236:239],v[64:67]\ns_mov_b32 s72,47104\nds_write_b64 v240,v[200:201] offset:45056\n"
"v_mfma_f32_16x16x32_bf16 v[92:95],a[208:211],a[236:239],v[92:95]\nds_read_b128 a[236:239],v239 offset:33792\nv_lshrrev_b32 v126,9,v239\nv_permlane16_swap_b32 v114,v122\nv_mfma_f32_16x16x32_bf16 v[12:15],a[196:199],a[240:243],v[12:15]\nbuffer_load_dwordx4 v226,s[56:59],s72 offen offset:3072 lds\nv_add_f32 v114,v114,v122\nv_mfma_f32_16x16x32_bf16 v[40:43],a[200:203],a[240:243],v[40:43]\n"
"ds_read_b64_tr_b16 a[220:221],v233 offset:8192\nv_permlane16_swap_b32 v130,v138\nv_add_f32 v130,v130,v138\nv_mfma_f32_16x16x32_bf16 v[68:71],a[204:207],a[240:243],v[68:71]\nds_read_b64_tr_b16 a[222:223],v233 offset:9216\nv_lshl_or_b32 v118,v118,1,v126\nv_mfma_f32_16x16x32_bf16 v[96:99],a[208:211],a[240:243],v[96:99]\nds_read_b128 a[240:243],v239 offset:35840\n"
"v_and_b32 v126,0xf0,v239\nv_lshl_or_b32 v118,v118,8,v126\nv_mfma_f32_16x16x32_bf16 v[16:19],a[196:199],a[244:247],v[16:19]\nds_read_b64_tr_b16 a[224:225],v234 offset:8192\nv_lshl_add_u32 v118,s70,2,v118\nv_mfma_f32_16x16x32_bf16 v[44:47],a[200:203],a[244:247],v[44:47]\nds_write_b64 v240,v[204:205] offset:47104\nv_mfma_f32_16x16x32_bf16 v[72:75],a[204:207],a[244:247],v[72:75]\n"
"v_mfma_f32_16x16x32_bf16 v[100:103],a[208:211],a[244:247],v[100:103]\nds_read_b128 a[244:247],v239 offset:37888\ns_waitcnt lgkmcnt(15)\nv_mfma_f32_16x16x32_bf16 v[20:23],a[196:199],a[248:251],v[20:23]\nds_read_b64_tr_b16 a[226:227],v234 offset:9216\ns_add_u32 m0,s64,8192\nv_mfma_f32_16x16x32_bf16 v[48:51],a[200:203],a[248:251],v[48:51]\ns_mov_b32 s73,57344\n"
"ds_write_b64 v240,v[208:209] offset:49152\nv_mfma_f32_16x16x32_bf16 v[76:79],a[204:207],a[248:251],v[76:79]\nv_mfma_f32_16x16x32_bf16 v[104:107],a[208:211],a[248:251],v[104:107]\nds_read_b128 a[248:251],v239 offset:39936\nv_mfma_f32_16x16x32_bf16 v[24:27],a[196:199],a[252:255],v[24:27]\nds_write_b64 v240,v[212:213] offset:51200\nv_mfma_f32_16x16x32_bf16 v[52:55],a[200:203],a[252:255],v[52:55]\nv_mfma_f32_16x16x32_bf16 v[80:83],a[204:207],a[252:255],v[80:83]\n"
"ds_write_b64 v240,v[216:217] offset:53248\nv_mfma_f32_16x16x32_bf16 v[108:111],a[208:211],a[252:255],v[108:111]\nds_read_b128 a[252:255],v239 offset:41984\ns_waitcnt lgkmcnt(5)\nv_mfma_f32_16x16x32_bf16 v[0:3],a[212:215],a[228:231],v[0:3]\nbuffer_load_dwordx4 v226,s[56:59],s73 offen offset:0 lds\ns_mov_b32 s74,70656\nv_mfma_f32_16x16x32_bf16 v[28:31],a[216:219],a[228:231],v[28:31]\n"
"s_waitcnt vmcnt(13)\nds_read_b64_tr_b16 a[196:197],v231 offset:12288\nv_mfma_f32_16x16x32_bf16 v[56:59],a[220:223],a[228:231],v[56:59]\nds_read_b64_tr_b16 a[198:199],v231 offset:13312\nv_mfma_f32_16x16x32_bf16 v[84:87],a[224:227],a[228:231],v[84:87]\nds_write_b64 v240,v[220:221] offset:55296\nds_write_b32 v118,v114 offset:61440\nds_write_b32 v118,v130 offset:62464\n"
"s_waitcnt lgkmcnt(0)\ns_barrier\nds_read_b128 a[228:231],v239 offset:43008\nv_mfma_f32_16x16x32_bf16 v[4:7],a[212:215],a[232:235],v[4:7]\nbuffer_load_dwordx4 v226,s[56:59],s74 offen offset:1024 lds\nv_accvgpr_read_b32 v112,a0\nv_mfma_f32_16x16x32_bf16 v[32:35],a[216:219],a[232:235],v[32:35]\nds_read_b64_tr_b16 a[200:201],v232 offset:12288\n"
"v_accvgpr_read_b32 v140,a1\nv_accvgpr_read_b32 v113,a2\nv_mfma_f32_16x16x32_bf16 v[60:63],a[220:223],a[232:235],v[60:63]\ns_mov_b32 s75,62464\nv_accvgpr_read_b32 v141,a3\nv_cvt_pk_bf16_f32 v112,v112,v140\nv_mfma_f32_16x16x32_bf16 v[88:91],a[224:227],a[232:235],v[88:91]\nds_read_b128 a[232:235],v239 offset:45056\n"
"v_cvt_pk_bf16_f32 v113,v113,v141\nv_accvgpr_read_b32 v114,a4\nv_mfma_f32_16x16x32_bf16 v[8:11],a[212:215],a[236:239],v[8:11]\nbuffer_load_dwordx4 v226,s[56:59],s75 offen offset:2048 lds\nv_accvgpr_read_b32 v142,a5\nv_mfma_f32_16x16x32_bf16 v[36:39],a[216:219],a[236:239],v[36:39]\nds_read_b64_tr_b16 a[202:203],v232 offset:13312\nv_accvgpr_read_b32 v115,a6\n"
"v_accvgpr_read_b32 v143,a7\nv_mfma_f32_16x16x32_bf16 v[64:67],a[220:223],a[236:239],v[64:67]\ns_mov_b32 s72,75776\nv_cvt_pk_bf16_f32 v114,v114,v142\nv_cvt_pk_bf16_f32 v115,v115,v143\nv_mfma_f32_16x16x32_bf16 v[92:95],a[224:227],a[236:239],v[92:95]\nds_read_b128 a[236:239],v239 offset:47104\nv_accvgpr_read_b32 v116,a8\n"
"v_accvgpr_read_b32 v144,a9\nv_mfma_f32_16x16x32_bf16 v[12:15],a[212:215],a[240:243],v[12:15]\nbuffer_load_dwordx4 v226,s[56:59],s72 offen offset:3072 lds\nv_accvgpr_read_b32 v117,a10\nv_mfma_f32_16x16x32_bf16 v[40:43],a[216:219],a[240:243],v[40:43]\nds_read_b64_tr_b16 a[204:205],v233 offset:12288\nv_accvgpr_read_b32 v145,a11\nv_cvt_pk_bf16_f32 v116,v116,v144\n"
"v_mfma_f32_16x16x32_bf16 v[68:71],a[220:223],a[240:243],v[68:71]\nds_read_b64_tr_b16 a[206:207],v233 offset:13312\nv_cvt_pk_bf16_f32 v117,v117,v145\nv_mfma_f32_16x16x32_bf16 v[96:99],a[224:227],a[240:243],v[96:99]\nds_read_b128 a[240:243],v239 offset:49152\nv_accvgpr_read_b32 v118,a12\nv_accvgpr_read_b32 v146,a13\nv_mfma_f32_16x16x32_bf16 v[16:19],a[212:215],a[244:247],v[16:19]\n"
"ds_read_b64_tr_b16 a[208:209],v234 offset:12288\nv_accvgpr_read_b32 v119,a14\nv_mfma_f32_16x16x32_bf16 v[44:47],a[216:219],a[244:247],v[44:47]\nv_accvgpr_read_b32 v147,a15\nv_cvt_pk_bf16_f32 v118,v118,v146\nv_cvt_pk_bf16_f32 v119,v119,v147\nv_mfma_f32_16x16x32_bf16 v[72:75],a[220:223],a[244:247],v[72:75]\nv_accvgpr_read_b32 v120,a16\n"
"v_accvgpr_read_b32 v140,a17\nv_mfma_f32_16x16x32_bf16 v[100:103],a[224:227],a[244:247],v[100:103]\nds_read_b128 a[244:247],v239 offset:51200\nv_accvgpr_read_b32 v121,a18\nv_accvgpr_read_b32 v141,a19\nv_mfma_f32_16x16x32_bf16 v[20:23],a[212:215],a[248:251],v[20:23]\nds_read_b64_tr_b16 a[210:211],v234 offset:13312\nv_cvt_pk_bf16_f32 v120,v120,v140\n"
"v_mfma_f32_16x16x32_bf16 v[48:51],a[216:219],a[248:251],v[48:51]\ns_add_u32 m0,s64,12288\nv_cvt_pk_bf16_f32 v121,v121,v141\nv_accvgpr_read_b32 v122,a20\nv_accvgpr_read_b32 v142,a21\nv_mfma_f32_16x16x32_bf16 v[76:79],a[220:223],a[248:251],v[76:79]\ns_mov_b32 s73,86016\nv_accvgpr_read_b32 v123,a22\n"
"v_accvgpr_read_b32 v143,a23\nv_mfma_f32_16x16x32_bf16 v[104:107],a[224:227],a[248:251],v[104:107]\nds_read_b128 a[248:251],v239 offset:53248\nv_cvt_pk_bf16_f32 v122,v122,v142\nv_cvt_pk_bf16_f32 v123,v123,v143\nv_mfma_f32_16x16x32_bf16 v[24:27],a[212:215],a[252:255],v[24:27]\nv_accvgpr_read_b32 v124,a24\nv_accvgpr_read_b32 v144,a25\n"
"v_mfma_f32_16x16x32_bf16 v[52:55],a[216:219],a[252:255],v[52:55]\nv_accvgpr_read_b32 v125,a26\nv_accvgpr_read_b32 v145,a27\nv_cvt_pk_bf16_f32 v124,v124,v144\nv_mfma_f32_16x16x32_bf16 v[80:83],a[220:223],a[252:255],v[80:83]\nv_cvt_pk_bf16_f32 v125,v125,v145\nv_accvgpr_read_b32 v126,a28\nv_mfma_f32_16x16x32_bf16 v[108:111],a[224:227],a[252:255],v[108:111]\n"
"ds_read_b128 a[252:255],v239 offset:55296\ns_waitcnt lgkmcnt(2)\nv_accvgpr_read_b32 v146,a29\nv_mfma_f32_16x16x32_bf16 v[0:3],a[196:199],a[228:231],v[0:3]\nbuffer_load_dwordx4 v226,s[56:59],s73 offen offset:0 lds\nv_accvgpr_read_b32 v127,a30\nv_mfma_f32_16x16x32_bf16 v[28:31],a[200:203],a[228:231],v[28:31]\ns_waitcnt vmcnt(13)\n"
"ds_read_b64_tr_b16 a[212:213],v231 offset:16384\nv_accvgpr_read_b32 v147,a31\nv_mfma_f32_16x16x32_bf16 v[56:59],a[204:207],a[228:231],v[56:59]\nds_read_b64_tr_b16 a[214:215],v231 offset:17408\nv_cvt_pk_bf16_f32 v126,v126,v146\nv_mfma_f32_16x16x32_bf16 v[84:87],a[208:211],a[228:231],v[84:87]\nds_read_b128 a[228:231],v239 offset:44032\nv_cvt_pk_bf16_f32 v127,v127,v147\n"
"v_accvgpr_read_b32 v128,a32\nv_mfma_f32_16x16x32_bf16 v[4:7],a[196:199],a[232:235],v[4:7]\ns_mov_b32 s74,99328\nbuffer_load_dwordx4 v226,s[56:59],s74 offen offset:1024 lds\nv_mfma_f32_16x16x32_bf16 v[32:35],a[200:203],a[232:235],v[32:35]\nds_read_b64_tr_b16 a[216:217],v232 offset:16384\nv_accvgpr_read_b32 v140,a33\nv_accvgpr_read_b32 v129,a34\n"
"v_mfma_f32_16x16x32_bf16 v[60:63],a[204:207],a[232:235],v[60:63]\ns_mov_b32 s75,91136\nv_accvgpr_read_b32 v141,a35\nv_cvt_pk_bf16_f32 v128,v128,v140\nv_mfma_f32_16x16x32_bf16 v[88:91],a[208:211],a[232:235],v[88:91]\nds_read_b128 a[232:235],v239 offset:46080\nv_cvt_pk_bf16_f32 v129,v129,v141\nv_accvgpr_read_b32 v130,a36\n"
"v_mfma_f32_16x16x32_bf16 v[8:11],a[196:199],a[236:239],v[8:11]\nbuffer_load_dwordx4 v226,s[56:59],s75 offen offset:2048 lds\nv_accvgpr_read_b32 v142,a37\nv_mfma_f32_16x16x32_bf16 v[36:39],a[200:203],a[236:239],v[36:39]\nds_read_b64_tr_b16 a[218:219],v232 offset:17408\nv_accvgpr_read_b32 v131,a38\nv_accvgpr_read_b32 v143,a39\nv_mfma_f32_16x16x32_bf16 v[64:67],a[204:207],a[236:239],v[64:67]\n"
"s_mov_b32 s72,104448\nv_cvt_pk_bf16_f32 v130,v130,v142\nv_cvt_pk_bf16_f32 v131,v131,v143\nv_mfma_f32_16x16x32_bf16 v[92:95],a[208:211],a[236:239],v[92:95]\nds_read_b128 a[236:239],v239 offset:48128\nv_accvgpr_read_b32 v132,a40\nv_accvgpr_read_b32 v144,a41\nv_mfma_f32_16x16x32_bf16 v[12:15],a[196:199],a[240:243],v[12:15]\n"
"buffer_load_dwordx4 v226,s[56:59],s72 offen offset:3072 lds\nv_accvgpr_read_b32 v133,a42\nv_mfma_f32_16x16x32_bf16 v[40:43],a[200:203],a[240:243],v[40:43]\nds_read_b64_tr_b16 a[220:221],v233 offset:16384\nv_accvgpr_read_b32 v145,a43\nv_cvt_pk_bf16_f32 v132,v132,v144\nv_mfma_f32_16x16x32_bf16 v[68:71],a[204:207],a[240:243],v[68:71]\nds_read_b64_tr_b16 a[222:223],v233 offset:17408\n"
"v_cvt_pk_bf16_f32 v133,v133,v145\nv_mfma_f32_16x16x32_bf16 v[96:99],a[208:211],a[240:243],v[96:99]\nds_read_b128 a[240:243],v239 offset:50176\nv_accvgpr_read_b32 v134,a44\nv_accvgpr_read_b32 v146,a45\nv_mfma_f32_16x16x32_bf16 v[16:19],a[196:199],a[244:247],v[16:19]\nds_read_b64_tr_b16 a[224:225],v234 offset:16384\nv_accvgpr_read_b32 v135,a46\n"
"v_mfma_f32_16x16x32_bf16 v[44:47],a[200:203],a[244:247],v[44:47]\nv_accvgpr_read_b32 v147,a47\nv_cvt_pk_bf16_f32 v134,v134,v146\nv_cvt_pk_bf16_f32 v135,v135,v147\nv_mfma_f32_16x16x32_bf16 v[72:75],a[204:207],a[244:247],v[72:75]\nv_accvgpr_read_b32 v136,a48\nv_accvgpr_read_b32 v140,a49\nv_mfma_f32_16x16x32_bf16 v[100:103],a[208:211],a[244:247],v[100:103]\n"
"ds_read_b128 a[244:247],v239 offset:52224\ns_waitcnt lgkmcnt(12)\nv_accvgpr_read_b32 v137,a50\nv_mfma_f32_16x16x32_bf16 v[20:23],a[196:199],a[248:251],v[20:23]\nds_read_b64_tr_b16 a[226:227],v234 offset:17408\nv_accvgpr_read_b32 v141,a51\nv_mfma_f32_16x16x32_bf16 v[48:51],a[200:203],a[248:251],v[48:51]\ns_add_u32 m0,s64,16384\n"
"v_cvt_pk_bf16_f32 v136,v136,v140\nv_cvt_pk_bf16_f32 v137,v137,v141\nv_accvgpr_read_b32 v138,a52\nv_mfma_f32_16x16x32_bf16 v[76:79],a[204:207],a[248:251],v[76:79]\ns_mov_b32 s73,114688\nv_accvgpr_read_b32 v142,a53\nv_accvgpr_read_b32 v139,a54\nv_mfma_f32_16x16x32_bf16 v[104:107],a[208:211],a[248:251],v[104:107]\n"
"ds_read_b128 a[248:251],v239 offset:54272\nv_accvgpr_read_b32 v143,a55\nv_cvt_pk_bf16_f32 v138,v138,v142\nv_mfma_f32_16x16x32_bf16 v[24:27],a[196:199],a[252:255],v[24:27]\nv_cvt_pk_bf16_f32 v139,v139,v143\nv_accvgpr_read_b32 v196,a56\nv_mfma_f32_16x16x32_bf16 v[52:55],a[200:203],a[252:255],v[52:55]\nv_accvgpr_read_b32 v144,a57\n"
"v_accvgpr_read_b32 v197,a58\nv_accvgpr_read_b32 v145,a59\nv_mfma_f32_16x16x32_bf16 v[80:83],a[204:207],a[252:255],v[80:83]\nv_cvt_pk_bf16_f32 v196,v196,v144\nv_cvt_pk_bf16_f32 v197,v197,v145\nv_mfma_f32_16x16x32_bf16 v[108:111],a[208:211],a[252:255],v[108:111]\nds_read_b128 a[252:255],v239 offset:56320\ns_waitcnt lgkmcnt(2)\n"
"v_accvgpr_read_b32 v198,a60\nv_mfma_f32_16x16x32_bf16 v[0:3],a[212:215],a[228:231],v[0:3]\nbuffer_load_dwordx4 v226,s[56:59],s73 offen offset:0 lds\nv_accvgpr_read_b32 v146,a61\nv_mfma_f32_16x16x32_bf16 v[28:31],a[216:219],a[228:231],v[28:31]\ns_waitcnt vmcnt(13)\nds_read_b64_tr_b16 a[196:197],v231 offset:0\nv_accvgpr_read_b32 v199,a62\n"
"v_mfma_f32_16x16x32_bf16 v[56:59],a[220:223],a[228:231],v[56:59]\nds_read_b64_tr_b16 a[198:199],v231 offset:1024\nv_accvgpr_read_b32 v147,a63\nv_mfma_f32_16x16x32_bf16 v[84:87],a[224:227],a[228:231],v[84:87]\ns_waitcnt lgkmcnt(2)\ns_barrier\nv_cvt_pk_bf16_f32 v198,v198,v146\nv_cvt_pk_bf16_f32 v199,v199,v147\n"
"v_mfma_f32_16x16x32_bf16 v[4:7],a[212:215],a[232:235],v[4:7]\ns_mov_b32 s74,128000\nbuffer_load_dwordx4 v226,s[56:59],s74 offen offset:1024 lds\nv_mfma_f32_16x16x32_bf16 v[32:35],a[216:219],a[232:235],v[32:35]\nds_read_b64_tr_b16 a[200:201],v232 offset:0\nv_accvgpr_read_b32 v200,a64\nv_accvgpr_read_b32 v140,a65\nv_mfma_f32_16x16x32_bf16 v[60:63],a[220:223],a[232:235],v[60:63]\n"
"s_mov_b32 s75,119808\nv_accvgpr_read_b32 v201,a66\nv_accvgpr_read_b32 v141,a67\nv_mfma_f32_16x16x32_bf16 v[88:91],a[224:227],a[232:235],v[88:91]\nv_cvt_pk_bf16_f32 v200,v200,v140\nv_cvt_pk_bf16_f32 v201,v201,v141\nv_accvgpr_read_b32 v202,a68\nv_mfma_f32_16x16x32_bf16 v[8:11],a[212:215],a[236:239],v[8:11]\n"
"buffer_load_dwordx4 v226,s[56:59],s75 offen offset:2048 lds\nv_accvgpr_read_b32 v142,a69\nv_mfma_f32_16x16x32_bf16 v[36:39],a[216:219],a[236:239],v[36:39]\nds_read_b64_tr_b16 a[202:203],v232 offset:1024\nv_accvgpr_read_b32 v203,a70\nv_accvgpr_read_b32 v143,a71\nv_mfma_f32_16x16x32_bf16 v[64:67],a[220:223],a[236:239],v[64:67]\ns_mov_b32 s72,133120\n"
"v_cvt_pk_bf16_f32 v202,v202,v142\nv_cvt_pk_bf16_f32 v203,v203,v143\nv_mfma_f32_16x16x32_bf16 v[92:95],a[224:227],a[236:239],v[92:95]\nv_accvgpr_read_b32 v204,a72\nv_accvgpr_read_b32 v144,a73\nv_accvgpr_read_b32 v205,a74\nv_mfma_f32_16x16x32_bf16 v[12:15],a[212:215],a[240:243],v[12:15]\nbuffer_load_dwordx4 v226,s[56:59],s72 offen offset:3072 lds\n"
"v_accvgpr_read_b32 v145,a75\nv_mfma_f32_16x16x32_bf16 v[40:43],a[216:219],a[240:243],v[40:43]\nds_read_b64_tr_b16 a[204:205],v233 offset:0\nv_cvt_pk_bf16_f32 v204,v204,v144\nv_cvt_pk_bf16_f32 v205,v205,v145\nv_mfma_f32_16x16x32_bf16 v[68:71],a[220:223],a[240:243],v[68:71]\nds_read_b64_tr_b16 a[206:207],v233 offset:1024\nv_accvgpr_read_b32 v206,a76\n"
"v_mfma_f32_16x16x32_bf16 v[96:99],a[224:227],a[240:243],v[96:99]\nv_accvgpr_read_b32 v146,a77\nv_accvgpr_read_b32 v207,a78\nv_accvgpr_read_b32 v147,a79\nv_mfma_f32_16x16x32_bf16 v[16:19],a[212:215],a[244:247],v[16:19]\nds_read_b64_tr_b16 a[208:209],v234 offset:0\nv_cvt_pk_bf16_f32 v206,v206,v146\nv_mfma_f32_16x16x32_bf16 v[44:47],a[216:219],a[244:247],v[44:47]\n"
"v_cvt_pk_bf16_f32 v207,v207,v147\nv_accvgpr_read_b32 v208,a80\nv_accvgpr_read_b32 v140,a81\nv_mfma_f32_16x16x32_bf16 v[72:75],a[220:223],a[244:247],v[72:75]\nv_accvgpr_read_b32 v209,a82\nv_accvgpr_read_b32 v141,a83\nv_mfma_f32_16x16x32_bf16 v[100:103],a[224:227],a[244:247],v[100:103]\nv_cvt_pk_bf16_f32 v208,v208,v140\n"
"v_cvt_pk_bf16_f32 v209,v209,v141\nv_accvgpr_read_b32 v210,a84\nv_mfma_f32_16x16x32_bf16 v[20:23],a[212:215],a[248:251],v[20:23]\nds_read_b64_tr_b16 a[210:211],v234 offset:1024\nv_accvgpr_read_b32 v142,a85\nv_mfma_f32_16x16x32_bf16 v[48:51],a[216:219],a[248:251],v[48:51]\ns_add_u32 m0,s64,0\nv_accvgpr_read_b32 v211,a86\n"
"v_accvgpr_read_b32 v143,a87\nv_cvt_pk_bf16_f32 v210,v210,v142\nv_mfma_f32_16x16x32_bf16 v[76:79],a[220:223],a[248:251],v[76:79]\ns_mov_b32 s73,143360\nv_cvt_pk_bf16_f32 v211,v211,v143\nv_accvgpr_read_b32 v212,a88\nv_mfma_f32_16x16x32_bf16 v[104:107],a[224:227],a[248:251],v[104:107]\nv_accvgpr_read_b32 v144,a89\n"
"v_accvgpr_read_b32 v213,a90\nv_accvgpr_read_b32 v145,a91\nv_mfma_f32_16x16x32_bf16 v[24:27],a[212:215],a[252:255],v[24:27]\nv_cvt_pk_bf16_f32 v212,v212,v144\nv_cvt_pk_bf16_f32 v213,v213,v145\nv_mfma_f32_16x16x32_bf16 v[52:55],a[216:219],a[252:255],v[52:55]\nv_accvgpr_read_b32 v214,a92\nv_accvgpr_read_b32 v146,a93\n"
"v_accvgpr_read_b32 v215,a94\nv_mfma_f32_16x16x32_bf16 v[80:83],a[220:223],a[252:255],v[80:83]\nv_accvgpr_read_b32 v147,a95\nv_cvt_pk_bf16_f32 v214,v214,v146\nv_mfma_f32_16x16x32_bf16 v[108:111],a[224:227],a[252:255],v[108:111]\nv_cvt_pk_bf16_f32 v215,v215,v147\nv_accvgpr_read_b32 v216,a96\nv_accvgpr_read_b32 v140,a97\n"
"v_accvgpr_read_b32 v217,a98\nv_accvgpr_read_b32 v141,a99\nv_cvt_pk_bf16_f32 v216,v216,v140\nv_cvt_pk_bf16_f32 v217,v217,v141\nv_accvgpr_read_b32 v218,a100\nv_accvgpr_read_b32 v142,a101\nv_accvgpr_read_b32 v219,a102\nv_accvgpr_read_b32 v143,a103\n"
"v_cvt_pk_bf16_f32 v218,v218,v142\nv_cvt_pk_bf16_f32 v219,v219,v143\nv_accvgpr_read_b32 v220,a104\nv_accvgpr_read_b32 v144,a105\nv_accvgpr_read_b32 v221,a106\nv_accvgpr_read_b32 v145,a107\nv_cvt_pk_bf16_f32 v220,v220,v144\nv_cvt_pk_bf16_f32 v221,v221,v145\n"
"v_accvgpr_read_b32 v222,a108\nv_accvgpr_read_b32 v146,a109\nv_accvgpr_read_b32 v223,a110\nv_accvgpr_read_b32 v147,a111\nv_cvt_pk_bf16_f32 v222,v222,v146\nv_cvt_pk_bf16_f32 v223,v223,v147\nv_and_b32 v255,0xf0,v239\nds_read_b128 v[140:143],v255 offset:59392\n"
"ds_read_b128 v[144:147],v255 offset:59648\nds_read_b128 v[148:151],v255 offset:59904\nds_read_b128 v[152:155],v255 offset:60160\nds_read_b128 v[156:159],v255 offset:60416\nds_read_b128 v[160:163],v255 offset:60672\nds_read_b128 v[164:167],v255 offset:60928\nds_read_b128 v[168:171],v255 offset:61440\nds_read_b128 v[172:175],v255 offset:61696\n"
"ds_read_b128 v[176:179],v255 offset:61952\nds_read_b128 v[180:183],v255 offset:62208\nds_read_b128 v[184:187],v255 offset:62464\nds_read_b128 v[188:191],v255 offset:62720\nds_read_b128 v[192:195],v255 offset:62976\ns_waitcnt lgkmcnt(0)\nv_add_f32 v140,v140,v141\nv_add_f32 v142,v142,v143\n"
"v_add_f32 v140,v140,v142\nv_add_f32 v168,v168,v169\nv_add_f32 v170,v170,v171\nv_add_f32 v168,v168,v170\nv_add_f32 v144,v144,v145\nv_add_f32 v146,v146,v147\nv_add_f32 v144,v144,v146\nv_add_f32 v172,v172,v173\n"
"v_add_f32 v174,v174,v175\nv_add_f32 v172,v172,v174\nv_add_f32 v148,v148,v149\nv_add_f32 v150,v150,v151\nv_add_f32 v148,v148,v150\nv_add_f32 v176,v176,v177\nv_add_f32 v178,v178,v179\nv_add_f32 v176,v176,v178\n"
"v_add_f32 v152,v152,v153\nv_add_f32 v154,v154,v155\nv_add_f32 v152,v152,v154\nv_add_f32 v180,v180,v181\nv_add_f32 v182,v182,v183\nv_add_f32 v180,v180,v182\nv_add_f32 v156,v156,v157\nv_add_f32 v158,v158,v159\n"
"v_add_f32 v156,v156,v158\nv_add_f32 v184,v184,v185\nv_add_f32 v186,v186,v187\nv_add_f32 v184,v184,v186\nv_add_f32 v160,v160,v161\nv_add_f32 v162,v162,v163\nv_add_f32 v160,v160,v162\nv_add_f32 v188,v188,v189\n"
"v_add_f32 v190,v190,v191\nv_add_f32 v188,v188,v190\nv_add_f32 v164,v164,v165\nv_add_f32 v166,v166,v167\nv_add_f32 v164,v164,v166\nv_add_f32 v192,v192,v193\nv_add_f32 v194,v194,v195\nv_add_f32 v192,v192,v194\n"
"v_fma_f32 v140,v140,v241,v168\nv_fma_f32 v144,v144,v242,v172\nv_fma_f32 v148,v148,v243,v176\nv_fma_f32 v152,v152,v244,v180\nv_fma_f32 v156,v156,v245,v184\nv_fma_f32 v160,v160,v246,v188\nv_fma_f32 v164,v164,v247,v192\nv_rcp_f32 v248,v140\n"
"v_rcp_f32 v249,v144\nv_rcp_f32 v250,v148\nv_rcp_f32 v251,v152\nv_rcp_f32 v252,v156\nv_rcp_f32 v253,v160\nv_rcp_f32 v254,v164\nv_mov_b32 v242,14336\nv_mad_u32_u24 v241,v242,s70,v239\n"
"v_mul_f32 v0,v248,v0\nv_mul_f32 v1,v248,v1\nv_mul_f32 v2,v248,v2\nv_mul_f32 v3,v248,v3\nv_cvt_pk_bf16_f32 v0,v0,v1\nv_cvt_pk_bf16_f32 v1,v2,v3\nv_mul_f32 v28,v248,v28\nv_mul_f32 v29,v248,v29\n"
"v_mul_f32 v30,v248,v30\nv_mul_f32 v31,v248,v31\nv_cvt_pk_bf16_f32 v2,v28,v29\nv_cvt_pk_bf16_f32 v3,v30,v31\nds_write_b128 v241,v[0:3] offset:0\nv_mul_f32 v56,v248,v56\nv_mul_f32 v57,v248,v57\nv_mul_f32 v58,v248,v58\n"
"v_mul_f32 v59,v248,v59\nv_cvt_pk_bf16_f32 v56,v56,v57\nv_cvt_pk_bf16_f32 v57,v58,v59\nv_mul_f32 v84,v248,v84\nv_mul_f32 v85,v248,v85\nv_mul_f32 v86,v248,v86\nv_mul_f32 v87,v248,v87\nv_cvt_pk_bf16_f32 v58,v84,v85\n"
"v_cvt_pk_bf16_f32 v59,v86,v87\nds_write_b128 v241,v[56:59] offset:1024\nv_mul_f32 v4,v249,v4\nv_mul_f32 v5,v249,v5\nv_mul_f32 v6,v249,v6\nv_mul_f32 v7,v249,v7\nv_cvt_pk_bf16_f32 v4,v4,v5\nv_cvt_pk_bf16_f32 v5,v6,v7\n"
"v_mul_f32 v32,v249,v32\nv_mul_f32 v33,v249,v33\nv_mul_f32 v34,v249,v34\nv_mul_f32 v35,v249,v35\nv_cvt_pk_bf16_f32 v6,v32,v33\nv_cvt_pk_bf16_f32 v7,v34,v35\nds_write_b128 v241,v[4:7] offset:2048\nv_mul_f32 v60,v249,v60\n"
"v_mul_f32 v61,v249,v61\nv_mul_f32 v62,v249,v62\nv_mul_f32 v63,v249,v63\nv_cvt_pk_bf16_f32 v60,v60,v61\nv_cvt_pk_bf16_f32 v61,v62,v63\nv_mul_f32 v88,v249,v88\nv_mul_f32 v89,v249,v89\nv_mul_f32 v90,v249,v90\n"
"v_mul_f32 v91,v249,v91\nv_cvt_pk_bf16_f32 v62,v88,v89\nv_cvt_pk_bf16_f32 v63,v90,v91\nds_write_b128 v241,v[60:63] offset:3072\nv_pk_mul_f32 v[8:9],v[8:9],v[250:251] op_sel:[0,0] op_sel_hi:[1,0]\nv_pk_mul_f32 v[10:11],v[10:11],v[250:251] op_sel:[0,0] op_sel_hi:[1,0]\nv_cvt_pk_bf16_f32 v8,v8,v9\nv_cvt_pk_bf16_f32 v9,v10,v11\n"
"v_pk_mul_f32 v[36:37],v[36:37],v[250:251] op_sel:[0,0] op_sel_hi:[1,0]\nv_pk_mul_f32 v[38:39],v[38:39],v[250:251] op_sel:[0,0] op_sel_hi:[1,0]\nv_cvt_pk_bf16_f32 v10,v36,v37\nv_cvt_pk_bf16_f32 v11,v38,v39\nds_write_b128 v241,v[8:11] offset:4096\nv_pk_mul_f32 v[64:65],v[64:65],v[250:251] op_sel:[0,0] op_sel_hi:[1,0]\nv_pk_mul_f32 v[66:67],v[66:67],v[250:251] op_sel:[0,0] op_sel_hi:[1,0]\nv_cvt_pk_bf16_f32 v64,v64,v65\n"
"v_cvt_pk_bf16_f32 v65,v66,v67\nv_pk_mul_f32 v[92:93],v[92:93],v[250:251] op_sel:[0,0] op_sel_hi:[1,0]\nv_pk_mul_f32 v[94:95],v[94:95],v[250:251] op_sel:[0,0] op_sel_hi:[1,0]\nv_cvt_pk_bf16_f32 v66,v92,v93\nv_cvt_pk_bf16_f32 v67,v94,v95\nds_write_b128 v241,v[64:67] offset:5120\nv_pk_mul_f32 v[12:13],v[12:13],v[250:251] op_sel:[0,1] op_sel_hi:[1,1]\nv_pk_mul_f32 v[14:15],v[14:15],v[250:251] op_sel:[0,1] op_sel_hi:[1,1]\n"
"v_cvt_pk_bf16_f32 v12,v12,v13\nv_cvt_pk_bf16_f32 v13,v14,v15\nv_pk_mul_f32 v[40:41],v[40:41],v[250:251] op_sel:[0,1] op_sel_hi:[1,1]\nv_pk_mul_f32 v[42:43],v[42:43],v[250:251] op_sel:[0,1] op_sel_hi:[1,1]\nv_cvt_pk_bf16_f32 v14,v40,v41\nv_cvt_pk_bf16_f32 v15,v42,v43\nds_write_b128 v241,v[12:15] offset:6144\nv_pk_mul_f32 v[68:69],v[68:69],v[250:251] op_sel:[0,1] op_sel_hi:[1,1]\n"
"v_pk_mul_f32 v[70:71],v[70:71],v[250:251] op_sel:[0,1] op_sel_hi:[1,1]\nv_cvt_pk_bf16_f32 v68,v68,v69\nv_cvt_pk_bf16_f32 v69,v70,v71\nv_pk_mul_f32 v[96:97],v[96:97],v[250:251] op_sel:[0,1] op_sel_hi:[1,1]\nv_pk_mul_f32 v[98:99],v[98:99],v[250:251] op_sel:[0,1] op_sel_hi:[1,1]\nv_cvt_pk_bf16_f32 v70,v96,v97\nv_cvt_pk_bf16_f32 v71,v98,v99\nds_write_b128 v241,v[68:71] offset:7168\n"
"v_pk_mul_f32 v[16:17],v[16:17],v[252:253] op_sel:[0,0] op_sel_hi:[1,0]\nv_pk_mul_f32 v[18:19],v[18:19],v[252:253] op_sel:[0,0] op_sel_hi:[1,0]\nv_cvt_pk_bf16_f32 v16,v16,v17\nv_cvt_pk_bf16_f32 v17,v18,v19\nv_pk_mul_f32 v[44:45],v[44:45],v[252:253] op_sel:[0,0] op_sel_hi:[1,0]\nv_pk_mul_f32 v[46:47],v[46:47],v[252:253] op_sel:[0,0] op_sel_hi:[1,0]\nv_cvt_pk_bf16_f32 v18,v44,v45\nv_cvt_pk_bf16_f32 v19,v46,v47\n"
"ds_write_b128 v241,v[16:19] offset:8192\nv_pk_mul_f32 v[72:73],v[72:73],v[252:253] op_sel:[0,0] op_sel_hi:[1,0]\nv_pk_mul_f32 v[74:75],v[74:75],v[252:253] op_sel:[0,0] op_sel_hi:[1,0]\nv_cvt_pk_bf16_f32 v72,v72,v73\nv_cvt_pk_bf16_f32 v73,v74,v75\nv_pk_mul_f32 v[100:101],v[100:101],v[252:253] op_sel:[0,0] op_sel_hi:[1,0]\nv_pk_mul_f32 v[102:103],v[102:103],v[252:253] op_sel:[0,0] op_sel_hi:[1,0]\nv_cvt_pk_bf16_f32 v74,v100,v101\n"
"v_cvt_pk_bf16_f32 v75,v102,v103\nds_write_b128 v241,v[72:75] offset:9216\nv_pk_mul_f32 v[20:21],v[20:21],v[252:253] op_sel:[0,1] op_sel_hi:[1,1]\nv_pk_mul_f32 v[22:23],v[22:23],v[252:253] op_sel:[0,1] op_sel_hi:[1,1]\nv_cvt_pk_bf16_f32 v20,v20,v21\nv_cvt_pk_bf16_f32 v21,v22,v23\nv_pk_mul_f32 v[48:49],v[48:49],v[252:253] op_sel:[0,1] op_sel_hi:[1,1]\nv_pk_mul_f32 v[50:51],v[50:51],v[252:253] op_sel:[0,1] op_sel_hi:[1,1]\n"
"v_cvt_pk_bf16_f32 v22,v48,v49\nv_cvt_pk_bf16_f32 v23,v50,v51\nds_write_b128 v241,v[20:23] offset:10240\nv_pk_mul_f32 v[76:77],v[76:77],v[252:253] op_sel:[0,1] op_sel_hi:[1,1]\nv_pk_mul_f32 v[78:79],v[78:79],v[252:253] op_sel:[0,1] op_sel_hi:[1,1]\nv_cvt_pk_bf16_f32 v76,v76,v77\nv_cvt_pk_bf16_f32 v77,v78,v79\nv_pk_mul_f32 v[104:105],v[104:105],v[252:253] op_sel:[0,1] op_sel_hi:[1,1]\n"
"v_pk_mul_f32 v[106:107],v[106:107],v[252:253] op_sel:[0,1] op_sel_hi:[1,1]\nv_cvt_pk_bf16_f32 v78,v104,v105\nv_cvt_pk_bf16_f32 v79,v106,v107\nds_write_b128 v241,v[76:79] offset:11264\nv_pk_mul_f32 v[24:25],v[24:25],v[254:255] op_sel:[0,0] op_sel_hi:[1,0]\nv_pk_mul_f32 v[26:27],v[26:27],v[254:255] op_sel:[0,0] op_sel_hi:[1,0]\nv_cvt_pk_bf16_f32 v24,v24,v25\nv_cvt_pk_bf16_f32 v25,v26,v27\n"
"v_pk_mul_f32 v[52:53],v[52:53],v[254:255] op_sel:[0,0] op_sel_hi:[1,0]\nv_pk_mul_f32 v[54:55],v[54:55],v[254:255] op_sel:[0,0] op_sel_hi:[1,0]\nv_cvt_pk_bf16_f32 v26,v52,v53\nv_cvt_pk_bf16_f32 v27,v54,v55\nds_write_b128 v241,v[24:27] offset:12288\nv_pk_mul_f32 v[80:81],v[80:81],v[254:255] op_sel:[0,0] op_sel_hi:[1,0]\nv_pk_mul_f32 v[82:83],v[82:83],v[254:255] op_sel:[0,0] op_sel_hi:[1,0]\nv_cvt_pk_bf16_f32 v80,v80,v81\n"
"v_cvt_pk_bf16_f32 v81,v82,v83\nv_pk_mul_f32 v[108:109],v[108:109],v[254:255] op_sel:[0,0] op_sel_hi:[1,0]\nv_pk_mul_f32 v[110:111],v[110:111],v[254:255] op_sel:[0,0] op_sel_hi:[1,0]\nv_cvt_pk_bf16_f32 v82,v108,v109\nv_cvt_pk_bf16_f32 v83,v110,v111\nds_write_b128 v241,v[80:83] offset:13312\ns_waitcnt lgkmcnt(0)\ns_barrier\n"
"ds_read_b128 a[228:231],v239 offset:0\nds_read_b128 a[232:235],v239 offset:2048\nds_read_b128 a[236:239],v239 offset:4096\nds_read_b128 a[240:243],v239 offset:6144\nds_read_b128 a[244:247],v239 offset:8192\nds_read_b128 a[248:251],v239 offset:10240\nds_read_b128 a[252:255],v239 offset:12288\ns_waitcnt lgkmcnt(6)\n"
"v_mfma_f32_16x16x32_bf16 a[0:3],a[196:199],a[228:231],0\nbuffer_load_dwordx4 v226,s[56:59],s73 offen offset:0 lds\nv_lshl_add_u32 v252,s70,11,v239\nv_mfma_f32_16x16x32_bf16 a[28:31],a[200:203],a[228:231],0\ns_waitcnt vmcnt(13)\nds_read_b64_tr_b16 a[212:213],v231 offset:4096\nv_mfma_f32_16x16x32_bf16 a[56:59],a[204:207],a[228:231],0\nds_read_b64_tr_b16 a[214:215],v231 offset:5120\n"
"v_lshl_add_u32 v253,s70,11,v237\nv_mfma_f32_16x16x32_bf16 a[84:87],a[208:211],a[228:231],0\nds_read_b128 a[228:231],v239 offset:1024\ns_waitcnt lgkmcnt(8)\nv_mfma_f32_16x16x32_bf16 a[4:7],a[196:199],a[232:235],0\ns_mov_b32 s74,156672\nbuffer_load_dwordx4 v226,s[56:59],s74 offen offset:1024 lds\nv_mfma_f32_16x16x32_bf16 a[32:35],a[200:203],a[232:235],0\n"
"ds_read_b64_tr_b16 a[216:217],v232 offset:4096\nv_lshl_add_u32 v254,s70,11,v238\nv_mfma_f32_16x16x32_bf16 a[60:63],a[204:207],a[232:235],0\ns_mov_b32 s75,148480\nv_mfma_f32_16x16x32_bf16 a[88:91],a[208:211],a[232:235],0\nds_read_b128 a[232:235],v239 offset:3072\ns_waitcnt lgkmcnt(9)\nv_mfma_f32_16x16x32_bf16 a[8:11],a[196:199],a[236:239],0\n"
"buffer_load_dwordx4 v226,s[56:59],s75 offen offset:2048 lds\ns_mov_b32 s72,161792\nv_mfma_f32_16x16x32_bf16 a[36:39],a[200:203],a[236:239],0\nds_read_b64_tr_b16 a[218:219],v232 offset:5120\nv_mfma_f32_16x16x32_bf16 a[64:67],a[204:207],a[236:239],0\nv_mfma_f32_16x16x32_bf16 a[92:95],a[208:211],a[236:239],0\nds_read_b128 a[236:239],v239 offset:5120\ns_waitcnt lgkmcnt(9)\n"
"v_mfma_f32_16x16x32_bf16 a[12:15],a[196:199],a[240:243],0\nbuffer_load_dwordx4 v226,s[56:59],s72 offen offset:3072 lds\nv_mfma_f32_16x16x32_bf16 a[40:43],a[200:203],a[240:243],0\nds_read_b64_tr_b16 a[220:221],v233 offset:4096\nv_mfma_f32_16x16x32_bf16 a[68:71],a[204:207],a[240:243],0\nds_read_b64_tr_b16 a[222:223],v233 offset:5120\nv_mfma_f32_16x16x32_bf16 a[96:99],a[208:211],a[240:243],0\nds_read_b128 a[240:243],v239 offset:7168\n"
"v_mfma_f32_16x16x32_bf16 a[16:19],a[196:199],a[244:247],0\nds_read_b64_tr_b16 a[224:225],v234 offset:4096\nv_mfma_f32_16x16x32_bf16 a[44:47],a[200:203],a[244:247],0\nv_mfma_f32_16x16x32_bf16 a[72:75],a[204:207],a[244:247],0\nv_mfma_f32_16x16x32_bf16 a[100:103],a[208:211],a[244:247],0\nds_read_b128 a[244:247],v239 offset:9216\ns_waitcnt lgkmcnt(12)\nv_mfma_f32_16x16x32_bf16 a[20:23],a[196:199],a[248:251],0\n"
"ds_read_b64_tr_b16 a[226:227],v234 offset:5120\ns_add_u32 m0,s64,4096\nv_mfma_f32_16x16x32_bf16 a[48:51],a[200:203],a[248:251],0\ns_mov_b32 s73,172032\nv_mfma_f32_16x16x32_bf16 a[76:79],a[204:207],a[248:251],0\nv_mfma_f32_16x16x32_bf16 a[104:107],a[208:211],a[248:251],0\nds_read_b128 a[248:251],v239 offset:11264\nv_mfma_f32_16x16x32_bf16 a[24:27],a[196:199],a[252:255],0\n"
"v_mfma_f32_16x16x32_bf16 a[52:55],a[200:203],a[252:255],0\nv_mfma_f32_16x16x32_bf16 a[80:83],a[204:207],a[252:255],0\nv_mfma_f32_16x16x32_bf16 a[108:111],a[208:211],a[252:255],0\nds_read_b128 a[252:255],v239 offset:13312\ns_waitcnt lgkmcnt(2)\nv_mfma_f32_16x16x32_bf16 a[0:3],a[212:215],a[228:231],a[0:3]\nbuffer_load_dwordx4 v226,s[56:59],s73 offen offset:0 lds\nv_mov_b32 v250,0xff800000\n"
"v_mfma_f32_16x16x32_bf16 a[28:31],a[216:219],a[228:231],a[28:31]\ns_waitcnt vmcnt(13)\nds_read_b64_tr_b16 a[196:197],v231 offset:8192\nv_mfma_f32_16x16x32_bf16 a[56:59],a[220:223],a[228:231],a[56:59]\nds_read_b64_tr_b16 a[198:199],v231 offset:9216\nv_lshrrev_b32 v248,2,v239\nv_mfma_f32_16x16x32_bf16 a[84:87],a[224:227],a[228:231],a[84:87]\nds_read_b128 a[228:231],v239 offset:14336\n"
"v_lshrrev_b32 v242,9,v239\nv_mfma_f32_16x16x32_bf16 a[4:7],a[212:215],a[232:235],a[4:7]\ns_mov_b32 s74,185344\nbuffer_load_dwordx4 v226,s[56:59],s74 offen offset:1024 lds\nv_mfma_f32_16x16x32_bf16 a[32:35],a[216:219],a[232:235],a[32:35]\nds_read_b64_tr_b16 a[200:201],v232 offset:8192\nv_and_b32 v241,0xf0,v239\nv_mfma_f32_16x16x32_bf16 a[60:63],a[220:223],a[232:235],a[60:63]\n"
"s_mov_b32 s75,177152\nv_lshl_or_b32 v241,v242,8,v241\nv_bfe_u32 v242,v239,8,1\nv_mfma_f32_16x16x32_bf16 a[88:91],a[224:227],a[232:235],a[88:91]\nds_read_b128 a[232:235],v239 offset:16384\nv_lshl_or_b32 v241,v242,3,v241\nv_mfma_f32_16x16x32_bf16 a[8:11],a[212:215],a[236:239],a[8:11]\nbuffer_load_dwordx4 v226,s[56:59],s75 offen offset:2048 lds\n"
"v_mov_b32 v242,14336\nv_mfma_f32_16x16x32_bf16 a[36:39],a[216:219],a[236:239],a[36:39]\nds_read_b64_tr_b16 a[202:203],v232 offset:9216\nv_mad_u32_u24 v241,v242,s70,v241\nv_mfma_f32_16x16x32_bf16 a[64:67],a[220:223],a[236:239],a[64:67]\ns_mov_b32 s72,190464\nv_mfma_f32_16x16x32_bf16 a[92:95],a[224:227],a[236:239],a[92:95]\nds_read_b128 a[236:239],v239 offset:18432\n"
"v_mfma_f32_16x16x32_bf16 a[12:15],a[212:215],a[240:243],a[12:15]\nbuffer_load_dwordx4 v226,s[56:59],s72 offen offset:3072 lds\nv_mfma_f32_16x16x32_bf16 a[40:43],a[216:219],a[240:243],a[40:43]\nds_read_b64_tr_b16 a[204:205],v233 offset:8192\nv_mfma_f32_16x16x32_bf16 a[68:71],a[220:223],a[240:243],a[68:71]\nds_read_b64_tr_b16 a[206:207],v233 offset:9216\nv_mfma_f32_16x16x32_bf16 a[96:99],a[224:227],a[240:243],a[96:99]\nds_read_b128 a[240:243],v239 offset:20480\n"
"v_mfma_f32_16x16x32_bf16 a[16:19],a[212:215],a[244:247],a[16:19]\nds_read_b64_tr_b16 a[208:209],v234 offset:8192\nv_mfma_f32_16x16x32_bf16 a[44:47],a[216:219],a[244:247],a[44:47]\nv_mfma_f32_16x16x32_bf16 a[72:75],a[220:223],a[244:247],a[72:75]\nv_mfma_f32_16x16x32_bf16 a[100:103],a[224:227],a[244:247],a[100:103]\nds_read_b128 a[244:247],v239 offset:22528\ns_waitcnt lgkmcnt(12)\nv_mfma_f32_16x16x32_bf16 a[20:23],a[212:215],a[248:251],a[20:23]\n"
"ds_read_b64_tr_b16 a[210:211],v234 offset:9216\ns_add_u32 m0,s64,8192\nv_mfma_f32_16x16x32_bf16 a[48:51],a[216:219],a[248:251],a[48:51]\ns_mov_b32 s73,200704\nv_mfma_f32_16x16x32_bf16 a[76:79],a[220:223],a[248:251],a[76:79]\nv_mfma_f32_16x16x32_bf16 a[104:107],a[224:227],a[248:251],a[104:107]\nds_read_b128 a[248:251],v239 offset:24576\nv_mfma_f32_16x16x32_bf16 a[24:27],a[212:215],a[252:255],a[24:27]\n"
"v_mfma_f32_16x16x32_bf16 a[52:55],a[216:219],a[252:255],a[52:55]\nv_mfma_f32_16x16x32_bf16 a[80:83],a[220:223],a[252:255],a[80:83]\nv_mfma_f32_16x16x32_bf16 a[108:111],a[224:227],a[252:255],a[108:111]\nds_read_b128 a[252:255],v239 offset:26624\ns_waitcnt lgkmcnt(2)\nv_mfma_f32_16x16x32_bf16 a[0:3],a[196:199],a[228:231],a[0:3]\nbuffer_load_dwordx4 v226,s[56:59],s73 offen offset:0 lds\ns_mov_b32 s74,214016\n"
"v_mfma_f32_16x16x32_bf16 a[28:31],a[200:203],a[228:231],a[28:31]\ns_waitcnt vmcnt(13)\nds_read_b64_tr_b16 a[212:213],v231 offset:12288\nv_mfma_f32_16x16x32_bf16 a[56:59],a[204:207],a[228:231],a[56:59]\nds_read_b64_tr_b16 a[214:215],v231 offset:13312\nv_mfma_f32_16x16x32_bf16 a[84:87],a[208:211],a[228:231],a[84:87]\nds_read_b128 a[228:231],v239 offset:15360\nv_mfma_f32_16x16x32_bf16 a[4:7],a[196:199],a[232:235],a[4:7]\n"
"buffer_load_dwordx4 v226,s[56:59],s74 offen offset:1024 lds\ns_mov_b32 s75,205824\nv_mfma_f32_16x16x32_bf16 a[32:35],a[200:203],a[232:235],a[32:35]\nds_read_b64_tr_b16 a[216:217],v232 offset:12288\nv_mfma_f32_16x16x32_bf16 a[60:63],a[204:207],a[232:235],a[60:63]\nv_mfma_f32_16x16x32_bf16 a[88:91],a[208:211],a[232:235],a[88:91]\nds_read_b128 a[232:235],v239 offset:17408\nv_mfma_f32_16x16x32_bf16 a[8:11],a[196:199],a[236:239],a[8:11]\n"
"buffer_load_dwordx4 v226,s[56:59],s75 offen offset:2048 lds\ns_mov_b32 s72,219136\nv_mfma_f32_16x16x32_bf16 a[36:39],a[200:203],a[236:239],a[36:39]\nds_read_b64_tr_b16 a[218:219],v232 offset:13312\nv_mfma_f32_16x16x32_bf16 a[64:67],a[204:207],a[236:239],a[64:67]\nv_mfma_f32_16x16x32_bf16 a[92:95],a[208:211],a[236:239],a[92:95]\nds_read_b128 a[236:239],v239 offset:19456\nv_mfma_f32_16x16x32_bf16 a[12:15],a[196:199],a[240:243],a[12:15]\n"
"buffer_load_dwordx4 v226,s[56:59],s72 offen offset:3072 lds\nv_mfma_f32_16x16x32_bf16 a[40:43],a[200:203],a[240:243],a[40:43]\nds_read_b64_tr_b16 a[220:221],v233 offset:12288\nv_mfma_f32_16x16x32_bf16 a[68:71],a[204:207],a[240:243],a[68:71]\nds_read_b64_tr_b16 a[222:223],v233 offset:13312\nv_mfma_f32_16x16x32_bf16 a[96:99],a[208:211],a[240:243],a[96:99]\nds_read_b128 a[240:243],v239 offset:21504\nv_mfma_f32_16x16x32_bf16 a[16:19],a[196:199],a[244:247],a[16:19]\n"
"ds_read_b64_tr_b16 a[224:225],v234 offset:12288\nv_mfma_f32_16x16x32_bf16 a[44:47],a[200:203],a[244:247],a[44:47]\nv_mfma_f32_16x16x32_bf16 a[72:75],a[204:207],a[244:247],a[72:75]\nv_mfma_f32_16x16x32_bf16 a[100:103],a[208:211],a[244:247],a[100:103]\nds_read_b128 a[244:247],v239 offset:23552\ns_waitcnt lgkmcnt(12)\nv_mfma_f32_16x16x32_bf16 a[20:23],a[196:199],a[248:251],a[20:23]\nds_read_b64_tr_b16 a[226:227],v234 offset:13312\n"
"v_add_u32 v226,s68,v226\nv_mfma_f32_16x16x32_bf16 a[48:51],a[200:203],a[248:251],a[48:51]\ns_add_u32 m0,s64,12288\ns_mov_b32 s73,0\nv_mfma_f32_16x16x32_bf16 a[76:79],a[204:207],a[248:251],a[76:79]\nv_mfma_f32_16x16x32_bf16 a[104:107],a[208:211],a[248:251],a[104:107]\nds_read_b128 a[248:251],v239 offset:25600\nv_mfma_f32_16x16x32_bf16 a[24:27],a[196:199],a[252:255],a[24:27]\n"
"v_mfma_f32_16x16x32_bf16 a[52:55],a[200:203],a[252:255],a[52:55]\nv_mfma_f32_16x16x32_bf16 a[80:83],a[204:207],a[252:255],a[80:83]\nv_mfma_f32_16x16x32_bf16 a[108:111],a[208:211],a[252:255],a[108:111]\nds_read_b128 a[252:255],v239 offset:27648\ns_waitcnt lgkmcnt(2)\nv_mfma_f32_16x16x32_bf16 a[0:3],a[212:215],a[228:231],a[0:3]\nbuffer_load_dwordx4 v226,s[56:59],s73 offen offset:0 lds\ns_mov_b32 s74,13312\n"
"v_mfma_f32_16x16x32_bf16 a[28:31],a[216:219],a[228:231],a[28:31]\ns_waitcnt vmcnt(13)\nds_read_b64_tr_b16 a[196:197],v231 offset:16384\nv_mfma_f32_16x16x32_bf16 a[56:59],a[220:223],a[228:231],a[56:59]\nds_read_b64_tr_b16 a[198:199],v231 offset:17408\nv_mfma_f32_16x16x32_bf16 a[84:87],a[224:227],a[228:231],a[84:87]\nds_read_b128 a[228:231],v239 offset:28672\nv_mfma_f32_16x16x32_bf16 a[4:7],a[212:215],a[232:235],a[4:7]\n"
"buffer_load_dwordx4 v226,s[56:59],s74 offen offset:1024 lds\ns_mov_b32 s75,5120\nv_mfma_f32_16x16x32_bf16 a[32:35],a[216:219],a[232:235],a[32:35]\nds_read_b64_tr_b16 a[200:201],v232 offset:16384\nv_mfma_f32_16x16x32_bf16 a[60:63],a[220:223],a[232:235],a[60:63]\nv_mfma_f32_16x16x32_bf16 a[88:91],a[224:227],a[232:235],a[88:91]\nds_read_b128 a[232:235],v239 offset:30720\nv_mfma_f32_16x16x32_bf16 a[8:11],a[212:215],a[236:239],a[8:11]\n"
"buffer_load_dwordx4 v226,s[56:59],s75 offen offset:2048 lds\ns_mov_b32 s72,18432\nv_mfma_f32_16x16x32_bf16 a[36:39],a[216:219],a[236:239],a[36:39]\nds_read_b64_tr_b16 a[202:203],v232 offset:17408\nv_mfma_f32_16x16x32_bf16 a[64:67],a[220:223],a[236:239],a[64:67]\nv_mfma_f32_16x16x32_bf16 a[92:95],a[224:227],a[236:239],a[92:95]\nds_read_b128 a[236:239],v239 offset:32768\nv_mfma_f32_16x16x32_bf16 a[12:15],a[212:215],a[240:243],a[12:15]\n"
"buffer_load_dwordx4 v226,s[56:59],s72 offen offset:3072 lds\nv_mfma_f32_16x16x32_bf16 a[40:43],a[216:219],a[240:243],a[40:43]\nds_read_b64_tr_b16 a[204:205],v233 offset:16384\nv_mfma_f32_16x16x32_bf16 a[68:71],a[220:223],a[240:243],a[68:71]\nds_read_b64_tr_b16 a[206:207],v233 offset:17408\nv_mfma_f32_16x16x32_bf16 a[96:99],a[224:227],a[240:243],a[96:99]\nds_read_b128 a[240:243],v239 offset:34816\nv_mfma_f32_16x16x32_bf16 a[16:19],a[212:215],a[244:247],a[16:19]\n"
"ds_read_b64_tr_b16 a[208:209],v234 offset:16384\nv_mfma_f32_16x16x32_bf16 a[44:47],a[216:219],a[244:247],a[44:47]\nv_mfma_f32_16x16x32_bf16 a[72:75],a[220:223],a[244:247],a[72:75]\nv_mfma_f32_16x16x32_bf16 a[100:103],a[224:227],a[244:247],a[100:103]\nds_read_b128 a[244:247],v239 offset:36864\ns_waitcnt lgkmcnt(12)\nv_mfma_f32_16x16x32_bf16 a[20:23],a[212:215],a[248:251],a[20:23]\nds_read_b64_tr_b16 a[210:211],v234 offset:17408\n"
"s_add_u32 m0,s64,16384\nv_mfma_f32_16x16x32_bf16 a[48:51],a[216:219],a[248:251],a[48:51]\ns_mov_b32 s73,28672\nv_mfma_f32_16x16x32_bf16 a[76:79],a[220:223],a[248:251],a[76:79]\nv_mfma_f32_16x16x32_bf16 a[104:107],a[224:227],a[248:251],a[104:107]\nds_read_b128 a[248:251],v239 offset:38912\nv_mfma_f32_16x16x32_bf16 a[24:27],a[212:215],a[252:255],a[24:27]\nv_mfma_f32_16x16x32_bf16 a[52:55],a[216:219],a[252:255],a[52:55]\n"
"v_mfma_f32_16x16x32_bf16 a[80:83],a[220:223],a[252:255],a[80:83]\nv_mfma_f32_16x16x32_bf16 a[108:111],a[224:227],a[252:255],a[108:111]\nds_read_b128 a[252:255],v239 offset:40960\ns_waitcnt lgkmcnt(2)\nv_mfma_f32_16x16x32_bf16 a[0:3],a[196:199],a[228:231],a[0:3]\nbuffer_load_dwordx4 v226,s[56:59],s73 offen offset:0 lds\ns_mov_b32 s74,41984\nv_mfma_f32_16x16x32_bf16 a[28:31],a[200:203],a[228:231],a[28:31]\n"
"s_waitcnt vmcnt(13)\nds_read_b64_tr_b16 a[212:213],v231 offset:0\nv_mfma_f32_16x16x32_bf16 a[56:59],a[204:207],a[228:231],a[56:59]\nds_read_b64_tr_b16 a[214:215],v231 offset:1024\nv_mfma_f32_16x16x32_bf16 a[84:87],a[208:211],a[228:231],a[84:87]\nds_read_b128 a[228:231],v239 offset:29696\nv_mfma_f32_16x16x32_bf16 a[4:7],a[196:199],a[232:235],a[4:7]\nbuffer_load_dwordx4 v226,s[56:59],s74 offen offset:1024 lds\n"
"s_mov_b32 s75,33792\nv_mfma_f32_16x16x32_bf16 a[32:35],a[200:203],a[232:235],a[32:35]\nds_read_b64_tr_b16 a[216:217],v232 offset:0\nv_mfma_f32_16x16x32_bf16 a[60:63],a[204:207],a[232:235],a[60:63]\nv_mfma_f32_16x16x32_bf16 a[88:91],a[208:211],a[232:235],a[88:91]\nds_read_b128 a[232:235],v239 offset:31744\nv_mfma_f32_16x16x32_bf16 a[8:11],a[196:199],a[236:239],a[8:11]\nbuffer_load_dwordx4 v226,s[56:59],s75 offen offset:2048 lds\n"
"s_mov_b32 s72,47104\nv_mfma_f32_16x16x32_bf16 a[36:39],a[200:203],a[236:239],a[36:39]\nds_read_b64_tr_b16 a[218:219],v232 offset:1024\nv_mfma_f32_16x16x32_bf16 a[64:67],a[204:207],a[236:239],a[64:67]\nv_mfma_f32_16x16x32_bf16 a[92:95],a[208:211],a[236:239],a[92:95]\nds_read_b128 a[236:239],v239 offset:33792\nv_mfma_f32_16x16x32_bf16 a[12:15],a[196:199],a[240:243],a[12:15]\nbuffer_load_dwordx4 v226,s[56:59],s72 offen offset:3072 lds\n"
"v_mfma_f32_16x16x32_bf16 a[40:43],a[200:203],a[240:243],a[40:43]\nds_read_b64_tr_b16 a[220:221],v233 offset:0\nv_mfma_f32_16x16x32_bf16 a[68:71],a[204:207],a[240:243],a[68:71]\nds_read_b64_tr_b16 a[222:223],v233 offset:1024\nv_mfma_f32_16x16x32_bf16 a[96:99],a[208:211],a[240:243],a[96:99]\nds_read_b128 a[240:243],v239 offset:35840\nv_mfma_f32_16x16x32_bf16 a[16:19],a[196:199],a[244:247],a[16:19]\nds_read_b64_tr_b16 a[224:225],v234 offset:0\n"
"v_mfma_f32_16x16x32_bf16 a[44:47],a[200:203],a[244:247],a[44:47]\nv_mfma_f32_16x16x32_bf16 a[72:75],a[204:207],a[244:247],a[72:75]\nv_mfma_f32_16x16x32_bf16 a[100:103],a[208:211],a[244:247],a[100:103]\nds_read_b128 a[244:247],v239 offset:37888\ns_waitcnt lgkmcnt(12)\nv_mfma_f32_16x16x32_bf16 a[20:23],a[196:199],a[248:251],a[20:23]\nds_read_b64_tr_b16 a[226:227],v234 offset:1024\ns_add_u32 m0,s64,0\n"
"v_mfma_f32_16x16x32_bf16 a[48:51],a[200:203],a[248:251],a[48:51]\ns_mov_b32 s73,57344\nv_mfma_f32_16x16x32_bf16 a[76:79],a[204:207],a[248:251],a[76:79]\nv_mfma_f32_16x16x32_bf16 a[104:107],a[208:211],a[248:251],a[104:107]\nds_read_b128 a[248:251],v239 offset:39936\nv_mfma_f32_16x16x32_bf16 a[24:27],a[196:199],a[252:255],a[24:27]\nv_mfma_f32_16x16x32_bf16 a[52:55],a[200:203],a[252:255],a[52:55]\nv_mfma_f32_16x16x32_bf16 a[80:83],a[204:207],a[252:255],a[80:83]\n"
"v_mfma_f32_16x16x32_bf16 a[108:111],a[208:211],a[252:255],a[108:111]\nds_read_b128 a[252:255],v239 offset:41984\ns_waitcnt lgkmcnt(2)\nv_mfma_f32_16x16x32_bf16 a[0:3],a[212:215],a[228:231],a[0:3]\nbuffer_load_dwordx4 v226,s[56:59],s73 offen offset:0 lds\ns_mov_b32 s74,70656\nv_mfma_f32_16x16x32_bf16 a[28:31],a[216:219],a[228:231],a[28:31]\ns_waitcnt vmcnt(13)\n"
"ds_read_b64_tr_b16 a[196:197],v231 offset:4096\nv_mfma_f32_16x16x32_bf16 a[56:59],a[220:223],a[228:231],a[56:59]\nds_read_b64_tr_b16 a[198:199],v231 offset:5120\nv_mfma_f32_16x16x32_bf16 a[84:87],a[224:227],a[228:231],a[84:87]\nds_read_b128 a[228:231],v239 offset:43008\nv_mfma_f32_16x16x32_bf16 a[4:7],a[212:215],a[232:235],a[4:7]\nbuffer_load_dwordx4 v226,s[56:59],s74 offen offset:1024 lds\ns_mov_b32 s75,62464\n"
"v_mfma_f32_16x16x32_bf16 a[32:35],a[216:219],a[232:235],a[32:35]\nds_read_b64_tr_b16 a[200:201],v232 offset:4096\nv_mfma_f32_16x16x32_bf16 a[60:63],a[220:223],a[232:235],a[60:63]\nv_mfma_f32_16x16x32_bf16 a[88:91],a[224:227],a[232:235],a[88:91]\nds_read_b128 a[232:235],v239 offset:45056\nv_mfma_f32_16x16x32_bf16 a[8:11],a[212:215],a[236:239],a[8:11]\nbuffer_load_dwordx4 v226,s[56:59],s75 offen offset:2048 lds\ns_mov_b32 s72,75776\n"
"v_mfma_f32_16x16x32_bf16 a[36:39],a[216:219],a[236:239],a[36:39]\nds_read_b64_tr_b16 a[202:203],v232 offset:5120\nv_mfma_f32_16x16x32_bf16 a[64:67],a[220:223],a[236:239],a[64:67]\nv_mfma_f32_16x16x32_bf16 a[92:95],a[224:227],a[236:239],a[92:95]\nds_read_b128 a[236:239],v239 offset:47104\nv_mfma_f32_16x16x32_bf16 a[12:15],a[212:215],a[240:243],a[12:15]\nbuffer_load_dwordx4 v226,s[56:59],s72 offen offset:3072 lds\nv_mfma_f32_16x16x32_bf16 a[40:43],a[216:219],a[240:243],a[40:43]\n"
"ds_read_b64_tr_b16 a[204:205],v233 offset:4096\nv_mfma_f32_16x16x32_bf16 a[68:71],a[220:223],a[240:243],a[68:71]\nds_read_b64_tr_b16 a[206:207],v233 offset:5120\nv_mfma_f32_16x16x32_bf16 a[96:99],a[224:227],a[240:243],a[96:99]\nds_read_b128 a[240:243],v239 offset:49152\nv_mfma_f32_16x16x32_bf16 a[16:19],a[212:215],a[244:247],a[16:19]\nds_read_b64_tr_b16 a[208:209],v234 offset:4096\nv_mfma_f32_16x16x32_bf16 a[44:47],a[216:219],a[244:247],a[44:47]\n"
"v_mfma_f32_16x16x32_bf16 a[72:75],a[220:223],a[244:247],a[72:75]\nv_mfma_f32_16x16x32_bf16 a[100:103],a[224:227],a[244:247],a[100:103]\nds_read_b128 a[244:247],v239 offset:51200\ns_waitcnt lgkmcnt(12)\nv_mfma_f32_16x16x32_bf16 a[20:23],a[212:215],a[248:251],a[20:23]\nds_read_b64_tr_b16 a[210:211],v234 offset:5120\ns_add_u32 m0,s64,4096\nv_mfma_f32_16x16x32_bf16 a[48:51],a[216:219],a[248:251],a[48:51]\n"
"s_mov_b32 s73,86016\nv_mfma_f32_16x16x32_bf16 a[76:79],a[220:223],a[248:251],a[76:79]\nv_mfma_f32_16x16x32_bf16 a[104:107],a[224:227],a[248:251],a[104:107]\nds_read_b128 a[248:251],v239 offset:53248\nv_mfma_f32_16x16x32_bf16 a[24:27],a[212:215],a[252:255],a[24:27]\nv_mfma_f32_16x16x32_bf16 a[52:55],a[216:219],a[252:255],a[52:55]\nv_mfma_f32_16x16x32_bf16 a[80:83],a[220:223],a[252:255],a[80:83]\nv_mfma_f32_16x16x32_bf16 a[108:111],a[224:227],a[252:255],a[108:111]\n"
"ds_read_b128 a[252:255],v239 offset:55296\ns_waitcnt lgkmcnt(2)\nv_mfma_f32_16x16x32_bf16 a[0:3],a[196:199],a[228:231],a[0:3]\nbuffer_load_dwordx4 v226,s[56:59],s73 offen offset:0 lds\ns_mov_b32 s74,99328\nv_mfma_f32_16x16x32_bf16 a[28:31],a[200:203],a[228:231],a[28:31]\ns_waitcnt vmcnt(13)\nds_read_b64_tr_b16 a[212:213],v231 offset:8192\n"
"v_mfma_f32_16x16x32_bf16 a[56:59],a[204:207],a[228:231],a[56:59]\nds_read_b64_tr_b16 a[214:215],v231 offset:9216\nv_mfma_f32_16x16x32_bf16 a[84:87],a[208:211],a[228:231],a[84:87]\nds_read_b128 a[228:231],v239 offset:44032\nv_mfma_f32_16x16x32_bf16 a[4:7],a[196:199],a[232:235],a[4:7]\nbuffer_load_dwordx4 v226,s[56:59],s74 offen offset:1024 lds\ns_mov_b32 s75,91136\nv_mfma_f32_16x16x32_bf16 a[32:35],a[200:203],a[232:235],a[32:35]\n"
"ds_read_b64_tr_b16 a[216:217],v232 offset:8192\nv_mfma_f32_16x16x32_bf16 a[60:63],a[204:207],a[232:235],a[60:63]\nv_mfma_f32_16x16x32_bf16 a[88:91],a[208:211],a[232:235],a[88:91]\nds_read_b128 a[232:235],v239 offset:46080\nv_mfma_f32_16x16x32_bf16 a[8:11],a[196:199],a[236:239],a[8:11]\nbuffer_load_dwordx4 v226,s[56:59],s75 offen offset:2048 lds\ns_mov_b32 s72,104448\nv_mfma_f32_16x16x32_bf16 a[36:39],a[200:203],a[236:239],a[36:39]\n"
"ds_read_b64_tr_b16 a[218:219],v232 offset:9216\nv_mfma_f32_16x16x32_bf16 a[64:67],a[204:207],a[236:239],a[64:67]\nv_mfma_f32_16x16x32_bf16 a[92:95],a[208:211],a[236:239],a[92:95]\nds_read_b128 a[236:239],v239 offset:48128\nv_mfma_f32_16x16x32_bf16 a[12:15],a[196:199],a[240:243],a[12:15]\nbuffer_load_dwordx4 v226,s[56:59],s72 offen offset:3072 lds\nv_mfma_f32_16x16x32_bf16 a[40:43],a[200:203],a[240:243],a[40:43]\nds_read_b64_tr_b16 a[220:221],v233 offset:8192\n"
"v_mfma_f32_16x16x32_bf16 a[68:71],a[204:207],a[240:243],a[68:71]\nds_read_b64_tr_b16 a[222:223],v233 offset:9216\nv_mfma_f32_16x16x32_bf16 a[96:99],a[208:211],a[240:243],a[96:99]\nds_read_b128 a[240:243],v239 offset:50176\nv_mfma_f32_16x16x32_bf16 a[16:19],a[196:199],a[244:247],a[16:19]\nds_read_b64_tr_b16 a[224:225],v234 offset:8192\nv_mfma_f32_16x16x32_bf16 a[44:47],a[200:203],a[244:247],a[44:47]\nv_mfma_f32_16x16x32_bf16 a[72:75],a[204:207],a[244:247],a[72:75]\n"
"v_mfma_f32_16x16x32_bf16 a[100:103],a[208:211],a[244:247],a[100:103]\nds_read_b128 a[244:247],v239 offset:52224\ns_waitcnt lgkmcnt(12)\nv_mfma_f32_16x16x32_bf16 a[20:23],a[196:199],a[248:251],a[20:23]\nds_read_b64_tr_b16 a[226:227],v234 offset:9216\ns_add_u32 m0,s64,8192\nv_mfma_f32_16x16x32_bf16 a[48:51],a[200:203],a[248:251],a[48:51]\ns_mov_b32 s73,114688\n"
"v_mfma_f32_16x16x32_bf16 a[76:79],a[204:207],a[248:251],a[76:79]\nv_mfma_f32_16x16x32_bf16 a[104:107],a[208:211],a[248:251],a[104:107]\nds_read_b128 a[248:251],v239 offset:54272\nv_mfma_f32_16x16x32_bf16 a[24:27],a[196:199],a[252:255],a[24:27]\nv_mfma_f32_16x16x32_bf16 a[52:55],a[200:203],a[252:255],a[52:55]\nv_mfma_f32_16x16x32_bf16 a[80:83],a[204:207],a[252:255],a[80:83]\nv_mfma_f32_16x16x32_bf16 a[108:111],a[208:211],a[252:255],a[108:111]\nds_read_b128 a[252:255],v239 offset:56320\n"
"s_waitcnt lgkmcnt(2)\nv_mfma_f32_16x16x32_bf16 a[0:3],a[212:215],a[228:231],a[0:3]\nbuffer_load_dwordx4 v226,s[56:59],s73 offen offset:0 lds\ns_mov_b32 s74,128000\nv_mfma_f32_16x16x32_bf16 a[28:31],a[216:219],a[228:231],a[28:31]\ns_waitcnt vmcnt(13)\nds_read_b64_tr_b16 a[196:197],v231 offset:12288\nv_mfma_f32_16x16x32_bf16 a[56:59],a[220:223],a[228:231],a[56:59]\n"
"v_mfma_f32_16x16x32_bf16 a[84:87],a[224:227],a[228:231],a[84:87]\nds_read_b128 a[228:231],v239 offset:0\nv_mfma_f32_16x16x32_bf16 a[4:7],a[212:215],a[232:235],a[4:7]\nbuffer_load_dwordx4 v226,s[56:59],s74 offen offset:1024 lds\ns_mov_b32 s75,119808\nv_mfma_f32_16x16x32_bf16 a[32:35],a[216:219],a[232:235],a[32:35]\nds_read_b64_tr_b16 a[198:199],v231 offset:13312\nv_mfma_f32_16x16x32_bf16 a[60:63],a[220:223],a[232:235],a[60:63]\n"
"v_mfma_f32_16x16x32_bf16 a[88:91],a[224:227],a[232:235],a[88:91]\nds_read_b128 a[232:235],v239 offset:2048\nv_mfma_f32_16x16x32_bf16 a[8:11],a[212:215],a[236:239],a[8:11]\nbuffer_load_dwordx4 v226,s[56:59],s75 offen offset:2048 lds\ns_mov_b32 s72,133120\nv_mfma_f32_16x16x32_bf16 a[36:39],a[216:219],a[236:239],a[36:39]\nds_read_b64_tr_b16 a[200:201],v232 offset:12288\nv_mfma_f32_16x16x32_bf16 a[64:67],a[220:223],a[236:239],a[64:67]\n"
"v_mfma_f32_16x16x32_bf16 a[92:95],a[224:227],a[236:239],a[92:95]\nds_read_b128 a[236:239],v239 offset:4096\nv_mfma_f32_16x16x32_bf16 a[12:15],a[212:215],a[240:243],a[12:15]\nbuffer_load_dwordx4 v226,s[56:59],s72 offen offset:3072 lds\nv_mfma_f32_16x16x32_bf16 a[40:43],a[216:219],a[240:243],a[40:43]\nds_read_b64_tr_b16 a[202:203],v232 offset:13312\nv_mfma_f32_16x16x32_bf16 a[68:71],a[220:223],a[240:243],a[68:71]\nv_mfma_f32_16x16x32_bf16 a[96:99],a[224:227],a[240:243],a[96:99]\n"
"ds_read_b128 a[240:243],v239 offset:6144\nv_mfma_f32_16x16x32_bf16 a[16:19],a[212:215],a[244:247],a[16:19]\nds_read_b64_tr_b16 a[204:205],v233 offset:12288\nv_mfma_f32_16x16x32_bf16 a[44:47],a[216:219],a[244:247],a[44:47]\nv_mfma_f32_16x16x32_bf16 a[72:75],a[220:223],a[244:247],a[72:75]\nv_mfma_f32_16x16x32_bf16 a[100:103],a[224:227],a[244:247],a[100:103]\nds_read_b128 a[244:247],v239 offset:8192\ns_waitcnt lgkmcnt(10)\n"
"v_mfma_f32_16x16x32_bf16 a[20:23],a[212:215],a[248:251],a[20:23]\nds_read_b64_tr_b16 a[206:207],v233 offset:13312\ns_add_u32 m0,s64,12288\nv_mfma_f32_16x16x32_bf16 a[48:51],a[216:219],a[248:251],a[48:51]\ns_mov_b32 s73,143360\nv_mfma_f32_16x16x32_bf16 a[76:79],a[220:223],a[248:251],a[76:79]\nv_mfma_f32_16x16x32_bf16 a[104:107],a[224:227],a[248:251],a[104:107]\nds_read_b128 a[248:251],v239 offset:10240\n"
"v_mfma_f32_16x16x32_bf16 a[24:27],a[212:215],a[252:255],a[24:27]\nv_mfma_f32_16x16x32_bf16 a[52:55],a[216:219],a[252:255],a[52:55]\nv_mfma_f32_16x16x32_bf16 a[80:83],a[220:223],a[252:255],a[80:83]\nv_mfma_f32_16x16x32_bf16 a[108:111],a[224:227],a[252:255],a[108:111]\nds_read_b128 a[252:255],v239 offset:12288\ns_waitcnt lgkmcnt(2)\nv_mfma_f32_16x16x32_bf16 a[112:115],a[196:199],a[228:231],0\nbuffer_load_dwordx4 v226,s[56:59],s73 offen offset:0 lds\n"
"s_mov_b32 s74,156672\nv_mfma_f32_16x16x32_bf16 a[140:143],a[200:203],a[228:231],0\ns_waitcnt vmcnt(13)\nds_read_b64_tr_b16 a[212:213],v231 offset:16384\nv_mfma_f32_16x16x32_bf16 a[168:171],a[204:207],a[228:231],0\nds_read_b128 a[228:231],v239 offset:1024\nv_mfma_f32_16x16x32_bf16 a[116:119],a[196:199],a[232:235],0\nbuffer_load_dwordx4 v226,s[56:59],s74 offen offset:1024 lds\n"
"s_mov_b32 s75,148480\nv_mfma_f32_16x16x32_bf16 a[144:147],a[200:203],a[232:235],0\nds_read_b64_tr_b16 a[214:215],v231 offset:17408\nv_mfma_f32_16x16x32_bf16 a[172:175],a[204:207],a[232:235],0\nds_read_b128 a[232:235],v239 offset:3072\nv_mfma_f32_16x16x32_bf16 a[120:123],a[196:199],a[236:239],0\nbuffer_load_dwordx4 v226,s[56:59],s75 offen offset:2048 lds\ns_mov_b32 s72,161792\n"
"v_mfma_f32_16x16x32_bf16 a[148:151],a[200:203],a[236:239],0\nds_read_b64_tr_b16 a[216:217],v232 offset:16384\nv_mfma_f32_16x16x32_bf16 a[176:179],a[204:207],a[236:239],0\nds_read_b128 a[236:239],v239 offset:5120\nv_mfma_f32_16x16x32_bf16 a[124:127],a[196:199],a[240:243],0\nbuffer_load_dwordx4 v226,s[56:59],s72 offen offset:3072 lds\nv_mfma_f32_16x16x32_bf16 a[152:155],a[200:203],a[240:243],0\nds_read_b64_tr_b16 a[218:219],v232 offset:17408\n"
"v_mfma_f32_16x16x32_bf16 a[180:183],a[204:207],a[240:243],0\nds_read_b128 a[240:243],v239 offset:7168\nv_mfma_f32_16x16x32_bf16 a[128:131],a[196:199],a[244:247],0\nds_read_b64_tr_b16 a[220:221],v233 offset:16384\nv_mfma_f32_16x16x32_bf16 a[156:159],a[200:203],a[244:247],0\nv_mfma_f32_16x16x32_bf16 a[184:187],a[204:207],a[244:247],0\nds_read_b128 a[244:247],v239 offset:9216\ns_waitcnt lgkmcnt(10)\n"
"v_mfma_f32_16x16x32_bf16 a[132:135],a[196:199],a[248:251],0\nds_read_b64_tr_b16 a[222:223],v233 offset:17408\ns_add_u32 m0,s64,16384\nv_mfma_f32_16x16x32_bf16 a[160:163],a[200:203],a[248:251],0\ns_mov_b32 s73,172032\nv_mfma_f32_16x16x32_bf16 a[188:191],a[204:207],a[248:251],0\nds_read_b128 a[248:251],v239 offset:11264\nv_mfma_f32_16x16x32_bf16 a[136:139],a[196:199],a[252:255],0\n"
"v_mfma_f32_16x16x32_bf16 a[164:167],a[200:203],a[252:255],0\nv_mfma_f32_16x16x32_bf16 a[192:195],a[204:207],a[252:255],0\nds_read_b128 a[252:255],v239 offset:13312\ns_waitcnt lgkmcnt(2)\nv_mfma_f32_16x16x32_bf16 a[112:115],a[212:215],a[228:231],a[112:115]\nbuffer_load_dwordx4 v226,s[56:59],s73 offen offset:0 lds\ns_mov_b32 s74,185344\nv_mfma_f32_16x16x32_bf16 a[140:143],a[216:219],a[228:231],a[140:143]\n"
"s_waitcnt vmcnt(13)\nds_read_b64_tr_b16 a[196:197],v231 offset:0\nv_mfma_f32_16x16x32_bf16 a[168:171],a[220:223],a[228:231],a[168:171]\nds_read_b128 a[228:231],v239 offset:14336\nv_mfma_f32_16x16x32_bf16 a[116:119],a[212:215],a[232:235],a[116:119]\nbuffer_load_dwordx4 v226,s[56:59],s74 offen offset:1024 lds\ns_mov_b32 s75,177152\nv_mfma_f32_16x16x32_bf16 a[144:147],a[216:219],a[232:235],a[144:147]\n"
"ds_read_b64_tr_b16 a[198:199],v231 offset:1024\nv_mfma_f32_16x16x32_bf16 a[172:175],a[220:223],a[232:235],a[172:175]\nds_read_b128 a[232:235],v239 offset:16384\nv_mfma_f32_16x16x32_bf16 a[120:123],a[212:215],a[236:239],a[120:123]\nbuffer_load_dwordx4 v226,s[56:59],s75 offen offset:2048 lds\ns_mov_b32 s72,190464\nv_mfma_f32_16x16x32_bf16 a[148:151],a[216:219],a[236:239],a[148:151]\nds_read_b64_tr_b16 a[200:201],v232 offset:0\n"
"v_mfma_f32_16x16x32_bf16 a[176:179],a[220:223],a[236:239],a[176:179]\nds_read_b128 a[236:239],v239 offset:18432\nv_mfma_f32_16x16x32_bf16 a[124:127],a[212:215],a[240:243],a[124:127]\nbuffer_load_dwordx4 v226,s[56:59],s72 offen offset:3072 lds\nv_mfma_f32_16x16x32_bf16 a[152:155],a[216:219],a[240:243],a[152:155]\nds_read_b64_tr_b16 a[202:203],v232 offset:1024\nv_mfma_f32_16x16x32_bf16 a[180:183],a[220:223],a[240:243],a[180:183]\nds_read_b128 a[240:243],v239 offset:20480\n"
"v_mfma_f32_16x16x32_bf16 a[128:131],a[212:215],a[244:247],a[128:131]\nds_read_b64_tr_b16 a[204:205],v233 offset:0\nv_mfma_f32_16x16x32_bf16 a[156:159],a[216:219],a[244:247],a[156:159]\nv_mfma_f32_16x16x32_bf16 a[184:187],a[220:223],a[244:247],a[184:187]\nds_read_b128 a[244:247],v239 offset:22528\ns_waitcnt lgkmcnt(10)\nv_mfma_f32_16x16x32_bf16 a[132:135],a[212:215],a[248:251],a[132:135]\nds_read_b64_tr_b16 a[206:207],v233 offset:1024\n"
"s_add_u32 m0,s64,0\nv_mfma_f32_16x16x32_bf16 a[160:163],a[216:219],a[248:251],a[160:163]\ns_mov_b32 s73,200704\nv_mfma_f32_16x16x32_bf16 a[188:191],a[220:223],a[248:251],a[188:191]\nds_read_b128 a[248:251],v239 offset:24576\nv_mfma_f32_16x16x32_bf16 a[136:139],a[212:215],a[252:255],a[136:139]\nv_mfma_f32_16x16x32_bf16 a[164:167],a[216:219],a[252:255],a[164:167]\nv_mfma_f32_16x16x32_bf16 a[192:195],a[220:223],a[252:255],a[192:195]\n"
"ds_read_b128 a[252:255],v239 offset:26624\ns_waitcnt lgkmcnt(2)\nv_mfma_f32_16x16x32_bf16 a[112:115],a[196:199],a[228:231],a[112:115]\nbuffer_load_dwordx4 v226,s[56:59],s73 offen offset:0 lds\ns_mov_b32 s74,214016\nv_mfma_f32_16x16x32_bf16 a[140:143],a[200:203],a[228:231],a[140:143]\ns_waitcnt vmcnt(13)\nds_read_b64_tr_b16 a[212:213],v231 offset:4096\n"
"v_mfma_f32_16x16x32_bf16 a[168:171],a[204:207],a[228:231],a[168:171]\nds_read_b128 a[228:231],v239 offset:15360\nv_mfma_f32_16x16x32_bf16 a[116:119],a[196:199],a[232:235],a[116:119]\nbuffer_load_dwordx4 v226,s[56:59],s74 offen offset:1024 lds\ns_mov_b32 s75,205824\nv_mfma_f32_16x16x32_bf16 a[144:147],a[200:203],a[232:235],a[144:147]\nds_read_b64_tr_b16 a[214:215],v231 offset:5120\nv_mfma_f32_16x16x32_bf16 a[172:175],a[204:207],a[232:235],a[172:175]\n"
"ds_read_b128 a[232:235],v239 offset:17408\nv_mfma_f32_16x16x32_bf16 a[120:123],a[196:199],a[236:239],a[120:123]\nbuffer_load_dwordx4 v226,s[56:59],s75 offen offset:2048 lds\ns_mov_b32 s72,219136\nv_mfma_f32_16x16x32_bf16 a[148:151],a[200:203],a[236:239],a[148:151]\nds_read_b64_tr_b16 a[216:217],v232 offset:4096\nv_mfma_f32_16x16x32_bf16 a[176:179],a[204:207],a[236:239],a[176:179]\nds_read_b128 a[236:239],v239 offset:19456\n"
"v_mfma_f32_16x16x32_bf16 a[124:127],a[196:199],a[240:243],a[124:127]\nbuffer_load_dwordx4 v226,s[56:59],s72 offen offset:3072 lds\nv_mfma_f32_16x16x32_bf16 a[152:155],a[200:203],a[240:243],a[152:155]\nds_read_b64_tr_b16 a[218:219],v232 offset:5120\nv_mfma_f32_16x16x32_bf16 a[180:183],a[204:207],a[240:243],a[180:183]\nds_read_b128 a[240:243],v239 offset:21504\nv_mfma_f32_16x16x32_bf16 a[128:131],a[196:199],a[244:247],a[128:131]\nds_read_b64_tr_b16 a[220:221],v233 offset:4096\n"
"v_mfma_f32_16x16x32_bf16 a[156:159],a[200:203],a[244:247],a[156:159]\nv_mfma_f32_16x16x32_bf16 a[184:187],a[204:207],a[244:247],a[184:187]\nds_read_b128 a[244:247],v239 offset:23552\ns_waitcnt lgkmcnt(10)\nv_mfma_f32_16x16x32_bf16 a[132:135],a[196:199],a[248:251],a[132:135]\nds_read_b64_tr_b16 a[222:223],v233 offset:5120\ns_add_u32 m0,s64,4096\nv_mfma_f32_16x16x32_bf16 a[160:163],a[200:203],a[248:251],a[160:163]\n"
"s_mov_b32 s73,262144\nv_mfma_f32_16x16x32_bf16 a[188:191],a[204:207],a[248:251],a[188:191]\nds_read_b128 a[248:251],v239 offset:25600\nv_mfma_f32_16x16x32_bf16 a[136:139],a[196:199],a[252:255],a[136:139]\nv_mfma_f32_16x16x32_bf16 a[164:167],a[200:203],a[252:255],a[164:167]\nv_mfma_f32_16x16x32_bf16 a[192:195],a[204:207],a[252:255],a[192:195]\nds_read_b128 a[252:255],v239 offset:27648\ns_waitcnt lgkmcnt(2)\n"
"v_mfma_f32_16x16x32_bf16 a[112:115],a[212:215],a[228:231],a[112:115]\nbuffer_load_dwordx4 v227,s[48:51],s73 offen offset:0 nt lds\ns_mov_b32 s74,269312\nv_mfma_f32_16x16x32_bf16 a[140:143],a[216:219],a[228:231],a[140:143]\ns_waitcnt vmcnt(13)\nds_read_b64_tr_b16 a[196:197],v231 offset:8192\nv_mfma_f32_16x16x32_bf16 a[168:171],a[220:223],a[228:231],a[168:171]\nds_read_b128 a[228:231],v239 offset:28672\n"
"v_mfma_f32_16x16x32_bf16 a[116:119],a[212:215],a[232:235],a[116:119]\nbuffer_load_dwordx4 v228,s[48:51],s74 offen offset:1024 nt lds\ns_mov_b32 s75,292864\nv_mfma_f32_16x16x32_bf16 a[144:147],a[216:219],a[232:235],a[144:147]\nds_read_b64_tr_b16 a[198:199],v231 offset:9216\nv_mfma_f32_16x16x32_bf16 a[172:175],a[220:223],a[232:235],a[172:175]\nds_read_b128 a[232:235],v239 offset:30720\nv_mfma_f32_16x16x32_bf16 a[120:123],a[212:215],a[236:239],a[120:123]\n"
"buffer_load_dwordx4 v227,s[48:51],s75 offen offset:2048 nt lds\ns_mov_b32 s72,300032\nv_mfma_f32_16x16x32_bf16 a[148:151],a[216:219],a[236:239],a[148:151]\nds_read_b64_tr_b16 a[200:201],v232 offset:8192\nv_mfma_f32_16x16x32_bf16 a[176:179],a[220:223],a[236:239],a[176:179]\nds_read_b128 a[236:239],v239 offset:32768\nv_mfma_f32_16x16x32_bf16 a[124:127],a[212:215],a[240:243],a[124:127]\nbuffer_load_dwordx4 v228,s[48:51],s72 offen offset:3072 nt lds\n"
"v_mfma_f32_16x16x32_bf16 a[152:155],a[216:219],a[240:243],a[152:155]\nds_read_b64_tr_b16 a[202:203],v232 offset:9216\nv_mfma_f32_16x16x32_bf16 a[180:183],a[220:223],a[240:243],a[180:183]\nds_read_b128 a[240:243],v239 offset:34816\nv_mfma_f32_16x16x32_bf16 a[128:131],a[212:215],a[244:247],a[128:131]\nds_read_b64_tr_b16 a[204:205],v233 offset:8192\nv_mfma_f32_16x16x32_bf16 a[156:159],a[216:219],a[244:247],a[156:159]\nv_mfma_f32_16x16x32_bf16 a[184:187],a[220:223],a[244:247],a[184:187]\n"
"ds_read_b128 a[244:247],v239 offset:36864\ns_waitcnt lgkmcnt(10)\nv_mfma_f32_16x16x32_bf16 a[132:135],a[212:215],a[248:251],a[132:135]\nds_read_b64_tr_b16 a[206:207],v233 offset:9216\ns_add_u32 m0,s64,8192\nv_mfma_f32_16x16x32_bf16 a[160:163],a[216:219],a[248:251],a[160:163]\ns_mov_b32 s73,327680\nv_mfma_f32_16x16x32_bf16 a[188:191],a[220:223],a[248:251],a[188:191]\n"
"ds_read_b128 a[248:251],v239 offset:38912\nv_mfma_f32_16x16x32_bf16 a[136:139],a[212:215],a[252:255],a[136:139]\nv_mfma_f32_16x16x32_bf16 a[164:167],a[216:219],a[252:255],a[164:167]\nv_mfma_f32_16x16x32_bf16 a[192:195],a[220:223],a[252:255],a[192:195]\nds_read_b128 a[252:255],v239 offset:40960\ns_waitcnt lgkmcnt(2)\nv_mfma_f32_16x16x32_bf16 a[112:115],a[196:199],a[228:231],a[112:115]\nbuffer_load_dwordx4 v227,s[48:51],s73 offen offset:0 nt lds\n"
"s_mov_b32 s74,334848\nv_mfma_f32_16x16x32_bf16 a[140:143],a[200:203],a[228:231],a[140:143]\ns_waitcnt vmcnt(13)\nds_read_b64_tr_b16 a[212:213],v231 offset:12288\nv_mfma_f32_16x16x32_bf16 a[168:171],a[204:207],a[228:231],a[168:171]\nds_read_b128 a[228:231],v239 offset:29696\nv_mfma_f32_16x16x32_bf16 a[116:119],a[196:199],a[232:235],a[116:119]\nbuffer_load_dwordx4 v228,s[48:51],s74 offen offset:1024 nt lds\n"
"s_mov_b32 s75,358400\nv_mfma_f32_16x16x32_bf16 a[144:147],a[200:203],a[232:235],a[144:147]\nds_read_b64_tr_b16 a[214:215],v231 offset:13312\nv_mfma_f32_16x16x32_bf16 a[172:175],a[204:207],a[232:235],a[172:175]\nds_read_b128 a[232:235],v239 offset:31744\nv_mfma_f32_16x16x32_bf16 a[120:123],a[196:199],a[236:239],a[120:123]\nbuffer_load_dwordx4 v227,s[48:51],s75 offen offset:2048 nt lds\ns_mov_b32 s72,365568\n"
"v_mfma_f32_16x16x32_bf16 a[148:151],a[200:203],a[236:239],a[148:151]\nds_read_b64_tr_b16 a[216:217],v232 offset:12288\nv_mfma_f32_16x16x32_bf16 a[176:179],a[204:207],a[236:239],a[176:179]\nds_read_b128 a[236:239],v239 offset:33792\nv_mfma_f32_16x16x32_bf16 a[124:127],a[196:199],a[240:243],a[124:127]\nbuffer_load_dwordx4 v228,s[48:51],s72 offen offset:3072 nt lds\nv_mfma_f32_16x16x32_bf16 a[152:155],a[200:203],a[240:243],a[152:155]\nds_read_b64_tr_b16 a[218:219],v232 offset:13312\n"
"v_mfma_f32_16x16x32_bf16 a[180:183],a[204:207],a[240:243],a[180:183]\nds_read_b128 a[240:243],v239 offset:35840\nv_mfma_f32_16x16x32_bf16 a[128:131],a[196:199],a[244:247],a[128:131]\nds_read_b64_tr_b16 a[220:221],v233 offset:12288\nv_mfma_f32_16x16x32_bf16 a[156:159],a[200:203],a[244:247],a[156:159]\nv_mfma_f32_16x16x32_bf16 a[184:187],a[204:207],a[244:247],a[184:187]\nds_read_b128 a[244:247],v239 offset:37888\ns_waitcnt lgkmcnt(10)\n"
"v_mfma_f32_16x16x32_bf16 a[132:135],a[196:199],a[248:251],a[132:135]\nds_read_b64_tr_b16 a[222:223],v233 offset:13312\ns_add_u32 m0,s64,12288\nv_mfma_f32_16x16x32_bf16 a[160:163],a[200:203],a[248:251],a[160:163]\ns_mov_b32 s73,262272\nv_mfma_f32_16x16x32_bf16 a[188:191],a[204:207],a[248:251],a[188:191]\nds_read_b128 a[248:251],v239 offset:39936\nv_mfma_f32_16x16x32_bf16 a[136:139],a[196:199],a[252:255],a[136:139]\n"
"v_mfma_f32_16x16x32_bf16 a[164:167],a[200:203],a[252:255],a[164:167]\nv_mfma_f32_16x16x32_bf16 a[192:195],a[204:207],a[252:255],a[192:195]\nds_read_b128 a[252:255],v239 offset:41984\ns_waitcnt lgkmcnt(2)\nv_mfma_f32_16x16x32_bf16 a[112:115],a[212:215],a[228:231],a[112:115]\nbuffer_load_dwordx4 v227,s[48:51],s73 offen offset:0 nt lds\ns_mov_b32 s74,269440\nv_mfma_f32_16x16x32_bf16 a[140:143],a[216:219],a[228:231],a[140:143]\n"
"s_waitcnt vmcnt(13)\nds_read_b64_tr_b16 a[196:197],v231 offset:16384\nv_mfma_f32_16x16x32_bf16 a[168:171],a[220:223],a[228:231],a[168:171]\nds_read_b128 a[228:231],v239 offset:43008\nv_mfma_f32_16x16x32_bf16 a[116:119],a[212:215],a[232:235],a[116:119]\nbuffer_load_dwordx4 v228,s[48:51],s74 offen offset:1024 nt lds\ns_mov_b32 s75,292992\nv_mfma_f32_16x16x32_bf16 a[144:147],a[216:219],a[232:235],a[144:147]\n"
"ds_read_b64_tr_b16 a[198:199],v231 offset:17408\nv_mfma_f32_16x16x32_bf16 a[172:175],a[220:223],a[232:235],a[172:175]\nds_read_b128 a[232:235],v239 offset:45056\nv_mfma_f32_16x16x32_bf16 a[120:123],a[212:215],a[236:239],a[120:123]\nbuffer_load_dwordx4 v227,s[48:51],s75 offen offset:2048 nt lds\ns_mov_b32 s72,300160\nv_mfma_f32_16x16x32_bf16 a[148:151],a[216:219],a[236:239],a[148:151]\nds_read_b64_tr_b16 a[200:201],v232 offset:16384\n"
"v_mfma_f32_16x16x32_bf16 a[176:179],a[220:223],a[236:239],a[176:179]\nds_read_b128 a[236:239],v239 offset:47104\nv_mfma_f32_16x16x32_bf16 a[124:127],a[212:215],a[240:243],a[124:127]\nbuffer_load_dwordx4 v228,s[48:51],s72 offen offset:3072 nt lds\nv_mfma_f32_16x16x32_bf16 a[152:155],a[216:219],a[240:243],a[152:155]\nds_read_b64_tr_b16 a[202:203],v232 offset:17408\nv_mfma_f32_16x16x32_bf16 a[180:183],a[220:223],a[240:243],a[180:183]\nds_read_b128 a[240:243],v239 offset:49152\n"
"v_mfma_f32_16x16x32_bf16 a[128:131],a[212:215],a[244:247],a[128:131]\nds_read_b64_tr_b16 a[204:205],v233 offset:16384\nv_mfma_f32_16x16x32_bf16 a[156:159],a[216:219],a[244:247],a[156:159]\nv_mfma_f32_16x16x32_bf16 a[184:187],a[220:223],a[244:247],a[184:187]\nds_read_b128 a[244:247],v239 offset:51200\ns_waitcnt lgkmcnt(10)\nv_mfma_f32_16x16x32_bf16 a[132:135],a[212:215],a[248:251],a[132:135]\nds_read_b64_tr_b16 a[206:207],v233 offset:17408\n"
"s_add_u32 m0,s64,16384\nv_mfma_f32_16x16x32_bf16 a[160:163],a[216:219],a[248:251],a[160:163]\ns_mov_b32 s73,327808\nv_mfma_f32_16x16x32_bf16 a[188:191],a[220:223],a[248:251],a[188:191]\nds_read_b128 a[248:251],v239 offset:53248\nv_mfma_f32_16x16x32_bf16 a[136:139],a[212:215],a[252:255],a[136:139]\nv_mfma_f32_16x16x32_bf16 a[164:167],a[216:219],a[252:255],a[164:167]\nv_mfma_f32_16x16x32_bf16 a[192:195],a[220:223],a[252:255],a[192:195]\n"
"ds_read_b128 a[252:255],v239 offset:55296\ns_waitcnt lgkmcnt(2)\nv_mfma_f32_16x16x32_bf16 a[112:115],a[196:199],a[228:231],a[112:115]\nbuffer_load_dwordx4 v227,s[48:51],s73 offen offset:0 nt lds\ns_mov_b32 s74,334976\nv_mfma_f32_16x16x32_bf16 a[140:143],a[200:203],a[228:231],a[140:143]\ns_waitcnt vmcnt(13)\nds_read_b64_tr_b16 a[212:213],v231 offset:0\n"
"v_mfma_f32_16x16x32_bf16 a[168:171],a[204:207],a[228:231],a[168:171]\nds_read_b128 a[228:231],v239 offset:44032\nv_mfma_f32_16x16x32_bf16 a[116:119],a[196:199],a[232:235],a[116:119]\nbuffer_load_dwordx4 v228,s[48:51],s74 offen offset:1024 nt lds\ns_mov_b32 s75,358528\nv_mfma_f32_16x16x32_bf16 a[144:147],a[200:203],a[232:235],a[144:147]\nds_read_b64_tr_b16 a[214:215],v231 offset:1024\nv_mfma_f32_16x16x32_bf16 a[172:175],a[204:207],a[232:235],a[172:175]\n"
"ds_read_b128 a[232:235],v239 offset:46080\nv_mfma_f32_16x16x32_bf16 a[120:123],a[196:199],a[236:239],a[120:123]\nbuffer_load_dwordx4 v227,s[48:51],s75 offen offset:2048 nt lds\ns_mov_b32 s72,365696\nv_mfma_f32_16x16x32_bf16 a[148:151],a[200:203],a[236:239],a[148:151]\nds_read_b64_tr_b16 a[216:217],v232 offset:0\nv_mfma_f32_16x16x32_bf16 a[176:179],a[204:207],a[236:239],a[176:179]\nds_read_b128 a[236:239],v239 offset:48128\n"
"v_mfma_f32_16x16x32_bf16 a[124:127],a[196:199],a[240:243],a[124:127]\nbuffer_load_dwordx4 v228,s[48:51],s72 offen offset:3072 nt lds\nv_mfma_f32_16x16x32_bf16 a[152:155],a[200:203],a[240:243],a[152:155]\nds_read_b64_tr_b16 a[218:219],v232 offset:1024\nv_mfma_f32_16x16x32_bf16 a[180:183],a[204:207],a[240:243],a[180:183]\nds_read_b128 a[240:243],v239 offset:50176\nv_mfma_f32_16x16x32_bf16 a[128:131],a[196:199],a[244:247],a[128:131]\nds_read_b64_tr_b16 a[220:221],v233 offset:0\n"
"v_mfma_f32_16x16x32_bf16 a[156:159],a[200:203],a[244:247],a[156:159]\nv_mfma_f32_16x16x32_bf16 a[184:187],a[204:207],a[244:247],a[184:187]\nds_read_b128 a[244:247],v239 offset:52224\ns_waitcnt lgkmcnt(10)\nv_mfma_f32_16x16x32_bf16 a[132:135],a[196:199],a[248:251],a[132:135]\nds_read_b64_tr_b16 a[222:223],v233 offset:1024\ns_add_u32 m0,s64,0\nv_mfma_f32_16x16x32_bf16 a[160:163],a[200:203],a[248:251],a[160:163]\n"
"s_mov_b32 s73,262400\nv_mfma_f32_16x16x32_bf16 a[188:191],a[204:207],a[248:251],a[188:191]\nds_read_b128 a[248:251],v239 offset:54272\nv_mfma_f32_16x16x32_bf16 a[136:139],a[196:199],a[252:255],a[136:139]\nv_mfma_f32_16x16x32_bf16 a[164:167],a[200:203],a[252:255],a[164:167]\nv_mfma_f32_16x16x32_bf16 a[192:195],a[204:207],a[252:255],a[192:195]\nds_read_b128 a[252:255],v239 offset:56320\ns_waitcnt lgkmcnt(2)\n"
"v_mfma_f32_16x16x32_bf16 a[112:115],a[212:215],a[228:231],a[112:115]\nbuffer_load_dwordx4 v227,s[48:51],s73 offen offset:0 nt lds\ns_mov_b32 s74,269568\nv_mfma_f32_16x16x32_bf16 a[140:143],a[216:219],a[228:231],a[140:143]\ns_waitcnt vmcnt(13)\nds_read_b128 a[196:199],v235 offset:4096\nv_mfma_f32_16x16x32_bf16 a[168:171],a[220:223],a[228:231],a[168:171]\ns_waitcnt lgkmcnt(1)\n"
"s_barrier\nv_mfma_f32_16x16x32_bf16 a[116:119],a[212:215],a[232:235],a[116:119]\nbuffer_load_dwordx4 v228,s[48:51],s74 offen offset:1024 nt lds\ns_mov_b32 s75,293120\nv_mfma_f32_16x16x32_bf16 a[144:147],a[216:219],a[232:235],a[144:147]\nds_read_b128 a[200:203],v235 offset:6144\nv_mfma_f32_16x16x32_bf16 a[172:175],a[220:223],a[232:235],a[172:175]\nds_write_b32 v248,v250 offset:57344\n"
"v_mfma_f32_16x16x32_bf16 a[120:123],a[212:215],a[236:239],a[120:123]\nbuffer_load_dwordx4 v227,s[48:51],s75 offen offset:2048 nt lds\ns_mov_b32 s72,300288\nv_mfma_f32_16x16x32_bf16 a[148:151],a[216:219],a[236:239],a[148:151]\nds_write_b32 v248,v250 offset:57600\nv_mfma_f32_16x16x32_bf16 a[176:179],a[220:223],a[236:239],a[176:179]\nv_mfma_f32_16x16x32_bf16 a[124:127],a[212:215],a[240:243],a[124:127]\nbuffer_load_dwordx4 v228,s[48:51],s72 offen offset:3072 nt lds\n"
"v_mfma_f32_16x16x32_bf16 a[152:155],a[216:219],a[240:243],a[152:155]\ns_waitcnt vmcnt(12)\nds_read_b128 a[204:207],v235 offset:8192\nv_mfma_f32_16x16x32_bf16 a[180:183],a[220:223],a[240:243],a[180:183]\nds_write_b64 v241,v[112:113] offset:0\nv_mfma_f32_16x16x32_bf16 a[128:131],a[212:215],a[244:247],a[128:131]\nds_read_b128 a[208:211],v235 offset:10240\nv_mfma_f32_16x16x32_bf16 a[156:159],a[216:219],a[244:247],a[156:159]\n"
"ds_write_b64 v241,v[126:127] offset:512\nv_mfma_f32_16x16x32_bf16 a[184:187],a[220:223],a[244:247],a[184:187]\nv_mfma_f32_16x16x32_bf16 a[132:135],a[212:215],a[248:251],a[132:135]\nds_write_b64 v241,v[196:197] offset:1024\nv_mfma_f32_16x16x32_bf16 a[160:163],a[216:219],a[248:251],a[160:163]\nv_mfma_f32_16x16x32_bf16 a[188:191],a[220:223],a[248:251],a[188:191]\nds_write_b64 v241,v[210:211] offset:1536\nv_mfma_f32_16x16x32_bf16 a[136:139],a[212:215],a[252:255],a[136:139]\n"
"v_mfma_f32_16x16x32_bf16 a[164:167],a[216:219],a[252:255],a[164:167]\nds_write_b64 v241,v[114:115] offset:2048\nv_mfma_f32_16x16x32_bf16 a[192:195],a[220:223],a[252:255],a[192:195]\nds_write_b64 v241,v[128:129] offset:2560\nds_write_b64 v241,v[198:199] offset:3072\nds_write_b64 v241,v[212:213] offset:3584\nds_write_b64 v241,v[116:117] offset:4096\nds_write_b64 v241,v[130:131] offset:4608\n"
"ds_write_b64 v241,v[200:201] offset:5120\nds_write_b64 v241,v[214:215] offset:5632\nds_write_b64 v241,v[118:119] offset:6144\nds_write_b64 v241,v[132:133] offset:6656\nds_write_b64 v241,v[202:203] offset:7168\nds_write_b64 v241,v[216:217] offset:7680\nds_write_b64 v241,v[120:121] offset:8192\nds_write_b64 v241,v[134:135] offset:8704\n"
"ds_write_b64 v241,v[204:205] offset:9216\nds_write_b64 v241,v[218:219] offset:9728\nds_write_b64 v241,v[122:123] offset:10240\nds_write_b64 v241,v[136:137] offset:10752\nds_write_b64 v241,v[206:207] offset:11264\nds_write_b64 v241,v[220:221] offset:11776\nds_write_b64 v241,v[124:125] offset:12288\nds_write_b64 v241,v[138:139] offset:12800\n"
"ds_write_b64 v241,v[208:209] offset:13312\nds_write_b64 v241,v[222:223] offset:13824\ns_waitcnt lgkmcnt(0)\ns_barrier\nds_read_b128 a[228:231],v239 offset:0\nds_read_b128 a[232:235],v239 offset:2048\nds_read_b128 a[236:239],v239 offset:4096\nds_read_b128 a[240:243],v239 offset:6144\n"
"ds_read_b128 a[244:247],v239 offset:8192\nds_read_b128 a[248:251],v239 offset:10240\nds_read_b128 a[252:255],v239 offset:12288\ns_waitcnt lgkmcnt(6)\nv_mfma_f32_16x16x32_bf16 v[0:3],a[196:199],a[228:231],0\nds_read_b128 a[212:215],v236 offset:4096\nv_mfma_f32_16x16x32_bf16 v[28:31],a[200:203],a[228:231],0\nv_mfma_f32_16x16x32_bf16 v[56:59],a[204:207],a[228:231],0\n"
"v_mfma_f32_16x16x32_bf16 v[84:87],a[208:211],a[228:231],0\nds_read_b128 a[228:231],v239 offset:1024\ns_waitcnt lgkmcnt(7)\nv_mfma_f32_16x16x32_bf16 v[4:7],a[196:199],a[232:235],0\nds_read_b128 a[216:219],v236 offset:6144\ns_add_u32 m0,s64,4096\nv_mfma_f32_16x16x32_bf16 v[32:35],a[200:203],a[232:235],0\ns_mov_b32 s73,327936\n"
"v_mfma_f32_16x16x32_bf16 v[60:63],a[204:207],a[232:235],0\nv_mfma_f32_16x16x32_bf16 v[88:91],a[208:211],a[232:235],0\nds_read_b128 a[232:235],v239 offset:3072\ns_waitcnt lgkmcnt(8)\nv_mfma_f32_16x16x32_bf16 v[8:11],a[196:199],a[236:239],0\nv_mfma_f32_16x16x32_bf16 v[36:39],a[200:203],a[236:239],0\nv_mfma_f32_16x16x32_bf16 v[64:67],a[204:207],a[236:239],0\nv_mfma_f32_16x16x32_bf16 v[92:95],a[208:211],a[236:239],0\n"
"ds_read_b128 a[236:239],v239 offset:5120\ns_waitcnt lgkmcnt(7)\nv_mfma_f32_16x16x32_bf16 v[12:15],a[196:199],a[240:243],0\nds_read_b128 a[220:223],v236 offset:8192\nv_mfma_f32_16x16x32_bf16 v[40:43],a[200:203],a[240:243],0\nv_mfma_f32_16x16x32_bf16 v[68:71],a[204:207],a[240:243],0\nv_mfma_f32_16x16x32_bf16 v[96:99],a[208:211],a[240:243],0\nds_read_b128 a[240:243],v239 offset:7168\n"
"v_mfma_f32_16x16x32_bf16 v[16:19],a[196:199],a[244:247],0\nds_read_b128 a[224:227],v236 offset:10240\nv_mfma_f32_16x16x32_bf16 v[44:47],a[200:203],a[244:247],0\nv_mfma_f32_16x16x32_bf16 v[72:75],a[204:207],a[244:247],0\nv_mfma_f32_16x16x32_bf16 v[100:103],a[208:211],a[244:247],0\nds_read_b128 a[244:247],v239 offset:9216\ns_waitcnt lgkmcnt(9)\nv_mfma_f32_16x16x32_bf16 v[20:23],a[196:199],a[248:251],0\n"
"v_mfma_f32_16x16x32_bf16 v[48:51],a[200:203],a[248:251],0\nv_mfma_f32_16x16x32_bf16 v[76:79],a[204:207],a[248:251],0\nv_mfma_f32_16x16x32_bf16 v[104:107],a[208:211],a[248:251],0\nds_read_b128 a[248:251],v239 offset:11264\nv_mfma_f32_16x16x32_bf16 v[24:27],a[196:199],a[252:255],0\nv_mfma_f32_16x16x32_bf16 v[52:55],a[200:203],a[252:255],0\nv_mfma_f32_16x16x32_bf16 v[80:83],a[204:207],a[252:255],0\nv_mfma_f32_16x16x32_bf16 v[108:111],a[208:211],a[252:255],0\n"
"ds_read_b128 a[252:255],v239 offset:13312\ns_waitcnt lgkmcnt(3)\nv_mfma_f32_16x16x32_bf16 v[0:3],a[212:215],a[228:231],v[0:3]\nbuffer_load_dwordx4 v227,s[48:51],s73 offen offset:0 nt lds\ns_mov_b32 s74,335104\nv_mfma_f32_16x16x32_bf16 v[28:31],a[216:219],a[228:231],v[28:31]\ns_waitcnt vmcnt(9)\nds_read_b128 a[196:199],v235 offset:12288\n"
"v_mfma_f32_16x16x32_bf16 v[56:59],a[220:223],a[228:231],v[56:59]\nv_mfma_f32_16x16x32_bf16 v[84:87],a[224:227],a[228:231],v[84:87]\nds_read_b128 a[228:231],v239 offset:14336\nv_mfma_f32_16x16x32_bf16 v[4:7],a[212:215],a[232:235],v[4:7]\nbuffer_load_dwordx4 v228,s[48:51],s74 offen offset:1024 nt lds\ns_mov_b32 s75,358656\nv_mfma_f32_16x16x32_bf16 v[32:35],a[216:219],a[232:235],v[32:35]\nds_read_b128 a[200:203],v235 offset:14336\n"
"v_mfma_f32_16x16x32_bf16 v[60:63],a[220:223],a[232:235],v[60:63]\nv_mfma_f32_16x16x32_bf16 v[88:91],a[224:227],a[232:235],v[88:91]\nds_read_b128 a[232:235],v239 offset:16384\nv_mfma_f32_16x16x32_bf16 v[8:11],a[212:215],a[236:239],v[8:11]\nbuffer_load_dwordx4 v227,s[48:51],s75 offen offset:2048 nt lds\ns_mov_b32 s72,365824\nv_mfma_f32_16x16x32_bf16 v[36:39],a[216:219],a[236:239],v[36:39]\nv_mfma_f32_16x16x32_bf16 v[64:67],a[220:223],a[236:239],v[64:67]\n"
"v_mfma_f32_16x16x32_bf16 v[92:95],a[224:227],a[236:239],v[92:95]\nds_read_b128 a[236:239],v239 offset:18432\ns_waitcnt lgkmcnt(7)\nv_mfma_f32_16x16x32_bf16 v[12:15],a[212:215],a[240:243],v[12:15]\nbuffer_load_dwordx4 v228,s[48:51],s72 offen offset:3072 nt lds\ns_add_u32 m0,s64,8192\nv_mfma_f32_16x16x32_bf16 v[40:43],a[216:219],a[240:243],v[40:43]\ns_waitcnt vmcnt(8)\n"
"ds_read_b128 a[204:207],v235 offset:16384\nv_mfma_f32_16x16x32_bf16 v[68:71],a[220:223],a[240:243],v[68:71]\ns_mov_b32 s73,262528\nv_mfma_f32_16x16x32_bf16 v[96:99],a[224:227],a[240:243],v[96:99]\nds_read_b128 a[240:243],v239 offset:20480\nv_mfma_f32_16x16x32_bf16 v[16:19],a[212:215],a[244:247],v[16:19]\nbuffer_load_dwordx4 v227,s[48:51],s73 offen offset:0 nt lds\ns_mov_b32 s74,269696\n"
"v_mfma_f32_16x16x32_bf16 v[44:47],a[216:219],a[244:247],v[44:47]\nds_read_b128 a[208:211],v235 offset:18432\nv_mfma_f32_16x16x32_bf16 v[72:75],a[220:223],a[244:247],v[72:75]\nv_mfma_f32_16x16x32_bf16 v[100:103],a[224:227],a[244:247],v[100:103]\nds_read_b128 a[244:247],v239 offset:22528\ns_waitcnt lgkmcnt(9)\nv_mfma_f32_16x16x32_bf16 v[20:23],a[212:215],a[248:251],v[20:23]\nbuffer_load_dwordx4 v228,s[48:51],s74 offen offset:1024 nt lds\n"
"s_mov_b32 s75,293248\nv_mfma_f32_16x16x32_bf16 v[48:51],a[216:219],a[248:251],v[48:51]\nv_mfma_f32_16x16x32_bf16 v[76:79],a[220:223],a[248:251],v[76:79]\nv_mfma_f32_16x16x32_bf16 v[104:107],a[224:227],a[248:251],v[104:107]\nds_read_b128 a[248:251],v239 offset:24576\nv_mfma_f32_16x16x32_bf16 v[24:27],a[212:215],a[252:255],v[24:27]\nbuffer_load_dwordx4 v227,s[48:51],s75 offen offset:2048 nt lds\ns_mov_b32 s72,300416\n"
"v_mfma_f32_16x16x32_bf16 v[52:55],a[216:219],a[252:255],v[52:55]\nv_mfma_f32_16x16x32_bf16 v[80:83],a[220:223],a[252:255],v[80:83]\nv_mfma_f32_16x16x32_bf16 v[108:111],a[224:227],a[252:255],v[108:111]\nds_read_b128 a[252:255],v239 offset:26624\ns_waitcnt lgkmcnt(3)\nv_mfma_f32_16x16x32_bf16 v[0:3],a[196:199],a[228:231],v[0:3]\nbuffer_load_dwordx4 v228,s[48:51],s72 offen offset:3072 nt lds\nv_mfma_f32_16x16x32_bf16 v[28:31],a[200:203],a[228:231],v[28:31]\n"
"ds_read_b128 a[212:215],v236 offset:12288\nv_mfma_f32_16x16x32_bf16 v[56:59],a[204:207],a[228:231],v[56:59]\nv_mfma_f32_16x16x32_bf16 v[84:87],a[208:211],a[228:231],v[84:87]\nds_read_b128 a[228:231],v239 offset:15360\nv_mfma_f32_16x16x32_bf16 v[4:7],a[196:199],a[232:235],v[4:7]\nds_read_b128 a[216:219],v236 offset:14336\ns_add_u32 m0,s64,12288\nv_mfma_f32_16x16x32_bf16 v[32:35],a[200:203],a[232:235],v[32:35]\n"
"s_mov_b32 s73,328064\nv_mfma_f32_16x16x32_bf16 v[60:63],a[204:207],a[232:235],v[60:63]\nv_mfma_f32_16x16x32_bf16 v[88:91],a[208:211],a[232:235],v[88:91]\nds_read_b128 a[232:235],v239 offset:17408\nv_mfma_f32_16x16x32_bf16 v[8:11],a[196:199],a[236:239],v[8:11]\nv_mfma_f32_16x16x32_bf16 v[36:39],a[200:203],a[236:239],v[36:39]\nv_mfma_f32_16x16x32_bf16 v[64:67],a[204:207],a[236:239],v[64:67]\nv_mfma_f32_16x16x32_bf16 v[92:95],a[208:211],a[236:239],v[92:95]\n"
"ds_read_b128 a[236:239],v239 offset:19456\ns_waitcnt lgkmcnt(7)\nv_mfma_f32_16x16x32_bf16 v[12:15],a[196:199],a[240:243],v[12:15]\nds_read_b128 a[220:223],v236 offset:16384\nv_mfma_f32_16x16x32_bf16 v[40:43],a[200:203],a[240:243],v[40:43]\nv_mfma_f32_16x16x32_bf16 v[68:71],a[204:207],a[240:243],v[68:71]\nv_mfma_f32_16x16x32_bf16 v[96:99],a[208:211],a[240:243],v[96:99]\nds_read_b128 a[240:243],v239 offset:21504\n"
"v_mfma_f32_16x16x32_bf16 v[16:19],a[196:199],a[244:247],v[16:19]\nds_read_b128 a[224:227],v236 offset:18432\nv_mfma_f32_16x16x32_bf16 v[44:47],a[200:203],a[244:247],v[44:47]\nv_mfma_f32_16x16x32_bf16 v[72:75],a[204:207],a[244:247],v[72:75]\nv_mfma_f32_16x16x32_bf16 v[100:103],a[208:211],a[244:247],v[100:103]\nds_read_b128 a[244:247],v239 offset:23552\ns_waitcnt lgkmcnt(9)\nv_mfma_f32_16x16x32_bf16 v[20:23],a[196:199],a[248:251],v[20:23]\n"
"v_mfma_f32_16x16x32_bf16 v[48:51],a[200:203],a[248:251],v[48:51]\nv_mfma_f32_16x16x32_bf16 v[76:79],a[204:207],a[248:251],v[76:79]\nv_mfma_f32_16x16x32_bf16 v[104:107],a[208:211],a[248:251],v[104:107]\nds_read_b128 a[248:251],v239 offset:25600\nv_mfma_f32_16x16x32_bf16 v[24:27],a[196:199],a[252:255],v[24:27]\nv_mfma_f32_16x16x32_bf16 v[52:55],a[200:203],a[252:255],v[52:55]\nv_mfma_f32_16x16x32_bf16 v[80:83],a[204:207],a[252:255],v[80:83]\nv_mfma_f32_16x16x32_bf16 v[108:111],a[208:211],a[252:255],v[108:111]\n"
"ds_read_b128 a[252:255],v239 offset:27648\ns_waitcnt lgkmcnt(3)\nv_mfma_f32_16x16x32_bf16 v[0:3],a[212:215],a[228:231],v[0:3]\nbuffer_load_dwordx4 v227,s[48:51],s73 offen offset:0 nt lds\ns_mov_b32 s74,335232\nv_mfma_f32_16x16x32_bf16 v[28:31],a[216:219],a[228:231],v[28:31]\ns_waitcnt vmcnt(9)\nds_read_b128 a[196:199],v235 offset:0\n"
"v_mfma_f32_16x16x32_bf16 v[56:59],a[220:223],a[228:231],v[56:59]\nv_mfma_f32_16x16x32_bf16 v[84:87],a[224:227],a[228:231],v[84:87]\nds_read_b128 a[228:231],v239 offset:28672\nv_mfma_f32_16x16x32_bf16 v[4:7],a[212:215],a[232:235],v[4:7]\nbuffer_load_dwordx4 v228,s[48:51],s74 offen offset:1024 nt lds\ns_mov_b32 s75,358784\nv_mfma_f32_16x16x32_bf16 v[32:35],a[216:219],a[232:235],v[32:35]\nds_read_b128 a[200:203],v235 offset:2048\n"
"v_mfma_f32_16x16x32_bf16 v[60:63],a[220:223],a[232:235],v[60:63]\nv_mfma_f32_16x16x32_bf16 v[88:91],a[224:227],a[232:235],v[88:91]\nds_read_b128 a[232:235],v239 offset:30720\nv_mfma_f32_16x16x32_bf16 v[8:11],a[212:215],a[236:239],v[8:11]\nbuffer_load_dwordx4 v227,s[48:51],s75 offen offset:2048 nt lds\ns_mov_b32 s72,365952\nv_mfma_f32_16x16x32_bf16 v[36:39],a[216:219],a[236:239],v[36:39]\nv_mfma_f32_16x16x32_bf16 v[64:67],a[220:223],a[236:239],v[64:67]\n"
"v_mfma_f32_16x16x32_bf16 v[92:95],a[224:227],a[236:239],v[92:95]\nds_read_b128 a[236:239],v239 offset:32768\ns_waitcnt lgkmcnt(7)\nv_mfma_f32_16x16x32_bf16 v[12:15],a[212:215],a[240:243],v[12:15]\nbuffer_load_dwordx4 v228,s[48:51],s72 offen offset:3072 nt lds\ns_add_u32 m0,s64,16384\nv_mfma_f32_16x16x32_bf16 v[40:43],a[216:219],a[240:243],v[40:43]\ns_waitcnt vmcnt(8)\n"
"ds_read_b128 a[204:207],v235 offset:4096\nv_mfma_f32_16x16x32_bf16 v[68:71],a[220:223],a[240:243],v[68:71]\ns_mov_b32 s73,393216\nv_mfma_f32_16x16x32_bf16 v[96:99],a[224:227],a[240:243],v[96:99]\nds_read_b128 a[240:243],v239 offset:34816\nv_mfma_f32_16x16x32_bf16 v[16:19],a[212:215],a[244:247],v[16:19]\nbuffer_load_dwordx4 v227,s[48:51],s73 offen offset:0 nt lds\ns_mov_b32 s74,400384\n"
"v_mfma_f32_16x16x32_bf16 v[44:47],a[216:219],a[244:247],v[44:47]\nds_read_b128 a[208:211],v235 offset:6144\nv_mfma_f32_16x16x32_bf16 v[72:75],a[220:223],a[244:247],v[72:75]\nv_mfma_f32_16x16x32_bf16 v[100:103],a[224:227],a[244:247],v[100:103]\nds_read_b128 a[244:247],v239 offset:36864\ns_waitcnt lgkmcnt(9)\nv_mfma_f32_16x16x32_bf16 v[20:23],a[212:215],a[248:251],v[20:23]\nbuffer_load_dwordx4 v228,s[48:51],s74 offen offset:1024 nt lds\n"
"s_mov_b32 s75,423936\nv_mfma_f32_16x16x32_bf16 v[48:51],a[216:219],a[248:251],v[48:51]\nv_mfma_f32_16x16x32_bf16 v[76:79],a[220:223],a[248:251],v[76:79]\nv_mfma_f32_16x16x32_bf16 v[104:107],a[224:227],a[248:251],v[104:107]\nds_read_b128 a[248:251],v239 offset:38912\nv_mfma_f32_16x16x32_bf16 v[24:27],a[212:215],a[252:255],v[24:27]\nbuffer_load_dwordx4 v227,s[48:51],s75 offen offset:2048 nt lds\ns_mov_b32 s72,431104\n"
"v_mfma_f32_16x16x32_bf16 v[52:55],a[216:219],a[252:255],v[52:55]\nv_mfma_f32_16x16x32_bf16 v[80:83],a[220:223],a[252:255],v[80:83]\nv_mfma_f32_16x16x32_bf16 v[108:111],a[224:227],a[252:255],v[108:111]\nds_read_b128 a[252:255],v239 offset:40960\ns_waitcnt lgkmcnt(3)\nv_mfma_f32_16x16x32_bf16 v[0:3],a[196:199],a[228:231],v[0:3]\nbuffer_load_dwordx4 v228,s[48:51],s72 offen offset:3072 nt lds\nv_mfma_f32_16x16x32_bf16 v[28:31],a[200:203],a[228:231],v[28:31]\n"
"ds_read_b128 a[212:215],v236 offset:0\nv_mfma_f32_16x16x32_bf16 v[56:59],a[204:207],a[228:231],v[56:59]\nv_mfma_f32_16x16x32_bf16 v[84:87],a[208:211],a[228:231],v[84:87]\nds_read_b128 a[228:231],v239 offset:29696\nv_mfma_f32_16x16x32_bf16 v[4:7],a[196:199],a[232:235],v[4:7]\nds_read_b128 a[216:219],v236 offset:2048\ns_add_u32 m0,s64,0\nv_mfma_f32_16x16x32_bf16 v[32:35],a[200:203],a[232:235],v[32:35]\n"
"s_mov_b32 s73,458752\nv_mfma_f32_16x16x32_bf16 v[60:63],a[204:207],a[232:235],v[60:63]\nv_mfma_f32_16x16x32_bf16 v[88:91],a[208:211],a[232:235],v[88:91]\nds_read_b128 a[232:235],v239 offset:31744\nv_mfma_f32_16x16x32_bf16 v[8:11],a[196:199],a[236:239],v[8:11]\nv_mfma_f32_16x16x32_bf16 v[36:39],a[200:203],a[236:239],v[36:39]\nv_mfma_f32_16x16x32_bf16 v[64:67],a[204:207],a[236:239],v[64:67]\nv_mfma_f32_16x16x32_bf16 v[92:95],a[208:211],a[236:239],v[92:95]\n"
"ds_read_b128 a[236:239],v239 offset:33792\ns_waitcnt lgkmcnt(7)\nv_mfma_f32_16x16x32_bf16 v[12:15],a[196:199],a[240:243],v[12:15]\nds_read_b128 a[220:223],v236 offset:4096\nv_mfma_f32_16x16x32_bf16 v[40:43],a[200:203],a[240:243],v[40:43]\nv_mfma_f32_16x16x32_bf16 v[68:71],a[204:207],a[240:243],v[68:71]\nv_mfma_f32_16x16x32_bf16 v[96:99],a[208:211],a[240:243],v[96:99]\nds_read_b128 a[240:243],v239 offset:35840\n"
"v_mfma_f32_16x16x32_bf16 v[16:19],a[196:199],a[244:247],v[16:19]\nds_read_b128 a[224:227],v236 offset:6144\nv_mfma_f32_16x16x32_bf16 v[44:47],a[200:203],a[244:247],v[44:47]\nv_mfma_f32_16x16x32_bf16 v[72:75],a[204:207],a[244:247],v[72:75]\nv_mfma_f32_16x16x32_bf16 v[100:103],a[208:211],a[244:247],v[100:103]\nds_read_b128 a[244:247],v239 offset:37888\ns_waitcnt lgkmcnt(9)\nv_mfma_f32_16x16x32_bf16 v[20:23],a[196:199],a[248:251],v[20:23]\n"
"v_mfma_f32_16x16x32_bf16 v[48:51],a[200:203],a[248:251],v[48:51]\nv_mfma_f32_16x16x32_bf16 v[76:79],a[204:207],a[248:251],v[76:79]\nv_mfma_f32_16x16x32_bf16 v[104:107],a[208:211],a[248:251],v[104:107]\nds_read_b128 a[248:251],v239 offset:39936\nv_mfma_f32_16x16x32_bf16 v[24:27],a[196:199],a[252:255],v[24:27]\nv_mfma_f32_16x16x32_bf16 v[52:55],a[200:203],a[252:255],v[52:55]\nv_mfma_f32_16x16x32_bf16 v[80:83],a[204:207],a[252:255],v[80:83]\nv_mfma_f32_16x16x32_bf16 v[108:111],a[208:211],a[252:255],v[108:111]\n"
"ds_read_b128 a[252:255],v239 offset:41984\ns_waitcnt lgkmcnt(3)\nv_mfma_f32_16x16x32_bf16 v[0:3],a[212:215],a[228:231],v[0:3]\nbuffer_load_dwordx4 v227,s[48:51],s73 offen offset:0 nt lds\ns_mov_b32 s74,465920\nv_mfma_f32_16x16x32_bf16 v[28:31],a[216:219],a[228:231],v[28:31]\ns_waitcnt vmcnt(9)\nds_read_b128 a[196:199],v235 offset:8192\n"
"v_mfma_f32_16x16x32_bf16 v[56:59],a[220:223],a[228:231],v[56:59]\nv_mfma_f32_16x16x32_bf16 v[84:87],a[224:227],a[228:231],v[84:87]\nds_read_b128 a[228:231],v239 offset:43008\nv_mfma_f32_16x16x32_bf16 v[4:7],a[212:215],a[232:235],v[4:7]\nbuffer_load_dwordx4 v228,s[48:51],s74 offen offset:1024 nt lds\ns_mov_b32 s75,489472\nv_mfma_f32_16x16x32_bf16 v[32:35],a[216:219],a[232:235],v[32:35]\nds_read_b128 a[200:203],v235 offset:10240\n"
"v_mfma_f32_16x16x32_bf16 v[60:63],a[220:223],a[232:235],v[60:63]\nv_mfma_f32_16x16x32_bf16 v[88:91],a[224:227],a[232:235],v[88:91]\nds_read_b128 a[232:235],v239 offset:45056\nv_mfma_f32_16x16x32_bf16 v[8:11],a[212:215],a[236:239],v[8:11]\nbuffer_load_dwordx4 v227,s[48:51],s75 offen offset:2048 nt lds\ns_mov_b32 s72,496640\nv_mfma_f32_16x16x32_bf16 v[36:39],a[216:219],a[236:239],v[36:39]\nv_mfma_f32_16x16x32_bf16 v[64:67],a[220:223],a[236:239],v[64:67]\n"
"v_mfma_f32_16x16x32_bf16 v[92:95],a[224:227],a[236:239],v[92:95]\nds_read_b128 a[236:239],v239 offset:47104\ns_waitcnt lgkmcnt(7)\nv_mfma_f32_16x16x32_bf16 v[12:15],a[212:215],a[240:243],v[12:15]\nbuffer_load_dwordx4 v228,s[48:51],s72 offen offset:3072 nt lds\ns_add_u32 m0,s64,4096\nv_mfma_f32_16x16x32_bf16 v[40:43],a[216:219],a[240:243],v[40:43]\ns_waitcnt vmcnt(8)\n"
"ds_read_b128 a[204:207],v235 offset:12288\nv_mfma_f32_16x16x32_bf16 v[68:71],a[220:223],a[240:243],v[68:71]\ns_mov_b32 s73,393344\nv_mfma_f32_16x16x32_bf16 v[96:99],a[224:227],a[240:243],v[96:99]\nds_read_b128 a[240:243],v239 offset:49152\nv_mfma_f32_16x16x32_bf16 v[16:19],a[212:215],a[244:247],v[16:19]\nbuffer_load_dwordx4 v227,s[48:51],s73 offen offset:0 nt lds\ns_mov_b32 s74,400512\n"
"v_mfma_f32_16x16x32_bf16 v[44:47],a[216:219],a[244:247],v[44:47]\nds_read_b128 a[208:211],v235 offset:14336\nv_mfma_f32_16x16x32_bf16 v[72:75],a[220:223],a[244:247],v[72:75]\nv_mfma_f32_16x16x32_bf16 v[100:103],a[224:227],a[244:247],v[100:103]\nds_read_b128 a[244:247],v239 offset:51200\ns_waitcnt lgkmcnt(9)\nv_mfma_f32_16x16x32_bf16 v[20:23],a[212:215],a[248:251],v[20:23]\nbuffer_load_dwordx4 v228,s[48:51],s74 offen offset:1024 nt lds\n"
"s_mov_b32 s75,424064\nv_mfma_f32_16x16x32_bf16 v[48:51],a[216:219],a[248:251],v[48:51]\nv_mfma_f32_16x16x32_bf16 v[76:79],a[220:223],a[248:251],v[76:79]\nv_mfma_f32_16x16x32_bf16 v[104:107],a[224:227],a[248:251],v[104:107]\nds_read_b128 a[248:251],v239 offset:53248\nv_mfma_f32_16x16x32_bf16 v[24:27],a[212:215],a[252:255],v[24:27]\nbuffer_load_dwordx4 v227,s[48:51],s75 offen offset:2048 nt lds\ns_mov_b32 s72,431232\n"
"v_mfma_f32_16x16x32_bf16 v[52:55],a[216:219],a[252:255],v[52:55]\nv_mfma_f32_16x16x32_bf16 v[80:83],a[220:223],a[252:255],v[80:83]\nv_mfma_f32_16x16x32_bf16 v[108:111],a[224:227],a[252:255],v[108:111]\nds_read_b128 a[252:255],v239 offset:55296\ns_waitcnt lgkmcnt(3)\nv_mfma_f32_16x16x32_bf16 v[0:3],a[196:199],a[228:231],v[0:3]\nbuffer_load_dwordx4 v228,s[48:51],s72 offen offset:3072 nt lds\nv_mfma_f32_16x16x32_bf16 v[28:31],a[200:203],a[228:231],v[28:31]\n"
"ds_read_b128 a[212:215],v236 offset:8192\nv_mfma_f32_16x16x32_bf16 v[56:59],a[204:207],a[228:231],v[56:59]\nv_mfma_f32_16x16x32_bf16 v[84:87],a[208:211],a[228:231],v[84:87]\nds_read_b128 a[228:231],v239 offset:44032\nv_mfma_f32_16x16x32_bf16 v[4:7],a[196:199],a[232:235],v[4:7]\nds_read_b128 a[216:219],v236 offset:10240\ns_add_u32 m0,s64,8192\nv_mfma_f32_16x16x32_bf16 v[32:35],a[200:203],a[232:235],v[32:35]\n"
"s_mov_b32 s73,458880\nv_mfma_f32_16x16x32_bf16 v[60:63],a[204:207],a[232:235],v[60:63]\nv_mfma_f32_16x16x32_bf16 v[88:91],a[208:211],a[232:235],v[88:91]\nds_read_b128 a[232:235],v239 offset:46080\nv_mfma_f32_16x16x32_bf16 v[8:11],a[196:199],a[236:239],v[8:11]\nv_mfma_f32_16x16x32_bf16 v[36:39],a[200:203],a[236:239],v[36:39]\nv_mfma_f32_16x16x32_bf16 v[64:67],a[204:207],a[236:239],v[64:67]\nv_mfma_f32_16x16x32_bf16 v[92:95],a[208:211],a[236:239],v[92:95]\n"
"ds_read_b128 a[236:239],v239 offset:48128\ns_waitcnt lgkmcnt(7)\nv_mfma_f32_16x16x32_bf16 v[12:15],a[196:199],a[240:243],v[12:15]\nds_read_b128 a[220:223],v236 offset:12288\nv_mfma_f32_16x16x32_bf16 v[40:43],a[200:203],a[240:243],v[40:43]\nv_mfma_f32_16x16x32_bf16 v[68:71],a[204:207],a[240:243],v[68:71]\nv_mfma_f32_16x16x32_bf16 v[96:99],a[208:211],a[240:243],v[96:99]\nds_read_b128 a[240:243],v239 offset:50176\n"
"v_mfma_f32_16x16x32_bf16 v[16:19],a[196:199],a[244:247],v[16:19]\nds_read_b128 a[224:227],v236 offset:14336\nv_mfma_f32_16x16x32_bf16 v[44:47],a[200:203],a[244:247],v[44:47]\nv_mfma_f32_16x16x32_bf16 v[72:75],a[204:207],a[244:247],v[72:75]\nv_mfma_f32_16x16x32_bf16 v[100:103],a[208:211],a[244:247],v[100:103]\nds_read_b128 a[244:247],v239 offset:52224\ns_waitcnt lgkmcnt(9)\nv_mfma_f32_16x16x32_bf16 v[20:23],a[196:199],a[248:251],v[20:23]\n"
"v_mfma_f32_16x16x32_bf16 v[48:51],a[200:203],a[248:251],v[48:51]\nv_mfma_f32_16x16x32_bf16 v[76:79],a[204:207],a[248:251],v[76:79]\nv_mfma_f32_16x16x32_bf16 v[104:107],a[208:211],a[248:251],v[104:107]\nds_read_b128 a[248:251],v239 offset:54272\nv_mfma_f32_16x16x32_bf16 v[24:27],a[196:199],a[252:255],v[24:27]\nv_mfma_f32_16x16x32_bf16 v[52:55],a[200:203],a[252:255],v[52:55]\nv_mfma_f32_16x16x32_bf16 v[80:83],a[204:207],a[252:255],v[80:83]\nv_mfma_f32_16x16x32_bf16 v[108:111],a[208:211],a[252:255],v[108:111]\n"
"ds_read_b128 a[252:255],v239 offset:56320\ns_waitcnt lgkmcnt(3)\nv_mfma_f32_16x16x32_bf16 v[0:3],a[212:215],a[228:231],v[0:3]\nbuffer_load_dwordx4 v227,s[48:51],s73 offen offset:0 nt lds\nv_bfe_u32 v255,v239,2,6\nv_mfma_f32_16x16x32_bf16 v[28:31],a[216:219],a[228:231],v[28:31]\ns_waitcnt vmcnt(9)\nds_read_b128 a[196:199],v235 offset:16384\n"
"v_mfma_f32_16x16x32_bf16 v[56:59],a[220:223],a[228:231],v[56:59]\ns_mov_b32 s74,466048\nv_mfma_f32_16x16x32_bf16 v[84:87],a[224:227],a[228:231],v[84:87]\nds_read_b128 a[228:231],v239 offset:0\nv_mfma_f32_16x16x32_bf16 v[4:7],a[212:215],a[232:235],v[4:7]\nbuffer_load_dwordx4 v228,s[48:51],s74 offen offset:1024 nt lds\ns_mov_b32 s75,489600\nv_mfma_f32_16x16x32_bf16 v[32:35],a[216:219],a[232:235],v[32:35]\n"
"ds_read_b128 a[200:203],v235 offset:18432\nv_mfma_f32_16x16x32_bf16 v[60:63],a[220:223],a[232:235],v[60:63]\nv_mfma_f32_16x16x32_bf16 v[88:91],a[224:227],a[232:235],v[88:91]\nds_read_b128 a[232:235],v239 offset:2048\nv_mfma_f32_16x16x32_bf16 v[8:11],a[212:215],a[236:239],v[8:11]\nbuffer_load_dwordx4 v227,s[48:51],s75 offen offset:2048 nt lds\nv_max3_f32 v241,v0,v1,v0\nv_mfma_f32_16x16x32_bf16 v[36:39],a[216:219],a[236:239],v[36:39]\n"
"s_mov_b32 s72,496768\nv_max3_f32 v241,v2,v3,v241\nv_max3_f32 v241,v28,v29,v241\nv_max3_f32 v241,v30,v31,v241\nv_mfma_f32_16x16x32_bf16 v[64:67],a[220:223],a[236:239],v[64:67]\nv_max3_f32 v241,v56,v57,v241\nv_max3_f32 v241,v58,v59,v241\nv_mfma_f32_16x16x32_bf16 v[92:95],a[224:227],a[236:239],v[92:95]\n"
"ds_read_b128 a[236:239],v239 offset:4096\ns_waitcnt lgkmcnt(7)\nv_max3_f32 v241,v84,v85,v241\nv_mfma_f32_16x16x32_bf16 v[12:15],a[212:215],a[240:243],v[12:15]\nbuffer_load_dwordx4 v228,s[48:51],s72 offen offset:3072 nt lds\nv_max3_f32 v241,v86,v87,v241\nv_mfma_f32_16x16x32_bf16 v[40:43],a[216:219],a[240:243],v[40:43]\ns_waitcnt vmcnt(8)\n"
"ds_read_b128 a[204:207],v235 offset:0\nv_mul_f32 v241,s69,v241\nv_mfma_f32_16x16x32_bf16 v[68:71],a[220:223],a[240:243],v[68:71]\ns_add_u32 m0,s64,12288\nds_max_f32 v255,v241 offset:57344\nv_mfma_f32_16x16x32_bf16 v[96:99],a[224:227],a[240:243],v[96:99]\nds_read_b128 a[240:243],v239 offset:6144\nv_max3_f32 v242,v4,v5,v4\n"
"v_max3_f32 v242,v6,v7,v242\nv_mfma_f32_16x16x32_bf16 v[16:19],a[212:215],a[244:247],v[16:19]\ns_mov_b32 s73,393472\nbuffer_load_dwordx4 v227,s[48:51],s73 offen offset:0 nt lds\nv_mfma_f32_16x16x32_bf16 v[44:47],a[216:219],a[244:247],v[44:47]\nds_read_b128 a[208:211],v235 offset:2048\nv_max3_f32 v242,v32,v33,v242\nv_max3_f32 v242,v34,v35,v242\n"
"v_mfma_f32_16x16x32_bf16 v[72:75],a[220:223],a[244:247],v[72:75]\ns_mov_b32 s74,400640\nv_max3_f32 v242,v60,v61,v242\nv_max3_f32 v242,v62,v63,v242\nv_mfma_f32_16x16x32_bf16 v[100:103],a[224:227],a[244:247],v[100:103]\nds_read_b128 a[244:247],v239 offset:8192\ns_waitcnt lgkmcnt(10)\nv_max3_f32 v242,v88,v89,v242\n"
"v_mfma_f32_16x16x32_bf16 v[20:23],a[212:215],a[248:251],v[20:23]\nbuffer_load_dwordx4 v228,s[48:51],s74 offen offset:1024 nt lds\nv_max3_f32 v242,v90,v91,v242\nv_mfma_f32_16x16x32_bf16 v[48:51],a[216:219],a[248:251],v[48:51]\ns_mov_b32 s75,424192\nv_mul_f32 v242,s69,v242\nv_max3_f32 v243,v8,v9,v8\nv_max3_f32 v243,v10,v11,v243\n"
"v_mfma_f32_16x16x32_bf16 v[76:79],a[220:223],a[248:251],v[76:79]\nds_max_f32 v255,v242 offset:57408\nv_mfma_f32_16x16x32_bf16 v[104:107],a[224:227],a[248:251],v[104:107]\nds_read_b128 a[248:251],v239 offset:10240\nv_max3_f32 v243,v36,v37,v243\nv_max3_f32 v243,v38,v39,v243\nv_mfma_f32_16x16x32_bf16 v[24:27],a[212:215],a[252:255],v[24:27]\nbuffer_load_dwordx4 v227,s[48:51],s75 offen offset:2048 nt lds\n"
"v_max3_f32 v243,v64,v65,v243\nv_mfma_f32_16x16x32_bf16 v[52:55],a[216:219],a[252:255],v[52:55]\ns_mov_b32 s72,431360\nv_max3_f32 v243,v66,v67,v243\nv_max3_f32 v243,v92,v93,v243\nv_max3_f32 v243,v94,v95,v243\nv_mfma_f32_16x16x32_bf16 v[80:83],a[220:223],a[252:255],v[80:83]\nv_mul_f32 v243,s69,v243\n"
"v_max3_f32 v244,v12,v13,v12\nv_mfma_f32_16x16x32_bf16 v[108:111],a[224:227],a[252:255],v[108:111]\nds_read_b128 a[252:255],v239 offset:12288\ns_waitcnt lgkmcnt(4)\nv_max3_f32 v244,v14,v15,v244\nv_mfma_f32_16x16x32_bf16 v[112:115],a[196:199],a[228:231],0\nbuffer_load_dwordx4 v228,s[48:51],s72 offen offset:3072 nt lds\nv_max3_f32 v244,v40,v41,v244\n"
"v_mfma_f32_16x16x32_bf16 v[140:143],a[200:203],a[228:231],0\nds_read_b128 a[212:215],v236 offset:16384\nv_max3_f32 v244,v42,v43,v244\nv_max3_f32 v244,v68,v69,v244\nv_mfma_f32_16x16x32_bf16 v[168:171],a[204:207],a[228:231],0\nds_max_f32 v255,v243 offset:57472\nv_mfma_f32_16x16x32_bf16 v[196:199],a[208:211],a[228:231],0\nds_read_b128 a[228:231],v239 offset:1024\n"
"v_max3_f32 v244,v70,v71,v244\nv_max3_f32 v244,v96,v97,v244\nv_mfma_f32_16x16x32_bf16 v[116:119],a[196:199],a[232:235],0\nds_read_b128 a[216:219],v236 offset:18432\nv_max3_f32 v244,v98,v99,v244\nv_mfma_f32_16x16x32_bf16 v[144:147],a[200:203],a[232:235],0\ns_add_u32 m0,s64,16384\nv_mul_f32 v244,s69,v244\n"
"v_max3_f32 v245,v16,v17,v16\nv_max3_f32 v245,v18,v19,v245\nv_mfma_f32_16x16x32_bf16 v[172:175],a[204:207],a[232:235],0\ns_mov_b32 s73,459008\nds_max_f32 v255,v244 offset:57536\nv_mfma_f32_16x16x32_bf16 v[200:203],a[208:211],a[232:235],0\nds_read_b128 a[232:235],v239 offset:3072\nv_max3_f32 v245,v44,v45,v245\n"
"v_max3_f32 v245,v46,v47,v245\nv_mfma_f32_16x16x32_bf16 v[120:123],a[196:199],a[236:239],0\nv_max3_f32 v245,v72,v73,v245\nv_max3_f32 v245,v74,v75,v245\nv_mfma_f32_16x16x32_bf16 v[148:151],a[200:203],a[236:239],0\nv_max3_f32 v245,v100,v101,v245\nv_max3_f32 v245,v102,v103,v245\nv_mul_f32 v245,s69,v245\n"
"v_mfma_f32_16x16x32_bf16 v[176:179],a[204:207],a[236:239],0\nds_max_f32 v255,v245 offset:57600\nv_mfma_f32_16x16x32_bf16 v[204:207],a[208:211],a[236:239],0\nds_read_b128 a[236:239],v239 offset:5120\ns_waitcnt lgkmcnt(11)\nv_max3_f32 v246,v20,v21,v20\nv_mfma_f32_16x16x32_bf16 v[124:127],a[196:199],a[240:243],0\nds_read_b128 a[220:223],v236 offset:0\n"
"v_max3_f32 v246,v22,v23,v246\nv_mfma_f32_16x16x32_bf16 v[152:155],a[200:203],a[240:243],0\nv_max3_f32 v246,v48,v49,v246\nv_max3_f32 v246,v50,v51,v246\nv_max3_f32 v246,v76,v77,v246\nv_mfma_f32_16x16x32_bf16 v[180:183],a[204:207],a[240:243],0\nv_max3_f32 v246,v78,v79,v246\nv_max3_f32 v246,v104,v105,v246\n"
"v_mfma_f32_16x16x32_bf16 v[208:211],a[208:211],a[240:243],0\nds_read_b128 a[240:243],v239 offset:7168\nv_max3_f32 v246,v106,v107,v246\nv_mul_f32 v246,s69,v246\nv_mfma_f32_16x16x32_bf16 v[128:131],a[196:199],a[244:247],0\nds_read_b128 a[224:227],v236 offset:2048\nv_max3_f32 v247,v24,v25,v24\nv_mfma_f32_16x16x32_bf16 v[156:159],a[200:203],a[244:247],0\n"
"ds_max_f32 v255,v246 offset:57664\nv_mfma_f32_16x16x32_bf16 v[184:187],a[204:207],a[244:247],0\nv_max3_f32 v247,v26,v27,v247\nv_max3_f32 v247,v52,v53,v247\nv_mfma_f32_16x16x32_bf16 v[212:215],a[208:211],a[244:247],0\nds_read_b128 a[244:247],v239 offset:9216\ns_waitcnt lgkmcnt(13)\nv_max3_f32 v247,v54,v55,v247\n"
"v_mfma_f32_16x16x32_bf16 v[132:135],a[196:199],a[248:251],0\nv_max3_f32 v247,v80,v81,v247\nv_max3_f32 v247,v82,v83,v247\nv_mfma_f32_16x16x32_bf16 v[160:163],a[200:203],a[248:251],0\nv_max3_f32 v247,v108,v109,v247\nv_max3_f32 v247,v110,v111,v247\nv_mul_f32 v247,s69,v247\nv_mfma_f32_16x16x32_bf16 v[188:191],a[204:207],a[248:251],0\n"
"ds_max_f32 v255,v247 offset:57728\nv_mfma_f32_16x16x32_bf16 v[216:219],a[208:211],a[248:251],0\nds_read_b128 a[248:251],v239 offset:11264\nv_mfma_f32_16x16x32_bf16 v[136:139],a[196:199],a[252:255],0\nv_mfma_f32_16x16x32_bf16 v[164:167],a[200:203],a[252:255],0\nv_mfma_f32_16x16x32_bf16 v[192:195],a[204:207],a[252:255],0\nv_mfma_f32_16x16x32_bf16 v[220:223],a[208:211],a[252:255],0\nds_read_b128 a[252:255],v239 offset:13312\n"
"s_waitcnt lgkmcnt(5)\ns_waitcnt lgkmcnt(1)\nv_mfma_f32_16x16x32_bf16 v[112:115],a[212:215],a[228:231],v[112:115]\nbuffer_load_dwordx4 v227,s[48:51],s73 offen offset:0 nt lds\ns_barrier\nv_mfma_f32_16x16x32_bf16 v[140:143],a[216:219],a[228:231],v[140:143]\ns_waitcnt vmcnt(9)\nds_read_b128 a[196:199],v235 offset:4096\n"
"v_mfma_f32_16x16x32_bf16 v[168:171],a[220:223],a[228:231],v[168:171]\ns_mov_b32 s74,466176\nds_read_b32 v241,v255 offset:57344\nv_mfma_f32_16x16x32_bf16 v[196:199],a[224:227],a[228:231],v[196:199]\nds_read_b128 a[228:231],v239 offset:14336\nds_read_b32 v242,v255 offset:57408\nv_mfma_f32_16x16x32_bf16 v[116:119],a[212:215],a[232:235],v[116:119]\nbuffer_load_dwordx4 v228,s[48:51],s74 offen offset:1024 nt lds\n"
"s_mov_b32 s75,489728\nv_mfma_f32_16x16x32_bf16 v[144:147],a[216:219],a[232:235],v[144:147]\nds_read_b128 a[200:203],v235 offset:6144\nds_read_b32 v243,v255 offset:57472\nv_mfma_f32_16x16x32_bf16 v[172:175],a[220:223],a[232:235],v[172:175]\nds_read_b32 v244,v255 offset:57536\nv_mfma_f32_16x16x32_bf16 v[200:203],a[224:227],a[232:235],v[200:203]\nds_read_b128 a[232:235],v239 offset:16384\n"
"ds_read_b32 v245,v255 offset:57600\nv_mfma_f32_16x16x32_bf16 v[120:123],a[212:215],a[236:239],v[120:123]\nbuffer_load_dwordx4 v227,s[48:51],s75 offen offset:2048 nt lds\ns_mov_b32 s72,496896\nv_mfma_f32_16x16x32_bf16 v[148:151],a[216:219],a[236:239],v[148:151]\nds_read_b32 v246,v255 offset:57664\nds_read_b32 v247,v255 offset:57728\nv_mfma_f32_16x16x32_bf16 v[176:179],a[220:223],a[236:239],v[176:179]\n"
"v_mfma_f32_16x16x32_bf16 v[204:207],a[224:227],a[236:239],v[204:207]\nds_read_b128 a[236:239],v239 offset:18432\nv_mfma_f32_16x16x32_bf16 v[124:127],a[212:215],a[240:243],v[124:127]\nbuffer_load_dwordx4 v228,s[48:51],s72 offen offset:3072 nt lds\ns_add_u32 m0,s64,0\nv_mfma_f32_16x16x32_bf16 v[152:155],a[216:219],a[240:243],v[152:155]\ns_waitcnt vmcnt(8)\nds_read_b128 a[204:207],v235 offset:8192\n"
"s_waitcnt lgkmcnt(2)\nv_mfma_f32_16x16x32_bf16 v[180:183],a[220:223],a[240:243],v[180:183]\ns_mov_b32 s73,393600\nv_fma_f32 v0,v0,s69,-v241\nv_fma_f32 v1,v1,s69,-v241\nv_mfma_f32_16x16x32_bf16 v[208:211],a[224:227],a[240:243],v[208:211]\nds_read_b128 a[240:243],v239 offset:20480\nv_fma_f32 v2,v2,s69,-v241\n"
"v_fma_f32 v3,v3,s69,-v241\nv_mfma_f32_16x16x32_bf16 v[128:131],a[212:215],a[244:247],v[128:131]\nbuffer_load_dwordx4 v227,s[48:51],s73 offen offset:0 nt lds\nv_fma_f32 v4,v4,s69,-v242\nv_mfma_f32_16x16x32_bf16 v[156:159],a[216:219],a[244:247],v[156:159]\nds_read_b128 a[208:211],v235 offset:10240\nv_exp_f32 v0,v0\nv_mfma_f32_16x16x32_bf16 v[184:187],a[220:223],a[244:247],v[184:187]\n"
"s_mov_b32 s74,400768\nv_exp_f32 v1,v1\nv_mfma_f32_16x16x32_bf16 v[212:215],a[224:227],a[244:247],v[212:215]\nds_read_b128 a[244:247],v239 offset:22528\nv_exp_f32 v2,v2\nv_mfma_f32_16x16x32_bf16 v[132:135],a[212:215],a[248:251],v[132:135]\nbuffer_load_dwordx4 v228,s[48:51],s74 offen offset:1024 nt lds\nv_cvt_pk_bf16_f32 v0,v0,v1\n"
"v_mfma_f32_16x16x32_bf16 v[160:163],a[216:219],a[248:251],v[160:163]\ns_mov_b32 s75,424320\nv_exp_f32 v3,v3\nv_fma_f32 v5,v5,s69,-v242\nv_mfma_f32_16x16x32_bf16 v[188:191],a[220:223],a[248:251],v[188:191]\nv_cvt_pk_bf16_f32 v1,v2,v3\nv_fma_f32 v6,v6,s69,-v242\nv_mfma_f32_16x16x32_bf16 v[216:219],a[224:227],a[248:251],v[216:219]\n"
"ds_read_b128 a[248:251],v239 offset:24576\nv_fma_f32 v7,v7,s69,-v242\nv_fma_f32 v8,v8,s69,-v243\nv_mfma_f32_16x16x32_bf16 v[136:139],a[212:215],a[252:255],v[136:139]\nbuffer_load_dwordx4 v227,s[48:51],s75 offen offset:2048 nt lds\nv_fma_f32 v9,v9,s69,-v243\nv_mfma_f32_16x16x32_bf16 v[164:167],a[216:219],a[252:255],v[164:167]\ns_mov_b32 s72,431488\n"
"v_exp_f32 v4,v4\nv_fma_f32 v10,v10,s69,-v243\nv_mfma_f32_16x16x32_bf16 v[192:195],a[220:223],a[252:255],v[192:195]\nv_exp_f32 v5,v5\nv_mfma_f32_16x16x32_bf16 v[220:223],a[224:227],a[252:255],v[220:223]\nds_read_b128 a[252:255],v239 offset:26624\ns_waitcnt lgkmcnt(3)\nv_cvt_pk_bf16_f32 v4,v4,v5\n"
"v_mfma_f32_16x16x32_bf16 v[112:115],a[196:199],a[228:231],v[112:115]\nbuffer_load_dwordx4 v228,s[48:51],s72 offen offset:3072 nt lds\nv_fma_f32 v11,v11,s69,-v243\nv_mfma_f32_16x16x32_bf16 v[140:143],a[200:203],a[228:231],v[140:143]\nds_read_b128 a[212:215],v236 offset:4096\nv_exp_f32 v6,v6\nv_mfma_f32_16x16x32_bf16 v[168:171],a[204:207],a[228:231],v[168:171]\nv_exp_f32 v7,v7\n"
"v_mfma_f32_16x16x32_bf16 v[196:199],a[208:211],a[228:231],v[196:199]\nds_read_b128 a[228:231],v239 offset:15360\nv_cvt_pk_bf16_f32 v5,v6,v7\nv_fma_f32 v12,v12,s69,-v244\nv_mfma_f32_16x16x32_bf16 v[116:119],a[196:199],a[232:235],v[116:119]\nds_read_b128 a[216:219],v236 offset:6144\nv_fma_f32 v13,v13,s69,-v244\nv_mfma_f32_16x16x32_bf16 v[144:147],a[200:203],a[232:235],v[144:147]\n"
"s_add_u32 m0,s64,4096\nv_exp_f32 v8,v8\nv_fma_f32 v14,v14,s69,-v244\nv_mfma_f32_16x16x32_bf16 v[172:175],a[204:207],a[232:235],v[172:175]\ns_mov_b32 s73,459136\nv_exp_f32 v9,v9\nv_mfma_f32_16x16x32_bf16 v[200:203],a[208:211],a[232:235],v[200:203]\nds_read_b128 a[232:235],v239 offset:17408\n"
"v_exp_f32 v10,v10\nv_mfma_f32_16x16x32_bf16 v[120:123],a[196:199],a[236:239],v[120:123]\nv_exp_f32 v11,v11\nv_mfma_f32_16x16x32_bf16 v[148:151],a[200:203],a[236:239],v[148:151]\nv_cvt_pk_bf16_f32 v8,v8,v9\nv_cvt_pk_bf16_f32 v9,v10,v11\nv_fma_f32 v15,v15,s69,-v244\nv_mfma_f32_16x16x32_bf16 v[176:179],a[204:207],a[236:239],v[176:179]\n"
"v_exp_f32 v12,v12\nv_mfma_f32_16x16x32_bf16 v[204:207],a[208:211],a[236:239],v[204:207]\nds_read_b128 a[236:239],v239 offset:19456\ns_waitcnt lgkmcnt(7)\nv_fma_f32 v16,v16,s69,-v245\nv_mfma_f32_16x16x32_bf16 v[124:127],a[196:199],a[240:243],v[124:127]\nds_read_b128 a[220:223],v236 offset:8192\nv_fma_f32 v17,v17,s69,-v245\n"
"v_mfma_f32_16x16x32_bf16 v[152:155],a[200:203],a[240:243],v[152:155]\nv_exp_f32 v13,v13\nv_fma_f32 v18,v18,s69,-v245\nv_mfma_f32_16x16x32_bf16 v[180:183],a[204:207],a[240:243],v[180:183]\nv_exp_f32 v14,v14\nv_mfma_f32_16x16x32_bf16 v[208:211],a[208:211],a[240:243],v[208:211]\nds_read_b128 a[240:243],v239 offset:21504\nv_exp_f32 v15,v15\n"
"v_mfma_f32_16x16x32_bf16 v[128:131],a[196:199],a[244:247],v[128:131]\nds_read_b128 a[224:227],v236 offset:10240\nv_cvt_pk_bf16_f32 v12,v12,v13\nv_mfma_f32_16x16x32_bf16 v[156:159],a[200:203],a[244:247],v[156:159]\nv_cvt_pk_bf16_f32 v13,v14,v15\nv_fma_f32 v19,v19,s69,-v245\nv_fma_f32 v20,v20,s69,-v246\nv_mfma_f32_16x16x32_bf16 v[184:187],a[204:207],a[244:247],v[184:187]\n"
"v_exp_f32 v16,v16\nv_mfma_f32_16x16x32_bf16 v[212:215],a[208:211],a[244:247],v[212:215]\nds_read_b128 a[244:247],v239 offset:23552\ns_waitcnt lgkmcnt(9)\nv_fma_f32 v21,v21,s69,-v246\nv_mfma_f32_16x16x32_bf16 v[132:135],a[196:199],a[248:251],v[132:135]\nv_exp_f32 v17,v17\nv_mfma_f32_16x16x32_bf16 v[160:163],a[200:203],a[248:251],v[160:163]\n"
"v_exp_f32 v18,v18\nv_cvt_pk_bf16_f32 v16,v16,v17\nv_mfma_f32_16x16x32_bf16 v[188:191],a[204:207],a[248:251],v[188:191]\nv_exp_f32 v19,v19\nv_mfma_f32_16x16x32_bf16 v[216:219],a[208:211],a[248:251],v[216:219]\nds_read_b128 a[248:251],v239 offset:25600\nv_cvt_pk_bf16_f32 v17,v18,v19\nv_fma_f32 v22,v22,s69,-v246\n"
"v_mfma_f32_16x16x32_bf16 v[136:139],a[196:199],a[252:255],v[136:139]\nv_fma_f32 v23,v23,s69,-v246\nv_fma_f32 v24,v24,s69,-v247\nv_mfma_f32_16x16x32_bf16 v[164:167],a[200:203],a[252:255],v[164:167]\nv_exp_f32 v20,v20\nv_fma_f32 v25,v25,s69,-v247\nv_mfma_f32_16x16x32_bf16 v[192:195],a[204:207],a[252:255],v[192:195]\nv_exp_f32 v21,v21\n"
"v_mfma_f32_16x16x32_bf16 v[220:223],a[208:211],a[252:255],v[220:223]\nds_read_b128 a[252:255],v239 offset:27648\ns_waitcnt lgkmcnt(3)\nv_cvt_pk_bf16_f32 v20,v20,v21\nv_mfma_f32_16x16x32_bf16 v[112:115],a[212:215],a[228:231],v[112:115]\nbuffer_load_dwordx4 v227,s[48:51],s73 offen offset:0 nt lds\nv_fma_f32 v26,v26,s69,-v247\nv_mfma_f32_16x16x32_bf16 v[140:143],a[216:219],a[228:231],v[140:143]\n"
"s_waitcnt vmcnt(9)\nds_read_b128 a[196:199],v235 offset:12288\nv_fma_f32 v27,v27,s69,-v247\nv_mfma_f32_16x16x32_bf16 v[168:171],a[220:223],a[228:231],v[168:171]\ns_mov_b32 s74,466304\nv_exp_f32 v22,v22\nv_mfma_f32_16x16x32_bf16 v[196:199],a[224:227],a[228:231],v[196:199]\nds_read_b128 a[228:231],v239 offset:28672\n"
"v_exp_f32 v23,v23\nv_mfma_f32_16x16x32_bf16 v[116:119],a[212:215],a[232:235],v[116:119]\nbuffer_load_dwordx4 v228,s[48:51],s74 offen offset:1024 nt lds\nv_cvt_pk_bf16_f32 v21,v22,v23\nv_mfma_f32_16x16x32_bf16 v[144:147],a[216:219],a[232:235],v[144:147]\nds_read_b128 a[200:203],v235 offset:14336\nv_exp_f32 v24,v24\nv_mfma_f32_16x16x32_bf16 v[172:175],a[220:223],a[232:235],v[172:175]\n"
"s_mov_b32 s75,489856\nv_exp_f32 v25,v25\nv_mfma_f32_16x16x32_bf16 v[200:203],a[224:227],a[232:235],v[200:203]\nds_read_b128 a[232:235],v239 offset:30720\nv_exp_f32 v26,v26\nv_mfma_f32_16x16x32_bf16 v[120:123],a[212:215],a[236:239],v[120:123]\nbuffer_load_dwordx4 v227,s[48:51],s75 offen offset:2048 nt lds\nv_cvt_pk_bf16_f32 v24,v24,v25\n"
"v_mfma_f32_16x16x32_bf16 v[148:151],a[216:219],a[236:239],v[148:151]\ns_mov_b32 s72,497024\nv_exp_f32 v27,v27\nv_fma_f32 v28,v28,s69,-v241\nv_mfma_f32_16x16x32_bf16 v[176:179],a[220:223],a[236:239],v[176:179]\nv_cvt_pk_bf16_f32 v25,v26,v27\nv_fma_f32 v29,v29,s69,-v241\nv_mfma_f32_16x16x32_bf16 v[204:207],a[224:227],a[236:239],v[204:207]\n"
"ds_read_b128 a[236:239],v239 offset:32768\ns_waitcnt lgkmcnt(7)\nv_fma_f32 v30,v30,s69,-v241\nv_mfma_f32_16x16x32_bf16 v[124:127],a[212:215],a[240:243],v[124:127]\nbuffer_load_dwordx4 v228,s[48:51],s72 offen offset:3072 nt lds\nv_fma_f32 v31,v31,s69,-v241\nv_mfma_f32_16x16x32_bf16 v[152:155],a[216:219],a[240:243],v[152:155]\ns_waitcnt vmcnt(8)\n"
"ds_read_b128 a[204:207],v235 offset:16384\nv_fma_f32 v32,v32,s69,-v242\nv_mfma_f32_16x16x32_bf16 v[180:183],a[220:223],a[240:243],v[180:183]\ns_add_u32 m0,s64,8192\nv_exp_f32 v28,v28\nv_mfma_f32_16x16x32_bf16 v[208:211],a[224:227],a[240:243],v[208:211]\nds_read_b128 a[240:243],v239 offset:34816\nv_exp_f32 v29,v29\n"
"v_mfma_f32_16x16x32_bf16 v[128:131],a[212:215],a[244:247],v[128:131]\ns_mov_b32 s73,262144\nbuffer_load_dwordx4 v225,s[52:55],s73 offen offset:0 nt lds\nv_mfma_f32_16x16x32_bf16 v[156:159],a[216:219],a[244:247],v[156:159]\nds_read_b128 a[208:211],v235 offset:18432\nv_exp_f32 v30,v30\nv_mfma_f32_16x16x32_bf16 v[184:187],a[220:223],a[244:247],v[184:187]\ns_mov_b32 s74,263168\n"
"v_exp_f32 v31,v31\nv_mfma_f32_16x16x32_bf16 v[212:215],a[224:227],a[244:247],v[212:215]\nds_read_b128 a[244:247],v239 offset:36864\ns_waitcnt lgkmcnt(9)\nv_cvt_pk_bf16_f32 v28,v28,v29\nv_mfma_f32_16x16x32_bf16 v[132:135],a[212:215],a[248:251],v[132:135]\nbuffer_load_dwordx4 v225,s[52:55],s74 offen offset:1024 nt lds\nv_cvt_pk_bf16_f32 v29,v30,v31\n"
"v_mfma_f32_16x16x32_bf16 v[160:163],a[216:219],a[248:251],v[160:163]\ns_mov_b32 s75,268288\nv_fma_f32 v33,v33,s69,-v242\nv_fma_f32 v34,v34,s69,-v242\nv_fma_f32 v35,v35,s69,-v242\nv_mfma_f32_16x16x32_bf16 v[188:191],a[220:223],a[248:251],v[188:191]\nv_exp_f32 v32,v32\nv_mfma_f32_16x16x32_bf16 v[216:219],a[224:227],a[248:251],v[216:219]\n"
"ds_read_b128 a[248:251],v239 offset:38912\nv_exp_f32 v33,v33\nv_mfma_f32_16x16x32_bf16 v[136:139],a[212:215],a[252:255],v[136:139]\nbuffer_load_dwordx4 v225,s[52:55],s75 offen offset:2048 nt lds\nv_cvt_pk_bf16_f32 v32,v32,v33\nv_mfma_f32_16x16x32_bf16 v[164:167],a[216:219],a[252:255],v[164:167]\ns_mov_b32 s72,269312\nv_exp_f32 v34,v34\n"
"v_fma_f32 v36,v36,s69,-v243\nv_mfma_f32_16x16x32_bf16 v[192:195],a[220:223],a[252:255],v[192:195]\nv_exp_f32 v35,v35\nv_mfma_f32_16x16x32_bf16 v[220:223],a[224:227],a[252:255],v[220:223]\nds_read_b128 a[252:255],v239 offset:40960\ns_waitcnt lgkmcnt(3)\nv_cvt_pk_bf16_f32 v33,v34,v35\nv_mfma_f32_16x16x32_bf16 v[112:115],a[196:199],a[228:231],v[112:115]\n"
"buffer_load_dwordx4 v225,s[52:55],s72 offen offset:3072 nt lds\nv_fma_f32 v37,v37,s69,-v243\nv_mfma_f32_16x16x32_bf16 v[140:143],a[200:203],a[228:231],v[140:143]\nds_read_b128 a[212:215],v236 offset:12288\nv_fma_f32 v38,v38,s69,-v243\nv_fma_f32 v39,v39,s69,-v243\nv_mfma_f32_16x16x32_bf16 v[168:171],a[204:207],a[228:231],v[168:171]\nv_exp_f32 v36,v36\n"
"v_mfma_f32_16x16x32_bf16 v[196:199],a[208:211],a[228:231],v[196:199]\nds_read_b128 a[228:231],v239 offset:29696\nv_exp_f32 v37,v37\nv_mfma_f32_16x16x32_bf16 v[116:119],a[196:199],a[232:235],v[116:119]\nds_read_b128 a[216:219],v236 offset:14336\nv_cvt_pk_bf16_f32 v36,v36,v37\nv_mfma_f32_16x16x32_bf16 v[144:147],a[200:203],a[232:235],v[144:147]\ns_add_u32 m0,s64,12288\n"
"v_exp_f32 v38,v38\nv_fma_f32 v40,v40,s69,-v244\nv_mfma_f32_16x16x32_bf16 v[172:175],a[204:207],a[232:235],v[172:175]\ns_mov_b32 s73,278528\nv_exp_f32 v39,v39\nv_mfma_f32_16x16x32_bf16 v[200:203],a[208:211],a[232:235],v[200:203]\nds_read_b128 a[232:235],v239 offset:31744\nv_cvt_pk_bf16_f32 v37,v38,v39\n"
"v_fma_f32 v41,v41,s69,-v244\nv_mfma_f32_16x16x32_bf16 v[120:123],a[196:199],a[236:239],v[120:123]\nv_fma_f32 v42,v42,s69,-v244\nv_fma_f32 v43,v43,s69,-v244\nv_mfma_f32_16x16x32_bf16 v[148:151],a[200:203],a[236:239],v[148:151]\nv_exp_f32 v40,v40\nv_fma_f32 v44,v44,s69,-v245\nv_mfma_f32_16x16x32_bf16 v[176:179],a[204:207],a[236:239],v[176:179]\n"
"v_exp_f32 v41,v41\nv_mfma_f32_16x16x32_bf16 v[204:207],a[208:211],a[236:239],v[204:207]\nds_read_b128 a[236:239],v239 offset:33792\ns_waitcnt lgkmcnt(7)\nv_cvt_pk_bf16_f32 v40,v40,v41\nv_mfma_f32_16x16x32_bf16 v[124:127],a[196:199],a[240:243],v[124:127]\nds_read_b128 a[220:223],v236 offset:16384\nv_fma_f32 v45,v45,s69,-v245\n"
"v_mfma_f32_16x16x32_bf16 v[152:155],a[200:203],a[240:243],v[152:155]\nv_exp_f32 v42,v42\nv_fma_f32 v46,v46,s69,-v245\nv_mfma_f32_16x16x32_bf16 v[180:183],a[204:207],a[240:243],v[180:183]\nv_exp_f32 v43,v43\nv_mfma_f32_16x16x32_bf16 v[208:211],a[208:211],a[240:243],v[208:211]\nds_read_b128 a[240:243],v239 offset:35840\nv_cvt_pk_bf16_f32 v41,v42,v43\n"
"v_fma_f32 v47,v47,s69,-v245\nv_mfma_f32_16x16x32_bf16 v[128:131],a[196:199],a[244:247],v[128:131]\nds_read_b128 a[224:227],v236 offset:18432\nv_fma_f32 v48,v48,s69,-v246\nv_mfma_f32_16x16x32_bf16 v[156:159],a[200:203],a[244:247],v[156:159]\nv_exp_f32 v44,v44\nv_fma_f32 v49,v49,s69,-v246\nv_mfma_f32_16x16x32_bf16 v[184:187],a[204:207],a[244:247],v[184:187]\n"
"v_exp_f32 v45,v45\nv_mfma_f32_16x16x32_bf16 v[212:215],a[208:211],a[244:247],v[212:215]\nds_read_b128 a[244:247],v239 offset:37888\ns_waitcnt lgkmcnt(9)\nv_cvt_pk_bf16_f32 v44,v44,v45\nv_mfma_f32_16x16x32_bf16 v[132:135],a[196:199],a[248:251],v[132:135]\nv_exp_f32 v46,v46\nv_mfma_f32_16x16x32_bf16 v[160:163],a[200:203],a[248:251],v[160:163]\n"
"v_exp_f32 v47,v47\nv_fma_f32 v50,v50,s69,-v246\nv_mfma_f32_16x16x32_bf16 v[188:191],a[204:207],a[248:251],v[188:191]\nv_cvt_pk_bf16_f32 v45,v46,v47\nv_fma_f32 v51,v51,s69,-v246\nv_mfma_f32_16x16x32_bf16 v[216:219],a[208:211],a[248:251],v[216:219]\nds_read_b128 a[248:251],v239 offset:39936\nv_exp_f32 v48,v48\n"
"v_mfma_f32_16x16x32_bf16 v[136:139],a[196:199],a[252:255],v[136:139]\nv_exp_f32 v49,v49\nv_mfma_f32_16x16x32_bf16 v[164:167],a[200:203],a[252:255],v[164:167]\nv_exp_f32 v50,v50\nv_cvt_pk_bf16_f32 v48,v48,v49\nv_mfma_f32_16x16x32_bf16 v[192:195],a[204:207],a[252:255],v[192:195]\nv_exp_f32 v51,v51\nv_mfma_f32_16x16x32_bf16 v[220:223],a[208:211],a[252:255],v[220:223]\n"
"ds_read_b128 a[252:255],v239 offset:41984\ns_waitcnt lgkmcnt(3)\nv_cvt_pk_bf16_f32 v49,v50,v51\nv_mfma_f32_16x16x32_bf16 v[112:115],a[212:215],a[228:231],v[112:115]\nbuffer_load_dwordx4 v225,s[52:55],s73 offen offset:0 nt lds\nv_fma_f32 v52,v52,s69,-v247\nv_mfma_f32_16x16x32_bf16 v[140:143],a[216:219],a[228:231],v[140:143]\ns_waitcnt vmcnt(9)\n"
"ds_read_b128 a[196:199],v235 offset:0\nv_fma_f32 v53,v53,s69,-v247\nv_mfma_f32_16x16x32_bf16 v[168:171],a[220:223],a[228:231],v[168:171]\ns_mov_b32 s74,279552\nv_fma_f32 v54,v54,s69,-v247\nv_fma_f32 v55,v55,s69,-v247\nv_mfma_f32_16x16x32_bf16 v[196:199],a[224:227],a[228:231],v[196:199]\nds_read_b128 a[228:231],v239 offset:43008\n"
"v_exp_f32 v52,v52\nv_mfma_f32_16x16x32_bf16 v[116:119],a[212:215],a[232:235],v[116:119]\nbuffer_load_dwordx4 v225,s[52:55],s74 offen offset:1024 nt lds\nv_fma_f32 v56,v56,s69,-v241\nv_mfma_f32_16x16x32_bf16 v[144:147],a[216:219],a[232:235],v[144:147]\nds_read_b128 a[200:203],v235 offset:2048\nv_exp_f32 v53,v53\nv_mfma_f32_16x16x32_bf16 v[172:175],a[220:223],a[232:235],v[172:175]\n"
"s_mov_b32 s75,284672\nv_exp_f32 v54,v54\nv_mfma_f32_16x16x32_bf16 v[200:203],a[224:227],a[232:235],v[200:203]\nds_read_b128 a[232:235],v239 offset:45056\nv_exp_f32 v55,v55\nv_mfma_f32_16x16x32_bf16 v[120:123],a[212:215],a[236:239],v[120:123]\nbuffer_load_dwordx4 v225,s[52:55],s75 offen offset:2048 nt lds\nv_cvt_pk_bf16_f32 v52,v52,v53\n"
"v_mfma_f32_16x16x32_bf16 v[148:151],a[216:219],a[236:239],v[148:151]\ns_mov_b32 s72,285696\nv_cvt_pk_bf16_f32 v53,v54,v55\nv_fma_f32 v57,v57,s69,-v241\nv_fma_f32 v58,v58,s69,-v241\nv_mfma_f32_16x16x32_bf16 v[176:179],a[220:223],a[236:239],v[176:179]\nv_fma_f32 v59,v59,s69,-v241\nv_fma_f32 v60,v60,s69,-v242\n"
"v_mfma_f32_16x16x32_bf16 v[204:207],a[224:227],a[236:239],v[204:207]\nds_read_b128 a[236:239],v239 offset:47104\ns_waitcnt lgkmcnt(7)\nv_fma_f32 v61,v61,s69,-v242\nv_mfma_f32_16x16x32_bf16 v[124:127],a[212:215],a[240:243],v[124:127]\nbuffer_load_dwordx4 v225,s[52:55],s72 offen offset:3072 nt lds\nv_fma_f32 v62,v62,s69,-v242\nv_mfma_f32_16x16x32_bf16 v[152:155],a[216:219],a[240:243],v[152:155]\n"
"s_waitcnt vmcnt(8)\nds_read_b128 a[204:207],v235 offset:4096\nv_fma_f32 v63,v63,s69,-v242\nv_mfma_f32_16x16x32_bf16 v[180:183],a[220:223],a[240:243],v[180:183]\ns_add_u32 m0,s64,16384\nv_exp_f32 v56,v56\nv_mfma_f32_16x16x32_bf16 v[208:211],a[224:227],a[240:243],v[208:211]\nds_read_b128 a[240:243],v239 offset:49152\n"
"v_exp_f32 v57,v57\nv_mfma_f32_16x16x32_bf16 v[128:131],a[212:215],a[244:247],v[128:131]\ns_mov_b32 s73,294912\nbuffer_load_dwordx4 v225,s[52:55],s73 offen offset:0 nt lds\nv_mfma_f32_16x16x32_bf16 v[156:159],a[216:219],a[244:247],v[156:159]\nds_read_b128 a[208:211],v235 offset:6144\nv_exp_f32 v58,v58\nv_mfma_f32_16x16x32_bf16 v[184:187],a[220:223],a[244:247],v[184:187]\n"
"s_mov_b32 s74,295936\nv_exp_f32 v59,v59\nv_mfma_f32_16x16x32_bf16 v[212:215],a[224:227],a[244:247],v[212:215]\nds_read_b128 a[244:247],v239 offset:51200\ns_waitcnt lgkmcnt(9)\nv_cvt_pk_bf16_f32 v56,v56,v57\nv_mfma_f32_16x16x32_bf16 v[132:135],a[212:215],a[248:251],v[132:135]\nbuffer_load_dwordx4 v225,s[52:55],s74 offen offset:1024 nt lds\n"
"v_cvt_pk_bf16_f32 v57,v58,v59\nv_mfma_f32_16x16x32_bf16 v[160:163],a[216:219],a[248:251],v[160:163]\ns_mov_b32 s75,301056\nv_exp_f32 v60,v60\nv_fma_f32 v64,v64,s69,-v243\nv_mfma_f32_16x16x32_bf16 v[188:191],a[220:223],a[248:251],v[188:191]\nv_exp_f32 v61,v61\nv_mfma_f32_16x16x32_bf16 v[216:219],a[224:227],a[248:251],v[216:219]\n"
"ds_read_b128 a[248:251],v239 offset:53248\nv_exp_f32 v62,v62\nv_mfma_f32_16x16x32_bf16 v[136:139],a[212:215],a[252:255],v[136:139]\nbuffer_load_dwordx4 v225,s[52:55],s75 offen offset:2048 nt lds\nv_cvt_pk_bf16_f32 v60,v60,v61\nv_mfma_f32_16x16x32_bf16 v[164:167],a[216:219],a[252:255],v[164:167]\ns_mov_b32 s72,302080\nv_exp_f32 v63,v63\n"
"v_fma_f32 v65,v65,s69,-v243\nv_mfma_f32_16x16x32_bf16 v[192:195],a[220:223],a[252:255],v[192:195]\nv_cvt_pk_bf16_f32 v61,v62,v63\nv_fma_f32 v66,v66,s69,-v243\nv_mfma_f32_16x16x32_bf16 v[220:223],a[224:227],a[252:255],v[220:223]\nds_read_b128 a[252:255],v239 offset:55296\ns_waitcnt lgkmcnt(3)\nv_fma_f32 v67,v67,s69,-v243\n"
"v_mfma_f32_16x16x32_bf16 v[112:115],a[196:199],a[228:231],v[112:115]\nbuffer_load_dwordx4 v225,s[52:55],s72 offen offset:3072 nt lds\nv_fma_f32 v68,v68,s69,-v244\nv_mfma_f32_16x16x32_bf16 v[140:143],a[200:203],a[228:231],v[140:143]\nds_read_b128 a[212:215],v236 offset:0\nv_exp_f32 v64,v64\nv_mfma_f32_16x16x32_bf16 v[168:171],a[204:207],a[228:231],v[168:171]\nv_exp_f32 v65,v65\n"
"v_mfma_f32_16x16x32_bf16 v[196:199],a[208:211],a[228:231],v[196:199]\nds_read_b128 a[228:231],v239 offset:44032\nv_exp_f32 v66,v66\nv_mfma_f32_16x16x32_bf16 v[116:119],a[196:199],a[232:235],v[116:119]\nds_read_b128 a[216:219],v236 offset:2048\nv_cvt_pk_bf16_f32 v64,v64,v65\nv_mfma_f32_16x16x32_bf16 v[144:147],a[200:203],a[232:235],v[144:147]\ns_add_u32 m0,s64,0\n"
"v_exp_f32 v67,v67\nv_fma_f32 v69,v69,s69,-v244\nv_mfma_f32_16x16x32_bf16 v[172:175],a[204:207],a[232:235],v[172:175]\ns_mov_b32 s73,311296\nv_cvt_pk_bf16_f32 v65,v66,v67\nv_fma_f32 v70,v70,s69,-v244\nv_mfma_f32_16x16x32_bf16 v[200:203],a[208:211],a[232:235],v[200:203]\nds_read_b128 a[232:235],v239 offset:46080\n"
"v_fma_f32 v71,v71,s69,-v244\nv_fma_f32 v72,v72,s69,-v245\nv_mfma_f32_16x16x32_bf16 v[120:123],a[196:199],a[236:239],v[120:123]\nv_exp_f32 v68,v68\nv_mfma_f32_16x16x32_bf16 v[148:151],a[200:203],a[236:239],v[148:151]\nv_exp_f32 v69,v69\nv_fma_f32 v73,v73,s69,-v245\nv_mfma_f32_16x16x32_bf16 v[176:179],a[204:207],a[236:239],v[176:179]\n"
"v_exp_f32 v70,v70\nv_mfma_f32_16x16x32_bf16 v[204:207],a[208:211],a[236:239],v[204:207]\nds_read_b128 a[236:239],v239 offset:48128\ns_waitcnt lgkmcnt(7)\nv_cvt_pk_bf16_f32 v68,v68,v69\nv_mfma_f32_16x16x32_bf16 v[124:127],a[196:199],a[240:243],v[124:127]\nds_read_b128 a[220:223],v236 offset:4096\nv_fma_f32 v74,v74,s69,-v245\n"
"v_mfma_f32_16x16x32_bf16 v[152:155],a[200:203],a[240:243],v[152:155]\nv_exp_f32 v71,v71\nv_fma_f32 v75,v75,s69,-v245\nv_mfma_f32_16x16x32_bf16 v[180:183],a[204:207],a[240:243],v[180:183]\nv_cvt_pk_bf16_f32 v69,v70,v71\nv_fma_f32 v76,v76,s69,-v246\nv_mfma_f32_16x16x32_bf16 v[208:211],a[208:211],a[240:243],v[208:211]\nds_read_b128 a[240:243],v239 offset:50176\n"
"v_exp_f32 v72,v72\nv_mfma_f32_16x16x32_bf16 v[128:131],a[196:199],a[244:247],v[128:131]\nds_read_b128 a[224:227],v236 offset:6144\nv_fma_f32 v77,v77,s69,-v246\nv_mfma_f32_16x16x32_bf16 v[156:159],a[200:203],a[244:247],v[156:159]\nv_exp_f32 v73,v73\nv_fma_f32 v78,v78,s69,-v246\nv_mfma_f32_16x16x32_bf16 v[184:187],a[204:207],a[244:247],v[184:187]\n"
"v_exp_f32 v74,v74\nv_mfma_f32_16x16x32_bf16 v[212:215],a[208:211],a[244:247],v[212:215]\nds_read_b128 a[244:247],v239 offset:52224\ns_waitcnt lgkmcnt(9)\nv_cvt_pk_bf16_f32 v72,v72,v73\nv_mfma_f32_16x16x32_bf16 v[132:135],a[196:199],a[248:251],v[132:135]\nv_exp_f32 v75,v75\nv_mfma_f32_16x16x32_bf16 v[160:163],a[200:203],a[248:251],v[160:163]\n"
"v_cvt_pk_bf16_f32 v73,v74,v75\nv_fma_f32 v79,v79,s69,-v246\nv_fma_f32 v80,v80,s69,-v247\nv_mfma_f32_16x16x32_bf16 v[188:191],a[204:207],a[248:251],v[188:191]\nv_exp_f32 v76,v76\nv_mfma_f32_16x16x32_bf16 v[216:219],a[208:211],a[248:251],v[216:219]\nds_read_b128 a[248:251],v239 offset:54272\nv_exp_f32 v77,v77\n"
"v_mfma_f32_16x16x32_bf16 v[136:139],a[196:199],a[252:255],v[136:139]\nv_exp_f32 v78,v78\nv_mfma_f32_16x16x32_bf16 v[164:167],a[200:203],a[252:255],v[164:167]\nv_exp_f32 v79,v79\nv_cvt_pk_bf16_f32 v76,v76,v77\nv_mfma_f32_16x16x32_bf16 v[192:195],a[204:207],a[252:255],v[192:195]\nv_cvt_pk_bf16_f32 v77,v78,v79\nv_fma_f32 v81,v81,s69,-v247\n"
"v_mfma_f32_16x16x32_bf16 v[220:223],a[208:211],a[252:255],v[220:223]\nds_read_b128 a[252:255],v239 offset:56320\ns_waitcnt lgkmcnt(3)\nv_fma_f32 v82,v82,s69,-v247\nv_mfma_f32_16x16x32_bf16 v[112:115],a[212:215],a[228:231],v[112:115]\nbuffer_load_dwordx4 v225,s[52:55],s73 offen offset:0 nt lds\nv_fma_f32 v83,v83,s69,-v247\nv_mfma_f32_16x16x32_bf16 v[140:143],a[216:219],a[228:231],v[140:143]\n"
"s_waitcnt vmcnt(9)\nds_read_b64_tr_b16 a[196:197],v231 offset:8192\nv_fma_f32 v84,v84,s69,-v241\nv_mfma_f32_16x16x32_bf16 v[168:171],a[220:223],a[228:231],v[168:171]\nds_read_b64_tr_b16 a[198:199],v231 offset:9216\nv_fma_f32 v85,v85,s69,-v241\nv_mfma_f32_16x16x32_bf16 v[196:199],a[224:227],a[228:231],v[196:199]\ns_waitcnt lgkmcnt(2)\n"
"s_barrier\nv_exp_f32 v80,v80\nv_mfma_f32_16x16x32_bf16 v[116:119],a[212:215],a[232:235],v[116:119]\ns_mov_b32 s74,312320\nbuffer_load_dwordx4 v225,s[52:55],s74 offen offset:1024 nt lds\nds_write_b64 v240,v[0:1] offset:0\nv_mfma_f32_16x16x32_bf16 v[144:147],a[216:219],a[232:235],v[144:147]\nds_read_b64_tr_b16 a[200:201],v232 offset:8192\n"
"v_exp_f32 v81,v81\nv_mfma_f32_16x16x32_bf16 v[172:175],a[220:223],a[232:235],v[172:175]\ns_mov_b32 s75,317440\nv_exp_f32 v82,v82\nv_mfma_f32_16x16x32_bf16 v[200:203],a[224:227],a[232:235],v[200:203]\nv_exp_f32 v83,v83\nv_cvt_pk_bf16_f32 v80,v80,v81\nv_mfma_f32_16x16x32_bf16 v[120:123],a[212:215],a[236:239],v[120:123]\n"
"buffer_load_dwordx4 v225,s[52:55],s75 offen offset:2048 nt lds\nds_write_b64 v240,v[4:5] offset:2048\nv_mfma_f32_16x16x32_bf16 v[148:151],a[216:219],a[236:239],v[148:151]\nds_read_b64_tr_b16 a[202:203],v232 offset:9216\nv_cvt_pk_bf16_f32 v81,v82,v83\nv_fma_f32 v86,v86,s69,-v241\nv_mfma_f32_16x16x32_bf16 v[176:179],a[220:223],a[236:239],v[176:179]\ns_mov_b32 s72,318464\n"
"v_fma_f32 v87,v87,s69,-v241\nv_fma_f32 v88,v88,s69,-v242\nv_mfma_f32_16x16x32_bf16 v[204:207],a[224:227],a[236:239],v[204:207]\nv_exp_f32 v84,v84\nv_fma_f32 v89,v89,s69,-v242\nv_mfma_f32_16x16x32_bf16 v[124:127],a[212:215],a[240:243],v[124:127]\nbuffer_load_dwordx4 v225,s[52:55],s72 offen offset:3072 nt lds\nds_write_b64 v240,v[8:9] offset:4096\n"
"v_mfma_f32_16x16x32_bf16 v[152:155],a[216:219],a[240:243],v[152:155]\nds_read_b64_tr_b16 a[204:205],v233 offset:8192\nv_exp_f32 v85,v85\nv_mfma_f32_16x16x32_bf16 v[180:183],a[220:223],a[240:243],v[180:183]\nds_read_b64_tr_b16 a[206:207],v233 offset:9216\nv_cvt_pk_bf16_f32 v84,v84,v85\nv_mfma_f32_16x16x32_bf16 v[208:211],a[224:227],a[240:243],v[208:211]\ns_add_u32 m0,s64,4096\n"
"v_exp_f32 v86,v86\nv_fma_f32 v90,v90,s69,-v242\nv_mfma_f32_16x16x32_bf16 v[128:131],a[212:215],a[244:247],v[128:131]\ns_mov_b32 s73,327680\nbuffer_load_dwordx4 v225,s[52:55],s73 offen offset:0 nt lds\nds_write_b64 v240,v[12:13] offset:6144\nv_mfma_f32_16x16x32_bf16 v[156:159],a[216:219],a[244:247],v[156:159]\nds_read_b64_tr_b16 a[208:209],v234 offset:8192\n"
"v_exp_f32 v87,v87\nv_mfma_f32_16x16x32_bf16 v[184:187],a[220:223],a[244:247],v[184:187]\ns_mov_b32 s74,328704\nv_cvt_pk_bf16_f32 v85,v86,v87\nv_fma_f32 v91,v91,s69,-v242\nv_mfma_f32_16x16x32_bf16 v[212:215],a[224:227],a[244:247],v[212:215]\nv_exp_f32 v88,v88\nv_fma_f32 v92,v92,s69,-v243\n"
"v_mfma_f32_16x16x32_bf16 v[132:135],a[212:215],a[248:251],v[132:135]\nbuffer_load_dwordx4 v225,s[52:55],s74 offen offset:1024 nt lds\nds_write_b64 v240,v[16:17] offset:8192\nv_mfma_f32_16x16x32_bf16 v[160:163],a[216:219],a[248:251],v[160:163]\nds_read_b64_tr_b16 a[210:211],v234 offset:9216\nv_exp_f32 v89,v89\nv_mfma_f32_16x16x32_bf16 v[188:191],a[220:223],a[248:251],v[188:191]\ns_mov_b32 s75,333824\n"
"v_exp_f32 v90,v90\nv_mfma_f32_16x16x32_bf16 v[216:219],a[224:227],a[248:251],v[216:219]\nv_exp_f32 v91,v91\nv_cvt_pk_bf16_f32 v88,v88,v89\nv_mfma_f32_16x16x32_bf16 v[136:139],a[212:215],a[252:255],v[136:139]\nbuffer_load_dwordx4 v225,s[52:55],s75 offen offset:2048 nt lds\nds_write_b64 v240,v[20:21] offset:10240\nv_mfma_f32_16x16x32_bf16 v[164:167],a[216:219],a[252:255],v[164:167]\n"
"s_mov_b32 s72,334848\nv_cvt_pk_bf16_f32 v89,v90,v91\nv_fma_f32 v93,v93,s69,-v243\nv_fma_f32 v94,v94,s69,-v243\nv_mfma_f32_16x16x32_bf16 v[192:195],a[220:223],a[252:255],v[192:195]\nv_fma_f32 v95,v95,s69,-v243\nv_fma_f32 v96,v96,s69,-v244\nv_mfma_f32_16x16x32_bf16 v[220:223],a[224:227],a[252:255],v[220:223]\n"
"v_exp_f32 v92,v92\nv_fma_f32 v97,v97,s69,-v244\nv_exp_f32 v93,v93\nv_exp_f32 v94,v94\nv_exp_f32 v95,v95\nv_cvt_pk_bf16_f32 v92,v92,v93\nv_cvt_pk_bf16_f32 v93,v94,v95\nv_fma_f32 v98,v98,s69,-v244\n"
"v_fma_f32 v99,v99,s69,-v244\nv_exp_f32 v96,v96\nv_exp_f32 v97,v97\nv_exp_f32 v98,v98\nv_exp_f32 v99,v99\nv_cvt_pk_bf16_f32 v96,v96,v97\nv_cvt_pk_bf16_f32 v97,v98,v99\nv_fma_f32 v100,v100,s69,-v245\n"
"v_fma_f32 v101,v101,s69,-v245\nv_fma_f32 v102,v102,s69,-v245\nv_fma_f32 v103,v103,s69,-v245\nv_exp_f32 v100,v100\nv_exp_f32 v101,v101\nv_exp_f32 v102,v102\nv_exp_f32 v103,v103\nv_cvt_pk_bf16_f32 v100,v100,v101\n"
"v_cvt_pk_bf16_f32 v101,v102,v103\nv_fma_f32 v104,v104,s69,-v246\nv_fma_f32 v105,v105,s69,-v246\nv_fma_f32 v106,v106,s69,-v246\nv_fma_f32 v107,v107,s69,-v246\nv_exp_f32 v104,v104\nv_exp_f32 v105,v105\nv_exp_f32 v106,v106\n"
"v_exp_f32 v107,v107\nv_cvt_pk_bf16_f32 v104,v104,v105\nv_cvt_pk_bf16_f32 v105,v106,v107\nv_fma_f32 v108,v108,s69,-v247\nv_fma_f32 v109,v109,s69,-v247\nv_fma_f32 v110,v110,s69,-v247\nv_fma_f32 v111,v111,s69,-v247\nv_exp_f32 v108,v108\n"
"v_exp_f32 v109,v109\nv_exp_f32 v110,v110\nv_exp_f32 v111,v111\nv_cvt_pk_bf16_f32 v108,v108,v109\nv_cvt_pk_bf16_f32 v109,v110,v111\nds_write_b64 v240,v[24:25] offset:12288\nv_max3_f32 v248,v112,v113,v112\nv_max3_f32 v248,v114,v115,v248\n"
"v_max3_f32 v248,v140,v141,v248\nv_max3_f32 v248,v142,v143,v248\nv_max3_f32 v248,v168,v169,v248\nv_max3_f32 v248,v170,v171,v248\nds_write_b64 v240,v[28:29] offset:14336\nv_max3_f32 v248,v196,v197,v248\nv_max3_f32 v248,v198,v199,v248\nv_mul_f32 v248,s69,v248\n"
"ds_max_f32 v255,v248 offset:57344\nv_max3_f32 v249,v116,v117,v116\nv_max3_f32 v249,v118,v119,v249\nbuffer_load_dwordx4 v225,s[52:55],s72 offen offset:3072 nt lds\nds_write_b64 v240,v[32:33] offset:16384\nv_max3_f32 v249,v144,v145,v249\nv_max3_f32 v249,v146,v147,v249\nv_max3_f32 v249,v172,v173,v249\n"
"v_max3_f32 v249,v174,v175,v249\nv_max3_f32 v249,v200,v201,v249\nv_max3_f32 v249,v202,v203,v249\nds_write_b64 v240,v[36:37] offset:18432\nv_mul_f32 v249,s69,v249\nds_max_f32 v255,v249 offset:57408\nv_max3_f32 v250,v120,v121,v120\nv_max3_f32 v250,v122,v123,v250\n"
"v_max3_f32 v250,v148,v149,v250\nv_max3_f32 v250,v150,v151,v250\nds_write_b64 v240,v[40:41] offset:20480\nv_max3_f32 v250,v176,v177,v250\nv_max3_f32 v250,v178,v179,v250\nv_max3_f32 v250,v204,v205,v250\nv_max3_f32 v250,v206,v207,v250\nv_mul_f32 v250,s69,v250\n"
"ds_max_f32 v255,v250 offset:57472\nds_write_b64 v240,v[44:45] offset:22528\nv_max3_f32 v251,v124,v125,v124\nv_max3_f32 v251,v126,v127,v251\nv_max3_f32 v251,v152,v153,v251\nv_max3_f32 v251,v154,v155,v251\nv_max3_f32 v251,v180,v181,v251\nv_max3_f32 v251,v182,v183,v251\n"
"ds_write_b64 v240,v[48:49] offset:24576\nv_max3_f32 v251,v208,v209,v251\nv_max3_f32 v251,v210,v211,v251\nv_mul_f32 v251,s69,v251\nds_max_f32 v255,v251 offset:57536\nv_max3_f32 v252,v128,v129,v128\nv_max3_f32 v252,v130,v131,v252\nds_write_b64 v240,v[52:53] offset:26624\n"
"v_max3_f32 v252,v156,v157,v252\nv_max3_f32 v252,v158,v159,v252\nv_max3_f32 v252,v184,v185,v252\nv_max3_f32 v252,v186,v187,v252\nv_max3_f32 v252,v212,v213,v252\nv_max3_f32 v252,v214,v215,v252\nds_write_b64 v240,v[56:57] offset:28672\nv_mul_f32 v252,s69,v252\n"
"ds_max_f32 v255,v252 offset:57600\nv_max3_f32 v253,v132,v133,v132\nv_max3_f32 v253,v134,v135,v253\nv_max3_f32 v253,v160,v161,v253\nv_max3_f32 v253,v162,v163,v253\nds_write_b64 v240,v[60:61] offset:30720\nv_max3_f32 v253,v188,v189,v253\nv_max3_f32 v253,v190,v191,v253\n"
"v_max3_f32 v253,v216,v217,v253\nv_max3_f32 v253,v218,v219,v253\nv_mul_f32 v253,s69,v253\nds_max_f32 v255,v253 offset:57664\nds_write_b64 v240,v[64:65] offset:32768\nv_max3_f32 v254,v136,v137,v136\nv_max3_f32 v254,v138,v139,v254\nv_max3_f32 v254,v164,v165,v254\n"
"v_max3_f32 v254,v166,v167,v254\nv_max3_f32 v254,v192,v193,v254\nv_max3_f32 v254,v194,v195,v254\nds_write_b64 v240,v[68:69] offset:34816\nv_max3_f32 v254,v220,v221,v254\nv_max3_f32 v254,v222,v223,v254\nv_mul_f32 v254,s69,v254\nds_max_f32 v255,v254 offset:57728\n"
"v_dot2_f32_bf16 v2,v0,s35,0\nv_dot2_f32_bf16 v2,v1,s35,v2\nds_write_b64 v240,v[72:73] offset:36864\nv_dot2_f32_bf16 v6,v4,s35,0\nv_dot2_f32_bf16 v6,v5,s35,v6\nv_dot2_f32_bf16 v10,v8,s35,0\nv_dot2_f32_bf16 v10,v9,s35,v10\nv_dot2_f32_bf16 v14,v12,s35,0\n"
"v_dot2_f32_bf16 v14,v13,s35,v14\nds_write_b64 v240,v[76:77] offset:38912\nv_dot2_f32_bf16 v18,v16,s35,0\nv_dot2_f32_bf16 v18,v17,s35,v18\nv_dot2_f32_bf16 v22,v20,s35,0\nv_dot2_f32_bf16 v22,v21,s35,v22\nv_dot2_f32_bf16 v26,v24,s35,0\nv_dot2_f32_bf16 v26,v25,s35,v26\n"
"ds_write_b64 v240,v[80:81] offset:40960\nv_dot2_f32_bf16 v2,v28,s35,v2\nv_dot2_f32_bf16 v2,v29,s35,v2\nv_dot2_f32_bf16 v6,v32,s35,v6\nv_dot2_f32_bf16 v6,v33,s35,v6\nv_dot2_f32_bf16 v10,v36,s35,v10\nv_dot2_f32_bf16 v10,v37,s35,v10\nds_write_b64 v240,v[84:85] offset:43008\n"
"v_dot2_f32_bf16 v14,v40,s35,v14\nv_dot2_f32_bf16 v14,v41,s35,v14\nv_dot2_f32_bf16 v18,v44,s35,v18\nv_dot2_f32_bf16 v18,v45,s35,v18\nv_dot2_f32_bf16 v22,v48,s35,v22\nv_dot2_f32_bf16 v22,v49,s35,v22\nds_write_b64 v240,v[88:89] offset:45056\nv_dot2_f32_bf16 v26,v52,s35,v26\n"
"v_dot2_f32_bf16 v26,v53,s35,v26\nv_dot2_f32_bf16 v2,v56,s35,v2\nv_dot2_f32_bf16 v2,v57,s35,v2\nv_dot2_f32_bf16 v6,v60,s35,v6\nv_dot2_f32_bf16 v6,v61,s35,v6\nds_write_b64 v240,v[92:93] offset:47104\nv_dot2_f32_bf16 v10,v64,s35,v10\nv_dot2_f32_bf16 v10,v65,s35,v10\n"
"v_dot2_f32_bf16 v14,v68,s35,v14\nv_dot2_f32_bf16 v14,v69,s35,v14\nv_dot2_f32_bf16 v18,v72,s35,v18\nv_dot2_f32_bf16 v18,v73,s35,v18\nds_write_b64 v240,v[96:97] offset:49152\nv_dot2_f32_bf16 v22,v76,s35,v22\nv_dot2_f32_bf16 v22,v77,s35,v22\nv_dot2_f32_bf16 v26,v80,s35,v26\n"
"v_dot2_f32_bf16 v26,v81,s35,v26\nv_dot2_f32_bf16 v2,v84,s35,v2\nv_dot2_f32_bf16 v2,v85,s35,v2\nds_write_b64 v240,v[100:101] offset:51200\nv_dot2_f32_bf16 v6,v88,s35,v6\nv_dot2_f32_bf16 v6,v89,s35,v6\nv_dot2_f32_bf16 v10,v92,s35,v10\nv_dot2_f32_bf16 v10,v93,s35,v10\n"
"v_dot2_f32_bf16 v14,v96,s35,v14\nv_dot2_f32_bf16 v14,v97,s35,v14\nds_write_b64 v240,v[104:105] offset:53248\nv_dot2_f32_bf16 v18,v100,s35,v18\nv_dot2_f32_bf16 v18,v101,s35,v18\nv_dot2_f32_bf16 v22,v104,s35,v22\nv_dot2_f32_bf16 v22,v105,s35,v22\nv_dot2_f32_bf16 v26,v108,s35,v26\n"
"v_dot2_f32_bf16 v26,v109,s35,v26\nds_write_b64 v240,v[108:109] offset:55296\nv_mov_b32 v3,0\nv_permlane32_swap_b32 v2,v6\nv_add_f32 v2,v2,v6\nv_permlane32_swap_b32 v10,v14\nv_add_f32 v10,v10,v14\nv_permlane32_swap_b32 v18,v22\n"
"v_add_f32 v18,v18,v22\nv_permlane32_swap_b32 v26,v3\nv_add_f32 v26,v26,v3\nv_bfe_u32 v6,v239,8,1\nv_lshrrev_b32 v14,9,v239\nv_permlane16_swap_b32 v2,v10\nv_add_f32 v2,v2,v10\nv_permlane16_swap_b32 v18,v26\n"
"v_add_f32 v18,v18,v26\nv_lshl_or_b32 v6,v6,1,v14\nv_and_b32 v14,0xf0,v239\nv_lshl_or_b32 v6,v6,8,v14\nv_lshl_add_u32 v6,s70,2,v6\nds_write_b32 v6,v2 offset:59392\nds_write_b32 v6,v18 offset:60416\ns_waitcnt lgkmcnt(0)\n"
"s_barrier\nds_read_b128 a[228:231],v239 offset:0\nds_read_b128 a[232:235],v239 offset:2048\nds_read_b128 a[236:239],v239 offset:4096\nds_read_b128 a[240:243],v239 offset:6144\nds_read_b128 a[244:247],v239 offset:8192\nds_read_b128 a[248:251],v239 offset:10240\nds_read_b128 a[252:255],v239 offset:12288\n"
"s_waitcnt lgkmcnt(6)\nv_mfma_f32_16x16x32_bf16 v[0:3],a[196:199],a[228:231],0\ns_waitcnt vmcnt(12)\nds_read_b64_tr_b16 a[212:213],v231 offset:12288\nv_mfma_f32_16x16x32_bf16 v[28:31],a[200:203],a[228:231],0\ns_add_u32 m0,s64,8192\nds_read_b32 v248,v255 offset:57344\nv_mfma_f32_16x16x32_bf16 v[56:59],a[204:207],a[228:231],0\n"
"ds_read_b64_tr_b16 a[214:215],v231 offset:13312\ns_mov_b32 s73,344064\nv_mfma_f32_16x16x32_bf16 v[84:87],a[208:211],a[228:231],0\nds_read_b128 a[228:231],v239 offset:1024\ns_waitcnt lgkmcnt(9)\nv_mfma_f32_16x16x32_bf16 v[4:7],a[196:199],a[232:235],0\nbuffer_load_dwordx4 v225,s[52:55],s73 offen offset:0 nt lds\ns_mov_b32 s74,345088\n"
"v_mfma_f32_16x16x32_bf16 v[32:35],a[200:203],a[232:235],0\nds_read_b64_tr_b16 a[216:217],v232 offset:12288\nds_read_b32 v249,v255 offset:57408\nv_mfma_f32_16x16x32_bf16 v[60:63],a[204:207],a[232:235],0\nds_read_b32 v250,v255 offset:57472\nv_mfma_f32_16x16x32_bf16 v[88:91],a[208:211],a[232:235],0\nds_read_b128 a[232:235],v239 offset:3072\ns_waitcnt lgkmcnt(12)\n"
"v_mfma_f32_16x16x32_bf16 v[8:11],a[196:199],a[236:239],0\nbuffer_load_dwordx4 v225,s[52:55],s74 offen offset:1024 nt lds\ns_mov_b32 s75,350208\nv_mfma_f32_16x16x32_bf16 v[36:39],a[200:203],a[236:239],0\nds_read_b64_tr_b16 a[218:219],v232 offset:13312\nds_read_b32 v251,v255 offset:57536\nv_mfma_f32_16x16x32_bf16 v[64:67],a[204:207],a[236:239],0\nds_read_b32 v252,v255 offset:57600\n"
"v_mfma_f32_16x16x32_bf16 v[92:95],a[208:211],a[236:239],0\nds_read_b128 a[236:239],v239 offset:5120\ns_waitcnt lgkmcnt(14)\nv_mfma_f32_16x16x32_bf16 v[12:15],a[196:199],a[240:243],0\nbuffer_load_dwordx4 v225,s[52:55],s75 offen offset:2048 nt lds\ns_mov_b32 s72,351232\nv_mfma_f32_16x16x32_bf16 v[40:43],a[200:203],a[240:243],0\nds_read_b64_tr_b16 a[220:221],v233 offset:12288\n"
"ds_read_b32 v253,v255 offset:57664\nv_mfma_f32_16x16x32_bf16 v[68:71],a[204:207],a[240:243],0\nds_read_b64_tr_b16 a[222:223],v233 offset:13312\nv_mfma_f32_16x16x32_bf16 v[96:99],a[208:211],a[240:243],0\nds_read_b128 a[240:243],v239 offset:7168\nds_read_b32 v254,v255 offset:57728\nv_mfma_f32_16x16x32_bf16 v[16:19],a[196:199],a[244:247],0\nbuffer_load_dwordx4 v225,s[52:55],s72 offen offset:3072 nt lds\n"
"v_mfma_f32_16x16x32_bf16 v[44:47],a[200:203],a[244:247],0\nds_read_b64_tr_b16 a[224:225],v234 offset:12288\nv_mfma_f32_16x16x32_bf16 v[72:75],a[204:207],a[244:247],0\nv_mfma_f32_16x16x32_bf16 v[100:103],a[208:211],a[244:247],0\nds_read_b128 a[244:247],v239 offset:9216\ns_waitcnt lgkmcnt(15)\ns_waitcnt lgkmcnt(2)\nv_mfma_f32_16x16x32_bf16 v[20:23],a[196:199],a[248:251],0\n"
"ds_read_b64_tr_b16 a[226:227],v234 offset:13312\nv_sub_f32 v241,v241,v248\nv_mfma_f32_16x16x32_bf16 v[48:51],a[200:203],a[248:251],0\ns_add_u32 m0,s64,12288\nv_sub_f32 v242,v242,v249\nv_sub_f32 v243,v243,v250\nv_sub_f32 v244,v244,v251\nv_mfma_f32_16x16x32_bf16 v[76:79],a[204:207],a[248:251],0\n"
"s_mov_b32 s73,360448\nv_sub_f32 v245,v245,v252\nv_sub_f32 v246,v246,v253\nv_mfma_f32_16x16x32_bf16 v[104:107],a[208:211],a[248:251],0\nds_read_b128 a[248:251],v239 offset:11264\nv_sub_f32 v247,v247,v254\nv_fma_f32 v112,v112,s69,-v248\nv_mfma_f32_16x16x32_bf16 v[24:27],a[196:199],a[252:255],0\n"
"v_exp_f32 v241,v241\nv_mfma_f32_16x16x32_bf16 v[52:55],a[200:203],a[252:255],0\nv_exp_f32 v242,v242\nv_fma_f32 v113,v113,s69,-v248\nv_mfma_f32_16x16x32_bf16 v[80:83],a[204:207],a[252:255],0\nv_exp_f32 v243,v243\nv_mfma_f32_16x16x32_bf16 v[108:111],a[208:211],a[252:255],0\nds_read_b128 a[252:255],v239 offset:13312\n"
"s_waitcnt lgkmcnt(2)\nv_fma_f32 v114,v114,s69,-v248\nv_mfma_f32_16x16x32_bf16 v[0:3],a[212:215],a[228:231],v[0:3]\nbuffer_load_dwordx4 v225,s[52:55],s73 offen offset:0 nt lds\nv_fma_f32 v115,v115,s69,-v248\nv_mfma_f32_16x16x32_bf16 v[28:31],a[216:219],a[228:231],v[28:31]\ns_waitcnt vmcnt(13)\nds_read_b64_tr_b16 a[196:197],v231 offset:16384\n"
"v_fma_f32 v116,v116,s69,-v249\nv_mfma_f32_16x16x32_bf16 v[56:59],a[220:223],a[228:231],v[56:59]\nds_read_b64_tr_b16 a[198:199],v231 offset:17408\nv_fma_f32 v117,v117,s69,-v249\nv_mfma_f32_16x16x32_bf16 v[84:87],a[224:227],a[228:231],v[84:87]\ns_waitcnt lgkmcnt(2)\ns_barrier\nds_read_b128 a[228:231],v239 offset:14336\n"
"v_mfma_f32_16x16x32_bf16 v[4:7],a[212:215],a[232:235],v[4:7]\ns_mov_b32 s74,361472\nbuffer_load_dwordx4 v225,s[52:55],s74 offen offset:1024 nt lds\nv_mfma_f32_16x16x32_bf16 v[32:35],a[216:219],a[232:235],v[32:35]\nds_read_b64_tr_b16 a[200:201],v232 offset:16384\nv_exp_f32 v244,v244\nv_mfma_f32_16x16x32_bf16 v[60:63],a[220:223],a[232:235],v[60:63]\ns_mov_b32 s75,366592\n"
"v_exp_f32 v245,v245\nv_mfma_f32_16x16x32_bf16 v[88:91],a[224:227],a[232:235],v[88:91]\nds_read_b128 a[232:235],v239 offset:16384\nv_exp_f32 v246,v246\nv_mfma_f32_16x16x32_bf16 v[8:11],a[212:215],a[236:239],v[8:11]\nbuffer_load_dwordx4 v225,s[52:55],s75 offen offset:2048 nt lds\nv_fma_f32 v118,v118,s69,-v249\nv_mfma_f32_16x16x32_bf16 v[36:39],a[216:219],a[236:239],v[36:39]\n"
"ds_read_b64_tr_b16 a[202:203],v232 offset:17408\nv_exp_f32 v247,v247\nv_mfma_f32_16x16x32_bf16 v[64:67],a[220:223],a[236:239],v[64:67]\ns_mov_b32 s72,367616\nv_exp_f32 v112,v112\nv_mfma_f32_16x16x32_bf16 v[92:95],a[224:227],a[236:239],v[92:95]\nds_read_b128 a[236:239],v239 offset:18432\nv_exp_f32 v113,v113\n"
"v_mfma_f32_16x16x32_bf16 v[12:15],a[212:215],a[240:243],v[12:15]\nbuffer_load_dwordx4 v225,s[52:55],s72 offen offset:3072 nt lds\nv_add_f32 v255,v112,v113\nv_mfma_f32_16x16x32_bf16 v[40:43],a[216:219],a[240:243],v[40:43]\nds_read_b64_tr_b16 a[204:205],v233 offset:16384\nv_exp_f32 v114,v114\nv_mfma_f32_16x16x32_bf16 v[68:71],a[220:223],a[240:243],v[68:71]\nds_read_b64_tr_b16 a[206:207],v233 offset:17408\n"
"v_cvt_pk_bf16_f32 v112,v112,v113\nv_mfma_f32_16x16x32_bf16 v[96:99],a[224:227],a[240:243],v[96:99]\nds_read_b128 a[240:243],v239 offset:20480\nv_exp_f32 v115,v115\nv_mfma_f32_16x16x32_bf16 v[16:19],a[212:215],a[244:247],v[16:19]\nds_read_b64_tr_b16 a[208:209],v234 offset:16384\nv_cvt_pk_bf16_f32 v113,v114,v115\nv_mfma_f32_16x16x32_bf16 v[44:47],a[216:219],a[244:247],v[44:47]\n"
"v_add_f32 v114,v114,v115\nv_add_f32 v114,v114,v255\nv_fma_f32 v119,v119,s69,-v249\nv_mfma_f32_16x16x32_bf16 v[72:75],a[220:223],a[244:247],v[72:75]\nv_exp_f32 v116,v116\nv_mfma_f32_16x16x32_bf16 v[100:103],a[224:227],a[244:247],v[100:103]\nds_read_b128 a[244:247],v239 offset:22528\nv_exp_f32 v117,v117\n"
"v_mfma_f32_16x16x32_bf16 v[20:23],a[212:215],a[248:251],v[20:23]\nds_read_b64_tr_b16 a[210:211],v234 offset:17408\nv_add_f32 v255,v116,v117\nv_mfma_f32_16x16x32_bf16 v[48:51],a[216:219],a[248:251],v[48:51]\ns_add_u32 m0,s64,16384\nv_exp_f32 v118,v118\nv_cvt_pk_bf16_f32 v116,v116,v117\nv_mfma_f32_16x16x32_bf16 v[76:79],a[220:223],a[248:251],v[76:79]\n"
"s_mov_b32 s73,376832\nv_exp_f32 v119,v119\nv_mfma_f32_16x16x32_bf16 v[104:107],a[224:227],a[248:251],v[104:107]\nds_read_b128 a[248:251],v239 offset:24576\nv_cvt_pk_bf16_f32 v117,v118,v119\nv_add_f32 v118,v118,v119\nv_mfma_f32_16x16x32_bf16 v[24:27],a[212:215],a[252:255],v[24:27]\nv_add_f32 v118,v118,v255\n"
"v_fma_f32 v120,v120,s69,-v250\nv_mfma_f32_16x16x32_bf16 v[52:55],a[216:219],a[252:255],v[52:55]\nv_fma_f32 v121,v121,s69,-v250\nv_fma_f32 v122,v122,s69,-v250\nv_fma_f32 v123,v123,s69,-v250\nv_mfma_f32_16x16x32_bf16 v[80:83],a[220:223],a[252:255],v[80:83]\nv_exp_f32 v120,v120\nv_mfma_f32_16x16x32_bf16 v[108:111],a[224:227],a[252:255],v[108:111]\n"
"ds_read_b128 a[252:255],v239 offset:26624\ns_waitcnt lgkmcnt(2)\nv_fma_f32 v124,v124,s69,-v251\nv_mfma_f32_16x16x32_bf16 v[0:3],a[196:199],a[228:231],v[0:3]\nbuffer_load_dwordx4 v225,s[52:55],s73 offen offset:0 nt lds\nv_fma_f32 v125,v125,s69,-v251\nv_mfma_f32_16x16x32_bf16 v[28:31],a[200:203],a[228:231],v[28:31]\ns_waitcnt vmcnt(13)\n"
"ds_read_b64_tr_b16 a[212:213],v231 offset:0\nv_fma_f32 v126,v126,s69,-v251\nv_mfma_f32_16x16x32_bf16 v[56:59],a[204:207],a[228:231],v[56:59]\nds_read_b64_tr_b16 a[214:215],v231 offset:1024\nv_fma_f32 v127,v127,s69,-v251\nv_mfma_f32_16x16x32_bf16 v[84:87],a[208:211],a[228:231],v[84:87]\nds_read_b128 a[228:231],v239 offset:15360\nv_exp_f32 v121,v121\n"
"v_mfma_f32_16x16x32_bf16 v[4:7],a[196:199],a[232:235],v[4:7]\ns_mov_b32 s74,377856\nbuffer_load_dwordx4 v225,s[52:55],s74 offen offset:1024 nt lds\nv_mfma_f32_16x16x32_bf16 v[32:35],a[200:203],a[232:235],v[32:35]\nds_read_b64_tr_b16 a[216:217],v232 offset:0\nv_exp_f32 v122,v122\nv_mfma_f32_16x16x32_bf16 v[60:63],a[204:207],a[232:235],v[60:63]\ns_mov_b32 s75,382976\n"
"v_exp_f32 v123,v123\nv_mfma_f32_16x16x32_bf16 v[88:91],a[208:211],a[232:235],v[88:91]\nds_read_b128 a[232:235],v239 offset:17408\nv_add_f32 v255,v120,v121\nv_cvt_pk_bf16_f32 v120,v120,v121\nv_mfma_f32_16x16x32_bf16 v[8:11],a[196:199],a[236:239],v[8:11]\nbuffer_load_dwordx4 v225,s[52:55],s75 offen offset:2048 nt lds\nv_cvt_pk_bf16_f32 v121,v122,v123\n"
"v_mfma_f32_16x16x32_bf16 v[36:39],a[200:203],a[236:239],v[36:39]\nds_read_b64_tr_b16 a[218:219],v232 offset:1024\nv_add_f32 v122,v122,v123\nv_add_f32 v122,v122,v255\nv_mfma_f32_16x16x32_bf16 v[64:67],a[204:207],a[236:239],v[64:67]\ns_mov_b32 s72,384000\nv_exp_f32 v124,v124\nv_mfma_f32_16x16x32_bf16 v[92:95],a[208:211],a[236:239],v[92:95]\n"
"ds_read_b128 a[236:239],v239 offset:19456\nv_exp_f32 v125,v125\nv_mfma_f32_16x16x32_bf16 v[12:15],a[196:199],a[240:243],v[12:15]\nbuffer_load_dwordx4 v225,s[52:55],s72 offen offset:3072 nt lds\nv_add_f32 v255,v124,v125\nv_mfma_f32_16x16x32_bf16 v[40:43],a[200:203],a[240:243],v[40:43]\nds_read_b64_tr_b16 a[220:221],v233 offset:0\nv_exp_f32 v126,v126\n"
"v_mfma_f32_16x16x32_bf16 v[68:71],a[204:207],a[240:243],v[68:71]\nds_read_b64_tr_b16 a[222:223],v233 offset:1024\nv_cvt_pk_bf16_f32 v124,v124,v125\nv_mfma_f32_16x16x32_bf16 v[96:99],a[208:211],a[240:243],v[96:99]\nds_read_b128 a[240:243],v239 offset:21504\nv_exp_f32 v127,v127\nv_mfma_f32_16x16x32_bf16 v[16:19],a[196:199],a[244:247],v[16:19]\nds_read_b64_tr_b16 a[224:225],v234 offset:0\n"
"v_cvt_pk_bf16_f32 v125,v126,v127\nv_mfma_f32_16x16x32_bf16 v[44:47],a[200:203],a[244:247],v[44:47]\nv_add_f32 v126,v126,v127\nv_add_f32 v126,v126,v255\nv_fma_f32 v128,v128,s69,-v252\nv_mfma_f32_16x16x32_bf16 v[72:75],a[204:207],a[244:247],v[72:75]\nv_fma_f32 v129,v129,s69,-v252\nv_fma_f32 v130,v130,s69,-v252\n"
"v_mfma_f32_16x16x32_bf16 v[100:103],a[208:211],a[244:247],v[100:103]\nds_read_b128 a[244:247],v239 offset:23552\ns_waitcnt lgkmcnt(12)\nv_fma_f32 v131,v131,s69,-v252\nv_mfma_f32_16x16x32_bf16 v[20:23],a[196:199],a[248:251],v[20:23]\nds_read_b64_tr_b16 a[226:227],v234 offset:1024\nv_fma_f32 v132,v132,s69,-v253\nv_mfma_f32_16x16x32_bf16 v[48:51],a[200:203],a[248:251],v[48:51]\n"
"s_add_u32 m0,s64,0\nv_exp_f32 v128,v128\nv_fma_f32 v133,v133,s69,-v253\nv_mfma_f32_16x16x32_bf16 v[76:79],a[204:207],a[248:251],v[76:79]\ns_mov_b32 s73,393216\nv_exp_f32 v129,v129\nv_mfma_f32_16x16x32_bf16 v[104:107],a[208:211],a[248:251],v[104:107]\nds_read_b128 a[248:251],v239 offset:25600\n"
"v_exp_f32 v130,v130\nv_mfma_f32_16x16x32_bf16 v[24:27],a[196:199],a[252:255],v[24:27]\nv_exp_f32 v131,v131\nv_mfma_f32_16x16x32_bf16 v[52:55],a[200:203],a[252:255],v[52:55]\nv_add_f32 v255,v128,v129\nv_cvt_pk_bf16_f32 v128,v128,v129\nv_cvt_pk_bf16_f32 v129,v130,v131\nv_mfma_f32_16x16x32_bf16 v[80:83],a[204:207],a[252:255],v[80:83]\n"
"v_add_f32 v130,v130,v131\nv_add_f32 v130,v130,v255\nv_mfma_f32_16x16x32_bf16 v[108:111],a[208:211],a[252:255],v[108:111]\nds_read_b128 a[252:255],v239 offset:27648\ns_waitcnt lgkmcnt(2)\nv_fma_f32 v134,v134,s69,-v253\nv_mfma_f32_16x16x32_bf16 v[0:3],a[212:215],a[228:231],v[0:3]\nbuffer_load_dwordx4 v225,s[52:55],s73 offen offset:0 nt lds\n"
"v_fma_f32 v135,v135,s69,-v253\nv_mfma_f32_16x16x32_bf16 v[28:31],a[216:219],a[228:231],v[28:31]\ns_waitcnt vmcnt(13)\nds_read_b64_tr_b16 a[196:197],v231 offset:4096\nv_fma_f32 v136,v136,s69,-v254\nv_mfma_f32_16x16x32_bf16 v[56:59],a[220:223],a[228:231],v[56:59]\nds_read_b64_tr_b16 a[198:199],v231 offset:5120\nv_fma_f32 v137,v137,s69,-v254\n"
"v_mfma_f32_16x16x32_bf16 v[84:87],a[224:227],a[228:231],v[84:87]\ns_waitcnt lgkmcnt(2)\ns_barrier\nds_read_b128 a[228:231],v239 offset:28672\nv_mfma_f32_16x16x32_bf16 v[4:7],a[212:215],a[232:235],v[4:7]\ns_mov_b32 s74,394240\nbuffer_load_dwordx4 v225,s[52:55],s74 offen offset:1024 nt lds\nv_mfma_f32_16x16x32_bf16 v[32:35],a[216:219],a[232:235],v[32:35]\n"
"ds_read_b64_tr_b16 a[200:201],v232 offset:4096\nv_exp_f32 v132,v132\nv_mfma_f32_16x16x32_bf16 v[60:63],a[220:223],a[232:235],v[60:63]\ns_mov_b32 s75,399360\nv_exp_f32 v133,v133\nv_mfma_f32_16x16x32_bf16 v[88:91],a[224:227],a[232:235],v[88:91]\nds_read_b128 a[232:235],v239 offset:30720\nv_exp_f32 v134,v134\n"
"v_mfma_f32_16x16x32_bf16 v[8:11],a[212:215],a[236:239],v[8:11]\nbuffer_load_dwordx4 v225,s[52:55],s75 offen offset:2048 nt lds\nv_add_f32 v255,v132,v133\nv_mfma_f32_16x16x32_bf16 v[36:39],a[216:219],a[236:239],v[36:39]\nds_read_b64_tr_b16 a[202:203],v232 offset:5120\nv_exp_f32 v135,v135\nv_mfma_f32_16x16x32_bf16 v[64:67],a[220:223],a[236:239],v[64:67]\ns_mov_b32 s72,400384\n"
"v_cvt_pk_bf16_f32 v132,v132,v133\nv_cvt_pk_bf16_f32 v133,v134,v135\nv_mfma_f32_16x16x32_bf16 v[92:95],a[224:227],a[236:239],v[92:95]\nds_read_b128 a[236:239],v239 offset:32768\nv_add_f32 v134,v134,v135\nv_add_f32 v134,v134,v255\nv_mfma_f32_16x16x32_bf16 v[12:15],a[212:215],a[240:243],v[12:15]\nbuffer_load_dwordx4 v225,s[52:55],s72 offen offset:3072 nt lds\n"
"v_fma_f32 v138,v138,s69,-v254\nv_mfma_f32_16x16x32_bf16 v[40:43],a[216:219],a[240:243],v[40:43]\nds_read_b64_tr_b16 a[204:205],v233 offset:4096\nv_fma_f32 v139,v139,s69,-v254\nv_fma_f32 v140,v140,s69,-v248\nv_mfma_f32_16x16x32_bf16 v[68:71],a[220:223],a[240:243],v[68:71]\nds_read_b64_tr_b16 a[206:207],v233 offset:5120\nv_fma_f32 v141,v141,s69,-v248\n"
"v_mfma_f32_16x16x32_bf16 v[96:99],a[224:227],a[240:243],v[96:99]\nds_read_b128 a[240:243],v239 offset:34816\nv_exp_f32 v136,v136\nv_mfma_f32_16x16x32_bf16 v[16:19],a[212:215],a[244:247],v[16:19]\nds_read_b64_tr_b16 a[208:209],v234 offset:4096\nv_fma_f32 v142,v142,s69,-v248\nv_mfma_f32_16x16x32_bf16 v[44:47],a[216:219],a[244:247],v[44:47]\nv_exp_f32 v137,v137\n"
"v_fma_f32 v143,v143,s69,-v248\nv_mfma_f32_16x16x32_bf16 v[72:75],a[220:223],a[244:247],v[72:75]\nv_exp_f32 v138,v138\nv_mfma_f32_16x16x32_bf16 v[100:103],a[224:227],a[244:247],v[100:103]\nds_read_b128 a[244:247],v239 offset:36864\nv_exp_f32 v139,v139\nv_mfma_f32_16x16x32_bf16 v[20:23],a[212:215],a[248:251],v[20:23]\nds_read_b64_tr_b16 a[210:211],v234 offset:5120\n"
"v_add_f32 v255,v136,v137\nv_mfma_f32_16x16x32_bf16 v[48:51],a[216:219],a[248:251],v[48:51]\ns_add_u32 m0,s64,4096\nv_cvt_pk_bf16_f32 v136,v136,v137\nv_cvt_pk_bf16_f32 v137,v138,v139\nv_add_f32 v138,v138,v139\nv_mfma_f32_16x16x32_bf16 v[76:79],a[220:223],a[248:251],v[76:79]\ns_mov_b32 s73,409600\n"
"v_add_f32 v138,v138,v255\nv_fma_f32 v144,v144,s69,-v249\nv_mfma_f32_16x16x32_bf16 v[104:107],a[224:227],a[248:251],v[104:107]\nds_read_b128 a[248:251],v239 offset:38912\nv_exp_f32 v140,v140\nv_mfma_f32_16x16x32_bf16 v[24:27],a[212:215],a[252:255],v[24:27]\nds_write_b64 v240,v[112:113] offset:0\nv_mfma_f32_16x16x32_bf16 v[52:55],a[216:219],a[252:255],v[52:55]\n"
"v_fma_f32 v145,v145,s69,-v249\nv_fma_f32 v146,v146,s69,-v249\nv_fma_f32 v147,v147,s69,-v249\nv_mfma_f32_16x16x32_bf16 v[80:83],a[220:223],a[252:255],v[80:83]\nds_write_b64 v240,v[116:117] offset:2048\nv_mfma_f32_16x16x32_bf16 v[108:111],a[224:227],a[252:255],v[108:111]\nds_read_b128 a[252:255],v239 offset:40960\ns_waitcnt lgkmcnt(4)\n"
"v_fma_f32 v148,v148,s69,-v250\nv_mfma_f32_16x16x32_bf16 v[0:3],a[196:199],a[228:231],v[0:3]\nbuffer_load_dwordx4 v225,s[52:55],s73 offen offset:0 nt lds\nv_fma_f32 v149,v149,s69,-v250\nv_mfma_f32_16x16x32_bf16 v[28:31],a[200:203],a[228:231],v[28:31]\ns_waitcnt vmcnt(13)\nds_read_b64_tr_b16 a[212:213],v231 offset:8192\nv_fma_f32 v150,v150,s69,-v250\n"
"v_mfma_f32_16x16x32_bf16 v[56:59],a[204:207],a[228:231],v[56:59]\nds_read_b64_tr_b16 a[214:215],v231 offset:9216\nv_fma_f32 v151,v151,s69,-v250\nv_mfma_f32_16x16x32_bf16 v[84:87],a[208:211],a[228:231],v[84:87]\nds_read_b128 a[228:231],v239 offset:29696\nv_exp_f32 v144,v144\nv_mfma_f32_16x16x32_bf16 v[4:7],a[196:199],a[232:235],v[4:7]\ns_mov_b32 s74,410624\n"
"buffer_load_dwordx4 v225,s[52:55],s74 offen offset:1024 nt lds\nv_mfma_f32_16x16x32_bf16 v[32:35],a[200:203],a[232:235],v[32:35]\nds_read_b64_tr_b16 a[216:217],v232 offset:8192\nv_exp_f32 v148,v148\nv_mfma_f32_16x16x32_bf16 v[60:63],a[204:207],a[232:235],v[60:63]\ns_mov_b32 s75,415744\nds_write_b64 v240,v[120:121] offset:4096\nv_mfma_f32_16x16x32_bf16 v[88:91],a[208:211],a[232:235],v[88:91]\n"
"ds_read_b128 a[232:235],v239 offset:31744\nv_fma_f32 v152,v152,s69,-v251\nv_fma_f32 v153,v153,s69,-v251\nv_mfma_f32_16x16x32_bf16 v[8:11],a[196:199],a[236:239],v[8:11]\nbuffer_load_dwordx4 v225,s[52:55],s75 offen offset:2048 nt lds\nv_fma_f32 v154,v154,s69,-v251\nv_mfma_f32_16x16x32_bf16 v[36:39],a[200:203],a[236:239],v[36:39]\nds_read_b64_tr_b16 a[218:219],v232 offset:9216\n"
"v_fma_f32 v155,v155,s69,-v251\nv_fma_f32 v156,v156,s69,-v252\nv_mfma_f32_16x16x32_bf16 v[64:67],a[204:207],a[236:239],v[64:67]\ns_mov_b32 s72,416768\nds_write_b64 v240,v[124:125] offset:6144\nv_mfma_f32_16x16x32_bf16 v[92:95],a[208:211],a[236:239],v[92:95]\nds_read_b128 a[236:239],v239 offset:33792\nv_exp_f32 v152,v152\n"
"v_mfma_f32_16x16x32_bf16 v[12:15],a[196:199],a[240:243],v[12:15]\nbuffer_load_dwordx4 v225,s[52:55],s72 offen offset:3072 nt lds\nv_fma_f32 v157,v157,s69,-v252\nv_mfma_f32_16x16x32_bf16 v[40:43],a[200:203],a[240:243],v[40:43]\nds_read_b64_tr_b16 a[220:221],v233 offset:8192\nv_fma_f32 v158,v158,s69,-v252\nv_fma_f32 v159,v159,s69,-v252\nv_mfma_f32_16x16x32_bf16 v[68:71],a[204:207],a[240:243],v[68:71]\n"
"ds_read_b64_tr_b16 a[222:223],v233 offset:9216\nv_fma_f32 v160,v160,s69,-v253\nv_mfma_f32_16x16x32_bf16 v[96:99],a[208:211],a[240:243],v[96:99]\nds_read_b128 a[240:243],v239 offset:35840\nv_exp_f32 v156,v156\nv_mfma_f32_16x16x32_bf16 v[16:19],a[196:199],a[244:247],v[16:19]\nds_read_b64_tr_b16 a[224:225],v234 offset:8192\nv_fma_f32 v161,v161,s69,-v253\n"
"v_mfma_f32_16x16x32_bf16 v[44:47],a[200:203],a[244:247],v[44:47]\nds_write_b64 v240,v[128:129] offset:8192\nv_mfma_f32_16x16x32_bf16 v[72:75],a[204:207],a[244:247],v[72:75]\nv_fma_f32 v162,v162,s69,-v253\nv_fma_f32 v163,v163,s69,-v253\nv_mfma_f32_16x16x32_bf16 v[100:103],a[208:211],a[244:247],v[100:103]\nds_read_b128 a[244:247],v239 offset:37888\ns_waitcnt lgkmcnt(15)\n"
"v_fma_f32 v164,v164,s69,-v254\nv_mfma_f32_16x16x32_bf16 v[20:23],a[196:199],a[248:251],v[20:23]\nds_read_b64_tr_b16 a[226:227],v234 offset:9216\nv_fma_f32 v165,v165,s69,-v254\nv_mfma_f32_16x16x32_bf16 v[48:51],a[200:203],a[248:251],v[48:51]\ns_add_u32 m0,s64,8192\nds_write_b64 v240,v[132:133] offset:10240\nv_mfma_f32_16x16x32_bf16 v[76:79],a[204:207],a[248:251],v[76:79]\n"
"s_mov_b32 s73,425984\nv_exp_f32 v160,v160\nv_mfma_f32_16x16x32_bf16 v[104:107],a[208:211],a[248:251],v[104:107]\nds_read_b128 a[248:251],v239 offset:39936\nv_fma_f32 v166,v166,s69,-v254\nv_fma_f32 v167,v167,s69,-v254\nv_mfma_f32_16x16x32_bf16 v[24:27],a[196:199],a[252:255],v[24:27]\nds_write_b64 v240,v[136:137] offset:12288\n"
"v_mfma_f32_16x16x32_bf16 v[52:55],a[200:203],a[252:255],v[52:55]\nv_exp_f32 v164,v164\nv_add_f32 v114,v140,v114\nv_mfma_f32_16x16x32_bf16 v[80:83],a[204:207],a[252:255],v[80:83]\nv_exp_f32 v141,v141\nv_mfma_f32_16x16x32_bf16 v[108:111],a[208:211],a[252:255],v[108:111]\nds_read_b128 a[252:255],v239 offset:41984\ns_waitcnt lgkmcnt(4)\n"
"v_add_f32 v114,v141,v114\nv_mfma_f32_16x16x32_bf16 v[0:3],a[212:215],a[228:231],v[0:3]\nbuffer_load_dwordx4 v225,s[52:55],s73 offen offset:0 nt lds\nv_cvt_pk_bf16_f32 v140,v140,v141\nv_mfma_f32_16x16x32_bf16 v[28:31],a[216:219],a[228:231],v[28:31]\ns_waitcnt vmcnt(13)\nds_read_b64_tr_b16 a[196:197],v231 offset:12288\nv_add_f32 v118,v144,v118\n"
"v_mfma_f32_16x16x32_bf16 v[56:59],a[220:223],a[228:231],v[56:59]\nds_read_b64_tr_b16 a[198:199],v231 offset:13312\nv_add_f32 v122,v148,v122\nv_mfma_f32_16x16x32_bf16 v[84:87],a[224:227],a[228:231],v[84:87]\ns_waitcnt lgkmcnt(2)\ns_barrier\nds_read_b128 a[228:231],v239 offset:43008\nv_mfma_f32_16x16x32_bf16 v[4:7],a[212:215],a[232:235],v[4:7]\n"
"s_mov_b32 s74,427008\nbuffer_load_dwordx4 v225,s[52:55],s74 offen offset:1024 nt lds\nv_mfma_f32_16x16x32_bf16 v[32:35],a[216:219],a[232:235],v[32:35]\nds_read_b64_tr_b16 a[200:201],v232 offset:12288\nv_exp_f32 v142,v142\nv_mfma_f32_16x16x32_bf16 v[60:63],a[220:223],a[232:235],v[60:63]\ns_mov_b32 s75,432128\nv_exp_f32 v143,v143\n"
"v_mfma_f32_16x16x32_bf16 v[88:91],a[224:227],a[232:235],v[88:91]\nds_read_b128 a[232:235],v239 offset:45056\nv_add_f32 v114,v142,v114\nv_cvt_pk_bf16_f32 v141,v142,v143\nv_mfma_f32_16x16x32_bf16 v[8:11],a[212:215],a[236:239],v[8:11]\nbuffer_load_dwordx4 v225,s[52:55],s75 offen offset:2048 nt lds\nv_add_f32 v114,v143,v114\nv_mfma_f32_16x16x32_bf16 v[36:39],a[216:219],a[236:239],v[36:39]\n"
"ds_read_b64_tr_b16 a[202:203],v232 offset:13312\nv_exp_f32 v145,v145\nv_mfma_f32_16x16x32_bf16 v[64:67],a[220:223],a[236:239],v[64:67]\ns_mov_b32 s72,433152\nv_exp_f32 v146,v146\nv_mfma_f32_16x16x32_bf16 v[92:95],a[224:227],a[236:239],v[92:95]\nds_read_b128 a[236:239],v239 offset:47104\nv_exp_f32 v147,v147\n"
"v_mfma_f32_16x16x32_bf16 v[12:15],a[212:215],a[240:243],v[12:15]\nbuffer_load_dwordx4 v225,s[52:55],s72 offen offset:3072 nt lds\nv_add_f32 v118,v145,v118\nv_mfma_f32_16x16x32_bf16 v[40:43],a[216:219],a[240:243],v[40:43]\nds_read_b64_tr_b16 a[204:205],v233 offset:12288\nv_cvt_pk_bf16_f32 v144,v144,v145\nv_add_f32 v118,v146,v118\nv_mfma_f32_16x16x32_bf16 v[68:71],a[220:223],a[240:243],v[68:71]\n"
"ds_read_b64_tr_b16 a[206:207],v233 offset:13312\nv_cvt_pk_bf16_f32 v145,v146,v147\nv_mfma_f32_16x16x32_bf16 v[96:99],a[224:227],a[240:243],v[96:99]\nds_read_b128 a[240:243],v239 offset:49152\nv_add_f32 v118,v147,v118\nv_add_f32 v126,v152,v126\nv_mfma_f32_16x16x32_bf16 v[16:19],a[212:215],a[244:247],v[16:19]\nds_read_b64_tr_b16 a[208:209],v234 offset:12288\n"
"v_add_f32 v130,v156,v130\nv_mfma_f32_16x16x32_bf16 v[44:47],a[216:219],a[244:247],v[44:47]\nv_exp_f32 v149,v149\nv_mfma_f32_16x16x32_bf16 v[72:75],a[220:223],a[244:247],v[72:75]\nv_exp_f32 v150,v150\nv_mfma_f32_16x16x32_bf16 v[100:103],a[224:227],a[244:247],v[100:103]\nds_read_b128 a[244:247],v239 offset:51200\nv_exp_f32 v151,v151\n"
"v_mfma_f32_16x16x32_bf16 v[20:23],a[212:215],a[248:251],v[20:23]\nds_read_b64_tr_b16 a[210:211],v234 offset:13312\nv_add_f32 v122,v149,v122\nv_mfma_f32_16x16x32_bf16 v[48:51],a[216:219],a[248:251],v[48:51]\ns_add_u32 m0,s64,12288\nv_cvt_pk_bf16_f32 v148,v148,v149\nv_add_f32 v122,v150,v122\nv_cvt_pk_bf16_f32 v149,v150,v151\n"
"v_mfma_f32_16x16x32_bf16 v[76:79],a[220:223],a[248:251],v[76:79]\ns_mov_b32 s73,442368\nv_add_f32 v122,v151,v122\nv_add_f32 v134,v160,v134\nv_mfma_f32_16x16x32_bf16 v[104:107],a[224:227],a[248:251],v[104:107]\nds_read_b128 a[248:251],v239 offset:53248\nv_exp_f32 v153,v153\nv_mfma_f32_16x16x32_bf16 v[24:27],a[212:215],a[252:255],v[24:27]\n"
"v_exp_f32 v154,v154\nv_mfma_f32_16x16x32_bf16 v[52:55],a[216:219],a[252:255],v[52:55]\nv_exp_f32 v155,v155\nv_add_f32 v126,v153,v126\nv_mfma_f32_16x16x32_bf16 v[80:83],a[220:223],a[252:255],v[80:83]\nv_cvt_pk_bf16_f32 v152,v152,v153\nv_add_f32 v126,v154,v126\nv_mfma_f32_16x16x32_bf16 v[108:111],a[224:227],a[252:255],v[108:111]\n"
"ds_read_b128 a[252:255],v239 offset:55296\ns_waitcnt lgkmcnt(2)\nv_cvt_pk_bf16_f32 v153,v154,v155\nv_mfma_f32_16x16x32_bf16 v[0:3],a[196:199],a[228:231],v[0:3]\nbuffer_load_dwordx4 v225,s[52:55],s73 offen offset:0 nt lds\nv_add_f32 v126,v155,v126\nv_mfma_f32_16x16x32_bf16 v[28:31],a[200:203],a[228:231],v[28:31]\ns_waitcnt vmcnt(13)\n"
"ds_read_b64_tr_b16 a[212:213],v231 offset:16384\nv_add_f32 v138,v164,v138\nv_mfma_f32_16x16x32_bf16 v[56:59],a[204:207],a[228:231],v[56:59]\nds_read_b64_tr_b16 a[214:215],v231 offset:17408\ns_mov_b32 s74,443392\nv_mfma_f32_16x16x32_bf16 v[84:87],a[208:211],a[228:231],v[84:87]\nds_read_b128 a[228:231],v239 offset:44032\nv_exp_f32 v157,v157\n"
"v_mfma_f32_16x16x32_bf16 v[4:7],a[196:199],a[232:235],v[4:7]\nbuffer_load_dwordx4 v225,s[52:55],s74 offen offset:1024 nt lds\nv_add_f32 v130,v157,v130\nv_mfma_f32_16x16x32_bf16 v[32:35],a[200:203],a[232:235],v[32:35]\nds_read_b64_tr_b16 a[216:217],v232 offset:16384\nv_exp_f32 v158,v158\nv_mfma_f32_16x16x32_bf16 v[60:63],a[204:207],a[232:235],v[60:63]\ns_mov_b32 s75,448512\n"
"v_exp_f32 v159,v159\nv_mfma_f32_16x16x32_bf16 v[88:91],a[208:211],a[232:235],v[88:91]\nds_read_b128 a[232:235],v239 offset:46080\nv_cvt_pk_bf16_f32 v156,v156,v157\nv_add_f32 v130,v158,v130\nv_mfma_f32_16x16x32_bf16 v[8:11],a[196:199],a[236:239],v[8:11]\nbuffer_load_dwordx4 v225,s[52:55],s75 offen offset:2048 nt lds\nv_cvt_pk_bf16_f32 v157,v158,v159\n"
"v_mfma_f32_16x16x32_bf16 v[36:39],a[200:203],a[236:239],v[36:39]\nds_read_b64_tr_b16 a[218:219],v232 offset:17408\nv_add_f32 v130,v159,v130\nv_fma_f32 v168,v168,s69,-v248\nv_mfma_f32_16x16x32_bf16 v[64:67],a[204:207],a[236:239],v[64:67]\ns_mov_b32 s72,449536\nv_exp_f32 v161,v161\nv_mfma_f32_16x16x32_bf16 v[92:95],a[208:211],a[236:239],v[92:95]\n"
"ds_read_b128 a[236:239],v239 offset:48128\nv_exp_f32 v162,v162\nv_mfma_f32_16x16x32_bf16 v[12:15],a[196:199],a[240:243],v[12:15]\nbuffer_load_dwordx4 v225,s[52:55],s72 offen offset:3072 nt lds\nv_add_f32 v134,v161,v134\nv_mfma_f32_16x16x32_bf16 v[40:43],a[200:203],a[240:243],v[40:43]\nds_read_b64_tr_b16 a[220:221],v233 offset:16384\nv_exp_f32 v163,v163\n"
"v_mfma_f32_16x16x32_bf16 v[68:71],a[204:207],a[240:243],v[68:71]\nds_read_b64_tr_b16 a[222:223],v233 offset:17408\nv_cvt_pk_bf16_f32 v160,v160,v161\nv_mfma_f32_16x16x32_bf16 v[96:99],a[208:211],a[240:243],v[96:99]\nds_read_b128 a[240:243],v239 offset:50176\nv_add_f32 v134,v162,v134\nv_cvt_pk_bf16_f32 v161,v162,v163\nv_mfma_f32_16x16x32_bf16 v[16:19],a[196:199],a[244:247],v[16:19]\n"
"ds_read_b64_tr_b16 a[224:225],v234 offset:16384\nv_add_f32 v134,v163,v134\nv_mfma_f32_16x16x32_bf16 v[44:47],a[200:203],a[244:247],v[44:47]\nv_exp_f32 v165,v165\nv_fma_f32 v169,v169,s69,-v248\nv_mfma_f32_16x16x32_bf16 v[72:75],a[204:207],a[244:247],v[72:75]\nv_exp_f32 v166,v166\nv_mfma_f32_16x16x32_bf16 v[100:103],a[208:211],a[244:247],v[100:103]\n"
"ds_read_b128 a[244:247],v239 offset:52224\ns_waitcnt lgkmcnt(12)\nv_add_f32 v138,v165,v138\nv_mfma_f32_16x16x32_bf16 v[20:23],a[196:199],a[248:251],v[20:23]\nds_read_b64_tr_b16 a[226:227],v234 offset:17408\nv_cvt_pk_bf16_f32 v164,v164,v165\nv_mfma_f32_16x16x32_bf16 v[48:51],a[200:203],a[248:251],v[48:51]\ns_add_u32 m0,s64,16384\n"
"v_exp_f32 v167,v167\nv_add_f32 v138,v166,v138\nv_mfma_f32_16x16x32_bf16 v[76:79],a[204:207],a[248:251],v[76:79]\ns_mov_b32 s73,458752\nv_cvt_pk_bf16_f32 v165,v166,v167\nv_add_f32 v138,v167,v138\nv_mfma_f32_16x16x32_bf16 v[104:107],a[208:211],a[248:251],v[104:107]\nds_read_b128 a[248:251],v239 offset:54272\n"
"v_fma_f32 v170,v170,s69,-v248\nv_fma_f32 v171,v171,s69,-v248\nv_mfma_f32_16x16x32_bf16 v[24:27],a[196:199],a[252:255],v[24:27]\nds_write_b64 v240,v[140:141] offset:14336\nv_mfma_f32_16x16x32_bf16 v[52:55],a[200:203],a[252:255],v[52:55]\nv_exp_f32 v168,v168\nv_fma_f32 v172,v172,s69,-v249\nv_mfma_f32_16x16x32_bf16 v[80:83],a[204:207],a[252:255],v[80:83]\n"
"ds_write_b64 v240,v[144:145] offset:16384\nv_mfma_f32_16x16x32_bf16 v[108:111],a[208:211],a[252:255],v[108:111]\nds_read_b128 a[252:255],v239 offset:56320\ns_waitcnt lgkmcnt(4)\nv_fma_f32 v173,v173,s69,-v249\nv_mfma_f32_16x16x32_bf16 v[0:3],a[212:215],a[228:231],v[0:3]\nbuffer_load_dwordx4 v225,s[52:55],s73 offen offset:0 nt lds\nv_fma_f32 v174,v174,s69,-v249\n"
"v_mfma_f32_16x16x32_bf16 v[28:31],a[216:219],a[228:231],v[28:31]\ns_waitcnt vmcnt(13)\nds_read_b64_tr_b16 a[196:197],v231 offset:0\nv_fma_f32 v175,v175,s69,-v249\nv_mfma_f32_16x16x32_bf16 v[56:59],a[220:223],a[228:231],v[56:59]\nds_read_b64_tr_b16 a[198:199],v231 offset:1024\nv_fma_f32 v176,v176,s69,-v250\nv_mfma_f32_16x16x32_bf16 v[84:87],a[224:227],a[228:231],v[84:87]\n"
"s_waitcnt lgkmcnt(2)\ns_barrier\nds_read_b128 a[228:231],v239 offset:0\nv_mfma_f32_16x16x32_bf16 v[4:7],a[212:215],a[232:235],v[4:7]\ns_mov_b32 s74,459776\nbuffer_load_dwordx4 v225,s[52:55],s74 offen offset:1024 nt lds\nv_mfma_f32_16x16x32_bf16 v[32:35],a[216:219],a[232:235],v[32:35]\nds_read_b64_tr_b16 a[200:201],v232 offset:0\n"
"v_exp_f32 v172,v172\nv_mfma_f32_16x16x32_bf16 v[60:63],a[220:223],a[232:235],v[60:63]\ns_mov_b32 s75,464896\nds_write_b64 v240,v[148:149] offset:18432\nv_mfma_f32_16x16x32_bf16 v[88:91],a[224:227],a[232:235],v[88:91]\nds_read_b128 a[232:235],v239 offset:2048\nv_fma_f32 v177,v177,s69,-v250\nv_fma_f32 v178,v178,s69,-v250\n"
"v_mfma_f32_16x16x32_bf16 v[8:11],a[212:215],a[236:239],v[8:11]\nbuffer_load_dwordx4 v225,s[52:55],s75 offen offset:2048 nt lds\nv_fma_f32 v179,v179,s69,-v250\nv_mfma_f32_16x16x32_bf16 v[36:39],a[216:219],a[236:239],v[36:39]\nds_read_b64_tr_b16 a[202:203],v232 offset:1024\nv_exp_f32 v176,v176\nv_mfma_f32_16x16x32_bf16 v[64:67],a[220:223],a[236:239],v[64:67]\ns_mov_b32 s72,465920\n"
"ds_write_b64 v240,v[152:153] offset:20480\nv_mfma_f32_16x16x32_bf16 v[92:95],a[224:227],a[236:239],v[92:95]\nds_read_b128 a[236:239],v239 offset:4096\nv_fma_f32 v180,v180,s69,-v251\nv_fma_f32 v181,v181,s69,-v251\nv_mfma_f32_16x16x32_bf16 v[12:15],a[212:215],a[240:243],v[12:15]\nbuffer_load_dwordx4 v225,s[52:55],s72 offen offset:3072 nt lds\nv_mul_f32 v0,v241,v0\n"
"v_mul_f32 v1,v241,v1\nv_mul_f32 v2,v241,v2\nv_mul_f32 v3,v241,v3\nv_mfma_f32_16x16x32_bf16 v[40:43],a[216:219],a[240:243],v[40:43]\nds_read_b64_tr_b16 a[204:205],v233 offset:0\nv_mul_f32 v28,v241,v28\nv_mul_f32 v29,v241,v29\nv_mul_f32 v30,v241,v30\n"
"v_mul_f32 v31,v241,v31\nv_mfma_f32_16x16x32_bf16 v[68:71],a[220:223],a[240:243],v[68:71]\nds_read_b64_tr_b16 a[206:207],v233 offset:1024\nv_mul_f32 v56,v241,v56\nv_mul_f32 v57,v241,v57\nv_mul_f32 v58,v241,v58\nv_mul_f32 v59,v241,v59\nv_mfma_f32_16x16x32_bf16 v[96:99],a[224:227],a[240:243],v[96:99]\n"
"ds_read_b128 a[240:243],v239 offset:6144\nv_mul_f32 v84,v241,v84\nv_mul_f32 v85,v241,v85\nv_mul_f32 v86,v241,v86\nv_mul_f32 v87,v241,v87\nv_mfma_f32_16x16x32_bf16 v[16:19],a[212:215],a[244:247],v[16:19]\nds_read_b64_tr_b16 a[208:209],v234 offset:0\nv_mul_f32 v4,v242,v4\n"
"v_mul_f32 v5,v242,v5\nv_mul_f32 v6,v242,v6\nv_mul_f32 v7,v242,v7\nv_mfma_f32_16x16x32_bf16 v[44:47],a[216:219],a[244:247],v[44:47]\nv_mul_f32 v32,v242,v32\nv_mul_f32 v33,v242,v33\nv_mul_f32 v34,v242,v34\nv_mul_f32 v35,v242,v35\n"
"v_mfma_f32_16x16x32_bf16 v[72:75],a[220:223],a[244:247],v[72:75]\nv_mul_f32 v60,v242,v60\nv_mul_f32 v61,v242,v61\nv_mul_f32 v62,v242,v62\nv_mul_f32 v63,v242,v63\nv_mfma_f32_16x16x32_bf16 v[100:103],a[224:227],a[244:247],v[100:103]\nds_read_b128 a[244:247],v239 offset:8192\nv_mul_f32 v88,v242,v88\n"
"v_mul_f32 v89,v242,v89\nv_mul_f32 v90,v242,v90\nv_mul_f32 v91,v242,v91\nv_mfma_f32_16x16x32_bf16 v[20:23],a[212:215],a[248:251],v[20:23]\nds_read_b64_tr_b16 a[210:211],v234 offset:1024\nv_mul_f32 v8,v243,v8\nv_mul_f32 v9,v243,v9\nv_mul_f32 v10,v243,v10\n"
"v_mul_f32 v11,v243,v11\nv_mfma_f32_16x16x32_bf16 v[48:51],a[216:219],a[248:251],v[48:51]\ns_add_u32 m0,s64,0\nv_mul_f32 v36,v243,v36\nv_mul_f32 v37,v243,v37\nv_mul_f32 v38,v243,v38\nv_mul_f32 v39,v243,v39\nv_mfma_f32_16x16x32_bf16 v[76:79],a[220:223],a[248:251],v[76:79]\n"
"s_mov_b32 s73,475136\nv_mul_f32 v64,v243,v64\nv_mul_f32 v65,v243,v65\nv_mul_f32 v66,v243,v66\nv_mul_f32 v67,v243,v67\nv_mfma_f32_16x16x32_bf16 v[104:107],a[224:227],a[248:251],v[104:107]\nds_read_b128 a[248:251],v239 offset:10240\nv_mul_f32 v92,v243,v92\n"
"v_mul_f32 v93,v243,v93\nv_mul_f32 v94,v243,v94\nv_mul_f32 v95,v243,v95\nv_mfma_f32_16x16x32_bf16 v[24:27],a[212:215],a[252:255],v[24:27]\nv_mul_f32 v12,v244,v12\nv_mul_f32 v13,v244,v13\nv_mul_f32 v14,v244,v14\nv_mul_f32 v15,v244,v15\n"
"v_mfma_f32_16x16x32_bf16 v[52:55],a[216:219],a[252:255],v[52:55]\nv_mul_f32 v40,v244,v40\nv_mul_f32 v41,v244,v41\nv_mul_f32 v42,v244,v42\nv_mul_f32 v43,v244,v43\nv_mfma_f32_16x16x32_bf16 v[80:83],a[220:223],a[252:255],v[80:83]\nv_mul_f32 v68,v244,v68\nv_mul_f32 v69,v244,v69\n"
"v_mul_f32 v70,v244,v70\nv_mul_f32 v71,v244,v71\nv_mfma_f32_16x16x32_bf16 v[108:111],a[224:227],a[252:255],v[108:111]\nds_read_b128 a[252:255],v239 offset:12288\ns_waitcnt lgkmcnt(2)\nv_mul_f32 v96,v244,v96\nv_mul_f32 v97,v244,v97\nv_mul_f32 v98,v244,v98\n"
"v_mul_f32 v99,v244,v99\nv_mfma_f32_16x16x32_bf16 v[0:3],a[196:199],a[228:231],v[0:3]\nbuffer_load_dwordx4 v225,s[52:55],s73 offen offset:0 nt lds\nv_mul_f32 v16,v245,v16\nv_mul_f32 v17,v245,v17\nv_mul_f32 v18,v245,v18\nv_mul_f32 v19,v245,v19\nv_mfma_f32_16x16x32_bf16 v[28:31],a[200:203],a[228:231],v[28:31]\n"
"s_waitcnt vmcnt(13)\nds_read_b64_tr_b16 a[212:213],v231 offset:4096\nv_mul_f32 v44,v245,v44\nv_mul_f32 v45,v245,v45\nv_mul_f32 v46,v245,v46\nv_mul_f32 v47,v245,v47\nv_mfma_f32_16x16x32_bf16 v[56:59],a[204:207],a[228:231],v[56:59]\nds_read_b64_tr_b16 a[214:215],v231 offset:5120\n"
"v_mul_f32 v72,v245,v72\nv_mul_f32 v73,v245,v73\nv_mul_f32 v74,v245,v74\nv_mul_f32 v75,v245,v75\nv_mfma_f32_16x16x32_bf16 v[84:87],a[208:211],a[228:231],v[84:87]\nds_read_b128 a[228:231],v239 offset:1024\nv_mul_f32 v100,v245,v100\nv_mul_f32 v101,v245,v101\n"
"v_mul_f32 v102,v245,v102\nv_mul_f32 v103,v245,v103\nv_mfma_f32_16x16x32_bf16 v[4:7],a[196:199],a[232:235],v[4:7]\ns_mov_b32 s74,476160\nbuffer_load_dwordx4 v225,s[52:55],s74 offen offset:1024 nt lds\nv_mul_f32 v20,v246,v20\nv_mul_f32 v21,v246,v21\nv_mul_f32 v22,v246,v22\n"
"v_mul_f32 v23,v246,v23\nv_mfma_f32_16x16x32_bf16 v[32:35],a[200:203],a[232:235],v[32:35]\nds_read_b64_tr_b16 a[216:217],v232 offset:4096\nv_mul_f32 v48,v246,v48\nv_mul_f32 v49,v246,v49\nv_mul_f32 v50,v246,v50\nv_mul_f32 v51,v246,v51\nv_mfma_f32_16x16x32_bf16 v[60:63],a[204:207],a[232:235],v[60:63]\n"
"s_mov_b32 s75,481280\nv_mul_f32 v76,v246,v76\nv_mul_f32 v77,v246,v77\nv_mul_f32 v78,v246,v78\nv_mul_f32 v79,v246,v79\nv_mfma_f32_16x16x32_bf16 v[88:91],a[208:211],a[232:235],v[88:91]\nds_read_b128 a[232:235],v239 offset:3072\nv_mul_f32 v104,v246,v104\n"
"v_mul_f32 v105,v246,v105\nv_mul_f32 v106,v246,v106\nv_mul_f32 v107,v246,v107\nv_mfma_f32_16x16x32_bf16 v[8:11],a[196:199],a[236:239],v[8:11]\nbuffer_load_dwordx4 v225,s[52:55],s75 offen offset:2048 nt lds\nv_mul_f32 v24,v247,v24\nv_mul_f32 v25,v247,v25\nv_mul_f32 v26,v247,v26\n"
"v_mul_f32 v27,v247,v27\nv_mfma_f32_16x16x32_bf16 v[36:39],a[200:203],a[236:239],v[36:39]\nds_read_b64_tr_b16 a[218:219],v232 offset:5120\nv_mul_f32 v52,v247,v52\nv_mul_f32 v53,v247,v53\nv_mul_f32 v54,v247,v54\nv_mul_f32 v55,v247,v55\nv_mfma_f32_16x16x32_bf16 v[64:67],a[204:207],a[236:239],v[64:67]\n"
"s_mov_b32 s72,482304\nv_mul_f32 v80,v247,v80\nv_mul_f32 v81,v247,v81\nv_mul_f32 v82,v247,v82\nv_mul_f32 v83,v247,v83\nv_mfma_f32_16x16x32_bf16 v[92:95],a[208:211],a[236:239],v[92:95]\nds_read_b128 a[236:239],v239 offset:5120\nv_mul_f32 v108,v247,v108\n"
"v_mul_f32 v109,v247,v109\nv_mul_f32 v110,v247,v110\nv_mul_f32 v111,v247,v111\nv_mfma_f32_16x16x32_bf16 v[12:15],a[196:199],a[240:243],v[12:15]\nbuffer_load_dwordx4 v225,s[52:55],s72 offen offset:3072 nt lds\nv_fma_f32 v182,v182,s69,-v251\nv_mfma_f32_16x16x32_bf16 v[40:43],a[200:203],a[240:243],v[40:43]\nds_read_b64_tr_b16 a[220:221],v233 offset:4096\n"
"v_fma_f32 v183,v183,s69,-v251\nv_fma_f32 v184,v184,s69,-v252\nv_mfma_f32_16x16x32_bf16 v[68:71],a[204:207],a[240:243],v[68:71]\nds_read_b64_tr_b16 a[222:223],v233 offset:5120\nv_fma_f32 v185,v185,s69,-v252\nv_mfma_f32_16x16x32_bf16 v[96:99],a[208:211],a[240:243],v[96:99]\nds_read_b128 a[240:243],v239 offset:7168\nv_exp_f32 v180,v180\n"
"v_mfma_f32_16x16x32_bf16 v[16:19],a[196:199],a[244:247],v[16:19]\nds_read_b64_tr_b16 a[224:225],v234 offset:4096\nv_fma_f32 v186,v186,s69,-v252\nv_mfma_f32_16x16x32_bf16 v[44:47],a[200:203],a[244:247],v[44:47]\nds_write_b64 v240,v[156:157] offset:22528\nv_mfma_f32_16x16x32_bf16 v[72:75],a[204:207],a[244:247],v[72:75]\nv_fma_f32 v187,v187,s69,-v252\nv_fma_f32 v188,v188,s69,-v253\n"
"v_mfma_f32_16x16x32_bf16 v[100:103],a[208:211],a[244:247],v[100:103]\nds_read_b128 a[244:247],v239 offset:9216\ns_waitcnt lgkmcnt(13)\nv_fma_f32 v189,v189,s69,-v253\nv_mfma_f32_16x16x32_bf16 v[20:23],a[196:199],a[248:251],v[20:23]\nds_read_b64_tr_b16 a[226:227],v234 offset:5120\nv_fma_f32 v190,v190,s69,-v253\nv_mfma_f32_16x16x32_bf16 v[48:51],a[200:203],a[248:251],v[48:51]\n"
"s_add_u32 m0,s64,4096\nv_exp_f32 v184,v184\nv_fma_f32 v191,v191,s69,-v253\nv_mfma_f32_16x16x32_bf16 v[76:79],a[204:207],a[248:251],v[76:79]\ns_mov_b32 s73,491520\nds_write_b64 v240,v[160:161] offset:24576\nv_mfma_f32_16x16x32_bf16 v[104:107],a[208:211],a[248:251],v[104:107]\nds_read_b128 a[248:251],v239 offset:11264\n"
"v_exp_f32 v188,v188\nv_mfma_f32_16x16x32_bf16 v[24:27],a[196:199],a[252:255],v[24:27]\nds_write_b64 v240,v[164:165] offset:26624\nv_mfma_f32_16x16x32_bf16 v[52:55],a[200:203],a[252:255],v[52:55]\nv_fma_f32 v192,v192,s69,-v254\nv_fma_f32 v193,v193,s69,-v254\nv_fma_f32 v194,v194,s69,-v254\nv_mfma_f32_16x16x32_bf16 v[80:83],a[204:207],a[252:255],v[80:83]\n"
"v_fma_f32 v195,v195,s69,-v254\nv_mfma_f32_16x16x32_bf16 v[108:111],a[208:211],a[252:255],v[108:111]\nds_read_b128 a[252:255],v239 offset:13312\ns_waitcnt lgkmcnt(4)\nv_mfma_f32_16x16x32_bf16 v[0:3],a[212:215],a[228:231],v[0:3]\nbuffer_load_dwordx4 v225,s[52:55],s73 offen offset:0 nt lds\ns_mov_b32 s74,492544\nv_mfma_f32_16x16x32_bf16 v[28:31],a[216:219],a[228:231],v[28:31]\n"
"s_waitcnt vmcnt(13)\nds_read_b64_tr_b16 a[196:197],v231 offset:8192\nv_mfma_f32_16x16x32_bf16 v[56:59],a[220:223],a[228:231],v[56:59]\nds_read_b64_tr_b16 a[198:199],v231 offset:9216\nv_mfma_f32_16x16x32_bf16 v[84:87],a[224:227],a[228:231],v[84:87]\nv_exp_f32 v192,v192\ns_waitcnt lgkmcnt(0)\ns_barrier\n"
"ds_read_b128 a[228:231],v239 offset:14336\nv_mfma_f32_16x16x32_bf16 v[4:7],a[212:215],a[232:235],v[4:7]\nbuffer_load_dwordx4 v225,s[52:55],s74 offen offset:1024 nt lds\nv_add_f32 v114,v168,v114\nv_mfma_f32_16x16x32_bf16 v[32:35],a[216:219],a[232:235],v[32:35]\nds_read_b64_tr_b16 a[200:201],v232 offset:8192\nv_exp_f32 v169,v169\nv_mfma_f32_16x16x32_bf16 v[60:63],a[220:223],a[232:235],v[60:63]\n"
"s_mov_b32 s75,497664\nv_exp_f32 v170,v170\nv_mfma_f32_16x16x32_bf16 v[88:91],a[224:227],a[232:235],v[88:91]\nds_read_b128 a[232:235],v239 offset:16384\nv_exp_f32 v171,v171\nv_mfma_f32_16x16x32_bf16 v[8:11],a[212:215],a[236:239],v[8:11]\nbuffer_load_dwordx4 v225,s[52:55],s75 offen offset:2048 nt lds\nv_add_f32 v114,v169,v114\n"
"v_mfma_f32_16x16x32_bf16 v[36:39],a[216:219],a[236:239],v[36:39]\nds_read_b64_tr_b16 a[202:203],v232 offset:9216\nv_cvt_pk_bf16_f32 v168,v168,v169\nv_add_f32 v114,v170,v114\nv_mfma_f32_16x16x32_bf16 v[64:67],a[220:223],a[236:239],v[64:67]\ns_mov_b32 s72,498688\nv_cvt_pk_bf16_f32 v169,v170,v171\nv_add_f32 v114,v171,v114\n"
"v_mfma_f32_16x16x32_bf16 v[92:95],a[224:227],a[236:239],v[92:95]\nds_read_b128 a[236:239],v239 offset:18432\nv_exp_f32 v173,v173\nv_mfma_f32_16x16x32_bf16 v[12:15],a[212:215],a[240:243],v[12:15]\nbuffer_load_dwordx4 v225,s[52:55],s72 offen offset:3072 nt lds\nv_add_f32 v118,v172,v118\nv_mfma_f32_16x16x32_bf16 v[40:43],a[216:219],a[240:243],v[40:43]\nds_read_b64_tr_b16 a[204:205],v233 offset:8192\n"
"v_exp_f32 v174,v174\nv_mfma_f32_16x16x32_bf16 v[68:71],a[220:223],a[240:243],v[68:71]\nds_read_b64_tr_b16 a[206:207],v233 offset:9216\nv_add_f32 v118,v173,v118\nv_mfma_f32_16x16x32_bf16 v[96:99],a[224:227],a[240:243],v[96:99]\nds_read_b128 a[240:243],v239 offset:20480\nv_exp_f32 v175,v175\nv_mfma_f32_16x16x32_bf16 v[16:19],a[212:215],a[244:247],v[16:19]\n"
"ds_read_b64_tr_b16 a[208:209],v234 offset:8192\nv_cvt_pk_bf16_f32 v172,v172,v173\nv_mfma_f32_16x16x32_bf16 v[44:47],a[216:219],a[244:247],v[44:47]\nv_add_f32 v118,v174,v118\nv_cvt_pk_bf16_f32 v173,v174,v175\nv_add_f32 v118,v175,v118\nv_mfma_f32_16x16x32_bf16 v[72:75],a[220:223],a[244:247],v[72:75]\nv_exp_f32 v177,v177\n"
"v_mfma_f32_16x16x32_bf16 v[100:103],a[224:227],a[244:247],v[100:103]\nds_read_b128 a[244:247],v239 offset:22528\nv_exp_f32 v178,v178\nv_mfma_f32_16x16x32_bf16 v[20:23],a[212:215],a[248:251],v[20:23]\nds_read_b64_tr_b16 a[210:211],v234 offset:9216\nv_add_f32 v122,v176,v122\nv_mfma_f32_16x16x32_bf16 v[48:51],a[216:219],a[248:251],v[48:51]\ns_add_u32 m0,s64,8192\n"
"v_exp_f32 v179,v179\nv_add_f32 v122,v177,v122\nv_mfma_f32_16x16x32_bf16 v[76:79],a[220:223],a[248:251],v[76:79]\ns_mov_b32 s73,507904\nv_cvt_pk_bf16_f32 v176,v176,v177\nv_add_f32 v122,v178,v122\nv_mfma_f32_16x16x32_bf16 v[104:107],a[224:227],a[248:251],v[104:107]\nds_read_b128 a[248:251],v239 offset:24576\n"
"v_cvt_pk_bf16_f32 v177,v178,v179\nv_add_f32 v122,v179,v122\nv_mfma_f32_16x16x32_bf16 v[24:27],a[212:215],a[252:255],v[24:27]\nv_exp_f32 v181,v181\nv_mfma_f32_16x16x32_bf16 v[52:55],a[216:219],a[252:255],v[52:55]\nv_exp_f32 v182,v182\nv_add_f32 v126,v180,v126\nv_mfma_f32_16x16x32_bf16 v[80:83],a[220:223],a[252:255],v[80:83]\n"
"v_exp_f32 v183,v183\nv_mfma_f32_16x16x32_bf16 v[108:111],a[224:227],a[252:255],v[108:111]\nds_read_b128 a[252:255],v239 offset:26624\ns_waitcnt lgkmcnt(2)\nv_add_f32 v126,v181,v126\nv_mfma_f32_16x16x32_bf16 v[0:3],a[196:199],a[228:231],v[0:3]\nbuffer_load_dwordx4 v225,s[52:55],s73 offen offset:0 nt lds\nv_cvt_pk_bf16_f32 v180,v180,v181\n"
"v_mfma_f32_16x16x32_bf16 v[28:31],a[200:203],a[228:231],v[28:31]\ns_waitcnt vmcnt(13)\nds_read_b64_tr_b16 a[212:213],v231 offset:12288\nv_add_f32 v126,v182,v126\nv_mfma_f32_16x16x32_bf16 v[56:59],a[204:207],a[228:231],v[56:59]\nds_read_b64_tr_b16 a[214:215],v231 offset:13312\nv_cvt_pk_bf16_f32 v181,v182,v183\nv_mfma_f32_16x16x32_bf16 v[84:87],a[208:211],a[228:231],v[84:87]\n"
"ds_read_b128 a[228:231],v239 offset:15360\nv_add_f32 v126,v183,v126\nv_add_f32 v130,v184,v130\nv_mfma_f32_16x16x32_bf16 v[4:7],a[196:199],a[232:235],v[4:7]\ns_mov_b32 s74,508928\nbuffer_load_dwordx4 v225,s[52:55],s74 offen offset:1024 nt lds\nv_mfma_f32_16x16x32_bf16 v[32:35],a[200:203],a[232:235],v[32:35]\nds_read_b64_tr_b16 a[216:217],v232 offset:12288\n"
"v_exp_f32 v185,v185\nv_mfma_f32_16x16x32_bf16 v[60:63],a[204:207],a[232:235],v[60:63]\ns_mov_b32 s75,514048\nv_exp_f32 v186,v186\nv_mfma_f32_16x16x32_bf16 v[88:91],a[208:211],a[232:235],v[88:91]\nds_read_b128 a[232:235],v239 offset:17408\nv_exp_f32 v187,v187\nv_mfma_f32_16x16x32_bf16 v[8:11],a[196:199],a[236:239],v[8:11]\n"
"buffer_load_dwordx4 v225,s[52:55],s75 offen offset:2048 nt lds\nv_add_f32 v130,v185,v130\nv_mfma_f32_16x16x32_bf16 v[36:39],a[200:203],a[236:239],v[36:39]\nds_read_b64_tr_b16 a[218:219],v232 offset:13312\nv_cvt_pk_bf16_f32 v184,v184,v185\nv_add_f32 v130,v186,v130\nv_mfma_f32_16x16x32_bf16 v[64:67],a[204:207],a[236:239],v[64:67]\ns_mov_b32 s72,515072\n"
"v_cvt_pk_bf16_f32 v185,v186,v187\nv_add_f32 v130,v187,v130\nv_mfma_f32_16x16x32_bf16 v[92:95],a[208:211],a[236:239],v[92:95]\nds_read_b128 a[236:239],v239 offset:19456\nv_exp_f32 v189,v189\nv_mfma_f32_16x16x32_bf16 v[12:15],a[196:199],a[240:243],v[12:15]\nbuffer_load_dwordx4 v225,s[52:55],s72 offen offset:3072 nt lds\nv_add_f32 v134,v188,v134\n"
"v_mfma_f32_16x16x32_bf16 v[40:43],a[200:203],a[240:243],v[40:43]\nds_read_b64_tr_b16 a[220:221],v233 offset:12288\nv_exp_f32 v190,v190\nv_mfma_f32_16x16x32_bf16 v[68:71],a[204:207],a[240:243],v[68:71]\nds_read_b64_tr_b16 a[222:223],v233 offset:13312\nv_add_f32 v134,v189,v134\nv_mfma_f32_16x16x32_bf16 v[96:99],a[208:211],a[240:243],v[96:99]\nds_read_b128 a[240:243],v239 offset:21504\n"
"v_exp_f32 v191,v191\nv_mfma_f32_16x16x32_bf16 v[16:19],a[196:199],a[244:247],v[16:19]\nds_read_b64_tr_b16 a[224:225],v234 offset:12288\nv_cvt_pk_bf16_f32 v188,v188,v189\nv_mfma_f32_16x16x32_bf16 v[44:47],a[200:203],a[244:247],v[44:47]\nv_add_f32 v134,v190,v134\nv_cvt_pk_bf16_f32 v189,v190,v191\nv_add_f32 v134,v191,v134\n"
"v_mfma_f32_16x16x32_bf16 v[72:75],a[204:207],a[244:247],v[72:75]\nv_exp_f32 v193,v193\nv_mfma_f32_16x16x32_bf16 v[100:103],a[208:211],a[244:247],v[100:103]\nds_read_b128 a[244:247],v239 offset:23552\ns_waitcnt lgkmcnt(12)\nv_add_f32 v138,v192,v138\nv_mfma_f32_16x16x32_bf16 v[20:23],a[196:199],a[248:251],v[20:23]\nds_read_b64_tr_b16 a[226:227],v234 offset:13312\n"
"v_add_f32 v138,v193,v138\nv_mfma_f32_16x16x32_bf16 v[48:51],a[200:203],a[248:251],v[48:51]\nv_subrev_u32 v226,s68,v226\nv_exp_f32 v194,v194\nv_mfma_f32_16x16x32_bf16 v[76:79],a[204:207],a[248:251],v[76:79]\ns_add_u32 m0,s64,12288\nv_exp_f32 v195,v195\nv_mfma_f32_16x16x32_bf16 v[104:107],a[208:211],a[248:251],v[104:107]\n"
"ds_read_b128 a[248:251],v239 offset:25600\nv_cvt_pk_bf16_f32 v192,v192,v193\nv_add_f32 v138,v194,v138\nv_mfma_f32_16x16x32_bf16 v[24:27],a[196:199],a[252:255],v[24:27]\ns_mov_b32 s73,229376\nv_cvt_pk_bf16_f32 v193,v194,v195\nv_add_f32 v138,v195,v138\nv_mfma_f32_16x16x32_bf16 v[52:55],a[200:203],a[252:255],v[52:55]\n"
"ds_write_b64 v240,v[168:169] offset:28672\nv_mfma_f32_16x16x32_bf16 v[80:83],a[204:207],a[252:255],v[80:83]\nv_fma_f32 v196,v196,s69,-v248\nv_fma_f32 v197,v197,s69,-v248\nv_mfma_f32_16x16x32_bf16 v[108:111],a[208:211],a[252:255],v[108:111]\nds_read_b128 a[252:255],v239 offset:27648\ns_waitcnt lgkmcnt(3)\nv_fma_f32 v198,v198,s69,-v248\n"
"v_mfma_f32_16x16x32_bf16 v[0:3],a[212:215],a[228:231],v[0:3]\nbuffer_load_dwordx4 v226,s[56:59],s73 offen offset:0 lds\nv_fma_f32 v199,v199,s69,-v248\nv_mfma_f32_16x16x32_bf16 v[28:31],a[216:219],a[228:231],v[28:31]\ns_waitcnt vmcnt(13)\nds_read_b64_tr_b16 a[196:197],v231 offset:16384\nv_fma_f32 v200,v200,s69,-v249\nv_mfma_f32_16x16x32_bf16 v[56:59],a[220:223],a[228:231],v[56:59]\n"
"ds_read_b64_tr_b16 a[198:199],v231 offset:17408\nv_fma_f32 v201,v201,s69,-v249\nv_mfma_f32_16x16x32_bf16 v[84:87],a[224:227],a[228:231],v[84:87]\nv_exp_f32 v196,v196\nds_write_b64 v240,v[172:173] offset:30720\nv_fma_f32 v202,v202,s69,-v249\nv_fma_f32 v203,v203,s69,-v249\nv_exp_f32 v200,v200\n"
"ds_write_b64 v240,v[176:177] offset:32768\nv_fma_f32 v204,v204,s69,-v250\nv_fma_f32 v205,v205,s69,-v250\nv_fma_f32 v206,v206,s69,-v250\nv_fma_f32 v207,v207,s69,-v250\nv_exp_f32 v204,v204\nds_write_b64 v240,v[180:181] offset:34816\nv_fma_f32 v208,v208,s69,-v251\n"
"v_fma_f32 v209,v209,s69,-v251\nv_fma_f32 v210,v210,s69,-v251\nv_fma_f32 v211,v211,s69,-v251\nv_exp_f32 v208,v208\nds_write_b64 v240,v[184:185] offset:36864\nv_fma_f32 v212,v212,s69,-v252\nv_fma_f32 v213,v213,s69,-v252\nv_fma_f32 v214,v214,s69,-v252\n"
"v_fma_f32 v215,v215,s69,-v252\nv_exp_f32 v212,v212\nds_write_b64 v240,v[188:189] offset:38912\nv_fma_f32 v216,v216,s69,-v253\nv_fma_f32 v217,v217,s69,-v253\nv_fma_f32 v218,v218,s69,-v253\nv_fma_f32 v219,v219,s69,-v253\nv_exp_f32 v216,v216\n"
"ds_write_b64 v240,v[192:193] offset:40960\nv_fma_f32 v220,v220,s69,-v254\nv_fma_f32 v221,v221,s69,-v254\nv_fma_f32 v222,v222,s69,-v254\nv_fma_f32 v223,v223,s69,-v254\nv_exp_f32 v220,v220\ns_waitcnt lgkmcnt(0)\ns_barrier\n"
"ds_read_b128 a[228:231],v239 offset:28672\nv_mfma_f32_16x16x32_bf16 v[4:7],a[212:215],a[232:235],v[4:7]\ns_mov_b32 s74,242688\nbuffer_load_dwordx4 v226,s[56:59],s74 offen offset:1024 lds\nv_mfma_f32_16x16x32_bf16 v[32:35],a[216:219],a[232:235],v[32:35]\nds_read_b64_tr_b16 a[200:201],v232 offset:16384\nv_exp_f32 v197,v197\nv_mfma_f32_16x16x32_bf16 v[60:63],a[220:223],a[232:235],v[60:63]\n"
"s_mov_b32 s75,234496\nv_exp_f32 v198,v198\nv_mfma_f32_16x16x32_bf16 v[88:91],a[224:227],a[232:235],v[88:91]\nds_read_b128 a[232:235],v239 offset:30720\nv_exp_f32 v199,v199\nv_mfma_f32_16x16x32_bf16 v[8:11],a[212:215],a[236:239],v[8:11]\nbuffer_load_dwordx4 v226,s[56:59],s75 offen offset:2048 lds\nv_add_f32 v114,v196,v114\n"
"v_mfma_f32_16x16x32_bf16 v[36:39],a[216:219],a[236:239],v[36:39]\nds_read_b64_tr_b16 a[202:203],v232 offset:17408\nv_add_f32 v114,v197,v114\nv_cvt_pk_bf16_f32 v196,v196,v197\nv_mfma_f32_16x16x32_bf16 v[64:67],a[220:223],a[236:239],v[64:67]\ns_mov_b32 s72,247808\nv_add_f32 v114,v198,v114\nv_cvt_pk_bf16_f32 v197,v198,v199\n"
"v_mfma_f32_16x16x32_bf16 v[92:95],a[224:227],a[236:239],v[92:95]\nds_read_b128 a[236:239],v239 offset:32768\nv_add_f32 v114,v199,v114\nv_add_f32 v118,v200,v118\nv_mfma_f32_16x16x32_bf16 v[12:15],a[212:215],a[240:243],v[12:15]\nbuffer_load_dwordx4 v226,s[56:59],s72 offen offset:3072 lds\nv_add_f32 v122,v204,v122\nv_mfma_f32_16x16x32_bf16 v[40:43],a[216:219],a[240:243],v[40:43]\n"
"ds_read_b64_tr_b16 a[204:205],v233 offset:16384\nv_exp_f32 v201,v201\nv_mfma_f32_16x16x32_bf16 v[68:71],a[220:223],a[240:243],v[68:71]\nds_read_b64_tr_b16 a[206:207],v233 offset:17408\nv_add_f32 v118,v201,v118\nv_mfma_f32_16x16x32_bf16 v[96:99],a[224:227],a[240:243],v[96:99]\nds_read_b128 a[240:243],v239 offset:34816\nv_exp_f32 v202,v202\n"
"v_mfma_f32_16x16x32_bf16 v[16:19],a[212:215],a[244:247],v[16:19]\nds_read_b64_tr_b16 a[208:209],v234 offset:16384\nv_cvt_pk_bf16_f32 v200,v200,v201\nv_mfma_f32_16x16x32_bf16 v[44:47],a[216:219],a[244:247],v[44:47]\nv_exp_f32 v203,v203\nv_add_f32 v118,v202,v118\nv_mfma_f32_16x16x32_bf16 v[72:75],a[220:223],a[244:247],v[72:75]\nv_cvt_pk_bf16_f32 v201,v202,v203\n"
"v_add_f32 v118,v203,v118\nv_mfma_f32_16x16x32_bf16 v[100:103],a[224:227],a[244:247],v[100:103]\nds_read_b128 a[244:247],v239 offset:36864\nv_exp_f32 v205,v205\nv_mfma_f32_16x16x32_bf16 v[20:23],a[212:215],a[248:251],v[20:23]\nds_read_b64_tr_b16 a[210:211],v234 offset:17408\nv_add_f32 v122,v205,v122\nv_mfma_f32_16x16x32_bf16 v[48:51],a[216:219],a[248:251],v[48:51]\n"
"s_add_u32 m0,s64,16384\nv_exp_f32 v206,v206\nv_cvt_pk_bf16_f32 v204,v204,v205\nv_mfma_f32_16x16x32_bf16 v[76:79],a[220:223],a[248:251],v[76:79]\ns_mov_b32 s73,258048\nv_exp_f32 v207,v207\nv_mfma_f32_16x16x32_bf16 v[104:107],a[224:227],a[248:251],v[104:107]\nds_read_b128 a[248:251],v239 offset:38912\n"
"v_add_f32 v122,v206,v122\nv_cvt_pk_bf16_f32 v205,v206,v207\nv_mfma_f32_16x16x32_bf16 v[24:27],a[212:215],a[252:255],v[24:27]\nv_add_f32 v122,v207,v122\nv_add_f32 v126,v208,v126\nv_mfma_f32_16x16x32_bf16 v[52:55],a[216:219],a[252:255],v[52:55]\nv_exp_f32 v209,v209\nv_add_f32 v130,v212,v130\n"
"v_mfma_f32_16x16x32_bf16 v[80:83],a[220:223],a[252:255],v[80:83]\nv_exp_f32 v210,v210\nv_mfma_f32_16x16x32_bf16 v[108:111],a[224:227],a[252:255],v[108:111]\nds_read_b128 a[252:255],v239 offset:40960\ns_waitcnt lgkmcnt(2)\nv_add_f32 v126,v209,v126\nv_mfma_f32_16x16x32_bf16 v[0:3],a[196:199],a[228:231],v[0:3]\nbuffer_load_dwordx4 v226,s[56:59],s73 offen offset:0 lds\n"
"v_cvt_pk_bf16_f32 v208,v208,v209\nv_mfma_f32_16x16x32_bf16 v[28:31],a[200:203],a[228:231],v[28:31]\ns_waitcnt vmcnt(13)\nds_read_b64_tr_b16 a[212:213],v231 offset:0\nv_add_f32 v126,v210,v126\nv_mfma_f32_16x16x32_bf16 v[56:59],a[204:207],a[228:231],v[56:59]\nds_read_b64_tr_b16 a[214:215],v231 offset:1024\nv_add_f32 v134,v216,v134\n"
"v_mfma_f32_16x16x32_bf16 v[84:87],a[208:211],a[228:231],v[84:87]\nds_read_b128 a[228:231],v239 offset:29696\nv_exp_f32 v211,v211\nv_mfma_f32_16x16x32_bf16 v[4:7],a[196:199],a[232:235],v[4:7]\ns_mov_b32 s74,271360\nbuffer_load_dwordx4 v226,s[56:59],s74 offen offset:1024 lds\nv_mfma_f32_16x16x32_bf16 v[32:35],a[200:203],a[232:235],v[32:35]\nds_read_b64_tr_b16 a[216:217],v232 offset:0\n"
"v_cvt_pk_bf16_f32 v209,v210,v211\nv_add_f32 v126,v211,v126\nv_mfma_f32_16x16x32_bf16 v[60:63],a[204:207],a[232:235],v[60:63]\ns_mov_b32 s75,263168\nv_exp_f32 v213,v213\nv_mfma_f32_16x16x32_bf16 v[88:91],a[208:211],a[232:235],v[88:91]\nds_read_b128 a[232:235],v239 offset:31744\nv_exp_f32 v214,v214\n"
"v_mfma_f32_16x16x32_bf16 v[8:11],a[196:199],a[236:239],v[8:11]\nbuffer_load_dwordx4 v226,s[56:59],s75 offen offset:2048 lds\nv_add_f32 v130,v213,v130\nv_mfma_f32_16x16x32_bf16 v[36:39],a[200:203],a[236:239],v[36:39]\nds_read_b64_tr_b16 a[218:219],v232 offset:1024\nv_exp_f32 v215,v215\nv_mfma_f32_16x16x32_bf16 v[64:67],a[204:207],a[236:239],v[64:67]\ns_mov_b32 s72,276480\n"
"v_cvt_pk_bf16_f32 v212,v212,v213\nv_add_f32 v130,v214,v130\nv_mfma_f32_16x16x32_bf16 v[92:95],a[208:211],a[236:239],v[92:95]\nds_read_b128 a[236:239],v239 offset:33792\nv_cvt_pk_bf16_f32 v213,v214,v215\nv_add_f32 v130,v215,v130\nv_mfma_f32_16x16x32_bf16 v[12:15],a[196:199],a[240:243],v[12:15]\nbuffer_load_dwordx4 v226,s[56:59],s72 offen offset:3072 lds\n"
"v_add_f32 v138,v220,v138\nv_mfma_f32_16x16x32_bf16 v[40:43],a[200:203],a[240:243],v[40:43]\nds_read_b64_tr_b16 a[220:221],v233 offset:0\nv_exp_f32 v217,v217\nv_mfma_f32_16x16x32_bf16 v[68:71],a[204:207],a[240:243],v[68:71]\nds_read_b64_tr_b16 a[222:223],v233 offset:1024\nv_add_f32 v134,v217,v134\nv_mfma_f32_16x16x32_bf16 v[96:99],a[208:211],a[240:243],v[96:99]\n"
"ds_read_b128 a[240:243],v239 offset:35840\nv_exp_f32 v218,v218\nv_mfma_f32_16x16x32_bf16 v[16:19],a[196:199],a[244:247],v[16:19]\nds_read_b64_tr_b16 a[224:225],v234 offset:0\nv_cvt_pk_bf16_f32 v216,v216,v217\nv_mfma_f32_16x16x32_bf16 v[44:47],a[200:203],a[244:247],v[44:47]\nv_exp_f32 v219,v219\nv_add_f32 v134,v218,v134\n"
"v_mfma_f32_16x16x32_bf16 v[72:75],a[204:207],a[244:247],v[72:75]\nv_cvt_pk_bf16_f32 v217,v218,v219\nv_add_f32 v134,v219,v134\nv_mfma_f32_16x16x32_bf16 v[100:103],a[208:211],a[244:247],v[100:103]\nds_read_b128 a[244:247],v239 offset:37888\ns_waitcnt lgkmcnt(12)\nv_mov_b32 v115,0\nv_mfma_f32_16x16x32_bf16 v[20:23],a[196:199],a[248:251],v[20:23]\n"
"ds_read_b64_tr_b16 a[226:227],v234 offset:1024\nv_permlane32_swap_b32 v114,v118\nv_mfma_f32_16x16x32_bf16 v[48:51],a[200:203],a[248:251],v[48:51]\ns_add_u32 m0,s64,0\nv_exp_f32 v221,v221\nv_add_f32 v114,v114,v118\nv_mfma_f32_16x16x32_bf16 v[76:79],a[204:207],a[248:251],v[76:79]\ns_mov_b32 s73,286720\n"
"v_exp_f32 v222,v222\nv_mfma_f32_16x16x32_bf16 v[104:107],a[208:211],a[248:251],v[104:107]\nds_read_b128 a[248:251],v239 offset:39936\nv_exp_f32 v223,v223\nv_mfma_f32_16x16x32_bf16 v[24:27],a[196:199],a[252:255],v[24:27]\nv_add_f32 v138,v221,v138\nv_cvt_pk_bf16_f32 v220,v220,v221\nv_mfma_f32_16x16x32_bf16 v[52:55],a[200:203],a[252:255],v[52:55]\n"
"v_add_f32 v138,v222,v138\nv_cvt_pk_bf16_f32 v221,v222,v223\nv_add_f32 v138,v223,v138\nv_mfma_f32_16x16x32_bf16 v[80:83],a[204:207],a[252:255],v[80:83]\nds_write_b64 v240,v[196:197] offset:43008\nv_mfma_f32_16x16x32_bf16 v[108:111],a[208:211],a[252:255],v[108:111]\nds_read_b128 a[252:255],v239 offset:41984\ns_waitcnt lgkmcnt(3)\n"
"v_permlane32_swap_b32 v122,v126\nv_mfma_f32_16x16x32_bf16 v[0:3],a[212:215],a[228:231],v[0:3]\nbuffer_load_dwordx4 v226,s[56:59],s73 offen offset:0 lds\nv_add_f32 v122,v122,v126\nv_mfma_f32_16x16x32_bf16 v[28:31],a[216:219],a[228:231],v[28:31]\ns_waitcnt vmcnt(13)\nds_read_b64_tr_b16 a[196:197],v231 offset:4096\nv_permlane32_swap_b32 v130,v134\n"
"v_mfma_f32_16x16x32_bf16 v[56:59],a[220:223],a[228:231],v[56:59]\nds_read_b64_tr_b16 a[198:199],v231 offset:5120\nv_add_f32 v130,v130,v134\nv_mfma_f32_16x16x32_bf16 v[84:87],a[224:227],a[228:231],v[84:87]\nds_write_b64 v240,v[200:201] offset:45056\nds_write_b64 v240,v[204:205] offset:47104\nds_write_b64 v240,v[208:209] offset:49152\nds_write_b64 v240,v[212:213] offset:51200\n"
"ds_write_b64 v240,v[216:217] offset:53248\nds_write_b64 v240,v[220:221] offset:55296\nv_permlane32_swap_b32 v138,v115\nv_add_f32 v138,v138,v115\nv_bfe_u32 v118,v239,8,1\nv_lshrrev_b32 v126,9,v239\nv_permlane16_swap_b32 v114,v122\nv_add_f32 v114,v114,v122\n"
"v_permlane16_swap_b32 v130,v138\nv_add_f32 v130,v130,v138\nv_lshl_or_b32 v118,v118,1,v126\nv_and_b32 v126,0xf0,v239\nv_lshl_or_b32 v118,v118,8,v126\nv_lshl_add_u32 v118,s70,2,v118\nds_write_b32 v118,v114 offset:61440\nds_write_b32 v118,v130 offset:62464\n"
"s_waitcnt lgkmcnt(0)\ns_barrier\nds_read_b128 a[228:231],v239 offset:43008\nv_mfma_f32_16x16x32_bf16 v[4:7],a[212:215],a[232:235],v[4:7]\ns_mov_b32 s74,300032\nbuffer_load_dwordx4 v226,s[56:59],s74 offen offset:1024 lds\nv_mfma_f32_16x16x32_bf16 v[32:35],a[216:219],a[232:235],v[32:35]\nds_read_b64_tr_b16 a[200:201],v232 offset:4096\n"
"v_and_b32 v255,0xf0,v239\nv_mfma_f32_16x16x32_bf16 v[60:63],a[220:223],a[232:235],v[60:63]\ns_mov_b32 s75,291840\nds_read_b128 v[140:143],v255 offset:59392\nv_mfma_f32_16x16x32_bf16 v[88:91],a[224:227],a[232:235],v[88:91]\nds_read_b128 a[232:235],v239 offset:45056\nds_read_b128 v[144:147],v255 offset:59648\nv_mfma_f32_16x16x32_bf16 v[8:11],a[212:215],a[236:239],v[8:11]\n"
"buffer_load_dwordx4 v226,s[56:59],s75 offen offset:2048 lds\ns_mov_b32 s72,305152\nv_mfma_f32_16x16x32_bf16 v[36:39],a[216:219],a[236:239],v[36:39]\nds_read_b64_tr_b16 a[202:203],v232 offset:5120\nds_read_b128 v[148:151],v255 offset:59904\nv_mfma_f32_16x16x32_bf16 v[64:67],a[220:223],a[236:239],v[64:67]\nds_read_b128 v[152:155],v255 offset:60160\nv_mfma_f32_16x16x32_bf16 v[92:95],a[224:227],a[236:239],v[92:95]\n"
"ds_read_b128 a[236:239],v239 offset:47104\nds_read_b128 v[156:159],v255 offset:60416\nv_mfma_f32_16x16x32_bf16 v[12:15],a[212:215],a[240:243],v[12:15]\nbuffer_load_dwordx4 v226,s[56:59],s72 offen offset:3072 lds\nv_mfma_f32_16x16x32_bf16 v[40:43],a[216:219],a[240:243],v[40:43]\nds_read_b64_tr_b16 a[204:205],v233 offset:4096\nds_read_b128 v[160:163],v255 offset:60672\nv_mfma_f32_16x16x32_bf16 v[68:71],a[220:223],a[240:243],v[68:71]\n"
"ds_read_b64_tr_b16 a[206:207],v233 offset:5120\nv_mfma_f32_16x16x32_bf16 v[96:99],a[224:227],a[240:243],v[96:99]\nds_read_b128 a[240:243],v239 offset:49152\nds_read_b128 v[164:167],v255 offset:60928\nv_mfma_f32_16x16x32_bf16 v[16:19],a[212:215],a[244:247],v[16:19]\nds_read_b64_tr_b16 a[208:209],v234 offset:4096\nv_mfma_f32_16x16x32_bf16 v[44:47],a[216:219],a[244:247],v[44:47]\nds_read_b128 v[168:171],v255 offset:61440\n"
"ds_read_b128 v[172:175],v255 offset:61696\nv_mfma_f32_16x16x32_bf16 v[72:75],a[220:223],a[244:247],v[72:75]\nds_read_b128 v[176:179],v255 offset:61952\nv_mfma_f32_16x16x32_bf16 v[100:103],a[224:227],a[244:247],v[100:103]\nds_read_b128 a[244:247],v239 offset:51200\nds_read_b128 v[180:183],v255 offset:62208\nv_mfma_f32_16x16x32_bf16 v[20:23],a[212:215],a[248:251],v[20:23]\nds_read_b64_tr_b16 a[210:211],v234 offset:5120\n"
"s_add_u32 m0,s64,4096\nv_mfma_f32_16x16x32_bf16 v[48:51],a[216:219],a[248:251],v[48:51]\ns_mov_b32 s73,315392\nds_read_b128 v[184:187],v255 offset:62464\nv_mfma_f32_16x16x32_bf16 v[76:79],a[220:223],a[248:251],v[76:79]\nds_read_b128 v[188:191],v255 offset:62720\nv_mfma_f32_16x16x32_bf16 v[104:107],a[224:227],a[248:251],v[104:107]\nds_read_b128 a[248:251],v239 offset:53248\n"
"ds_read_b128 v[192:195],v255 offset:62976\nv_mfma_f32_16x16x32_bf16 v[24:27],a[212:215],a[252:255],v[24:27]\nv_mfma_f32_16x16x32_bf16 v[52:55],a[216:219],a[252:255],v[52:55]\nv_mfma_f32_16x16x32_bf16 v[80:83],a[220:223],a[252:255],v[80:83]\nv_mfma_f32_16x16x32_bf16 v[108:111],a[224:227],a[252:255],v[108:111]\nds_read_b128 a[252:255],v239 offset:55296\ns_waitcnt lgkmcnt(5)\nv_mfma_f32_16x16x32_bf16 v[0:3],a[196:199],a[228:231],v[0:3]\n"
"buffer_load_dwordx4 v226,s[56:59],s73 offen offset:0 lds\ns_waitcnt lgkmcnt(1)\nv_mfma_f32_16x16x32_bf16 v[28:31],a[200:203],a[228:231],v[28:31]\ns_waitcnt vmcnt(13)\nds_read_b64_tr_b16 a[212:213],v231 offset:8192\nv_add_f32 v140,v140,v141\nv_mfma_f32_16x16x32_bf16 v[56:59],a[204:207],a[228:231],v[56:59]\nds_read_b64_tr_b16 a[214:215],v231 offset:9216\n"
"v_add_f32 v142,v142,v143\nv_mfma_f32_16x16x32_bf16 v[84:87],a[208:211],a[228:231],v[84:87]\nds_read_b128 a[228:231],v239 offset:44032\nv_add_f32 v140,v140,v142\nv_add_f32 v168,v168,v169\nv_mfma_f32_16x16x32_bf16 v[4:7],a[196:199],a[232:235],v[4:7]\ns_mov_b32 s74,328704\nbuffer_load_dwordx4 v226,s[56:59],s74 offen offset:1024 lds\n"
"v_mfma_f32_16x16x32_bf16 v[32:35],a[200:203],a[232:235],v[32:35]\nds_read_b64_tr_b16 a[216:217],v232 offset:8192\nv_add_f32 v170,v170,v171\nv_add_f32 v168,v168,v170\nv_mfma_f32_16x16x32_bf16 v[60:63],a[204:207],a[232:235],v[60:63]\ns_mov_b32 s75,320512\nv_add_f32 v144,v144,v145\nv_add_f32 v146,v146,v147\n"
"v_mfma_f32_16x16x32_bf16 v[88:91],a[208:211],a[232:235],v[88:91]\nds_read_b128 a[232:235],v239 offset:46080\nv_add_f32 v144,v144,v146\nv_add_f32 v172,v172,v173\nv_mfma_f32_16x16x32_bf16 v[8:11],a[196:199],a[236:239],v[8:11]\nbuffer_load_dwordx4 v226,s[56:59],s75 offen offset:2048 lds\nv_add_f32 v174,v174,v175\nv_mfma_f32_16x16x32_bf16 v[36:39],a[200:203],a[236:239],v[36:39]\n"
"ds_read_b64_tr_b16 a[218:219],v232 offset:9216\nv_add_f32 v172,v172,v174\nv_add_f32 v148,v148,v149\nv_mfma_f32_16x16x32_bf16 v[64:67],a[204:207],a[236:239],v[64:67]\ns_mov_b32 s72,333824\nv_add_f32 v150,v150,v151\nv_add_f32 v148,v148,v150\nv_mfma_f32_16x16x32_bf16 v[92:95],a[208:211],a[236:239],v[92:95]\n"
"ds_read_b128 a[236:239],v239 offset:48128\nv_add_f32 v176,v176,v177\nv_add_f32 v178,v178,v179\nv_mfma_f32_16x16x32_bf16 v[12:15],a[196:199],a[240:243],v[12:15]\nbuffer_load_dwordx4 v226,s[56:59],s72 offen offset:3072 lds\nv_add_f32 v176,v176,v178\nv_mfma_f32_16x16x32_bf16 v[40:43],a[200:203],a[240:243],v[40:43]\nds_read_b64_tr_b16 a[220:221],v233 offset:8192\n"
"v_add_f32 v152,v152,v153\nv_add_f32 v154,v154,v155\nv_mfma_f32_16x16x32_bf16 v[68:71],a[204:207],a[240:243],v[68:71]\nds_read_b64_tr_b16 a[222:223],v233 offset:9216\nv_add_f32 v152,v152,v154\nv_mfma_f32_16x16x32_bf16 v[96:99],a[208:211],a[240:243],v[96:99]\nds_read_b128 a[240:243],v239 offset:50176\nv_add_f32 v180,v180,v181\n"
"v_add_f32 v182,v182,v183\nv_mfma_f32_16x16x32_bf16 v[16:19],a[196:199],a[244:247],v[16:19]\nds_read_b64_tr_b16 a[224:225],v234 offset:8192\nv_add_f32 v180,v180,v182\nv_mfma_f32_16x16x32_bf16 v[44:47],a[200:203],a[244:247],v[44:47]\nv_add_f32 v156,v156,v157\nv_add_f32 v158,v158,v159\nv_add_f32 v156,v156,v158\n"
"v_mfma_f32_16x16x32_bf16 v[72:75],a[204:207],a[244:247],v[72:75]\nv_add_f32 v184,v184,v185\nv_add_f32 v186,v186,v187\nv_mfma_f32_16x16x32_bf16 v[100:103],a[208:211],a[244:247],v[100:103]\nds_read_b128 a[244:247],v239 offset:52224\ns_waitcnt lgkmcnt(12)\nv_add_f32 v184,v184,v186\nv_mfma_f32_16x16x32_bf16 v[20:23],a[196:199],a[248:251],v[20:23]\n"
"ds_read_b64_tr_b16 a[226:227],v234 offset:9216\nv_add_f32 v160,v160,v161\nv_mfma_f32_16x16x32_bf16 v[48:51],a[200:203],a[248:251],v[48:51]\ns_add_u32 m0,s64,8192\nv_add_f32 v162,v162,v163\nv_add_f32 v160,v160,v162\nv_add_f32 v188,v188,v189\nv_mfma_f32_16x16x32_bf16 v[76:79],a[204:207],a[248:251],v[76:79]\n"
"s_mov_b32 s73,344064\nv_add_f32 v190,v190,v191\nv_add_f32 v188,v188,v190\nv_mfma_f32_16x16x32_bf16 v[104:107],a[208:211],a[248:251],v[104:107]\nds_read_b128 a[248:251],v239 offset:54272\nv_add_f32 v164,v164,v165\nv_add_f32 v166,v166,v167\nv_mfma_f32_16x16x32_bf16 v[24:27],a[196:199],a[252:255],v[24:27]\n"
"v_add_f32 v164,v164,v166\nv_add_f32 v192,v192,v193\nv_mfma_f32_16x16x32_bf16 v[52:55],a[200:203],a[252:255],v[52:55]\nv_add_f32 v194,v194,v195\nv_add_f32 v192,v192,v194\nv_fma_f32 v140,v140,v241,v168\nv_mfma_f32_16x16x32_bf16 v[80:83],a[204:207],a[252:255],v[80:83]\nv_fma_f32 v144,v144,v242,v172\n"
"v_fma_f32 v148,v148,v243,v176\nv_mfma_f32_16x16x32_bf16 v[108:111],a[208:211],a[252:255],v[108:111]\nds_read_b128 a[252:255],v239 offset:56320\ns_waitcnt lgkmcnt(2)\nv_fma_f32 v152,v152,v244,v180\nv_mfma_f32_16x16x32_bf16 v[0:3],a[212:215],a[228:231],v[0:3]\nbuffer_load_dwordx4 v226,s[56:59],s73 offen offset:0 lds\nv_fma_f32 v156,v156,v245,v184\n"
"v_mfma_f32_16x16x32_bf16 v[28:31],a[216:219],a[228:231],v[28:31]\ns_waitcnt vmcnt(13)\nds_read_b64_tr_b16 a[196:197],v231 offset:12288\nv_fma_f32 v160,v160,v246,v188\nv_mfma_f32_16x16x32_bf16 v[56:59],a[220:223],a[228:231],v[56:59]\nds_read_b64_tr_b16 a[198:199],v231 offset:13312\nv_fma_f32 v164,v164,v247,v192\nv_mfma_f32_16x16x32_bf16 v[84:87],a[224:227],a[228:231],v[84:87]\n"
"s_waitcnt lgkmcnt(2)\ns_barrier\nv_rcp_f32 v248,v140\nv_mfma_f32_16x16x32_bf16 v[4:7],a[212:215],a[232:235],v[4:7]\ns_mov_b32 s74,357376\nbuffer_load_dwordx4 v226,s[56:59],s74 offen offset:1024 lds\nv_mfma_f32_16x16x32_bf16 v[32:35],a[216:219],a[232:235],v[32:35]\nds_read_b64_tr_b16 a[200:201],v232 offset:12288\n"
"v_rcp_f32 v249,v144\nv_mfma_f32_16x16x32_bf16 v[60:63],a[220:223],a[232:235],v[60:63]\ns_mov_b32 s75,349184\nv_rcp_f32 v250,v148\nv_mfma_f32_16x16x32_bf16 v[88:91],a[224:227],a[232:235],v[88:91]\nv_rcp_f32 v251,v152\nv_mov_b32 v242,14336\nv_mfma_f32_16x16x32_bf16 v[8:11],a[212:215],a[236:239],v[8:11]\n"
"buffer_load_dwordx4 v226,s[56:59],s75 offen offset:2048 lds\nv_mad_u32_u24 v241,v242,s70,v239\nv_mfma_f32_16x16x32_bf16 v[36:39],a[216:219],a[236:239],v[36:39]\nds_read_b64_tr_b16 a[202:203],v232 offset:13312\nv_rcp_f32 v252,v156\nv_mfma_f32_16x16x32_bf16 v[64:67],a[220:223],a[236:239],v[64:67]\ns_mov_b32 s72,362496\nv_rcp_f32 v253,v160\n"
"v_mfma_f32_16x16x32_bf16 v[92:95],a[224:227],a[236:239],v[92:95]\nv_rcp_f32 v254,v164\nv_mul_f32 v0,v248,v0\nv_mfma_f32_16x16x32_bf16 v[12:15],a[212:215],a[240:243],v[12:15]\nbuffer_load_dwordx4 v226,s[56:59],s72 offen offset:3072 lds\nv_mul_f32 v1,v248,v1\nv_mfma_f32_16x16x32_bf16 v[40:43],a[216:219],a[240:243],v[40:43]\nds_read_b64_tr_b16 a[204:205],v233 offset:12288\n"
"v_mul_f32 v2,v248,v2\nv_mul_f32 v3,v248,v3\nv_mfma_f32_16x16x32_bf16 v[68:71],a[220:223],a[240:243],v[68:71]\nds_read_b64_tr_b16 a[206:207],v233 offset:13312\nv_cvt_pk_bf16_f32 v0,v0,v1\nv_mfma_f32_16x16x32_bf16 v[96:99],a[224:227],a[240:243],v[96:99]\nv_cvt_pk_bf16_f32 v1,v2,v3\nv_mul_f32 v28,v248,v28\n"
"v_mul_f32 v29,v248,v29\nv_mfma_f32_16x16x32_bf16 v[16:19],a[212:215],a[244:247],v[16:19]\nds_read_b64_tr_b16 a[208:209],v234 offset:12288\nv_mul_f32 v30,v248,v30\nv_mfma_f32_16x16x32_bf16 v[44:47],a[216:219],a[244:247],v[44:47]\nv_mul_f32 v31,v248,v31\nv_cvt_pk_bf16_f32 v2,v28,v29\nv_cvt_pk_bf16_f32 v3,v30,v31\n"
"v_mfma_f32_16x16x32_bf16 v[72:75],a[220:223],a[244:247],v[72:75]\nds_write_b128 v241,v[0:3] offset:0\nv_mfma_f32_16x16x32_bf16 v[100:103],a[224:227],a[244:247],v[100:103]\nv_mul_f32 v56,v248,v56\nv_mul_f32 v57,v248,v57\nv_mul_f32 v58,v248,v58\nv_mfma_f32_16x16x32_bf16 v[20:23],a[212:215],a[248:251],v[20:23]\nds_read_b64_tr_b16 a[210:211],v234 offset:13312\n"
"v_mul_f32 v59,v248,v59\nv_mfma_f32_16x16x32_bf16 v[48:51],a[216:219],a[248:251],v[48:51]\ns_add_u32 m0,s64,12288\nv_cvt_pk_bf16_f32 v56,v56,v57\nv_cvt_pk_bf16_f32 v57,v58,v59\nv_mul_f32 v84,v248,v84\nv_mfma_f32_16x16x32_bf16 v[76:79],a[220:223],a[248:251],v[76:79]\ns_mov_b32 s73,372736\n"
"v_mul_f32 v85,v248,v85\nv_mul_f32 v86,v248,v86\nv_mfma_f32_16x16x32_bf16 v[104:107],a[224:227],a[248:251],v[104:107]\nv_mul_f32 v87,v248,v87\nv_cvt_pk_bf16_f32 v58,v84,v85\nv_cvt_pk_bf16_f32 v59,v86,v87\nv_mfma_f32_16x16x32_bf16 v[24:27],a[212:215],a[252:255],v[24:27]\nds_write_b128 v241,v[56:59] offset:1024\n"
"v_mfma_f32_16x16x32_bf16 v[52:55],a[216:219],a[252:255],v[52:55]\nv_mul_f32 v4,v249,v4\nv_mul_f32 v5,v249,v5\nv_mul_f32 v6,v249,v6\nv_mfma_f32_16x16x32_bf16 v[80:83],a[220:223],a[252:255],v[80:83]\nv_mul_f32 v7,v249,v7\nv_cvt_pk_bf16_f32 v4,v4,v5\nv_mfma_f32_16x16x32_bf16 v[108:111],a[224:227],a[252:255],v[108:111]\n"
"v_cvt_pk_bf16_f32 v5,v6,v7\nv_mul_f32 v32,v249,v32\nv_mul_f32 v33,v249,v33\nv_mul_f32 v34,v249,v34\nv_mul_f32 v35,v249,v35\nv_cvt_pk_bf16_f32 v6,v32,v33\nv_cvt_pk_bf16_f32 v7,v34,v35\nds_write_b128 v241,v[4:7] offset:2048\n"
"v_mul_f32 v60,v249,v60\nv_mul_f32 v61,v249,v61\nv_mul_f32 v62,v249,v62\nv_mul_f32 v63,v249,v63\nv_cvt_pk_bf16_f32 v60,v60,v61\nv_cvt_pk_bf16_f32 v61,v62,v63\nv_mul_f32 v88,v249,v88\nv_mul_f32 v89,v249,v89\n"
"v_mul_f32 v90,v249,v90\nv_mul_f32 v91,v249,v91\nv_cvt_pk_bf16_f32 v62,v88,v89\nv_cvt_pk_bf16_f32 v63,v90,v91\nds_write_b128 v241,v[60:63] offset:3072\nv_pk_mul_f32 v[8:9],v[8:9],v[250:251] op_sel:[0,0] op_sel_hi:[1,0]\nv_pk_mul_f32 v[10:11],v[10:11],v[250:251] op_sel:[0,0] op_sel_hi:[1,0]\nv_cvt_pk_bf16_f32 v8,v8,v9\n"
"v_cvt_pk_bf16_f32 v9,v10,v11\nv_pk_mul_f32 v[36:37],v[36:37],v[250:251] op_sel:[0,0] op_sel_hi:[1,0]\nv_pk_mul_f32 v[38:39],v[38:39],v[250:251] op_sel:[0,0] op_sel_hi:[1,0]\nv_cvt_pk_bf16_f32 v10,v36,v37\nv_cvt_pk_bf16_f32 v11,v38,v39\nds_write_b128 v241,v[8:11] offset:4096\nv_pk_mul_f32 v[64:65],v[64:65],v[250:251] op_sel:[0,0] op_sel_hi:[1,0]\nv_pk_mul_f32 v[66:67],v[66:67],v[250:251] op_sel:[0,0] op_sel_hi:[1,0]\n"
"v_cvt_pk_bf16_f32 v64,v64,v65\nv_cvt_pk_bf16_f32 v65,v66,v67\nv_pk_mul_f32 v[92:93],v[92:93],v[250:251] op_sel:[0,0] op_sel_hi:[1,0]\nv_pk_mul_f32 v[94:95],v[94:95],v[250:251] op_sel:[0,0] op_sel_hi:[1,0]\nv_cvt_pk_bf16_f32 v66,v92,v93\nv_cvt_pk_bf16_f32 v67,v94,v95\nds_write_b128 v241,v[64:67] offset:5120\nv_pk_mul_f32 v[12:13],v[12:13],v[250:251] op_sel:[0,1] op_sel_hi:[1,1]\n"
"v_pk_mul_f32 v[14:15],v[14:15],v[250:251] op_sel:[0,1] op_sel_hi:[1,1]\nv_cvt_pk_bf16_f32 v12,v12,v13\nv_cvt_pk_bf16_f32 v13,v14,v15\nv_pk_mul_f32 v[40:41],v[40:41],v[250:251] op_sel:[0,1] op_sel_hi:[1,1]\nv_pk_mul_f32 v[42:43],v[42:43],v[250:251] op_sel:[0,1] op_sel_hi:[1,1]\nv_cvt_pk_bf16_f32 v14,v40,v41\nv_cvt_pk_bf16_f32 v15,v42,v43\nds_write_b128 v241,v[12:15] offset:6144\n"
"v_pk_mul_f32 v[68:69],v[68:69],v[250:251] op_sel:[0,1] op_sel_hi:[1,1]\nv_pk_mul_f32 v[70:71],v[70:71],v[250:251] op_sel:[0,1] op_sel_hi:[1,1]\nv_cvt_pk_bf16_f32 v68,v68,v69\nv_cvt_pk_bf16_f32 v69,v70,v71\nv_pk_mul_f32 v[96:97],v[96:97],v[250:251] op_sel:[0,1] op_sel_hi:[1,1]\nv_pk_mul_f32 v[98:99],v[98:99],v[250:251] op_sel:[0,1] op_sel_hi:[1,1]\nv_cvt_pk_bf16_f32 v70,v96,v97\nv_cvt_pk_bf16_f32 v71,v98,v99\n"
"ds_write_b128 v241,v[68:71] offset:7168\nv_pk_mul_f32 v[16:17],v[16:17],v[252:253] op_sel:[0,0] op_sel_hi:[1,0]\nv_pk_mul_f32 v[18:19],v[18:19],v[252:253] op_sel:[0,0] op_sel_hi:[1,0]\nv_cvt_pk_bf16_f32 v16,v16,v17\nv_cvt_pk_bf16_f32 v17,v18,v19\nv_pk_mul_f32 v[44:45],v[44:45],v[252:253] op_sel:[0,0] op_sel_hi:[1,0]\nv_pk_mul_f32 v[46:47],v[46:47],v[252:253] op_sel:[0,0] op_sel_hi:[1,0]\nv_cvt_pk_bf16_f32 v18,v44,v45\n"
"v_cvt_pk_bf16_f32 v19,v46,v47\nds_write_b128 v241,v[16:19] offset:8192\nv_pk_mul_f32 v[72:73],v[72:73],v[252:253] op_sel:[0,0] op_sel_hi:[1,0]\nv_pk_mul_f32 v[74:75],v[74:75],v[252:253] op_sel:[0,0] op_sel_hi:[1,0]\nv_cvt_pk_bf16_f32 v72,v72,v73\nv_cvt_pk_bf16_f32 v73,v74,v75\nv_pk_mul_f32 v[100:101],v[100:101],v[252:253] op_sel:[0,0] op_sel_hi:[1,0]\nv_pk_mul_f32 v[102:103],v[102:103],v[252:253] op_sel:[0,0] op_sel_hi:[1,0]\n"
"v_cvt_pk_bf16_f32 v74,v100,v101\nv_cvt_pk_bf16_f32 v75,v102,v103\nds_write_b128 v241,v[72:75] offset:9216\nv_pk_mul_f32 v[20:21],v[20:21],v[252:253] op_sel:[0,1] op_sel_hi:[1,1]\nv_pk_mul_f32 v[22:23],v[22:23],v[252:253] op_sel:[0,1] op_sel_hi:[1,1]\nv_cvt_pk_bf16_f32 v20,v20,v21\nv_cvt_pk_bf16_f32 v21,v22,v23\nv_pk_mul_f32 v[48:49],v[48:49],v[252:253] op_sel:[0,1] op_sel_hi:[1,1]\n"
"v_pk_mul_f32 v[50:51],v[50:51],v[252:253] op_sel:[0,1] op_sel_hi:[1,1]\nv_cvt_pk_bf16_f32 v22,v48,v49\nv_cvt_pk_bf16_f32 v23,v50,v51\nds_write_b128 v241,v[20:23] offset:10240\nv_pk_mul_f32 v[76:77],v[76:77],v[252:253] op_sel:[0,1] op_sel_hi:[1,1]\nv_pk_mul_f32 v[78:79],v[78:79],v[252:253] op_sel:[0,1] op_sel_hi:[1,1]\nv_cvt_pk_bf16_f32 v76,v76,v77\nv_cvt_pk_bf16_f32 v77,v78,v79\n"
"v_pk_mul_f32 v[104:105],v[104:105],v[252:253] op_sel:[0,1] op_sel_hi:[1,1]\nv_pk_mul_f32 v[106:107],v[106:107],v[252:253] op_sel:[0,1] op_sel_hi:[1,1]\nv_cvt_pk_bf16_f32 v78,v104,v105\nv_cvt_pk_bf16_f32 v79,v106,v107\nds_write_b128 v241,v[76:79] offset:11264\nv_pk_mul_f32 v[24:25],v[24:25],v[254:255] op_sel:[0,0] op_sel_hi:[1,0]\nv_pk_mul_f32 v[26:27],v[26:27],v[254:255] op_sel:[0,0] op_sel_hi:[1,0]\nv_cvt_pk_bf16_f32 v24,v24,v25\n"
"v_cvt_pk_bf16_f32 v25,v26,v27\nv_pk_mul_f32 v[52:53],v[52:53],v[254:255] op_sel:[0,0] op_sel_hi:[1,0]\nv_pk_mul_f32 v[54:55],v[54:55],v[254:255] op_sel:[0,0] op_sel_hi:[1,0]\nv_cvt_pk_bf16_f32 v26,v52,v53\nv_cvt_pk_bf16_f32 v27,v54,v55\nds_write_b128 v241,v[24:27] offset:12288\nv_pk_mul_f32 v[80:81],v[80:81],v[254:255] op_sel:[0,0] op_sel_hi:[1,0]\nv_pk_mul_f32 v[82:83],v[82:83],v[254:255] op_sel:[0,0] op_sel_hi:[1,0]\n"
"v_cvt_pk_bf16_f32 v80,v80,v81\nv_cvt_pk_bf16_f32 v81,v82,v83\nv_pk_mul_f32 v[108:109],v[108:109],v[254:255] op_sel:[0,0] op_sel_hi:[1,0]\nv_pk_mul_f32 v[110:111],v[110:111],v[254:255] op_sel:[0,0] op_sel_hi:[1,0]\nv_cvt_pk_bf16_f32 v82,v108,v109\nv_cvt_pk_bf16_f32 v83,v110,v111\nds_write_b128 v241,v[80:83] offset:13312\ns_waitcnt lgkmcnt(0)\n"
"s_barrier\nds_read_b128 a[228:231],v239 offset:0\nds_read_b128 a[232:235],v239 offset:2048\nds_read_b128 a[236:239],v239 offset:4096\nds_read_b128 a[240:243],v239 offset:6144\nds_read_b128 a[244:247],v239 offset:8192\nds_read_b128 a[248:251],v239 offset:10240\nds_read_b128 a[252:255],v239 offset:12288\n"
"s_waitcnt lgkmcnt(6)\nv_mfma_f32_16x16x32_bf16 a[0:3],a[196:199],a[228:231],a[0:3]\nbuffer_load_dwordx4 v226,s[56:59],s73 offen offset:0 lds\ns_mov_b32 s74,386048\nv_mfma_f32_16x16x32_bf16 a[28:31],a[200:203],a[228:231],a[28:31]\ns_waitcnt vmcnt(13)\nds_read_b64_tr_b16 a[212:213],v231 offset:16384\nv_mfma_f32_16x16x32_bf16 a[56:59],a[204:207],a[228:231],a[56:59]\n"
"ds_read_b64_tr_b16 a[214:215],v231 offset:17408\nv_mfma_f32_16x16x32_bf16 a[84:87],a[208:211],a[228:231],a[84:87]\nds_read_b128 a[228:231],v239 offset:1024\ns_waitcnt lgkmcnt(8)\nv_mfma_f32_16x16x32_bf16 a[4:7],a[196:199],a[232:235],a[4:7]\nbuffer_load_dwordx4 v226,s[56:59],s74 offen offset:1024 lds\ns_mov_b32 s75,377856\nv_mfma_f32_16x16x32_bf16 a[32:35],a[200:203],a[232:235],a[32:35]\n"
"ds_read_b64_tr_b16 a[216:217],v232 offset:16384\nv_mfma_f32_16x16x32_bf16 a[60:63],a[204:207],a[232:235],a[60:63]\nv_mfma_f32_16x16x32_bf16 a[88:91],a[208:211],a[232:235],a[88:91]\nds_read_b128 a[232:235],v239 offset:3072\ns_waitcnt lgkmcnt(9)\nv_mfma_f32_16x16x32_bf16 a[8:11],a[196:199],a[236:239],a[8:11]\nbuffer_load_dwordx4 v226,s[56:59],s75 offen offset:2048 lds\ns_mov_b32 s72,391168\n"
"v_mfma_f32_16x16x32_bf16 a[36:39],a[200:203],a[236:239],a[36:39]\nds_read_b64_tr_b16 a[218:219],v232 offset:17408\nv_mfma_f32_16x16x32_bf16 a[64:67],a[204:207],a[236:239],a[64:67]\nv_mfma_f32_16x16x32_bf16 a[92:95],a[208:211],a[236:239],a[92:95]\nds_read_b128 a[236:239],v239 offset:5120\ns_waitcnt lgkmcnt(9)\nv_mfma_f32_16x16x32_bf16 a[12:15],a[196:199],a[240:243],a[12:15]\nbuffer_load_dwordx4 v226,s[56:59],s72 offen offset:3072 lds\n"
"v_mfma_f32_16x16x32_bf16 a[40:43],a[200:203],a[240:243],a[40:43]\nds_read_b64_tr_b16 a[220:221],v233 offset:16384\nv_mfma_f32_16x16x32_bf16 a[68:71],a[204:207],a[240:243],a[68:71]\nds_read_b64_tr_b16 a[222:223],v233 offset:17408\nv_mfma_f32_16x16x32_bf16 a[96:99],a[208:211],a[240:243],a[96:99]\nds_read_b128 a[240:243],v239 offset:7168\nv_mfma_f32_16x16x32_bf16 a[16:19],a[196:199],a[244:247],a[16:19]\nds_read_b64_tr_b16 a[224:225],v234 offset:16384\n"
"v_mfma_f32_16x16x32_bf16 a[44:47],a[200:203],a[244:247],a[44:47]\nv_mfma_f32_16x16x32_bf16 a[72:75],a[204:207],a[244:247],a[72:75]\nv_mfma_f32_16x16x32_bf16 a[100:103],a[208:211],a[244:247],a[100:103]\nds_read_b128 a[244:247],v239 offset:9216\ns_waitcnt lgkmcnt(12)\nv_mfma_f32_16x16x32_bf16 a[20:23],a[196:199],a[248:251],a[20:23]\nds_read_b64_tr_b16 a[226:227],v234 offset:17408\ns_add_u32 m0,s64,16384\n"
"v_mfma_f32_16x16x32_bf16 a[48:51],a[200:203],a[248:251],a[48:51]\ns_mov_b32 s73,401408\nv_mfma_f32_16x16x32_bf16 a[76:79],a[204:207],a[248:251],a[76:79]\nv_mfma_f32_16x16x32_bf16 a[104:107],a[208:211],a[248:251],a[104:107]\nds_read_b128 a[248:251],v239 offset:11264\nv_mfma_f32_16x16x32_bf16 a[24:27],a[196:199],a[252:255],a[24:27]\nv_mfma_f32_16x16x32_bf16 a[52:55],a[200:203],a[252:255],a[52:55]\nv_mfma_f32_16x16x32_bf16 a[80:83],a[204:207],a[252:255],a[80:83]\n"
"v_mfma_f32_16x16x32_bf16 a[108:111],a[208:211],a[252:255],a[108:111]\nds_read_b128 a[252:255],v239 offset:13312\ns_waitcnt lgkmcnt(2)\nv_mfma_f32_16x16x32_bf16 a[0:3],a[212:215],a[228:231],a[0:3]\nbuffer_load_dwordx4 v226,s[56:59],s73 offen offset:0 lds\ns_mov_b32 s74,414720\nv_mfma_f32_16x16x32_bf16 a[28:31],a[216:219],a[228:231],a[28:31]\ns_waitcnt vmcnt(13)\n"
"ds_read_b64_tr_b16 a[196:197],v231 offset:0\nv_mfma_f32_16x16x32_bf16 a[56:59],a[220:223],a[228:231],a[56:59]\nds_read_b64_tr_b16 a[198:199],v231 offset:1024\nv_mfma_f32_16x16x32_bf16 a[84:87],a[224:227],a[228:231],a[84:87]\nds_read_b128 a[228:231],v239 offset:14336\nv_mfma_f32_16x16x32_bf16 a[4:7],a[212:215],a[232:235],a[4:7]\nbuffer_load_dwordx4 v226,s[56:59],s74 offen offset:1024 lds\ns_mov_b32 s75,406528\n"
"v_mfma_f32_16x16x32_bf16 a[32:35],a[216:219],a[232:235],a[32:35]\nds_read_b64_tr_b16 a[200:201],v232 offset:0\nv_mfma_f32_16x16x32_bf16 a[60:63],a[220:223],a[232:235],a[60:63]\nv_mfma_f32_16x16x32_bf16 a[88:91],a[224:227],a[232:235],a[88:91]\nds_read_b128 a[232:235],v239 offset:16384\nv_mfma_f32_16x16x32_bf16 a[8:11],a[212:215],a[236:239],a[8:11]\nbuffer_load_dwordx4 v226,s[56:59],s75 offen offset:2048 lds\ns_mov_b32 s72,419840\n"
"v_mfma_f32_16x16x32_bf16 a[36:39],a[216:219],a[236:239],a[36:39]\nds_read_b64_tr_b16 a[202:203],v232 offset:1024\nv_mfma_f32_16x16x32_bf16 a[64:67],a[220:223],a[236:239],a[64:67]\nv_mfma_f32_16x16x32_bf16 a[92:95],a[224:227],a[236:239],a[92:95]\nds_read_b128 a[236:239],v239 offset:18432\nv_mfma_f32_16x16x32_bf16 a[12:15],a[212:215],a[240:243],a[12:15]\nbuffer_load_dwordx4 v226,s[56:59],s72 offen offset:3072 lds\nv_mfma_f32_16x16x32_bf16 a[40:43],a[216:219],a[240:243],a[40:43]\n"
"ds_read_b64_tr_b16 a[204:205],v233 offset:0\nv_mfma_f32_16x16x32_bf16 a[68:71],a[220:223],a[240:243],a[68:71]\nds_read_b64_tr_b16 a[206:207],v233 offset:1024\nv_mfma_f32_16x16x32_bf16 a[96:99],a[224:227],a[240:243],a[96:99]\nds_read_b128 a[240:243],v239 offset:20480\nv_mfma_f32_16x16x32_bf16 a[16:19],a[212:215],a[244:247],a[16:19]\nds_read_b64_tr_b16 a[208:209],v234 offset:0\nv_mfma_f32_16x16x32_bf16 a[44:47],a[216:219],a[244:247],a[44:47]\n"
"v_mfma_f32_16x16x32_bf16 a[72:75],a[220:223],a[244:247],a[72:75]\nv_mfma_f32_16x16x32_bf16 a[100:103],a[224:227],a[244:247],a[100:103]\nds_read_b128 a[244:247],v239 offset:22528\ns_waitcnt lgkmcnt(12)\nv_mfma_f32_16x16x32_bf16 a[20:23],a[212:215],a[248:251],a[20:23]\nds_read_b64_tr_b16 a[210:211],v234 offset:1024\ns_add_u32 m0,s64,0\nv_mfma_f32_16x16x32_bf16 a[48:51],a[216:219],a[248:251],a[48:51]\n"
"s_mov_b32 s73,430080\nv_mfma_f32_16x16x32_bf16 a[76:79],a[220:223],a[248:251],a[76:79]\nv_mfma_f32_16x16x32_bf16 a[104:107],a[224:227],a[248:251],a[104:107]\nds_read_b128 a[248:251],v239 offset:24576\nv_mfma_f32_16x16x32_bf16 a[24:27],a[212:215],a[252:255],a[24:27]\nv_mfma_f32_16x16x32_bf16 a[52:55],a[216:219],a[252:255],a[52:55]\nv_mfma_f32_16x16x32_bf16 a[80:83],a[220:223],a[252:255],a[80:83]\nv_mfma_f32_16x16x32_bf16 a[108:111],a[224:227],a[252:255],a[108:111]\n"
"ds_read_b128 a[252:255],v239 offset:26624\ns_waitcnt lgkmcnt(2)\nv_mfma_f32_16x16x32_bf16 a[0:3],a[196:199],a[228:231],a[0:3]\nbuffer_load_dwordx4 v226,s[56:59],s73 offen offset:0 lds\nv_lshrrev_b32 v200,2,v239\nv_mfma_f32_16x16x32_bf16 a[28:31],a[200:203],a[228:231],a[28:31]\ns_waitcnt vmcnt(13)\nds_read_b64_tr_b16 a[212:213],v231 offset:4096\n"
"v_mfma_f32_16x16x32_bf16 a[56:59],a[204:207],a[228:231],a[56:59]\nds_read_b64_tr_b16 a[214:215],v231 offset:5120\nv_lshlrev_b32 v201,3,v239\nv_mfma_f32_16x16x32_bf16 a[84:87],a[208:211],a[228:231],a[84:87]\nds_read_b128 a[228:231],v239 offset:15360\nv_lshl_add_u32 v200,%12,7,v200\nv_mfma_f32_16x16x32_bf16 a[4:7],a[196:199],a[232:235],a[4:7]\ns_mov_b32 s74,443392\n"
"buffer_load_dwordx4 v226,s[56:59],s74 offen offset:1024 lds\nv_mfma_f32_16x16x32_bf16 a[32:35],a[200:203],a[232:235],a[32:35]\nds_read_b64_tr_b16 a[216:217],v232 offset:4096\nv_lshl_add_u32 v201,%12,2,v201\nv_mfma_f32_16x16x32_bf16 a[60:63],a[204:207],a[232:235],a[60:63]\ns_mov_b32 s75,435200\nv_lshrrev_b32 v202,7,v239\nv_add_u32 v202,s70,v202\n"
"v_mfma_f32_16x16x32_bf16 a[88:91],a[208:211],a[232:235],a[88:91]\nds_read_b128 a[232:235],v239 offset:17408\nv_lshl_add_u32 v200,v202,16,v200\nv_mfma_f32_16x16x32_bf16 a[8:11],a[196:199],a[236:239],a[8:11]\nbuffer_load_dwordx4 v226,s[56:59],s75 offen offset:2048 lds\nv_lshl_add_u32 v201,v202,16,v201\nv_mfma_f32_16x16x32_bf16 a[36:39],a[200:203],a[236:239],a[36:39]\nds_read_b64_tr_b16 a[218:219],v232 offset:5120\n"
"s_mov_b32 s72,448512\nv_mfma_f32_16x16x32_bf16 a[64:67],a[204:207],a[236:239],a[64:67]\nbuffer_load_dword v203,v200,s[96:99],0 offen sc1\nv_mfma_f32_16x16x32_bf16 a[92:95],a[208:211],a[236:239],a[92:95]\nds_read_b128 a[236:239],v239 offset:19456\nv_mfma_f32_16x16x32_bf16 a[12:15],a[196:199],a[240:243],a[12:15]\nbuffer_load_dwordx4 v226,s[56:59],s72 offen offset:3072 lds\nv_mfma_f32_16x16x32_bf16 a[40:43],a[200:203],a[240:243],a[40:43]\n"
"ds_read_b64_tr_b16 a[220:221],v233 offset:4096\nv_mfma_f32_16x16x32_bf16 a[68:71],a[204:207],a[240:243],a[68:71]\nds_read_b64_tr_b16 a[222:223],v233 offset:5120\nv_mfma_f32_16x16x32_bf16 a[96:99],a[208:211],a[240:243],a[96:99]\nds_read_b128 a[240:243],v239 offset:21504\nv_mfma_f32_16x16x32_bf16 a[16:19],a[196:199],a[244:247],a[16:19]\nds_read_b64_tr_b16 a[224:225],v234 offset:4096\nv_mfma_f32_16x16x32_bf16 a[44:47],a[200:203],a[244:247],a[44:47]\n"
"buffer_load_dword v204,v201,s[96:99],0 offen sc1\nv_mfma_f32_16x16x32_bf16 a[72:75],a[204:207],a[244:247],a[72:75]\nv_mfma_f32_16x16x32_bf16 a[100:103],a[208:211],a[244:247],a[100:103]\nds_read_b128 a[244:247],v239 offset:23552\ns_waitcnt lgkmcnt(12)\nv_mfma_f32_16x16x32_bf16 a[20:23],a[196:199],a[248:251],a[20:23]\nds_read_b64_tr_b16 a[226:227],v234 offset:5120\nv_add_u32 v226,s68,v226\n"
"v_mfma_f32_16x16x32_bf16 a[48:51],a[200:203],a[248:251],a[48:51]\ns_add_u32 m0,s64,4096\ns_mov_b32 s73,229376\nv_mfma_f32_16x16x32_bf16 a[76:79],a[204:207],a[248:251],a[76:79]\nv_mfma_f32_16x16x32_bf16 a[104:107],a[208:211],a[248:251],a[104:107]\nds_read_b128 a[248:251],v239 offset:25600\nv_mfma_f32_16x16x32_bf16 a[24:27],a[196:199],a[252:255],a[24:27]\nv_mfma_f32_16x16x32_bf16 a[52:55],a[200:203],a[252:255],a[52:55]\n"
"v_mfma_f32_16x16x32_bf16 a[80:83],a[204:207],a[252:255],a[80:83]\nv_mfma_f32_16x16x32_bf16 a[108:111],a[208:211],a[252:255],a[108:111]\nds_read_b128 a[252:255],v239 offset:27648\ns_waitcnt lgkmcnt(2)\nv_mfma_f32_16x16x32_bf16 a[0:3],a[212:215],a[228:231],a[0:3]\nbuffer_load_dwordx4 v226,s[56:59],s73 offen offset:0 lds\ns_mov_b32 s74,242688\nv_mfma_f32_16x16x32_bf16 a[28:31],a[216:219],a[228:231],a[28:31]\n"
"s_waitcnt vmcnt(15)\nds_read_b64_tr_b16 a[196:197],v231 offset:8192\nv_mfma_f32_16x16x32_bf16 a[56:59],a[220:223],a[228:231],a[56:59]\nds_read_b64_tr_b16 a[198:199],v231 offset:9216\nv_mfma_f32_16x16x32_bf16 a[84:87],a[224:227],a[228:231],a[84:87]\nds_read_b128 a[228:231],v239 offset:28672\nv_mfma_f32_16x16x32_bf16 a[4:7],a[212:215],a[232:235],a[4:7]\nbuffer_load_dwordx4 v226,s[56:59],s74 offen offset:1024 lds\n"
"s_mov_b32 s75,234496\nv_mfma_f32_16x16x32_bf16 a[32:35],a[216:219],a[232:235],a[32:35]\nds_read_b64_tr_b16 a[200:201],v232 offset:8192\nv_mfma_f32_16x16x32_bf16 a[60:63],a[220:223],a[232:235],a[60:63]\nv_mfma_f32_16x16x32_bf16 a[88:91],a[224:227],a[232:235],a[88:91]\nds_read_b128 a[232:235],v239 offset:30720\nv_mfma_f32_16x16x32_bf16 a[8:11],a[212:215],a[236:239],a[8:11]\nbuffer_load_dwordx4 v226,s[56:59],s75 offen offset:2048 lds\n"
"s_mov_b32 s72,247808\nv_mfma_f32_16x16x32_bf16 a[36:39],a[216:219],a[236:239],a[36:39]\nds_read_b64_tr_b16 a[202:203],v232 offset:9216\nv_mfma_f32_16x16x32_bf16 a[64:67],a[220:223],a[236:239],a[64:67]\nv_mfma_f32_16x16x32_bf16 a[92:95],a[224:227],a[236:239],a[92:95]\nds_read_b128 a[236:239],v239 offset:32768\nv_mfma_f32_16x16x32_bf16 a[12:15],a[212:215],a[240:243],a[12:15]\nbuffer_load_dwordx4 v226,s[56:59],s72 offen offset:3072 lds\n"
"v_mfma_f32_16x16x32_bf16 a[40:43],a[216:219],a[240:243],a[40:43]\nds_read_b64_tr_b16 a[204:205],v233 offset:8192\nv_mfma_f32_16x16x32_bf16 a[68:71],a[220:223],a[240:243],a[68:71]\nds_read_b64_tr_b16 a[206:207],v233 offset:9216\nv_mfma_f32_16x16x32_bf16 a[96:99],a[224:227],a[240:243],a[96:99]\nds_read_b128 a[240:243],v239 offset:34816\nv_mfma_f32_16x16x32_bf16 a[16:19],a[212:215],a[244:247],a[16:19]\nds_read_b64_tr_b16 a[208:209],v234 offset:8192\n"
"v_mfma_f32_16x16x32_bf16 a[44:47],a[216:219],a[244:247],a[44:47]\nv_mfma_f32_16x16x32_bf16 a[72:75],a[220:223],a[244:247],a[72:75]\nv_mfma_f32_16x16x32_bf16 a[100:103],a[224:227],a[244:247],a[100:103]\nds_read_b128 a[244:247],v239 offset:36864\ns_waitcnt lgkmcnt(12)\nv_mfma_f32_16x16x32_bf16 a[20:23],a[212:215],a[248:251],a[20:23]\nds_read_b64_tr_b16 a[210:211],v234 offset:9216\ns_add_u32 m0,s64,8192\n"
"v_mfma_f32_16x16x32_bf16 a[48:51],a[216:219],a[248:251],a[48:51]\ns_mov_b32 s73,258048\nv_mfma_f32_16x16x32_bf16 a[76:79],a[220:223],a[248:251],a[76:79]\nv_mfma_f32_16x16x32_bf16 a[104:107],a[224:227],a[248:251],a[104:107]\nds_read_b128 a[248:251],v239 offset:38912\nv_mfma_f32_16x16x32_bf16 a[24:27],a[212:215],a[252:255],a[24:27]\nv_mfma_f32_16x16x32_bf16 a[52:55],a[216:219],a[252:255],a[52:55]\nv_mfma_f32_16x16x32_bf16 a[80:83],a[220:223],a[252:255],a[80:83]\n"
"v_mfma_f32_16x16x32_bf16 a[108:111],a[224:227],a[252:255],a[108:111]\nds_read_b128 a[252:255],v239 offset:40960\ns_waitcnt lgkmcnt(2)\nv_mfma_f32_16x16x32_bf16 a[0:3],a[196:199],a[228:231],a[0:3]\nbuffer_load_dwordx4 v226,s[56:59],s73 offen offset:0 lds\ns_mov_b32 s74,271360\nv_mfma_f32_16x16x32_bf16 a[28:31],a[200:203],a[228:231],a[28:31]\ns_waitcnt vmcnt(15)\n"
"ds_read_b64_tr_b16 a[212:213],v231 offset:12288\nv_mfma_f32_16x16x32_bf16 a[56:59],a[204:207],a[228:231],a[56:59]\nds_read_b64_tr_b16 a[214:215],v231 offset:13312\nv_mfma_f32_16x16x32_bf16 a[84:87],a[208:211],a[228:231],a[84:87]\nds_read_b128 a[228:231],v239 offset:29696\nv_mfma_f32_16x16x32_bf16 a[4:7],a[196:199],a[232:235],a[4:7]\nbuffer_load_dwordx4 v226,s[56:59],s74 offen offset:1024 lds\ns_mov_b32 s75,263168\n"
"v_mfma_f32_16x16x32_bf16 a[32:35],a[200:203],a[232:235],a[32:35]\nds_read_b64_tr_b16 a[216:217],v232 offset:12288\nv_mfma_f32_16x16x32_bf16 a[60:63],a[204:207],a[232:235],a[60:63]\nv_mfma_f32_16x16x32_bf16 a[88:91],a[208:211],a[232:235],a[88:91]\nds_read_b128 a[232:235],v239 offset:31744\nv_mfma_f32_16x16x32_bf16 a[8:11],a[196:199],a[236:239],a[8:11]\nbuffer_load_dwordx4 v226,s[56:59],s75 offen offset:2048 lds\ns_mov_b32 s72,276480\n"
"v_mfma_f32_16x16x32_bf16 a[36:39],a[200:203],a[236:239],a[36:39]\nds_read_b64_tr_b16 a[218:219],v232 offset:13312\nv_mfma_f32_16x16x32_bf16 a[64:67],a[204:207],a[236:239],a[64:67]\nv_mfma_f32_16x16x32_bf16 a[92:95],a[208:211],a[236:239],a[92:95]\nds_read_b128 a[236:239],v239 offset:33792\nv_mfma_f32_16x16x32_bf16 a[12:15],a[196:199],a[240:243],a[12:15]\nbuffer_load_dwordx4 v226,s[56:59],s72 offen offset:3072 lds\nv_mfma_f32_16x16x32_bf16 a[40:43],a[200:203],a[240:243],a[40:43]\n"
"ds_read_b64_tr_b16 a[220:221],v233 offset:12288\nv_mfma_f32_16x16x32_bf16 a[68:71],a[204:207],a[240:243],a[68:71]\nds_read_b64_tr_b16 a[222:223],v233 offset:13312\nv_mfma_f32_16x16x32_bf16 a[96:99],a[208:211],a[240:243],a[96:99]\nds_read_b128 a[240:243],v239 offset:35840\nv_mfma_f32_16x16x32_bf16 a[16:19],a[196:199],a[244:247],a[16:19]\nds_read_b64_tr_b16 a[224:225],v234 offset:12288\nv_mfma_f32_16x16x32_bf16 a[44:47],a[200:203],a[244:247],a[44:47]\n"
"v_mfma_f32_16x16x32_bf16 a[72:75],a[204:207],a[244:247],a[72:75]\nv_mfma_f32_16x16x32_bf16 a[100:103],a[208:211],a[244:247],a[100:103]\nds_read_b128 a[244:247],v239 offset:37888\ns_waitcnt lgkmcnt(12)\nv_mfma_f32_16x16x32_bf16 a[20:23],a[196:199],a[248:251],a[20:23]\nds_read_b64_tr_b16 a[226:227],v234 offset:13312\ns_add_u32 m0,s64,12288\nv_mfma_f32_16x16x32_bf16 a[48:51],a[200:203],a[248:251],a[48:51]\n"
"s_mov_b32 s73,286720\nv_mfma_f32_16x16x32_bf16 a[76:79],a[204:207],a[248:251],a[76:79]\nv_mfma_f32_16x16x32_bf16 a[104:107],a[208:211],a[248:251],a[104:107]\nds_read_b128 a[248:251],v239 offset:39936\nv_mfma_f32_16x16x32_bf16 a[24:27],a[196:199],a[252:255],a[24:27]\nv_mfma_f32_16x16x32_bf16 a[52:55],a[200:203],a[252:255],a[52:55]\nv_mfma_f32_16x16x32_bf16 a[80:83],a[204:207],a[252:255],a[80:83]\nv_mfma_f32_16x16x32_bf16 a[108:111],a[208:211],a[252:255],a[108:111]\n"
"ds_read_b128 a[252:255],v239 offset:41984\ns_waitcnt lgkmcnt(2)\nv_mfma_f32_16x16x32_bf16 a[0:3],a[212:215],a[228:231],a[0:3]\nbuffer_load_dwordx4 v226,s[56:59],s73 offen offset:0 lds\ns_mov_b32 s74,300032\nv_mfma_f32_16x16x32_bf16 a[28:31],a[216:219],a[228:231],a[28:31]\ns_waitcnt vmcnt(15)\nds_read_b64_tr_b16 a[196:197],v231 offset:16384\n"
"v_mfma_f32_16x16x32_bf16 a[56:59],a[220:223],a[228:231],a[56:59]\nds_read_b64_tr_b16 a[198:199],v231 offset:17408\nv_mfma_f32_16x16x32_bf16 a[84:87],a[224:227],a[228:231],a[84:87]\nds_read_b128 a[228:231],v239 offset:43008\nv_mfma_f32_16x16x32_bf16 a[4:7],a[212:215],a[232:235],a[4:7]\nbuffer_load_dwordx4 v226,s[56:59],s74 offen offset:1024 lds\ns_mov_b32 s75,291840\nv_mfma_f32_16x16x32_bf16 a[32:35],a[216:219],a[232:235],a[32:35]\n"
"ds_read_b64_tr_b16 a[200:201],v232 offset:16384\nv_mfma_f32_16x16x32_bf16 a[60:63],a[220:223],a[232:235],a[60:63]\nv_mfma_f32_16x16x32_bf16 a[88:91],a[224:227],a[232:235],a[88:91]\nds_read_b128 a[232:235],v239 offset:45056\nv_mfma_f32_16x16x32_bf16 a[8:11],a[212:215],a[236:239],a[8:11]\nbuffer_load_dwordx4 v226,s[56:59],s75 offen offset:2048 lds\ns_mov_b32 s72,305152\nv_mfma_f32_16x16x32_bf16 a[36:39],a[216:219],a[236:239],a[36:39]\n"
"ds_read_b64_tr_b16 a[202:203],v232 offset:17408\nv_mfma_f32_16x16x32_bf16 a[64:67],a[220:223],a[236:239],a[64:67]\nv_mfma_f32_16x16x32_bf16 a[92:95],a[224:227],a[236:239],a[92:95]\nds_read_b128 a[236:239],v239 offset:47104\nv_mfma_f32_16x16x32_bf16 a[12:15],a[212:215],a[240:243],a[12:15]\nbuffer_load_dwordx4 v226,s[56:59],s72 offen offset:3072 lds\nv_mfma_f32_16x16x32_bf16 a[40:43],a[216:219],a[240:243],a[40:43]\nds_read_b64_tr_b16 a[204:205],v233 offset:16384\n"
"v_mfma_f32_16x16x32_bf16 a[68:71],a[220:223],a[240:243],a[68:71]\nds_read_b64_tr_b16 a[206:207],v233 offset:17408\nv_mfma_f32_16x16x32_bf16 a[96:99],a[224:227],a[240:243],a[96:99]\nds_read_b128 a[240:243],v239 offset:49152\nv_mfma_f32_16x16x32_bf16 a[16:19],a[212:215],a[244:247],a[16:19]\nds_read_b64_tr_b16 a[208:209],v234 offset:16384\nv_mfma_f32_16x16x32_bf16 a[44:47],a[216:219],a[244:247],a[44:47]\nv_mfma_f32_16x16x32_bf16 a[72:75],a[220:223],a[244:247],a[72:75]\n"
"v_mfma_f32_16x16x32_bf16 a[100:103],a[224:227],a[244:247],a[100:103]\nds_read_b128 a[244:247],v239 offset:51200\ns_waitcnt lgkmcnt(12)\nv_mfma_f32_16x16x32_bf16 a[20:23],a[212:215],a[248:251],a[20:23]\nds_read_b64_tr_b16 a[210:211],v234 offset:17408\ns_add_u32 m0,s64,16384\nv_mfma_f32_16x16x32_bf16 a[48:51],a[216:219],a[248:251],a[48:51]\ns_mov_b32 s73,315392\n"
"v_mfma_f32_16x16x32_bf16 a[76:79],a[220:223],a[248:251],a[76:79]\nv_mfma_f32_16x16x32_bf16 a[104:107],a[224:227],a[248:251],a[104:107]\nds_read_b128 a[248:251],v239 offset:53248\nv_mfma_f32_16x16x32_bf16 a[24:27],a[212:215],a[252:255],a[24:27]\nv_mfma_f32_16x16x32_bf16 a[52:55],a[216:219],a[252:255],a[52:55]\nv_mfma_f32_16x16x32_bf16 a[80:83],a[220:223],a[252:255],a[80:83]\nv_mfma_f32_16x16x32_bf16 a[108:111],a[224:227],a[252:255],a[108:111]\nds_read_b128 a[252:255],v239 offset:55296\n"
"s_waitcnt lgkmcnt(2)\nv_mfma_f32_16x16x32_bf16 a[0:3],a[196:199],a[228:231],a[0:3]\nbuffer_load_dwordx4 v226,s[56:59],s73 offen offset:0 lds\ns_mov_b32 s74,328704\nv_mfma_f32_16x16x32_bf16 a[28:31],a[200:203],a[228:231],a[28:31]\ns_waitcnt vmcnt(14)\nds_read_b64_tr_b16 a[212:213],v231 offset:0\nv_mfma_f32_16x16x32_bf16 a[56:59],a[204:207],a[228:231],a[56:59]\n"
"ds_read_b64_tr_b16 a[214:215],v231 offset:1024\nv_mfma_f32_16x16x32_bf16 a[84:87],a[208:211],a[228:231],a[84:87]\nds_read_b128 a[228:231],v239 offset:44032\nv_mfma_f32_16x16x32_bf16 a[4:7],a[196:199],a[232:235],a[4:7]\nbuffer_load_dwordx4 v226,s[56:59],s74 offen offset:1024 lds\ns_mov_b32 s75,320512\nv_mfma_f32_16x16x32_bf16 a[32:35],a[200:203],a[232:235],a[32:35]\nds_read_b64_tr_b16 a[216:217],v232 offset:0\n"
"v_mfma_f32_16x16x32_bf16 a[60:63],a[204:207],a[232:235],a[60:63]\nv_mfma_f32_16x16x32_bf16 a[88:91],a[208:211],a[232:235],a[88:91]\nds_read_b128 a[232:235],v239 offset:46080\nv_mfma_f32_16x16x32_bf16 a[8:11],a[196:199],a[236:239],a[8:11]\nbuffer_load_dwordx4 v226,s[56:59],s75 offen offset:2048 lds\ns_mov_b32 s72,333824\nv_mfma_f32_16x16x32_bf16 a[36:39],a[200:203],a[236:239],a[36:39]\nds_read_b64_tr_b16 a[218:219],v232 offset:1024\n"
"v_mfma_f32_16x16x32_bf16 a[64:67],a[204:207],a[236:239],a[64:67]\nv_mfma_f32_16x16x32_bf16 a[92:95],a[208:211],a[236:239],a[92:95]\nds_read_b128 a[236:239],v239 offset:48128\nv_mfma_f32_16x16x32_bf16 a[12:15],a[196:199],a[240:243],a[12:15]\nbuffer_load_dwordx4 v226,s[56:59],s72 offen offset:3072 lds\nv_mfma_f32_16x16x32_bf16 a[40:43],a[200:203],a[240:243],a[40:43]\nds_read_b64_tr_b16 a[220:221],v233 offset:0\nv_mfma_f32_16x16x32_bf16 a[68:71],a[204:207],a[240:243],a[68:71]\n"
"ds_read_b64_tr_b16 a[222:223],v233 offset:1024\nv_mfma_f32_16x16x32_bf16 a[96:99],a[208:211],a[240:243],a[96:99]\nds_read_b128 a[240:243],v239 offset:50176\nv_mfma_f32_16x16x32_bf16 a[16:19],a[196:199],a[244:247],a[16:19]\nds_read_b64_tr_b16 a[224:225],v234 offset:0\nv_mfma_f32_16x16x32_bf16 a[44:47],a[200:203],a[244:247],a[44:47]\nv_mfma_f32_16x16x32_bf16 a[72:75],a[204:207],a[244:247],a[72:75]\nv_mfma_f32_16x16x32_bf16 a[100:103],a[208:211],a[244:247],a[100:103]\n"
"ds_read_b128 a[244:247],v239 offset:52224\ns_waitcnt lgkmcnt(12)\nv_mfma_f32_16x16x32_bf16 a[20:23],a[196:199],a[248:251],a[20:23]\nds_read_b64_tr_b16 a[226:227],v234 offset:1024\ns_add_u32 m0,s64,0\nv_mfma_f32_16x16x32_bf16 a[48:51],a[200:203],a[248:251],a[48:51]\ns_mov_b32 s73,344064\nv_mfma_f32_16x16x32_bf16 a[76:79],a[204:207],a[248:251],a[76:79]\n"
"v_mfma_f32_16x16x32_bf16 a[104:107],a[208:211],a[248:251],a[104:107]\nds_read_b128 a[248:251],v239 offset:54272\nv_mfma_f32_16x16x32_bf16 a[24:27],a[196:199],a[252:255],a[24:27]\nv_mfma_f32_16x16x32_bf16 a[52:55],a[200:203],a[252:255],a[52:55]\nv_mfma_f32_16x16x32_bf16 a[80:83],a[204:207],a[252:255],a[80:83]\nv_mfma_f32_16x16x32_bf16 a[108:111],a[208:211],a[252:255],a[108:111]\nds_read_b128 a[252:255],v239 offset:56320\ns_waitcnt lgkmcnt(2)\n"
"v_mfma_f32_16x16x32_bf16 a[0:3],a[212:215],a[228:231],a[0:3]\nbuffer_load_dwordx4 v226,s[56:59],s73 offen offset:0 lds\ns_mov_b32 s74,357376\nv_mfma_f32_16x16x32_bf16 a[28:31],a[216:219],a[228:231],a[28:31]\ns_waitcnt vmcnt(13)\nds_read_b64_tr_b16 a[196:197],v231 offset:4096\nv_mfma_f32_16x16x32_bf16 a[56:59],a[220:223],a[228:231],a[56:59]\nv_mfma_f32_16x16x32_bf16 a[84:87],a[224:227],a[228:231],a[84:87]\n"
"ds_read_b128 a[228:231],v239 offset:0\nv_mfma_f32_16x16x32_bf16 a[4:7],a[212:215],a[232:235],a[4:7]\nbuffer_load_dwordx4 v226,s[56:59],s74 offen offset:1024 lds\ns_mov_b32 s75,349184\nv_mfma_f32_16x16x32_bf16 a[32:35],a[216:219],a[232:235],a[32:35]\nds_read_b64_tr_b16 a[198:199],v231 offset:5120\nv_mfma_f32_16x16x32_bf16 a[60:63],a[220:223],a[232:235],a[60:63]\nv_mfma_f32_16x16x32_bf16 a[88:91],a[224:227],a[232:235],a[88:91]\n"
"ds_read_b128 a[232:235],v239 offset:2048\nv_mfma_f32_16x16x32_bf16 a[8:11],a[212:215],a[236:239],a[8:11]\nbuffer_load_dwordx4 v226,s[56:59],s75 offen offset:2048 lds\ns_mov_b32 s72,362496\nv_mfma_f32_16x16x32_bf16 a[36:39],a[216:219],a[236:239],a[36:39]\nds_read_b64_tr_b16 a[200:201],v232 offset:4096\nv_mfma_f32_16x16x32_bf16 a[64:67],a[220:223],a[236:239],a[64:67]\nv_mfma_f32_16x16x32_bf16 a[92:95],a[224:227],a[236:239],a[92:95]\n"
"ds_read_b128 a[236:239],v239 offset:4096\nv_mfma_f32_16x16x32_bf16 a[12:15],a[212:215],a[240:243],a[12:15]\nbuffer_load_dwordx4 v226,s[56:59],s72 offen offset:3072 lds\nv_mfma_f32_16x16x32_bf16 a[40:43],a[216:219],a[240:243],a[40:43]\nds_read_b64_tr_b16 a[202:203],v232 offset:5120\nv_mfma_f32_16x16x32_bf16 a[68:71],a[220:223],a[240:243],a[68:71]\nv_mfma_f32_16x16x32_bf16 a[96:99],a[224:227],a[240:243],a[96:99]\nds_read_b128 a[240:243],v239 offset:6144\n"
"v_mfma_f32_16x16x32_bf16 a[16:19],a[212:215],a[244:247],a[16:19]\nds_read_b64_tr_b16 a[204:205],v233 offset:4096\nv_mfma_f32_16x16x32_bf16 a[44:47],a[216:219],a[244:247],a[44:47]\nv_mfma_f32_16x16x32_bf16 a[72:75],a[220:223],a[244:247],a[72:75]\nv_mfma_f32_16x16x32_bf16 a[100:103],a[224:227],a[244:247],a[100:103]\nds_read_b128 a[244:247],v239 offset:8192\ns_waitcnt lgkmcnt(10)\nv_mfma_f32_16x16x32_bf16 a[20:23],a[212:215],a[248:251],a[20:23]\n"
"ds_read_b64_tr_b16 a[206:207],v233 offset:5120\ns_add_u32 m0,s64,4096\nv_mfma_f32_16x16x32_bf16 a[48:51],a[216:219],a[248:251],a[48:51]\ns_mov_b32 s73,372736\nv_mfma_f32_16x16x32_bf16 a[76:79],a[220:223],a[248:251],a[76:79]\nv_mfma_f32_16x16x32_bf16 a[104:107],a[224:227],a[248:251],a[104:107]\nds_read_b128 a[248:251],v239 offset:10240\nv_mfma_f32_16x16x32_bf16 a[24:27],a[212:215],a[252:255],a[24:27]\n"
"v_mfma_f32_16x16x32_bf16 a[52:55],a[216:219],a[252:255],a[52:55]\nv_mfma_f32_16x16x32_bf16 a[80:83],a[220:223],a[252:255],a[80:83]\nv_mfma_f32_16x16x32_bf16 a[108:111],a[224:227],a[252:255],a[108:111]\nds_read_b128 a[252:255],v239 offset:12288\ns_waitcnt lgkmcnt(2)\nv_mfma_f32_16x16x32_bf16 a[112:115],a[196:199],a[228:231],a[112:115]\nbuffer_load_dwordx4 v226,s[56:59],s73 offen offset:0 lds\nv_lshrrev_b32 v255,1,v239\n"
"v_mfma_f32_16x16x32_bf16 a[140:143],a[200:203],a[228:231],a[140:143]\ns_waitcnt vmcnt(13)\nds_read_b64_tr_b16 a[212:213],v231 offset:8192\nv_mfma_f32_16x16x32_bf16 a[168:171],a[204:207],a[228:231],a[168:171]\nds_read_b128 a[228:231],v239 offset:1024\ns_mov_b32 s74,386048\nv_mfma_f32_16x16x32_bf16 a[116:119],a[196:199],a[232:235],a[116:119]\nbuffer_load_dwordx4 v226,s[56:59],s74 offen offset:1024 lds\n"
"s_mov_b32 s75,377856\nv_mfma_f32_16x16x32_bf16 a[144:147],a[200:203],a[232:235],a[144:147]\nds_read_b64_tr_b16 a[214:215],v231 offset:9216\nv_mfma_f32_16x16x32_bf16 a[172:175],a[204:207],a[232:235],a[172:175]\nds_read_b128 a[232:235],v239 offset:3072\nv_mfma_f32_16x16x32_bf16 a[120:123],a[196:199],a[236:239],a[120:123]\nbuffer_load_dwordx4 v226,s[56:59],s75 offen offset:2048 lds\ns_mov_b32 s72,391168\n"
"v_mfma_f32_16x16x32_bf16 a[148:151],a[200:203],a[236:239],a[148:151]\nds_read_b64_tr_b16 a[216:217],v232 offset:8192\nv_mfma_f32_16x16x32_bf16 a[176:179],a[204:207],a[236:239],a[176:179]\nds_read_b128 a[236:239],v239 offset:5120\nv_mfma_f32_16x16x32_bf16 a[124:127],a[196:199],a[240:243],a[124:127]\nbuffer_load_dwordx4 v226,s[56:59],s72 offen offset:3072 lds\nv_mfma_f32_16x16x32_bf16 a[152:155],a[200:203],a[240:243],a[152:155]\nds_read_b64_tr_b16 a[218:219],v232 offset:9216\n"
"v_mfma_f32_16x16x32_bf16 a[180:183],a[204:207],a[240:243],a[180:183]\nds_read_b128 a[240:243],v239 offset:7168\nv_mfma_f32_16x16x32_bf16 a[128:131],a[196:199],a[244:247],a[128:131]\nds_read_b64_tr_b16 a[220:221],v233 offset:8192\nv_mfma_f32_16x16x32_bf16 a[156:159],a[200:203],a[244:247],a[156:159]\nv_mfma_f32_16x16x32_bf16 a[184:187],a[204:207],a[244:247],a[184:187]\nds_read_b128 a[244:247],v239 offset:9216\ns_waitcnt lgkmcnt(10)\n"
"v_mfma_f32_16x16x32_bf16 a[132:135],a[196:199],a[248:251],a[132:135]\nds_read_b64_tr_b16 a[222:223],v233 offset:9216\ns_add_u32 m0,s64,8192\nv_mfma_f32_16x16x32_bf16 a[160:163],a[200:203],a[248:251],a[160:163]\ns_mov_b32 s73,401408\nv_mfma_f32_16x16x32_bf16 a[188:191],a[204:207],a[248:251],a[188:191]\nds_read_b128 a[248:251],v239 offset:11264\nv_mfma_f32_16x16x32_bf16 a[136:139],a[196:199],a[252:255],a[136:139]\n"
"v_mfma_f32_16x16x32_bf16 a[164:167],a[200:203],a[252:255],a[164:167]\nv_mfma_f32_16x16x32_bf16 a[192:195],a[204:207],a[252:255],a[192:195]\nds_read_b128 a[252:255],v239 offset:13312\ns_waitcnt lgkmcnt(2)\nv_mfma_f32_16x16x32_bf16 a[112:115],a[212:215],a[228:231],a[112:115]\nbuffer_load_dwordx4 v226,s[56:59],s73 offen offset:0 lds\nv_accvgpr_read_b32 v0,a0\nv_mfma_f32_16x16x32_bf16 a[140:143],a[216:219],a[228:231],a[140:143]\n"
"s_waitcnt vmcnt(13)\nds_read_b64_tr_b16 a[196:197],v231 offset:12288\nv_mfma_f32_16x16x32_bf16 a[168:171],a[220:223],a[228:231],a[168:171]\nds_read_b128 a[228:231],v239 offset:14336\nv_accvgpr_read_b32 v1,a1\nv_mfma_f32_16x16x32_bf16 a[116:119],a[212:215],a[232:235],a[116:119]\ns_mov_b32 s74,414720\nbuffer_load_dwordx4 v226,s[56:59],s74 offen offset:1024 lds\n"
"v_mfma_f32_16x16x32_bf16 a[144:147],a[216:219],a[232:235],a[144:147]\nds_read_b64_tr_b16 a[198:199],v231 offset:13312\nv_accvgpr_read_b32 v2,a2\nv_mfma_f32_16x16x32_bf16 a[172:175],a[220:223],a[232:235],a[172:175]\nds_read_b128 a[232:235],v239 offset:16384\nv_accvgpr_read_b32 v3,a3\nv_mfma_f32_16x16x32_bf16 a[120:123],a[212:215],a[236:239],a[120:123]\ns_mov_b32 s75,406528\n"
"buffer_load_dwordx4 v226,s[56:59],s75 offen offset:2048 lds\nv_mfma_f32_16x16x32_bf16 a[148:151],a[216:219],a[236:239],a[148:151]\nds_read_b64_tr_b16 a[200:201],v232 offset:12288\nv_accvgpr_read_b32 v4,a28\nv_mfma_f32_16x16x32_bf16 a[176:179],a[220:223],a[236:239],a[176:179]\nds_read_b128 a[236:239],v239 offset:18432\nv_accvgpr_read_b32 v5,a29\nv_mfma_f32_16x16x32_bf16 a[124:127],a[212:215],a[240:243],a[124:127]\n"
"s_mov_b32 s72,419840\nbuffer_load_dwordx4 v226,s[56:59],s72 offen offset:3072 lds\nv_mfma_f32_16x16x32_bf16 a[152:155],a[216:219],a[240:243],a[152:155]\nds_read_b64_tr_b16 a[202:203],v232 offset:13312\nv_accvgpr_read_b32 v6,a30\nv_mfma_f32_16x16x32_bf16 a[180:183],a[220:223],a[240:243],a[180:183]\nds_read_b128 a[240:243],v239 offset:20480\nv_accvgpr_read_b32 v7,a31\n"
"v_mfma_f32_16x16x32_bf16 a[128:131],a[212:215],a[244:247],a[128:131]\nds_read_b64_tr_b16 a[204:205],v233 offset:12288\nv_cvt_pk_bf16_f32 v0,v0,v1\nv_mfma_f32_16x16x32_bf16 a[156:159],a[216:219],a[244:247],a[156:159]\nv_cvt_pk_bf16_f32 v1,v2,v3\nv_cvt_pk_bf16_f32 v2,v4,v5\nv_mfma_f32_16x16x32_bf16 a[184:187],a[220:223],a[244:247],a[184:187]\nds_read_b128 a[244:247],v239 offset:22528\n"
"s_waitcnt lgkmcnt(10)\nv_mfma_f32_16x16x32_bf16 a[132:135],a[212:215],a[248:251],a[132:135]\nds_read_b64_tr_b16 a[206:207],v233 offset:13312\nv_cvt_pk_bf16_f32 v3,v6,v7\nv_mfma_f32_16x16x32_bf16 a[160:163],a[216:219],a[248:251],a[160:163]\ns_add_u32 m0,s64,12288\ns_add_u32 s71,s71,0\nv_accvgpr_read_b32 v8,a56\n"
"v_mfma_f32_16x16x32_bf16 a[188:191],a[220:223],a[248:251],a[188:191]\nds_read_b128 a[248:251],v239 offset:24576\nv_accvgpr_read_b32 v9,a57\nv_mfma_f32_16x16x32_bf16 a[136:139],a[212:215],a[252:255],a[136:139]\ns_mov_b32 s73,430080\nbuffer_store_dwordx4 v[0:3],v239,s[60:63],s71 offen sc1\nv_mfma_f32_16x16x32_bf16 a[164:167],a[216:219],a[252:255],a[164:167]\nv_accvgpr_read_b32 v10,a58\n"
"v_accvgpr_read_b32 v11,a59\nv_mfma_f32_16x16x32_bf16 a[192:195],a[220:223],a[252:255],a[192:195]\nds_read_b128 a[252:255],v239 offset:26624\ns_waitcnt lgkmcnt(2)\nv_mfma_f32_16x16x32_bf16 a[112:115],a[196:199],a[228:231],a[112:115]\nbuffer_load_dwordx4 v226,s[56:59],s73 offen offset:0 lds\nv_accvgpr_read_b32 v12,a84\nv_mfma_f32_16x16x32_bf16 a[140:143],a[200:203],a[228:231],a[140:143]\n"
"s_waitcnt vmcnt(14)\nds_read_b64_tr_b16 a[212:213],v231 offset:16384\nv_mfma_f32_16x16x32_bf16 a[168:171],a[204:207],a[228:231],a[168:171]\nds_read_b128 a[228:231],v239 offset:15360\nv_accvgpr_read_b32 v13,a85\nv_mfma_f32_16x16x32_bf16 a[116:119],a[196:199],a[232:235],a[116:119]\ns_mov_b32 s74,443392\nbuffer_load_dwordx4 v226,s[56:59],s74 offen offset:1024 lds\n"
"v_mfma_f32_16x16x32_bf16 a[144:147],a[200:203],a[232:235],a[144:147]\nds_read_b64_tr_b16 a[214:215],v231 offset:17408\nv_accvgpr_read_b32 v14,a86\nv_mfma_f32_16x16x32_bf16 a[172:175],a[204:207],a[232:235],a[172:175]\nds_read_b128 a[232:235],v239 offset:17408\nv_accvgpr_read_b32 v15,a87\nv_mfma_f32_16x16x32_bf16 a[120:123],a[196:199],a[236:239],a[120:123]\ns_mov_b32 s75,435200\n"
"buffer_load_dwordx4 v226,s[56:59],s75 offen offset:2048 lds\nv_mfma_f32_16x16x32_bf16 a[148:151],a[200:203],a[236:239],a[148:151]\nds_read_b64_tr_b16 a[216:217],v232 offset:16384\nv_cvt_pk_bf16_f32 v8,v8,v9\nv_mfma_f32_16x16x32_bf16 a[176:179],a[204:207],a[236:239],a[176:179]\nds_read_b128 a[236:239],v239 offset:19456\nv_cvt_pk_bf16_f32 v9,v10,v11\nv_mfma_f32_16x16x32_bf16 a[124:127],a[196:199],a[240:243],a[124:127]\n"
"s_mov_b32 s72,448512\nbuffer_load_dwordx4 v226,s[56:59],s72 offen offset:3072 lds\nv_mfma_f32_16x16x32_bf16 a[152:155],a[200:203],a[240:243],a[152:155]\nds_read_b64_tr_b16 a[218:219],v232 offset:17408\nv_cvt_pk_bf16_f32 v10,v12,v13\nv_mfma_f32_16x16x32_bf16 a[180:183],a[204:207],a[240:243],a[180:183]\nds_read_b128 a[240:243],v239 offset:21504\nv_cvt_pk_bf16_f32 v11,v14,v15\n"
"v_mfma_f32_16x16x32_bf16 a[128:131],a[196:199],a[244:247],a[128:131]\nds_read_b64_tr_b16 a[220:221],v233 offset:16384\ns_add_u32 s71,s71,1024\nv_mfma_f32_16x16x32_bf16 a[156:159],a[200:203],a[244:247],a[156:159]\nbuffer_store_dwordx4 v[8:11],v239,s[60:63],s71 offen sc1\nv_mfma_f32_16x16x32_bf16 a[184:187],a[204:207],a[244:247],a[184:187]\nds_read_b128 a[244:247],v239 offset:23552\ns_waitcnt lgkmcnt(10)\n"
"v_mfma_f32_16x16x32_bf16 a[132:135],a[196:199],a[248:251],a[132:135]\nds_read_b64_tr_b16 a[222:223],v233 offset:17408\nv_accvgpr_read_b32 v16,a4\nv_mfma_f32_16x16x32_bf16 a[160:163],a[200:203],a[248:251],a[160:163]\nv_accvgpr_read_b32 v17,a5\nv_accvgpr_read_b32 v18,a6\nv_mfma_f32_16x16x32_bf16 a[188:191],a[204:207],a[248:251],a[188:191]\nds_read_b128 a[248:251],v239 offset:25600\n"
"v_accvgpr_read_b32 v19,a7\nv_mfma_f32_16x16x32_bf16 a[136:139],a[196:199],a[252:255],a[136:139]\nv_accvgpr_read_b32 v20,a32\nv_accvgpr_read_b32 v21,a33\nv_mfma_f32_16x16x32_bf16 a[164:167],a[200:203],a[252:255],a[164:167]\nv_accvgpr_read_b32 v22,a34\nv_accvgpr_read_b32 v23,a35\nv_mfma_f32_16x16x32_bf16 a[192:195],a[204:207],a[252:255],a[192:195]\n"
"ds_read_b128 a[252:255],v239 offset:27648\ns_waitcnt lgkmcnt(2)\nv_mfma_f32_16x16x32_bf16 a[112:115],a[212:215],a[228:231],a[112:115]\ns_waitcnt vmcnt(14)\nds_read_b64_tr_b16 a[196:197],v231 offset:0\nv_mfma_f32_16x16x32_bf16 a[140:143],a[216:219],a[228:231],a[140:143]\nv_cvt_pk_bf16_f32 v16,v16,v17\nv_cvt_pk_bf16_f32 v17,v18,v19\n"
"v_mfma_f32_16x16x32_bf16 a[168:171],a[220:223],a[228:231],a[168:171]\nds_read_b128 a[228:231],v239 offset:28672\nv_cvt_pk_bf16_f32 v18,v20,v21\nv_mfma_f32_16x16x32_bf16 a[116:119],a[212:215],a[232:235],a[116:119]\nds_read_b64_tr_b16 a[198:199],v231 offset:1024\nv_cvt_pk_bf16_f32 v19,v22,v23\nv_mfma_f32_16x16x32_bf16 a[144:147],a[216:219],a[232:235],a[144:147]\ns_add_u32 s71,s71,1024\n"
"buffer_store_dwordx4 v[16:19],v239,s[60:63],s71 offen sc1\nv_mfma_f32_16x16x32_bf16 a[172:175],a[220:223],a[232:235],a[172:175]\nds_read_b128 a[232:235],v239 offset:30720\nv_accvgpr_read_b32 v24,a60\nv_mfma_f32_16x16x32_bf16 a[120:123],a[212:215],a[236:239],a[120:123]\nds_read_b64_tr_b16 a[200:201],v232 offset:0\nv_accvgpr_read_b32 v25,a61\nv_mfma_f32_16x16x32_bf16 a[148:151],a[216:219],a[236:239],a[148:151]\n"
"v_accvgpr_read_b32 v26,a62\nv_accvgpr_read_b32 v27,a63\nv_mfma_f32_16x16x32_bf16 a[176:179],a[220:223],a[236:239],a[176:179]\nds_read_b128 a[236:239],v239 offset:32768\nv_accvgpr_read_b32 v28,a88\nv_mfma_f32_16x16x32_bf16 a[124:127],a[212:215],a[240:243],a[124:127]\nds_read_b64_tr_b16 a[202:203],v232 offset:1024\nv_accvgpr_read_b32 v29,a89\n"
"v_mfma_f32_16x16x32_bf16 a[152:155],a[216:219],a[240:243],a[152:155]\nv_accvgpr_read_b32 v30,a90\nv_accvgpr_read_b32 v31,a91\nv_mfma_f32_16x16x32_bf16 a[180:183],a[220:223],a[240:243],a[180:183]\nds_read_b128 a[240:243],v239 offset:34816\nv_cvt_pk_bf16_f32 v24,v24,v25\nv_mfma_f32_16x16x32_bf16 a[128:131],a[212:215],a[244:247],a[128:131]\nds_read_b64_tr_b16 a[204:205],v233 offset:0\n"
"v_cvt_pk_bf16_f32 v25,v26,v27\nv_mfma_f32_16x16x32_bf16 a[156:159],a[216:219],a[244:247],a[156:159]\nv_cvt_pk_bf16_f32 v26,v28,v29\nv_cvt_pk_bf16_f32 v27,v30,v31\nv_mfma_f32_16x16x32_bf16 a[184:187],a[220:223],a[244:247],a[184:187]\nds_read_b128 a[244:247],v239 offset:36864\ns_waitcnt lgkmcnt(10)\nv_mfma_f32_16x16x32_bf16 a[132:135],a[212:215],a[248:251],a[132:135]\n"
"ds_read_b64_tr_b16 a[206:207],v233 offset:1024\ns_add_u32 s71,s71,1024\nv_mfma_f32_16x16x32_bf16 a[160:163],a[216:219],a[248:251],a[160:163]\nbuffer_store_dwordx4 v[24:27],v239,s[60:63],s71 offen sc1\nv_mfma_f32_16x16x32_bf16 a[188:191],a[220:223],a[248:251],a[188:191]\nds_read_b128 a[248:251],v239 offset:38912\nv_accvgpr_read_b32 v32,a8\nv_mfma_f32_16x16x32_bf16 a[136:139],a[212:215],a[252:255],a[136:139]\n"
"v_accvgpr_read_b32 v33,a9\nv_accvgpr_read_b32 v34,a10\nv_mfma_f32_16x16x32_bf16 a[164:167],a[216:219],a[252:255],a[164:167]\nv_accvgpr_read_b32 v35,a11\nv_accvgpr_read_b32 v36,a36\nv_mfma_f32_16x16x32_bf16 a[192:195],a[220:223],a[252:255],a[192:195]\nds_read_b128 a[252:255],v239 offset:40960\ns_waitcnt lgkmcnt(2)\n"
"v_mfma_f32_16x16x32_bf16 a[112:115],a[196:199],a[228:231],a[112:115]\ns_waitcnt vmcnt(12)\nds_read_b64_tr_b16 a[212:213],v231 offset:4096\nv_mfma_f32_16x16x32_bf16 a[140:143],a[200:203],a[228:231],a[140:143]\nv_accvgpr_read_b32 v37,a37\nv_accvgpr_read_b32 v38,a38\nv_mfma_f32_16x16x32_bf16 a[168:171],a[204:207],a[228:231],a[168:171]\nds_read_b128 a[228:231],v239 offset:29696\n"
"v_accvgpr_read_b32 v39,a39\nv_mfma_f32_16x16x32_bf16 a[116:119],a[196:199],a[232:235],a[116:119]\nds_read_b64_tr_b16 a[214:215],v231 offset:5120\nv_cvt_pk_bf16_f32 v32,v32,v33\nv_mfma_f32_16x16x32_bf16 a[144:147],a[200:203],a[232:235],a[144:147]\nv_cvt_pk_bf16_f32 v33,v34,v35\nv_cvt_pk_bf16_f32 v34,v36,v37\nv_mfma_f32_16x16x32_bf16 a[172:175],a[204:207],a[232:235],a[172:175]\n"
"ds_read_b128 a[232:235],v239 offset:31744\nv_cvt_pk_bf16_f32 v35,v38,v39\nv_mfma_f32_16x16x32_bf16 a[120:123],a[196:199],a[236:239],a[120:123]\nds_read_b64_tr_b16 a[216:217],v232 offset:4096\ns_add_u32 s71,s71,1024\nv_mfma_f32_16x16x32_bf16 a[148:151],a[200:203],a[236:239],a[148:151]\nbuffer_store_dwordx4 v[32:35],v239,s[60:63],s71 offen sc1\nv_mfma_f32_16x16x32_bf16 a[176:179],a[204:207],a[236:239],a[176:179]\n"
"ds_read_b128 a[236:239],v239 offset:33792\nv_accvgpr_read_b32 v40,a64\nv_mfma_f32_16x16x32_bf16 a[124:127],a[196:199],a[240:243],a[124:127]\nds_read_b64_tr_b16 a[218:219],v232 offset:5120\nv_accvgpr_read_b32 v41,a65\nv_mfma_f32_16x16x32_bf16 a[152:155],a[200:203],a[240:243],a[152:155]\nv_accvgpr_read_b32 v42,a66\nv_accvgpr_read_b32 v43,a67\n"
"v_mfma_f32_16x16x32_bf16 a[180:183],a[204:207],a[240:243],a[180:183]\nds_read_b128 a[240:243],v239 offset:35840\nv_accvgpr_read_b32 v44,a92\nv_mfma_f32_16x16x32_bf16 a[128:131],a[196:199],a[244:247],a[128:131]\nds_read_b64_tr_b16 a[220:221],v233 offset:4096\nv_accvgpr_read_b32 v45,a93\nv_mfma_f32_16x16x32_bf16 a[156:159],a[200:203],a[244:247],a[156:159]\nv_accvgpr_read_b32 v46,a94\n"
"v_accvgpr_read_b32 v47,a95\nv_mfma_f32_16x16x32_bf16 a[184:187],a[204:207],a[244:247],a[184:187]\nds_read_b128 a[244:247],v239 offset:37888\ns_waitcnt lgkmcnt(10)\nv_mfma_f32_16x16x32_bf16 a[132:135],a[196:199],a[248:251],a[132:135]\nds_read_b64_tr_b16 a[222:223],v233 offset:5120\nv_cvt_pk_bf16_f32 v40,v40,v41\nv_mfma_f32_16x16x32_bf16 a[160:163],a[200:203],a[248:251],a[160:163]\n"
"v_cvt_pk_bf16_f32 v41,v42,v43\nv_cvt_pk_bf16_f32 v42,v44,v45\nv_mfma_f32_16x16x32_bf16 a[188:191],a[204:207],a[248:251],a[188:191]\nds_read_b128 a[248:251],v239 offset:39936\nv_cvt_pk_bf16_f32 v43,v46,v47\nv_mfma_f32_16x16x32_bf16 a[136:139],a[196:199],a[252:255],a[136:139]\ns_add_u32 s71,s71,1024\nbuffer_store_dwordx4 v[40:43],v239,s[60:63],s71 offen sc1\n"
"v_mfma_f32_16x16x32_bf16 a[164:167],a[200:203],a[252:255],a[164:167]\nv_accvgpr_read_b32 v48,a12\nv_accvgpr_read_b32 v49,a13\nv_mfma_f32_16x16x32_bf16 a[192:195],a[204:207],a[252:255],a[192:195]\nds_read_b128 a[252:255],v239 offset:41984\ns_waitcnt lgkmcnt(2)\nv_mfma_f32_16x16x32_bf16 a[112:115],a[212:215],a[228:231],a[112:115]\ns_waitcnt vmcnt(10)\n"
"ds_read_b64_tr_b16 a[196:197],v231 offset:8192\nv_mfma_f32_16x16x32_bf16 a[140:143],a[216:219],a[228:231],a[140:143]\nv_accvgpr_read_b32 v50,a14\nv_accvgpr_read_b32 v51,a15\nv_mfma_f32_16x16x32_bf16 a[168:171],a[220:223],a[228:231],a[168:171]\nds_read_b128 a[228:231],v239 offset:43008\nv_accvgpr_read_b32 v52,a40\nv_mfma_f32_16x16x32_bf16 a[116:119],a[212:215],a[232:235],a[116:119]\n"
"ds_read_b64_tr_b16 a[198:199],v231 offset:9216\nv_accvgpr_read_b32 v53,a41\nv_mfma_f32_16x16x32_bf16 a[144:147],a[216:219],a[232:235],a[144:147]\nv_accvgpr_read_b32 v54,a42\nv_accvgpr_read_b32 v55,a43\nv_mfma_f32_16x16x32_bf16 a[172:175],a[220:223],a[232:235],a[172:175]\nds_read_b128 a[232:235],v239 offset:45056\nv_cvt_pk_bf16_f32 v48,v48,v49\n"
"v_mfma_f32_16x16x32_bf16 a[120:123],a[212:215],a[236:239],a[120:123]\nds_read_b64_tr_b16 a[200:201],v232 offset:8192\nv_cvt_pk_bf16_f32 v49,v50,v51\nv_mfma_f32_16x16x32_bf16 a[148:151],a[216:219],a[236:239],a[148:151]\nv_cvt_pk_bf16_f32 v50,v52,v53\nv_cvt_pk_bf16_f32 v51,v54,v55\nv_mfma_f32_16x16x32_bf16 a[176:179],a[220:223],a[236:239],a[176:179]\nds_read_b128 a[236:239],v239 offset:47104\n"
"s_add_u32 s71,s71,1024\nv_mfma_f32_16x16x32_bf16 a[124:127],a[212:215],a[240:243],a[124:127]\nds_read_b64_tr_b16 a[202:203],v232 offset:9216\nv_accvgpr_read_b32 v56,a68\nv_mfma_f32_16x16x32_bf16 a[152:155],a[216:219],a[240:243],a[152:155]\nbuffer_store_dwordx4 v[48:51],v239,s[60:63],s71 offen sc1\nv_mfma_f32_16x16x32_bf16 a[180:183],a[220:223],a[240:243],a[180:183]\nds_read_b128 a[240:243],v239 offset:49152\n"
"v_accvgpr_read_b32 v57,a69\nv_mfma_f32_16x16x32_bf16 a[128:131],a[212:215],a[244:247],a[128:131]\nds_read_b64_tr_b16 a[204:205],v233 offset:8192\nv_accvgpr_read_b32 v58,a70\nv_mfma_f32_16x16x32_bf16 a[156:159],a[216:219],a[244:247],a[156:159]\nv_accvgpr_read_b32 v59,a71\nv_accvgpr_read_b32 v60,a96\nv_mfma_f32_16x16x32_bf16 a[184:187],a[220:223],a[244:247],a[184:187]\n"
"ds_read_b128 a[244:247],v239 offset:51200\ns_waitcnt lgkmcnt(10)\nv_mfma_f32_16x16x32_bf16 a[132:135],a[212:215],a[248:251],a[132:135]\nds_read_b64_tr_b16 a[206:207],v233 offset:9216\nv_accvgpr_read_b32 v61,a97\nv_mfma_f32_16x16x32_bf16 a[160:163],a[216:219],a[248:251],a[160:163]\nv_accvgpr_read_b32 v62,a98\nv_accvgpr_read_b32 v63,a99\n"
"v_mfma_f32_16x16x32_bf16 a[188:191],a[220:223],a[248:251],a[188:191]\nds_read_b128 a[248:251],v239 offset:53248\nv_cvt_pk_bf16_f32 v56,v56,v57\nv_mfma_f32_16x16x32_bf16 a[136:139],a[212:215],a[252:255],a[136:139]\nv_cvt_pk_bf16_f32 v57,v58,v59\nv_cvt_pk_bf16_f32 v58,v60,v61\nv_mfma_f32_16x16x32_bf16 a[164:167],a[216:219],a[252:255],a[164:167]\nv_cvt_pk_bf16_f32 v59,v62,v63\n"
"s_add_u32 s71,s71,1024\nv_mfma_f32_16x16x32_bf16 a[192:195],a[220:223],a[252:255],a[192:195]\nds_read_b128 a[252:255],v239 offset:55296\ns_waitcnt lgkmcnt(2)\nv_mfma_f32_16x16x32_bf16 a[112:115],a[196:199],a[228:231],a[112:115]\ns_waitcnt vmcnt(6)\nds_read_b64_tr_b16 a[212:213],v231 offset:12288\nv_mfma_f32_16x16x32_bf16 a[140:143],a[200:203],a[228:231],a[140:143]\n"
"buffer_store_dwordx4 v[56:59],v239,s[60:63],s71 offen sc1\nv_mfma_f32_16x16x32_bf16 a[168:171],a[204:207],a[228:231],a[168:171]\nds_read_b128 a[228:231],v239 offset:44032\nv_accvgpr_read_b32 v64,a16\nv_mfma_f32_16x16x32_bf16 a[116:119],a[196:199],a[232:235],a[116:119]\nds_read_b64_tr_b16 a[214:215],v231 offset:13312\nv_accvgpr_read_b32 v65,a17\nv_mfma_f32_16x16x32_bf16 a[144:147],a[200:203],a[232:235],a[144:147]\n"
"v_accvgpr_read_b32 v66,a18\nv_accvgpr_read_b32 v67,a19\nv_mfma_f32_16x16x32_bf16 a[172:175],a[204:207],a[232:235],a[172:175]\nds_read_b128 a[232:235],v239 offset:46080\nv_accvgpr_read_b32 v68,a44\nv_mfma_f32_16x16x32_bf16 a[120:123],a[196:199],a[236:239],a[120:123]\nds_read_b64_tr_b16 a[216:217],v232 offset:12288\nv_accvgpr_read_b32 v69,a45\n"
"v_mfma_f32_16x16x32_bf16 a[148:151],a[200:203],a[236:239],a[148:151]\nv_accvgpr_read_b32 v70,a46\nv_accvgpr_read_b32 v71,a47\nv_mfma_f32_16x16x32_bf16 a[176:179],a[204:207],a[236:239],a[176:179]\nds_read_b128 a[236:239],v239 offset:48128\nv_cvt_pk_bf16_f32 v64,v64,v65\nv_mfma_f32_16x16x32_bf16 a[124:127],a[196:199],a[240:243],a[124:127]\nds_read_b64_tr_b16 a[218:219],v232 offset:13312\n"
"v_cvt_pk_bf16_f32 v65,v66,v67\nv_mfma_f32_16x16x32_bf16 a[152:155],a[200:203],a[240:243],a[152:155]\nv_cvt_pk_bf16_f32 v66,v68,v69\nv_cvt_pk_bf16_f32 v67,v70,v71\nv_mfma_f32_16x16x32_bf16 a[180:183],a[204:207],a[240:243],a[180:183]\nds_read_b128 a[240:243],v239 offset:50176\ns_add_u32 s71,s71,1024\nv_mfma_f32_16x16x32_bf16 a[128:131],a[196:199],a[244:247],a[128:131]\n"
"ds_read_b64_tr_b16 a[220:221],v233 offset:12288\nv_accvgpr_read_b32 v72,a72\nv_mfma_f32_16x16x32_bf16 a[156:159],a[200:203],a[244:247],a[156:159]\nbuffer_store_dwordx4 v[64:67],v239,s[60:63],s71 offen sc1\nv_mfma_f32_16x16x32_bf16 a[184:187],a[204:207],a[244:247],a[184:187]\nds_read_b128 a[244:247],v239 offset:52224\ns_waitcnt lgkmcnt(10)\nv_mfma_f32_16x16x32_bf16 a[132:135],a[196:199],a[248:251],a[132:135]\n"
"ds_read_b64_tr_b16 a[222:223],v233 offset:13312\nv_accvgpr_read_b32 v73,a73\nv_mfma_f32_16x16x32_bf16 a[160:163],a[200:203],a[248:251],a[160:163]\nv_accvgpr_read_b32 v74,a74\nv_accvgpr_read_b32 v75,a75\nv_mfma_f32_16x16x32_bf16 a[188:191],a[204:207],a[248:251],a[188:191]\nds_read_b128 a[248:251],v239 offset:54272\nv_accvgpr_read_b32 v76,a100\n"
"v_mfma_f32_16x16x32_bf16 a[136:139],a[196:199],a[252:255],a[136:139]\nv_accvgpr_read_b32 v77,a101\nv_accvgpr_read_b32 v78,a102\nv_mfma_f32_16x16x32_bf16 a[164:167],a[200:203],a[252:255],a[164:167]\nv_accvgpr_read_b32 v79,a103\nv_cvt_pk_bf16_f32 v72,v72,v73\nv_mfma_f32_16x16x32_bf16 a[192:195],a[204:207],a[252:255],a[192:195]\nds_read_b128 a[252:255],v239 offset:56320\n"
"s_waitcnt lgkmcnt(2)\nv_mfma_f32_16x16x32_bf16 a[112:115],a[212:215],a[228:231],a[112:115]\nv_cvt_pk_bf16_f32 v73,v74,v75\nv_cvt_pk_bf16_f32 v74,v76,v77\nv_mfma_f32_16x16x32_bf16 a[140:143],a[216:219],a[228:231],a[140:143]\nv_cvt_pk_bf16_f32 v75,v78,v79\ns_add_u32 s71,s71,1024\nv_mfma_f32_16x16x32_bf16 a[168:171],a[220:223],a[228:231],a[168:171]\n"
"buffer_store_dwordx4 v[72:75],v239,s[60:63],s71 offen sc1\nv_mfma_f32_16x16x32_bf16 a[116:119],a[212:215],a[232:235],a[116:119]\nv_accvgpr_read_b32 v80,a20\nv_accvgpr_read_b32 v81,a21\nv_mfma_f32_16x16x32_bf16 a[144:147],a[216:219],a[232:235],a[144:147]\nv_accvgpr_read_b32 v82,a22\nv_accvgpr_read_b32 v83,a23\nv_mfma_f32_16x16x32_bf16 a[172:175],a[220:223],a[232:235],a[172:175]\n"
"v_accvgpr_read_b32 v84,a48\nv_accvgpr_read_b32 v85,a49\nv_mfma_f32_16x16x32_bf16 a[120:123],a[212:215],a[236:239],a[120:123]\nv_accvgpr_read_b32 v86,a50\nv_accvgpr_read_b32 v87,a51\nv_mfma_f32_16x16x32_bf16 a[148:151],a[216:219],a[236:239],a[148:151]\nv_cvt_pk_bf16_f32 v80,v80,v81\nv_cvt_pk_bf16_f32 v81,v82,v83\n"
"v_mfma_f32_16x16x32_bf16 a[176:179],a[220:223],a[236:239],a[176:179]\nv_cvt_pk_bf16_f32 v82,v84,v85\nv_cvt_pk_bf16_f32 v83,v86,v87\nv_mfma_f32_16x16x32_bf16 a[124:127],a[212:215],a[240:243],a[124:127]\ns_add_u32 s71,s71,1024\nbuffer_store_dwordx4 v[80:83],v239,s[60:63],s71 offen sc1\nv_accvgpr_read_b32 v88,a76\nv_accvgpr_read_b32 v89,a77\n"
"v_accvgpr_read_b32 v90,a78\nv_accvgpr_read_b32 v91,a79\nv_accvgpr_read_b32 v92,a104\nv_accvgpr_read_b32 v93,a105\nv_accvgpr_read_b32 v94,a106\nv_accvgpr_read_b32 v95,a107\nv_cvt_pk_bf16_f32 v88,v88,v89\nv_cvt_pk_bf16_f32 v89,v90,v91\n"
"v_cvt_pk_bf16_f32 v90,v92,v93\nv_cvt_pk_bf16_f32 v91,v94,v95\ns_add_u32 s71,s71,1024\nbuffer_store_dwordx4 v[88:91],v239,s[60:63],s71 offen sc1\nv_accvgpr_read_b32 v0,a24\nv_accvgpr_read_b32 v1,a25\nv_accvgpr_read_b32 v2,a26\nv_accvgpr_read_b32 v3,a27\n"
"v_accvgpr_read_b32 v4,a52\nv_accvgpr_read_b32 v5,a53\nv_mfma_f32_16x16x32_bf16 a[152:155],a[216:219],a[240:243],a[152:155]\nv_accvgpr_read_b32 v6,a54\nv_accvgpr_read_b32 v7,a55\nv_cvt_pk_bf16_f32 v0,v0,v1\nv_cvt_pk_bf16_f32 v1,v2,v3\nv_cvt_pk_bf16_f32 v2,v4,v5\n"
"v_cvt_pk_bf16_f32 v3,v6,v7\ns_add_u32 s71,s71,1024\nbuffer_store_dwordx4 v[0:3],v239,s[60:63],s71 offen sc1\nv_accvgpr_read_b32 v8,a80\nv_accvgpr_read_b32 v9,a81\nv_accvgpr_read_b32 v10,a82\nv_accvgpr_read_b32 v11,a83\nv_accvgpr_read_b32 v12,a108\n"
"v_accvgpr_read_b32 v13,a109\nv_accvgpr_read_b32 v14,a110\nv_accvgpr_read_b32 v15,a111\nv_cvt_pk_bf16_f32 v8,v8,v9\nv_cvt_pk_bf16_f32 v9,v10,v11\nv_cvt_pk_bf16_f32 v10,v12,v13\nv_cvt_pk_bf16_f32 v11,v14,v15\ns_add_u32 s71,s71,1024\n"
"buffer_store_dwordx4 v[8:11],v239,s[60:63],s71 offen sc1\nv_mfma_f32_16x16x32_bf16 a[180:183],a[220:223],a[240:243],a[180:183]\nv_accvgpr_read_b32 v16,a112\nv_accvgpr_read_b32 v17,a113\nv_accvgpr_read_b32 v18,a114\nv_accvgpr_read_b32 v19,a115\nv_accvgpr_read_b32 v20,a140\nv_accvgpr_read_b32 v21,a141\n"
"v_accvgpr_read_b32 v22,a142\nv_accvgpr_read_b32 v23,a143\nv_cvt_pk_bf16_f32 v16,v16,v17\nv_cvt_pk_bf16_f32 v17,v18,v19\nv_cvt_pk_bf16_f32 v18,v20,v21\nv_cvt_pk_bf16_f32 v19,v22,v23\ns_add_u32 s71,s71,1024\nbuffer_store_dwordx4 v[16:19],v239,s[60:63],s71 offen sc1\n"
"v_accvgpr_read_b32 v24,a168\nv_accvgpr_read_b32 v25,a169\nv_accvgpr_read_b32 v26,a170\nv_accvgpr_read_b32 v27,a171\nv_cvt_pk_bf16_f32 v24,v24,v25\nv_cvt_pk_bf16_f32 v25,v26,v27\ns_add_u32 s71,s71,1024\nbuffer_store_dwordx2 v[24:25],v255,s[60:63],s71 offen sc1\n"
"v_mfma_f32_16x16x32_bf16 a[128:131],a[212:215],a[244:247],a[128:131]\nv_accvgpr_read_b32 v32,a116\nv_accvgpr_read_b32 v33,a117\nv_accvgpr_read_b32 v34,a118\nv_accvgpr_read_b32 v35,a119\nv_accvgpr_read_b32 v36,a144\nv_accvgpr_read_b32 v37,a145\nv_accvgpr_read_b32 v38,a146\n"
"v_accvgpr_read_b32 v39,a147\nv_mfma_f32_16x16x32_bf16 a[156:159],a[216:219],a[244:247],a[156:159]\nv_cvt_pk_bf16_f32 v32,v32,v33\nv_cvt_pk_bf16_f32 v33,v34,v35\nv_cvt_pk_bf16_f32 v34,v36,v37\nv_cvt_pk_bf16_f32 v35,v38,v39\ns_add_u32 s71,s71,512\nbuffer_store_dwordx4 v[32:35],v239,s[60:63],s71 offen sc1\n"
"v_accvgpr_read_b32 v40,a172\nv_mfma_f32_16x16x32_bf16 a[184:187],a[220:223],a[244:247],a[184:187]\ns_waitcnt lgkmcnt(0)\nv_accvgpr_read_b32 v41,a173\nv_accvgpr_read_b32 v42,a174\nv_accvgpr_read_b32 v43,a175\nv_cvt_pk_bf16_f32 v40,v40,v41\nv_cvt_pk_bf16_f32 v41,v42,v43\n"
"s_add_u32 s71,s71,1024\nbuffer_store_dwordx2 v[40:41],v255,s[60:63],s71 offen sc1\nv_mfma_f32_16x16x32_bf16 a[132:135],a[212:215],a[248:251],a[132:135]\nv_accvgpr_read_b32 v48,a120\nv_accvgpr_read_b32 v49,a121\nv_accvgpr_read_b32 v50,a122\nv_accvgpr_read_b32 v51,a123\nv_accvgpr_read_b32 v52,a148\n"
"v_accvgpr_read_b32 v53,a149\nv_accvgpr_read_b32 v54,a150\nv_accvgpr_read_b32 v55,a151\nv_mfma_f32_16x16x32_bf16 a[160:163],a[216:219],a[248:251],a[160:163]\nv_cvt_pk_bf16_f32 v48,v48,v49\nv_cvt_pk_bf16_f32 v49,v50,v51\nv_cvt_pk_bf16_f32 v50,v52,v53\nv_cvt_pk_bf16_f32 v51,v54,v55\n"
"s_add_u32 s71,s71,512\nbuffer_store_dwordx4 v[48:51],v239,s[60:63],s71 offen sc1\nv_accvgpr_read_b32 v56,a176\nv_mfma_f32_16x16x32_bf16 a[188:191],a[220:223],a[248:251],a[188:191]\nv_accvgpr_read_b32 v57,a177\nv_accvgpr_read_b32 v58,a178\nv_accvgpr_read_b32 v59,a179\nv_cvt_pk_bf16_f32 v56,v56,v57\n"
"v_cvt_pk_bf16_f32 v57,v58,v59\ns_add_u32 s71,s71,1024\nbuffer_store_dwordx2 v[56:57],v255,s[60:63],s71 offen sc1\nv_mfma_f32_16x16x32_bf16 a[136:139],a[212:215],a[252:255],a[136:139]\nv_accvgpr_read_b32 v64,a124\nv_accvgpr_read_b32 v65,a125\nv_accvgpr_read_b32 v66,a126\nv_accvgpr_read_b32 v67,a127\n"
"v_accvgpr_read_b32 v68,a152\nv_accvgpr_read_b32 v69,a153\nv_accvgpr_read_b32 v70,a154\nv_accvgpr_read_b32 v71,a155\nv_mfma_f32_16x16x32_bf16 a[164:167],a[216:219],a[252:255],a[164:167]\nv_cvt_pk_bf16_f32 v64,v64,v65\nv_cvt_pk_bf16_f32 v65,v66,v67\nv_cvt_pk_bf16_f32 v66,v68,v69\n"
"v_cvt_pk_bf16_f32 v67,v70,v71\ns_add_u32 s71,s71,512\nbuffer_store_dwordx4 v[64:67],v239,s[60:63],s71 offen sc1\nv_accvgpr_read_b32 v72,a180\nv_mfma_f32_16x16x32_bf16 a[192:195],a[220:223],a[252:255],a[192:195]\nv_accvgpr_read_b32 v73,a181\nv_accvgpr_read_b32 v74,a182\nv_accvgpr_read_b32 v75,a183\n"
"v_cvt_pk_bf16_f32 v72,v72,v73\nv_cvt_pk_bf16_f32 v73,v74,v75\ns_add_u32 s71,s71,1024\nbuffer_store_dwordx2 v[72:73],v255,s[60:63],s71 offen sc1\nv_accvgpr_read_b32 v80,a128\nv_accvgpr_read_b32 v81,a129\nv_accvgpr_read_b32 v82,a130\nv_accvgpr_read_b32 v83,a131\n"
"v_accvgpr_read_b32 v84,a156\nv_accvgpr_read_b32 v85,a157\nv_accvgpr_read_b32 v86,a158\nv_accvgpr_read_b32 v87,a159\nv_cvt_pk_bf16_f32 v80,v80,v81\nv_cvt_pk_bf16_f32 v81,v82,v83\nv_cvt_pk_bf16_f32 v82,v84,v85\nv_cvt_pk_bf16_f32 v83,v86,v87\n"
"s_add_u32 s71,s71,512\nbuffer_store_dwordx4 v[80:83],v239,s[60:63],s71 offen sc1\nv_accvgpr_read_b32 v88,a184\nv_accvgpr_read_b32 v89,a185\nv_accvgpr_read_b32 v90,a186\nv_accvgpr_read_b32 v91,a187\nv_cvt_pk_bf16_f32 v88,v88,v89\nv_cvt_pk_bf16_f32 v89,v90,v91\n"
"s_add_u32 s71,s71,1024\nbuffer_store_dwordx2 v[88:89],v255,s[60:63],s71 offen sc1\nv_accvgpr_read_b32 v0,a132\nv_accvgpr_read_b32 v1,a133\nv_accvgpr_read_b32 v2,a134\nv_accvgpr_read_b32 v3,a135\nv_accvgpr_read_b32 v4,a160\nv_accvgpr_read_b32 v5,a161\n"
"v_accvgpr_read_b32 v6,a162\nv_accvgpr_read_b32 v7,a163\nv_cvt_pk_bf16_f32 v0,v0,v1\nv_cvt_pk_bf16_f32 v1,v2,v3\nv_cvt_pk_bf16_f32 v2,v4,v5\nv_cvt_pk_bf16_f32 v3,v6,v7\ns_add_u32 s71,s71,512\nbuffer_store_dwordx4 v[0:3],v239,s[60:63],s71 offen sc1\n"
"v_accvgpr_read_b32 v8,a188\nv_accvgpr_read_b32 v9,a189\nv_accvgpr_read_b32 v10,a190\nv_accvgpr_read_b32 v11,a191\nv_cvt_pk_bf16_f32 v8,v8,v9\nv_cvt_pk_bf16_f32 v9,v10,v11\ns_add_u32 s71,s71,1024\nbuffer_store_dwordx2 v[8:9],v255,s[60:63],s71 offen sc1\n"
"v_accvgpr_read_b32 v16,a136\nv_accvgpr_read_b32 v17,a137\nv_accvgpr_read_b32 v18,a138\nv_accvgpr_read_b32 v19,a139\nv_accvgpr_read_b32 v20,a164\nv_accvgpr_read_b32 v21,a165\nv_accvgpr_read_b32 v22,a166\nv_accvgpr_read_b32 v23,a167\n"
"v_cvt_pk_bf16_f32 v16,v16,v17\nv_cvt_pk_bf16_f32 v17,v18,v19\nv_cvt_pk_bf16_f32 v18,v20,v21\nv_cvt_pk_bf16_f32 v19,v22,v23\ns_add_u32 s71,s71,512\nbuffer_store_dwordx4 v[16:19],v239,s[60:63],s71 offen sc1\nv_accvgpr_read_b32 v24,a192\nv_accvgpr_read_b32 v25,a193\n"
"v_accvgpr_read_b32 v26,a194\nv_accvgpr_read_b32 v27,a195\nv_cvt_pk_bf16_f32 v24,v24,v25\nv_cvt_pk_bf16_f32 v25,v26,v27\ns_add_u32 s71,s71,1024\nbuffer_store_dwordx2 v[24:25],v255,s[60:63],s71 offen sc1\ns_waitcnt vmcnt(0)\ns_barrier\n"
"s_cmp_lg_u32 s70,0\ns_cbranch_scc1 3f\nbuffer_store_dword v203,v201,s[96:99],0 offen offset:1024 sc1\ns_mov_b32 s72,16777216\n1:\nbuffer_load_dword v205,v200,s[96:99],0 offen offset:1024 sc1\nbuffer_load_dword v206,v200,s[96:99],0 offen sc1\ns_waitcnt vmcnt(1)\n"
"v_cmp_ne_u32 vcc,v205,v204\ns_nop 4\ns_cbranch_vccz 3f\ns_waitcnt vmcnt(0)\nv_cmp_ne_u32 vcc,v206,v203\ns_nop 4\ns_cbranch_vccz 2f\nv_mov_b32 v203,v206\n"
"buffer_store_dword v203,v201,s[96:99],0 offen offset:1024 sc1\n2:\ns_sub_u32 s72,s72,1\ns_cmp_lg_u32 s72,0\ns_cbranch_scc1 1b\n3:\ns_waitcnt vmcnt(0)\ns_barrier\n"
"78:\n"
  :: "s"(x), "s"(wq), "s"(k), "s"(v), "s"(wo), "s"(part), "s"(w), "s"(won), "s"(w), "s"((unsigned)(size_t)lds), "s"(b * 4 + w), "s"((hp & 1) * 4 + w), "s"(hp), "s"(flags), "s"(ob)
  : "memory", "vcc", "scc", "v0","v1","v2","v3","v4","v5","v6","v7","v8","v9","v10","v11","v12","v13","v14","v15","v16","v17","v18","v19","v20","v21","v22","v23","v24","v25","v26","v27","v28","v29","v30","v31","v32","v33","v34","v35","v36","v37","v38","v39","v40","v41","v42","v43","v44","v45","v46","v47","v48","v49","v50","v51","v52","v53","v54","v55","v56","v57","v58","v59","v60","v61","v62","v63","v64","v65","v66","v67","v68","v69","v70","v71","v72","v73","v74","v75","v76","v77","v78","v79","v80","v81","v82","v83","v84","v85","v86","v87","v88","v89","v90","v91","v92","v93","v94","v95","v96","v97","v98","v99","v100","v101","v102","v103","v104","v105","v106","v107","v108","v109","v110","v111","v112","v113","v114","v115","v116","v117","v118","v119","v120","v121","v122","v123","v124","v125","v126","v127","v128","v129","v130","v131","v132","v133","v134","v135","v136","v137","v138","v139","v140","v141","v142","v143","v144","v145","v146","v147","v148","v149","v150","v151","v152","v153","v154","v155","v156","v157","v158","v159","v160","v161","v162","v163","v164","v165","v166","v167","v168","v169","v170","v171","v172","v173","v174","v175","v176","v177","v178","v179","v180","v181","v182","v183","v184","v185","v186","v187","v188","v189","v190","v191","v192","v193","v194","v195","v196","v197","v198","v199","v200","v201","v202","v203","v204","v205","v206","v207","v208","v209","v210","v211","v212","v213","v214","v215","v216","v217","v218","v219","v220","v221","v222","v223","v224","v225","v226","v227","v228","v229","v230","v231","v232","v233","v234","v235","v236","v237","v238","v239","v240","v241","v242","v243","v244","v245","v246","v247","v248","v249","v250","v251","v252","v253","v254","v255","a0","a1","a2","a3","a4","a5","a6","a7","a8","a9","a10","a11","a12","a13","a14","a15","a16","a17","a18","a19","a20","a21","a22","a23","a24","a25","a26","a27","a28","a29","a30","a31","a32","a33","a34","a35","a36","a37","a38","a39","a40","a41","a42","a43","a44","a45","a46","a47","a48","a49","a50","a51","a52","a53","a54","a55","a56","a57","a58","a59","a60","a61","a62","a63","a64","a65","a66","a67","a68","a69","a70","a71","a72","a73","a74","a75","a76","a77","a78","a79","a80","a81","a82","a83","a84","a85","a86","a87","a88","a89","a90","a91","a92","a93","a94","a95","a96","a97","a98","a99","a100","a101","a102","a103","a104","a105","a106","a107","a108","a109","a110","a111","a112","a113","a114","a115","a116","a117","a118","a119","a120","a121","a122","a123","a124","a125","a126","a127","a128","a129","a130","a131","a132","a133","a134","a135","a136","a137","a138","a139","a140","a141","a142","a143","a144","a145","a146","a147","a148","a149","a150","a151","a152","a153","a154","a155","a156","a157","a158","a159","a160","a161","a162","a163","a164","a165","a166","a167","a168","a169","a170","a171","a172","a173","a174","a175","a176","a177","a178","a179","a180","a181","a182","a183","a184","a185","a186","a187","a188","a189","a190","a191","a192","a193","a194","a195","a196","a197","a198","a199","a200","a201","a202","a203","a204","a205","a206","a207","a208","a209","a210","a211","a212","a213","a214","a215","a216","a217","a218","a219","a220","a221","a222","a223","a224","a225","a226","a227","a228","a229","a230","a231","a232","a233","a234","a235","a236","a237","a238","a239","a240","a241","a242","a243","a244","a245","a246","a247","a248","a249","a250","a251","a252","a253","a254","a255","s34","s35","s36","s37","s38","s39","s40","s41","s42","s43","s44","s45","s46","s47","s48","s49","s50","s51","s52","s53","s54","s55","s56","s57","s58","s59","s60","s61","s62","s63","s64","s65","s66","s67","s68","s69","s70","s71","s72","s73","s74","s75","s76","s77","s78","s79","s80","s81","s82","s83","s84","s85","s86","s87","s88","s89","s90","s91","s92","s93","s94","s95","s96","s97","s98","s99");
  if (1) {
    unsigned hq = hp, wv = w; char* pp = part; char* fl = flags; char* op = ob;
    asm volatile("" : "+s"(hq), "+s"(wv), "+s"(pp), "+s"(fl), "+s"(op) :: "memory");
    const unsigned lane = __builtin_amdgcn_mbcnt_hi(~0u, __builtin_amdgcn_mbcnt_lo(~0u, 0u));
    const __amdgpu_buffer_rsrc_t rp = __builtin_amdgcn_make_buffer_rsrc(pp - (size_t)hq * 100352, 0, 802816, 0x20000);
    const __amdgpu_buffer_rsrc_t rf = __builtin_amdgcn_make_buffer_rsrc(fl, 0, 2048, 0x20000);
    const unsigned oob = (wv == 0 && lane < 8) ? 0u : 0x10000u;
    const unsigned q = __builtin_amdgcn_raw_buffer_load_b32(rf, oob + hq * 128 + lane * 4, 0, 16);
    u4 r[4][8];
    const unsigned e0 = hq * 784 + wv * 196 + lane;
#pragma unroll
    for (int i = 0; i < 4; i++)
#pragma unroll
      for (int h = 0; h < 8; h++) {
        __builtin_amdgcn_sched_barrier(0);
        r[i][h] = __builtin_amdgcn_raw_buffer_load_b128(rp, (i < 3 || lane < 4) ? (e0 + i * 64) * 16 : 0x80000000u, h * 100352, 16);
      }
    const __amdgpu_buffer_rsrc_t ro = __builtin_amdgcn_make_buffer_rsrc(op, 0, 200704 - 896, 0x20000);
    u2 at[4]; u4 o[4];
    __builtin_amdgcn_sched_barrier(0);
#pragma unroll
    for (int i = 0; i < 4; i++) at[i] = where(e0 + i * 64) | ((i < 3 || lane < 4) ? 0u : 0x80000000u);
#pragma unroll
    for (int i = 0; i < 4; i++) { __builtin_amdgcn_sched_barrier(0); o[i] = sum8(r[i]); }
    __builtin_amdgcn_sched_barrier(0);
#pragma unroll
    for (int i = 0; i < 4; i++) {
      __builtin_amdgcn_raw_buffer_store_b64(o[i].xy, ro, at[i].x, 0, 0);
      __builtin_amdgcn_raw_buffer_store_b64(o[i].zw, ro, at[i].y, 0, 0);
    }
    __builtin_amdgcn_raw_buffer_store_b32(q, rf, oob + 1024 + lane * 128 + hq * 4, 0, 16);
  }
}

extern "C" void kernel_launch(void* const* d_in, const int* in_sizes, int n_in, void* d_out, int out_size,
                              void* d_ws, size_t ws_size, hipStream_t stream) {
  Args a;
  for (int i = 0; i < 10; i++) a.p[i] = (const char*)d_in[i];
  a.part = (char*)d_ws;
  a.out = (char*)d_out;
  attn_fused<<<256, 256, 0, stream>>>(a);
}
```
